```python
import torch

def _softmax(x, axis=-1):
    return torch.softmax(x, dim=axis)
def _gelu(x):
    return torch.nn.functional.gelu(x, approximate="tanh")
def _silu(x):
    return torch.nn.functional.silu(x)
def _sigmoid(x):
    return torch.sigmoid(x)
def _layernorm(x, axis=-1, eps=1e-5):
    m = x.mean(dim=axis, keepdim=True)
    v = x.var(dim=axis, keepdim=True, unbiased=False)
    return (x - m) / torch.sqrt(v + eps)
def _scatter_add(tgt, idx, src, axis=0):
    return tgt.clone().index_add_(axis, idx, src)
def _segment_sum(src, seg, n):
    out = torch.zeros((n,) + src.shape[1:], dtype=src.dtype)
    return out.index_add_(0, seg, src)
def _pad(x, pw):
    flat = tuple(v for pair in reversed(pw) for v in pair)
    return torch.nn.functional.pad(x, flat)

kernel_name = 'standalone__cheap_equivariant_egnn__graph'

def setup_inputs():

    _w = (lambda a: (lambda t: t.to(torch.bfloat16) if t.is_floating_point() else t)(torch.as_tensor(a)))
    N, deg, D, De = 50000, 16, 64, max(16, 1)
    E = N * deg
    return {
        "h": _w((lambda *s: torch.randn(*s, dtype=torch.bfloat16) * 0.2)(N, D)),
        "pos": _w((lambda *s: torch.randn(*s, dtype=torch.bfloat16) * 0.8)(N, 3)),
        "e_attr": _w((lambda *s: torch.randn(*s, dtype=torch.bfloat16) * 0.8)(E, De)),
        "src": _w((lambda lo, hi, *s: torch.randint(lo, hi, s))(0, N, E).to(torch.int64)),
        "dst": _w((lambda lo, hi, *s: torch.randint(lo, hi, s))(0, N, E).to(torch.int64)),
        "w_e": _w(0.1 * (lambda *s: torch.randn(*s, dtype=torch.bfloat16) * 0.8)(2 * D + 1 + De, D)),
        "w_x": _w(0.1 * (lambda *s: torch.randn(*s, dtype=torch.bfloat16) * 0.2)(D, 1)),
        "w_h": _w(0.1 * (lambda *s: torch.randn(*s, dtype=torch.bfloat16) * 0.2)(2 * D, D)),
    }

def reference(h, pos, e_attr, src, dst, w_e, w_x, w_h):
    N, D = h.shape[0], h.shape[1]
    diff = pos[dst] - pos[src]
    dist2 = torch.sum(diff * diff, axis=-1, keepdims=True)
    e_in = torch.concatenate([h[dst], h[src], dist2, e_attr], axis=-1)
    m_ij = _silu(e_in @ w_e)
    coord_scale = m_ij @ w_x
    pos_new = pos + _segment_sum(diff * coord_scale, dst, N)
    m_i = _segment_sum(m_ij, dst, N)
    h_new = h + _silu(torch.concatenate([h, m_i], axis=-1) @ w_h)
    return h_new, pos_new
```

```cpp
#include <hip/hip_runtime.h>
#include <stdint.h>
#include <type_traits>

#define FOLD_LOG2E 1

namespace {

typedef __bf16 bf16x8 __attribute__((ext_vector_type(8)));
typedef __bf16 bf16x2 __attribute__((ext_vector_type(2)));
typedef float f32x16 __attribute__((ext_vector_type(16)));
typedef float f32x4 __attribute__((ext_vector_type(4)));
typedef unsigned u32x4 __attribute__((ext_vector_type(4)));
typedef unsigned u32x2 __attribute__((ext_vector_type(2)));
typedef unsigned u32x8 __attribute__((ext_vector_type(8)));
typedef unsigned u32x16 __attribute__((ext_vector_type(16)));
typedef unsigned u32x32 __attribute__((ext_vector_type(32)));
typedef short s16x4 __attribute__((ext_vector_type(4)));
typedef __attribute__((address_space(3))) s16x4* lds_s16x4_p;

#define DI __device__ __forceinline__
#define MFMA(a, b, c) __builtin_amdgcn_mfma_f32_32x32x16_bf16((a), (b), (c), 0, 0, 0)

#define POLL_SLEEP 2
#define HPF 0
#define EPF 2
#define OUT_AUX 16
constexpr int NBK = 256;
constexpr int LG = 16;
constexpr int NB = 196;
constexpr int CH = 3136;
constexpr float LOG2E = 1.4426950408889634f;
constexpr float LN2 = 0.6931471805599453f;

constexpr size_t WS_EPOCH = 0;
constexpr size_t WS_TAB = 256;
constexpr size_t WS_REC = WS_TAB + (size_t)NBK * NBK * 8;

constexpr int W_STH = 0, W_STE = 4096, W_STP = 5120, W_SI = 5376, W_TI = W_SI + 26 * 80, WSZ = W_TI + 256;
constexpr int DUMMY = 25;
constexpr int MIP = 144, MISZ = 32 * MIP;
constexpr int MIL = 80;
constexpr int SEG = 17 * 256;
constexpr int L_CNT = 0;
constexpr int L_BASE = L_CNT + 2048;
constexpr int L_FLAG = L_CNT + 512;
constexpr int L_REC = L_BASE + 2048;
constexpr int L_LUT = L_REC + 8 * SEG + 512;
constexpr int L_WAVE = L_LUT + 26 * 80;
constexpr int L_MI = L_WAVE + 8 * WSZ;
constexpr int L_WJ = L_MI + 8 * MISZ;
constexpr int L_WH = L_WJ + 6 * 1024;
constexpr int L_TOTAL = L_WH + 16 * 1024;
static_assert(L_TOTAL <= 163840, "LDS");
static_assert(WSZ % 16 == 0 && L_MI % 16 == 0 && L_LUT % 16 == 0, "align");

#pragma clang diagnostic ignored "-Wint-to-pointer-cast"
#define LDS(T, off) (*(__attribute__((address_space(3))) T*)(unsigned)(off))
DI unsigned mul24(unsigned a, unsigned b) { return (unsigned)__umul24(a, b); }
DI float bf2f(unsigned lo16) { return __uint_as_float(lo16 << 16); }
DI unsigned pk_bf16(float a, float b) { bf16x2 v = {(__bf16)a, (__bf16)b}; return __builtin_bit_cast(unsigned, v); }
DI bf16x8 as_bf(u32x4 v) { return __builtin_bit_cast(bf16x8, v); }
DI int opaque(int x) { asm volatile("" : "+v"(x)); return x; }
DI int lane_id() {
  int x; asm volatile("v_mbcnt_lo_u32_b32 %0, -1, 0\n\tv_mbcnt_hi_u32_b32 %0, -1, %0" : "=v"(x)); return x;
}
DI void wait_vm0() { asm volatile("s_waitcnt vmcnt(0)" ::: "memory"); }
DI void wait_lgkm0() { asm volatile("s_waitcnt lgkmcnt(0)" ::: "memory"); }
DI void barrier_lds() { asm volatile("s_waitcnt lgkmcnt(0)\n\ts_barrier" ::: "memory"); }
DI void dma16(const void* g, unsigned off, unsigned l) {
  unsigned keep;
  asm volatile("s_mov_b32 %0, m0\n\ts_mov_b32 m0, %3\n\ts_nop 0\n\tglobal_load_lds_dwordx4 %1, %2\n\ts_mov_b32 m0, %0"
               : "=&s"(keep) : "v"(off), "s"(g), "s"(l) : "memory");
}
DI void dma4(const void* g, unsigned off, unsigned l) {
  unsigned keep;
  asm volatile("s_mov_b32 %0, m0\n\ts_mov_b32 m0, %3\n\ts_nop 0\n\tglobal_load_lds_dword %1, %2\n\ts_mov_b32 m0, %0"
               : "=&s"(keep) : "v"(off), "s"(g), "s"(l) : "memory");
}
DI void dma_tile(const void* hm, const void* ea, const void* ps, unsigned o0, unsigned o1, unsigned o2, unsigned o3,
                 unsigned oe, unsigned op, unsigned l) {
  unsigned keep;
  asm volatile("s_mov_b32 %0, m0\n\t"
               "s_add_u32 m0, %10, 0x1000\n\ts_nop 0\n\tglobal_load_lds_dwordx4 %5, %8\n\t"
               "s_mov_b32 m0, %10\n\ts_nop 0\n\tglobal_load_lds_dwordx4 %1, %7\n\t"
               "s_add_u32 m0, %10, 0x400\n\ts_nop 0\n\tglobal_load_lds_dwordx4 %2, %7\n\t"
               "s_add_u32 m0, %10, 0x800\n\ts_nop 0\n\tglobal_load_lds_dwordx4 %3, %7\n\t"
               "s_add_u32 m0, %10, 0xc00\n\ts_nop 0\n\tglobal_load_lds_dwordx4 %4, %7\n\t"
               "s_add_u32 m0, %10, 0x1400\n\ts_nop 0\n\tglobal_load_lds_dword %6, %9\n\t"
               "s_mov_b32 m0, %0"
               : "=&s"(keep) : "v"(o0), "v"(o1), "v"(o2), "v"(o3), "v"(oe), "v"(op), "s"(hm), "s"(ea), "s"(ps), "s"(l)
               : "memory", "scc");
}
template <int CTRL, int ROWMASK> DI unsigned dpp_add(unsigned x) {
  return x + (unsigned)__builtin_amdgcn_update_dpp(0, (int)x, CTRL, ROWMASK, 0xf, false);
}
DI unsigned wave_scan_incl(unsigned x) {
  x = dpp_add<0x111, 0xf>(x); x = dpp_add<0x112, 0xf>(x); x = dpp_add<0x114, 0xf>(x); x = dpp_add<0x118, 0xf>(x);
  x = dpp_add<0x142, 0xa>(x); x = dpp_add<0x143, 0xc>(x);
  return x;
}
#define STAMP(i)
#define STAMP_ARG

#define CLOBBER_V "v100", "v101", "v102", "v103", "v104", "v105", "v106", "v107", "v108", "v109", "v110", "v111", "v112", "v113", "v114", "v115", "v116", "v117", "v118", "v119", "v120", "v121", "v122", "v123", "v124", "v125", "v126", "v127", "v128", "v129", "v130", "v131", "v132", "v133", "v134", "v135", "v136", "v137", "v138", "v139", "v140", "v141", "v142", "v143", "v144", "v145", "v146", "v147", "v148", "v149", "v150", "v151", "v152", "v153", "v154", "v155", "v156", "v157", "v158", "v159", "v160", "v161", "v162", "v163", "v164", "v165", "v166", "v167", "v168", "v169", "v170", "v171", "v172", "v173", "v174", "v175", "v176", "v177", "v178", "v179", "v180", "v181", "v182", "v183", "v184", "v185", "v186", "v187", "v188", "v189", "v190", "v191", "v192", "v193", "v194", "v195", "v196", "v197", "v198", "v199", "v200", "v201", "v202", "v203", "v204", "v205", "v206", "v207", "v208", "v209", "v210", "v211", "v212", "v213", "v214", "v215", "v216", "v217", "v218", "v219", "v220", "v221", "v222", "v223", "v224", "v225", "v226", "v227", "v228", "v229", "v230", "v231"
#define LOOP_ASM_FOLD \
  "s_mov_b32 %[keep], m0\n\t" \
  "s_mov_b64 vcc, %[hsel]\n\t" \
  "ds_read2_b32 v[220:221], v252 offset0:0 offset1:2\n\t" \
  "ds_read2_b32 v[222:223], v252 offset0:4 offset1:6\n\t" \
  "ds_read_b64 v[226:227], v253\n\t" \
  "ds_read_b32 v232, v254\n\t" \
  "ds_read_b128 v[212:215], v247 offset:0\n\t" \
  "v_mov_b32 v176, 0\n\t" \
  "v_mov_b32 v177, 0\n\t" \
  "v_mov_b32 v178, 0\n\t" \
  "v_mov_b32 v179, 0\n\t" \
  "v_mov_b32 v180, 0\n\t" \
  "v_mov_b32 v181, 0\n\t" \
  "v_mov_b32 v182, 0\n\t" \
  "v_mov_b32 v183, 0\n\t" \
  "v_mov_b32 v184, 0\n\t" \
  "v_mov_b32 v185, 0\n\t" \
  "v_mov_b32 v186, 0\n\t" \
  "v_mov_b32 v187, 0\n\t" \
  "v_mov_b32 v188, 0\n\t" \
  "v_mov_b32 v189, 0\n\t" \
  "v_mov_b32 v190, 0\n\t" \
  "v_mov_b32 v191, 0\n\t" \
  "v_mov_b32 v230, 0\n\t" \
  "v_mov_b32 v231, 0\n\t" \
  "v_mov_b32 v208, 0\n\t" \
  "v_mov_b32 v209, 0\n\t" \
  "v_mov_b32 v210, 0\n\t" \
  "v_mov_b32 v211, 0\n\t" \
  "ds_write_b128 v243, v[184:187] offset:0\n\t" \
  "s_nop 0\n\t" \
  "v_mfma_f32_32x32x16_bf16 v[132:147], v[184:187], v[188:191], 0\n\t" \
  "v_add_u32 v252, 0x100, v252\n\t" \
  "v_add_u32 v253, 0x100, v253\n\t" \
  "v_add_u32 v254, 0x100, v254\n\t" \
  "s_waitcnt lgkmcnt(0)\n\t" \
  "v_lshrrev_b32 v232, 20, v232\n\t" \
  "v_add_u32 v233, %[posd], v232\n\t" \
  "ds_read_b128 v[216:219], v233\n\t" \
  "v_add_u32 v233, v240, v232\n\t" \
  "ds_read_b128 v[172:175], v233 offset:32\n\t" \
  "ds_read_b128 v[168:171], v233\n\t" \
  "v_add_u32 v233, v241, v232\n\t" \
  "ds_write_b16_d16_hi v233, v255 offset:5376\n\t" \
  "v_mad_u32_u16 v220, v220, %[c128], v248\n\t" \
  "v_mad_u32_u16 v221, v221, %[c128], v249\n\t" \
  "v_mad_u32_u16 v222, v222, %[c128], v248\n\t" \
  "v_mad_u32_u16 v223, v223, %[c128], v249\n\t" \
  "v_or_b32 v226, v226, v250\n\t" \
  "v_mad_u32_u16 v227, v227, 6, v251\n\t" \
  "s_waitcnt vmcnt(0)\n\t" \
  "ds_read_b128 v[148:151], v234\n\t" \
  "ds_read_b128 v[152:155], v235\n\t" \
  "ds_read_b128 v[156:159], v236\n\t" \
  "ds_read_b128 v[160:163], v237\n\t" \
  "ds_read_b128 v[164:167], v238 offset:4096\n\t" \
  "ds_read_b64 v[224:225], v239 offset:5120\n\t" \
  "s_waitcnt lgkmcnt(0)\n\t" \
  "s_cmp_lt_i32 %[nt], 1\n\t" \
  "s_cbranch_scc1 7f\n\t" \
  "s_mov_b32 m0, %[b2]\n\t" \
  "s_nop 0\n\t" \
  "global_load_lds_dwordx4 v226, %[ea]\n\t" \
  "global_load_lds_dword v227, %[pos] offset:1024\n\t" \
  "s_mov_b32 m0, %[b1]\n\t" \
  "s_nop 0\n\t" \
  "global_load_lds_dwordx4 v220, %[h0]\n\t" \
  "global_load_lds_dwordx4 v221, %[h1] offset:1024\n\t" \
  "global_load_lds_dwordx4 v222, %[h2] offset:2048\n\t" \
  "global_load_lds_dwordx4 v223, %[h3] offset:3072\n\t" \
  "7:\n\t" \
  "ds_read2_b32 v[220:221], v252 offset0:0 offset1:2\n\t" \
  "ds_read2_b32 v[222:223], v252 offset0:4 offset1:6\n\t" \
  "ds_read_b64 v[226:227], v253\n\t" \
  "ds_read_b32 v232, v254\n\t" \
  "v_add_u32 v252, 0x100, v252\n\t" \
  "v_add_u32 v253, 0x100, v253\n\t" \
  "v_add_u32 v254, 0x100, v254\n\t" \
  "v_and_b32 v225, 0xffff0000, v225\n\t" \
  "v_lshlrev_b32 v219, 16, v224\n\t" \
  "v_and_b32 v224, 0xffff0000, v224\n\t" \
  "v_sub_f32 v229, v218, v225\n\t" \
  "v_sub_f32 v216, v216, v219\n\t" \
  "v_sub_f32 v217, v217, v224\n\t" \
  "v_mul_f32 v218, v229, v229\n\t" \
  "v_cndmask_b32 v228, v216, v217, vcc\n\t" \
  "v_fmac_f32 v218, v216, v216\n\t" \
  "v_fmac_f32 v218, v217, v217\n\t" \
  "v_cvt_pk_bf16_f32 v218, v218, v218\n\t" \
  "v_cndmask_b32_sdwa v175, v175, v218, vcc dst_sel:WORD_1 dst_unused:UNUSED_PRESERVE src0_sel:WORD_1 src1_sel:WORD_1\n\t" \
  "v_mfma_f32_32x32x16_bf16 v[100:115], v[4:7], v[148:151], 0\n\t" \
  "v_mfma_f32_32x32x16_bf16 v[100:115], v[12:15], v[152:155], v[100:115]\n\t" \
  "v_mfma_f32_32x32x16_bf16 v[100:115], v[20:23], v[156:159], v[100:115]\n\t" \
  "v_mfma_f32_32x32x16_bf16 v[100:115], v[28:31], v[160:163], v[100:115]\n\t" \
  "v_mfma_f32_32x32x16_bf16 v[100:115], v[36:39], v[168:171], v[100:115]\n\t" \
  "v_mfma_f32_32x32x16_bf16 v[100:115], v[212:215], v[164:167], v[100:115]\n\t" \
  "v_mfma_f32_32x32x16_bf16 v[100:115], v[44:47], v[172:175], v[100:115]\n\t" \
  "s_cmp_eq_u32 %[nt], 0\n\t" \
  "s_nop 10\n\t" \
  "v_exp_f32 v192, v100\n\t" \
  "v_exp_f32 v193, v101\n\t" \
  "v_exp_f32 v194, v102\n\t" \
  "v_exp_f32 v195, v103\n\t" \
  "s_nop 0\n\t" \
  "v_add_f32 v192, 1.0, v192\n\t" \
  "v_add_f32 v193, 1.0, v193\n\t" \
  "v_add_f32 v194, 1.0, v194\n\t" \
  "v_add_f32 v195, 1.0, v195\n\t" \
  "s_cbranch_scc1 6f\n\t" \
  "s_cmp_lt_i32 %[thr], 0\n\t" \
  "s_cbranch_scc1 1f\n\t" \
  "s_setprio 1\n\t" \
  "1:\n\t" \
  "ds_write_b128 v243, v[188:191] offset:16\n\t" \
  "ds_read_b64_tr_b16 v[200:201], v244 offset:0\n\t" \
  "ds_read_b64_tr_b16 v[202:203], v244 offset:320\n\t" \
  "ds_read_b64_tr_b16 v[204:205], v244 offset:1280\n\t" \
  "ds_read_b64_tr_b16 v[206:207], v244 offset:1600\n\t" \
  "ds_read_b128 v[212:215], v247 offset:1024\n\t" \
  "v_mfma_f32_32x32x16_bf16 v[132:147], v[208:211], v[188:191], v[132:147]\n\t" \
  "v_rcp_f32 v192, v192\n\t" \
  "v_rcp_f32 v193, v193\n\t" \
  "v_rcp_f32 v194, v194\n\t" \
  "v_rcp_f32 v195, v195\n\t" \
  "v_mfma_f32_32x32x16_bf16 v[116:131], v[8:11], v[148:151], 0\n\t" \
  "v_exp_f32 v196, v104\n\t" \
  "v_exp_f32 v197, v105\n\t" \
  "v_exp_f32 v198, v106\n\t" \
  "v_exp_f32 v199, v107\n\t" \
  "v_mfma_f32_32x32x16_bf16 v[116:131], v[16:19], v[152:155], v[116:131]\n\t" \
  "v_mul_f32 v192, v100, v192\n\t" \
  "v_mul_f32 v193, v101, v193\n\t" \
  "v_mul_f32 v194, v102, v194\n\t" \
  "v_mul_f32 v195, v103, v195\n\t" \
  "v_add_f32 v196, 1.0, v196\n\t" \
  "v_add_f32 v197, 1.0, v197\n\t" \
  "v_add_f32 v198, 1.0, v198\n\t" \
  "v_add_f32 v199, 1.0, v199\n\t" \
  "v_cvt_pk_bf16_f32 v184, v192, v193\n\t" \
  "v_cvt_pk_bf16_f32 v185, v194, v195\n\t" \
  "v_mfma_f32_32x32x16_bf16 v[116:131], v[24:27], v[156:159], v[116:131]\n\t" \
  "v_rcp_f32 v196, v196\n\t" \
  "v_rcp_f32 v197, v197\n\t" \
  "v_rcp_f32 v198, v198\n\t" \
  "v_rcp_f32 v199, v199\n\t" \
  "s_waitcnt lgkmcnt(3)\n\t" \
  "v_mfma_f32_32x32x16_bf16 v[68:83], v[200:203], v[176:179], v[68:83]\n\t" \
  "v_exp_f32 v192, v108\n\t" \
  "v_exp_f32 v193, v109\n\t" \
  "v_exp_f32 v194, v110\n\t" \
  "v_exp_f32 v195, v111\n\t" \
  "v_mfma_f32_32x32x16_bf16 v[116:131], v[32:35], v[160:163], v[116:131]\n\t" \
  "v_mul_f32 v196, v104, v196\n\t" \
  "v_mul_f32 v197, v105, v197\n\t" \
  "v_mul_f32 v198, v106, v198\n\t" \
  "v_mul_f32 v199, v107, v199\n\t" \
  "v_mul_f32 v230, v230, v132\n\t" \
  "v_mul_f32 v231, v231, v132\n\t" \
  "v_cvt_pk_bf16_f32 v230, v230, v231\n\t" \
  "ds_write_b16 v245, v230 offset:7456\n\t" \
  "ds_write_b16_d16_hi v241, v230 offset:7584\n\t" \
  "ds_read_b128 v[200:203], v246 offset:7456\n\t" \
  "ds_read_b128 v[208:211], v246 offset:7488\n\t" \
  "v_mov_b32 v230, v228\n\t" \
  "v_mov_b32 v231, v229\n\t" \
  "v_add_f32 v192, 1.0, v192\n\t" \
  "v_add_f32 v193, 1.0, v193\n\t" \
  "v_add_f32 v194, 1.0, v194\n\t" \
  "v_add_f32 v195, 1.0, v195\n\t" \
  "v_cvt_pk_bf16_f32 v186, v196, v197\n\t" \
  "v_cvt_pk_bf16_f32 v187, v198, v199\n\t" \
  "v_mfma_f32_32x32x16_bf16 v[116:131], v[40:43], v[168:171], v[116:131]\n\t" \
  "v_rcp_f32 v192, v192\n\t" \
  "v_rcp_f32 v193, v193\n\t" \
  "v_rcp_f32 v194, v194\n\t" \
  "v_rcp_f32 v195, v195\n\t" \
  "s_sub_u32 %[nt], %[nt], 1\n\t" \
  "s_cmp_lg_u32 %[nt], %[thr]\n\t" \
  "s_cbranch_scc1 3f\n\t" \
  "s_setprio 0\n\t" \
  "3:\n\t" \
  "s_waitcnt lgkmcnt(4)\n\t" \
  "v_mfma_f32_32x32x16_bf16 v[116:131], v[212:215], v[164:167], v[116:131]\n\t" \
  "v_exp_f32 v196, v112\n\t" \
  "v_exp_f32 v197, v113\n\t" \
  "v_exp_f32 v198, v114\n\t" \
  "v_exp_f32 v199, v115\n\t" \
  "v_mfma_f32_32x32x16_bf16 v[116:131], v[48:51], v[172:175], v[116:131]\n\t" \
  "v_mul_f32 v192, v108, v192\n\t" \
  "v_mul_f32 v193, v109, v193\n\t" \
  "v_mul_f32 v194, v110, v194\n\t" \
  "v_mul_f32 v195, v111, v195\n\t" \
  "s_waitcnt vmcnt(0)\n\t" \
  "ds_read_b128 v[148:151], v234\n\t" \
  "ds_read_b128 v[152:155], v235\n\t" \
  "ds_read_b128 v[156:159], v236\n\t" \
  "ds_read_b128 v[160:163], v237\n\t" \
  "ds_read_b128 v[164:167], v238 offset:4096\n\t" \
  "ds_read_b64 v[224:225], v239 offset:5120\n\t" \
  "ds_write_b128 v243, v[184:187] offset:0\n\t" \
  "ds_read_b128 v[212:215], v247 offset:2048\n\t" \
  "v_add_f32 v196, 1.0, v196\n\t" \
  "v_add_f32 v197, 1.0, v197\n\t" \
  "v_add_f32 v198, 1.0, v198\n\t" \
  "v_add_f32 v199, 1.0, v199\n\t" \
  "v_cvt_pk_bf16_f32 v188, v192, v193\n\t" \
  "v_cvt_pk_bf16_f32 v189, v194, v195\n\t" \
  "v_mad_u32_u16 v220, v220, %[c128], v248\n\t" \
  "v_mad_u32_u16 v221, v221, %[c128], v249\n\t" \
  "v_mad_u32_u16 v222, v222, %[c128], v248\n\t" \
  "v_mfma_f32_32x32x16_bf16 v[68:83], v[204:207], v[180:183], v[68:83]\n\t" \
  "v_rcp_f32 v196, v196\n\t" \
  "v_rcp_f32 v197, v197\n\t" \
  "v_rcp_f32 v198, v198\n\t" \
  "v_rcp_f32 v199, v199\n\t" \
  "v_mad_u32_u16 v223, v223, %[c128], v249\n\t" \
  "v_or_b32 v226, v226, v250\n\t" \
  "v_mad_u32_u16 v227, v227, 6, v251\n\t" \
  "s_waitcnt lgkmcnt(9)\n\t" \
  "v_mfma_f32_32x32x16_bf16 v[84:99], v[200:203], v[176:179], v[84:99]\n\t" \
  "v_exp_f32 v192, v116\n\t" \
  "v_exp_f32 v193, v117\n\t" \
  "v_exp_f32 v194, v118\n\t" \
  "v_exp_f32 v195, v119\n\t" \
  "s_waitcnt lgkmcnt(8)\n\t" \
  "v_mfma_f32_32x32x16_bf16 v[84:99], v[208:211], v[180:183], v[84:99]\n\t" \
  "v_mul_f32 v196, v112, v196\n\t" \
  "v_mul_f32 v197, v113, v197\n\t" \
  "v_mul_f32 v198, v114, v198\n\t" \
  "v_mul_f32 v199, v115, v199\n\t" \
  "s_waitcnt lgkmcnt(2)\n\t" \
  "s_cmp_lt_i32 %[nt], 1\n\t" \
  "s_cbranch_scc1 4f\n\t" \
  "s_mov_b32 m0, %[b2]\n\t" \
  "s_nop 0\n\t" \
  "global_load_lds_dwordx4 v226, %[ea]\n\t" \
  "global_load_lds_dword v227, %[pos] offset:1024\n\t" \
  "s_mov_b32 m0, %[b1]\n\t" \
  "s_nop 0\n\t" \
  "global_load_lds_dwordx4 v220, %[h0]\n\t" \
  "global_load_lds_dwordx4 v221, %[h1] offset:1024\n\t" \
  "global_load_lds_dwordx4 v222, %[h2] offset:2048\n\t" \
  "global_load_lds_dwordx4 v223, %[h3] offset:3072\n\t" \
  "4:\n\t" \
  "v_add_f32 v192, 1.0, v192\n\t" \
  "v_add_f32 v193, 1.0, v193\n\t" \
  "v_add_f32 v194, 1.0, v194\n\t" \
  "v_add_f32 v195, 1.0, v195\n\t" \
  "v_cvt_pk_bf16_f32 v190, v196, v197\n\t" \
  "v_cvt_pk_bf16_f32 v191, v198, v199\n\t" \
  "v_lshrrev_b32 v232, 20, v232\n\t" \
  "v_add_u32 v220, %[posd], v232\n\t" \
  "v_add_u32 v221, v240, v232\n\t" \
  "ds_write_b128 v243, v[188:191] offset:16\n\t" \
  "ds_read_b128 v[176:179], v242 offset:5376\n\t" \
  "ds_read_b128 v[180:183], v242 offset:5408\n\t" \
  "ds_write_b16 v233, v255 offset:5376\n\t" \
  "ds_read_b128 v[216:219], v220\n\t" \
  "ds_read_b128 v[172:175], v221 offset:32\n\t" \
  "ds_read_b128 v[168:171], v221\n\t" \
  "ds_read_b64_tr_b16 v[200:201], v244 offset:0\n\t" \
  "ds_read_b64_tr_b16 v[202:203], v244 offset:320\n\t" \
  "ds_read_b64_tr_b16 v[204:205], v244 offset:1280\n\t" \
  "ds_read_b64_tr_b16 v[206:207], v244 offset:1600\n\t" \
  "ds_read_b128 v[208:211], v247 offset:3072\n\t" \
  "v_add_u32 v233, v241, v232\n\t" \
  "ds_write_b16_d16_hi v233, v255 offset:5376\n\t" \
  "s_waitcnt lgkmcnt(13)\n\t" \
  "v_mfma_f32_32x32x16_bf16 v[132:147], v[212:215], v[184:187], 0\n\t" \
  "v_rcp_f32 v192, v192\n\t" \
  "v_rcp_f32 v193, v193\n\t" \
  "v_rcp_f32 v194, v194\n\t" \
  "v_rcp_f32 v195, v195\n\t" \
  "v_mfma_f32_32x32x16_bf16 v[100:115], v[4:7], v[148:151], 0\n\t" \
  "v_exp_f32 v196, v120\n\t" \
  "v_exp_f32 v197, v121\n\t" \
  "v_exp_f32 v198, v122\n\t" \
  "v_exp_f32 v199, v123\n\t" \
  "v_mfma_f32_32x32x16_bf16 v[100:115], v[12:15], v[152:155], v[100:115]\n\t" \
  "v_mul_f32 v192, v116, v192\n\t" \
  "v_mul_f32 v193, v117, v193\n\t" \
  "v_mul_f32 v194, v118, v194\n\t" \
  "v_mul_f32 v195, v119, v195\n\t" \
  "v_add_f32 v196, 1.0, v196\n\t" \
  "v_add_f32 v197, 1.0, v197\n\t" \
  "v_add_f32 v198, 1.0, v198\n\t" \
  "v_add_f32 v199, 1.0, v199\n\t" \
  "v_cvt_pk_bf16_f32 v184, v192, v193\n\t" \
  "v_cvt_pk_bf16_f32 v185, v194, v195\n\t" \
  "v_and_b32 v225, 0xffff0000, v225\n\t" \
  "s_waitcnt lgkmcnt(8)\n\t" \
  "v_lshlrev_b32 v219, 16, v224\n\t" \
  "v_and_b32 v224, 0xffff0000, v224\n\t" \
  "v_sub_f32 v229, v218, v225\n\t" \
  "v_sub_f32 v216, v216, v219\n\t" \
  "v_sub_f32 v217, v217, v224\n\t" \
  "v_mfma_f32_32x32x16_bf16 v[100:115], v[20:23], v[156:159], v[100:115]\n\t" \
  "v_rcp_f32 v196, v196\n\t" \
  "v_rcp_f32 v197, v197\n\t" \
  "v_rcp_f32 v198, v198\n\t" \
  "v_rcp_f32 v199, v199\n\t" \
  "v_mul_f32 v218, v229, v229\n\t" \
  "v_cndmask_b32 v228, v216, v217, vcc\n\t" \
  "v_fmac_f32 v218, v216, v216\n\t" \
  "v_fmac_f32 v218, v217, v217\n\t" \
  "v_cvt_pk_bf16_f32 v218, v218, v218\n\t" \
  "s_waitcnt lgkmcnt(7)\n\t" \
  "v_cndmask_b32_sdwa v175, v175, v218, vcc dst_sel:WORD_1 dst_unused:UNUSED_PRESERVE src0_sel:WORD_1 src1_sel:WORD_1\n\t" \
  "s_waitcnt lgkmcnt(1)\n\t" \
  "v_mfma_f32_32x32x16_bf16 v[132:147], v[208:211], v[188:191], v[132:147]\n\t" \
  "v_exp_f32 v192, v124\n\t" \
  "v_exp_f32 v193, v125\n\t" \
  "v_exp_f32 v194, v126\n\t" \
  "v_exp_f32 v195, v127\n\t" \
  "ds_read_b128 v[212:215], v247 offset:0\n\t" \
  "ds_read2_b32 v[220:221], v252 offset0:0 offset1:2\n\t" \
  "ds_read2_b32 v[222:223], v252 offset0:4 offset1:6\n\t" \
  "ds_read_b64 v[226:227], v253\n\t" \
  "ds_read_b32 v232, v254\n\t" \
  "v_mfma_f32_32x32x16_bf16 v[100:115], v[28:31], v[160:163], v[100:115]\n\t" \
  "v_mul_f32 v196, v120, v196\n\t" \
  "v_mul_f32 v197, v121, v197\n\t" \
  "v_mul_f32 v198, v122, v198\n\t" \
  "v_mul_f32 v199, v123, v199\n\t" \
  "v_add_u32 v252, 0x100, v252\n\t" \
  "v_add_u32 v253, 0x100, v253\n\t" \
  "v_add_u32 v254, 0x100, v254\n\t" \
  "v_add_f32 v192, 1.0, v192\n\t" \
  "v_add_f32 v193, 1.0, v193\n\t" \
  "v_add_f32 v194, 1.0, v194\n\t" \
  "v_add_f32 v195, 1.0, v195\n\t" \
  "v_cvt_pk_bf16_f32 v186, v196, v197\n\t" \
  "v_cvt_pk_bf16_f32 v187, v198, v199\n\t" \
  "v_mfma_f32_32x32x16_bf16 v[100:115], v[36:39], v[168:171], v[100:115]\n\t" \
  "v_rcp_f32 v192, v192\n\t" \
  "v_rcp_f32 v193, v193\n\t" \
  "v_rcp_f32 v194, v194\n\t" \
  "v_rcp_f32 v195, v195\n\t" \
  "v_mfma_f32_32x32x16_bf16 v[100:115], v[44:47], v[172:175], v[100:115]\n\t" \
  "v_exp_f32 v196, v128\n\t" \
  "v_exp_f32 v197, v129\n\t" \
  "v_exp_f32 v198, v130\n\t" \
  "v_exp_f32 v199, v131\n\t" \
  "s_waitcnt lgkmcnt(4)\n\t" \
  "v_mfma_f32_32x32x16_bf16 v[100:115], v[212:215], v[164:167], v[100:115]\n\t" \
  "v_mul_f32 v192, v124, v192\n\t" \
  "v_mul_f32 v193, v125, v193\n\t" \
  "v_mul_f32 v194, v126, v194\n\t" \
  "v_mul_f32 v195, v127, v195\n\t" \
  "ds_write_b128 v243, v[184:187] offset:0\n\t" \
  "ds_read_b128 v[208:211], v247 offset:5120\n\t" \
  "ds_read_b128 v[212:215], v247 offset:4096\n\t" \
  "v_add_f32 v196, 1.0, v196\n\t" \
  "v_add_f32 v197, 1.0, v197\n\t" \
  "v_add_f32 v198, 1.0, v198\n\t" \
  "v_add_f32 v199, 1.0, v199\n\t" \
  "v_cvt_pk_bf16_f32 v188, v192, v193\n\t" \
  "v_cvt_pk_bf16_f32 v189, v194, v195\n\t" \
  "v_mfma_f32_32x32x16_bf16 v[52:67], v[200:203], v[176:179], v[52:67]\n\t" \
  "v_rcp_f32 v196, v196\n\t" \
  "v_rcp_f32 v197, v197\n\t" \
  "v_rcp_f32 v198, v198\n\t" \
  "v_rcp_f32 v199, v199\n\t" \
  "v_mfma_f32_32x32x16_bf16 v[52:67], v[204:207], v[180:183], v[52:67]\n\t" \
  "v_exp_f32 v192, v100\n\t" \
  "v_exp_f32 v193, v101\n\t" \
  "v_exp_f32 v194, v102\n\t" \
  "v_exp_f32 v195, v103\n\t" \
  "s_cmp_eq_u32 %[nt], 0\n\t" \
  "s_waitcnt lgkmcnt(0)\n\t" \
  "v_mfma_f32_32x32x16_bf16 v[132:147], v[212:215], v[184:187], v[132:147]\n\t" \
  "v_mul_f32 v196, v128, v196\n\t" \
  "v_mul_f32 v197, v129, v197\n\t" \
  "v_mul_f32 v198, v130, v198\n\t" \
  "v_mul_f32 v199, v131, v199\n\t" \
  "v_add_f32 v192, 1.0, v192\n\t" \
  "v_add_f32 v193, 1.0, v193\n\t" \
  "v_add_f32 v194, 1.0, v194\n\t" \
  "v_add_f32 v195, 1.0, v195\n\t" \
  "v_cvt_pk_bf16_f32 v190, v196, v197\n\t" \
  "v_cvt_pk_bf16_f32 v191, v198, v199\n\t" \
  "s_cbranch_scc0 1b\n\t" \
  "s_cmp_lg_u32 %[last], 0\n\t" \
  "s_cbranch_scc1 8f\n\t" \
  "ds_write_b128 v243, v[188:191] offset:16\n\t" \
  "ds_read_b64_tr_b16 v[200:201], v244 offset:0\n\t" \
  "ds_read_b64_tr_b16 v[202:203], v244 offset:320\n\t" \
  "ds_read_b64_tr_b16 v[204:205], v244 offset:1280\n\t" \
  "ds_read_b64_tr_b16 v[206:207], v244 offset:1600\n\t" \
  "v_mfma_f32_32x32x16_bf16 v[132:147], v[208:211], v[188:191], v[132:147]\n\t" \
  "s_waitcnt lgkmcnt(0)\n\t" \
  "v_mfma_f32_32x32x16_bf16 v[68:83], v[200:203], v[176:179], v[68:83]\n\t" \
  "v_mfma_f32_32x32x16_bf16 v[68:83], v[204:207], v[180:183], v[68:83]\n\t" \
  "s_nop 8\n\t" \
  "v_mul_f32 v230, v230, v132\n\t" \
  "v_mul_f32 v231, v231, v132\n\t" \
  "v_cvt_pk_bf16_f32 v230, v230, v231\n\t" \
  "ds_write_b16 v245, v230 offset:7456\n\t" \
  "ds_write_b16_d16_hi v241, v230 offset:7584\n\t" \
  "ds_read_b128 v[200:203], v246 offset:7456\n\t" \
  "ds_read_b128 v[208:211], v246 offset:7488\n\t" \
  "s_waitcnt lgkmcnt(0)\n\t" \
  "v_mfma_f32_32x32x16_bf16 v[84:99], v[200:203], v[176:179], v[84:99]\n\t" \
  "v_mfma_f32_32x32x16_bf16 v[84:99], v[208:211], v[180:183], v[84:99]\n\t" \
  "s_branch 9f\n\t" \
  "6:\n\t" \
  "s_cmp_lg_u32 %[last], 0\n\t" \
  "s_cbranch_scc0 9f\n\t" \
  "s_waitcnt lgkmcnt(0)\n\t" \
  "v_cndmask_b32 v224, 0, 24, vcc\n\t" \
  "v_xor_b32 v220, v234, v224\n\t" \
  "v_xor_b32 v221, v235, v224\n\t" \
  "v_xor_b32 v222, v236, v224\n\t" \
  "v_xor_b32 v223, v237, v224\n\t" \
  "v_xor_b32 v216, 16, v220\n\t" \
  "v_xor_b32 v217, 16, v221\n\t" \
  "v_xor_b32 v218, 16, v222\n\t" \
  "v_xor_b32 v219, 16, v223\n\t" \
  "ds_read_b64 v[148:149], v220\n\t" \
  "ds_read_b64 v[150:151], v216\n\t" \
  "ds_read_b64 v[152:153], v221\n\t" \
  "ds_read_b64 v[154:155], v217\n\t" \
  "ds_read_b64 v[156:157], v222\n\t" \
  "ds_read_b64 v[158:159], v218\n\t" \
  "ds_read_b64 v[160:161], v223\n\t" \
  "ds_read_b64 v[162:163], v219\n\t" \
  "ds_read_b128 v[4:7], v247 offset:6144\n\t" \
  "ds_read_b128 v[8:11], v247 offset:7168\n\t" \
  "ds_read_b128 v[12:15], v247 offset:8192\n\t" \
  "ds_read_b128 v[16:19], v247 offset:9216\n\t" \
  "ds_read_b128 v[20:23], v247 offset:10240\n\t" \
  "ds_read_b128 v[24:27], v247 offset:11264\n\t" \
  "v_subrev_u32 v225, %[b1], v241\n\t" \
  "v_lshl_add_u32 v225, v225, 5, v243\n\t" \
  "v_sub_u32 v225, v225, v224\n\t" \
  "s_nop 4\n\t" \
  "v_cvt_pk_bf16_f32 v184, v52, v53\n\t" \
  "v_cvt_pk_bf16_f32 v185, v54, v55\n\t" \
  "v_cvt_pk_bf16_f32 v186, v56, v57\n\t" \
  "v_cvt_pk_bf16_f32 v187, v58, v59\n\t" \
  "v_cvt_pk_bf16_f32 v188, v60, v61\n\t" \
  "v_cvt_pk_bf16_f32 v189, v62, v63\n\t" \
  "v_cvt_pk_bf16_f32 v190, v64, v65\n\t" \
  "v_cvt_pk_bf16_f32 v191, v66, v67\n\t" \
  "v_cvt_pk_bf16_f32 v36, v68, v69\n\t" \
  "v_cvt_pk_bf16_f32 v37, v70, v71\n\t" \
  "v_cvt_pk_bf16_f32 v38, v72, v73\n\t" \
  "v_cvt_pk_bf16_f32 v39, v74, v75\n\t" \
  "v_cvt_pk_bf16_f32 v40, v76, v77\n\t" \
  "v_cvt_pk_bf16_f32 v41, v78, v79\n\t" \
  "v_cvt_pk_bf16_f32 v42, v80, v81\n\t" \
  "v_cvt_pk_bf16_f32 v43, v82, v83\n\t" \
  "s_waitcnt lgkmcnt(5)\n\t" \
  "v_mfma_f32_32x32x16_bf16 v[100:115], v[4:7], v[148:151], 0\n\t" \
  "s_waitcnt lgkmcnt(4)\n\t" \
  "v_mfma_f32_32x32x16_bf16 v[116:131], v[8:11], v[148:151], 0\n\t" \
  "ds_read_b128 v[28:31], v247 offset:12288\n\t" \
  "ds_read_b128 v[32:35], v247 offset:13312\n\t" \
  "s_waitcnt lgkmcnt(5)\n\t" \
  "v_mfma_f32_32x32x16_bf16 v[100:115], v[12:15], v[152:155], v[100:115]\n\t" \
  "s_waitcnt lgkmcnt(4)\n\t" \
  "v_mfma_f32_32x32x16_bf16 v[116:131], v[16:19], v[152:155], v[116:131]\n\t" \
  "ds_read_b128 v[4:7], v247 offset:14336\n\t" \
  "ds_read_b128 v[8:11], v247 offset:15360\n\t" \
  "s_waitcnt lgkmcnt(5)\n\t" \
  "v_mfma_f32_32x32x16_bf16 v[100:115], v[20:23], v[156:159], v[100:115]\n\t" \
  "s_waitcnt lgkmcnt(4)\n\t" \
  "v_mfma_f32_32x32x16_bf16 v[116:131], v[24:27], v[156:159], v[116:131]\n\t" \
  "ds_read_b128 v[12:15], v247 offset:16384\n\t" \
  "ds_read_b128 v[16:19], v247 offset:17408\n\t" \
  "s_waitcnt lgkmcnt(5)\n\t" \
  "v_mfma_f32_32x32x16_bf16 v[100:115], v[28:31], v[160:163], v[100:115]\n\t" \
  "s_waitcnt lgkmcnt(4)\n\t" \
  "v_mfma_f32_32x32x16_bf16 v[116:131], v[32:35], v[160:163], v[116:131]\n\t" \
  "ds_read_b128 v[20:23], v247 offset:18432\n\t" \
  "ds_read_b128 v[24:27], v247 offset:19456\n\t" \
  "s_waitcnt lgkmcnt(5)\n\t" \
  "v_mfma_f32_32x32x16_bf16 v[100:115], v[4:7], v[184:187], v[100:115]\n\t" \
  "ds_read_b128 v[28:31], v247 offset:20480\n\t" \
  "ds_read_b128 v[32:35], v247 offset:21504\n\t" \
  "s_waitcnt lgkmcnt(5)\n\t" \
  "v_mfma_f32_32x32x16_bf16 v[100:115], v[12:15], v[188:191], v[100:115]\n\t" \
  "s_waitcnt lgkmcnt(3)\n\t" \
  "v_mfma_f32_32x32x16_bf16 v[100:115], v[20:23], v[36:39], v[100:115]\n\t" \
  "s_waitcnt lgkmcnt(1)\n\t" \
  "v_mfma_f32_32x32x16_bf16 v[100:115], v[28:31], v[40:43], v[100:115]\n\t" \
  "s_waitcnt lgkmcnt(0)\n\t" \
  "s_branch 10f\n\t" \
  "8:\n\t" \
  "ds_write_b128 v243, v[188:191] offset:16\n\t" \
  "ds_read_b64_tr_b16 v[200:201], v244 offset:0\n\t" \
  "ds_read_b64_tr_b16 v[202:203], v244 offset:320\n\t" \
  "ds_read_b64_tr_b16 v[204:205], v244 offset:1280\n\t" \
  "ds_read_b64_tr_b16 v[206:207], v244 offset:1600\n\t" \
  "v_mfma_f32_32x32x16_bf16 v[132:147], v[208:211], v[188:191], v[132:147]\n\t" \
  "v_cndmask_b32 v224, 0, 24, vcc\n\t" \
  "v_xor_b32 v220, v234, v224\n\t" \
  "v_xor_b32 v221, v235, v224\n\t" \
  "v_xor_b32 v222, v236, v224\n\t" \
  "v_xor_b32 v223, v237, v224\n\t" \
  "v_xor_b32 v216, 16, v220\n\t" \
  "v_xor_b32 v217, 16, v221\n\t" \
  "v_xor_b32 v218, 16, v222\n\t" \
  "v_xor_b32 v219, 16, v223\n\t" \
  "ds_read_b64 v[148:149], v220\n\t" \
  "ds_read_b64 v[150:151], v216\n\t" \
  "ds_read_b64 v[152:153], v221\n\t" \
  "ds_read_b64 v[154:155], v217\n\t" \
  "ds_read_b64 v[156:157], v222\n\t" \
  "ds_read_b64 v[158:159], v218\n\t" \
  "ds_read_b64 v[160:161], v223\n\t" \
  "ds_read_b64 v[162:163], v219\n\t" \
  "ds_read_b128 v[4:7], v247 offset:6144\n\t" \
  "ds_read_b128 v[8:11], v247 offset:7168\n\t" \
  "s_waitcnt lgkmcnt(11)\n\t" \
  "ds_read_b128 v[12:15], v247 offset:8192\n\t" \
  "ds_read_b128 v[16:19], v247 offset:9216\n\t" \
  "ds_read_b128 v[20:23], v247 offset:10240\n\t" \
  "ds_read_b128 v[24:27], v247 offset:11264\n\t" \
  "v_subrev_u32 v225, %[b1], v241\n\t" \
  "v_lshl_add_u32 v225, v225, 5, v243\n\t" \
  "v_sub_u32 v225, v225, v224\n\t" \
  "v_mfma_f32_32x32x16_bf16 v[68:83], v[200:203], v[176:179], v[68:83]\n\t" \
  "s_waitcnt lgkmcnt(5)\n\t" \
  "v_mfma_f32_32x32x16_bf16 v[100:115], v[4:7], v[148:151], 0\n\t" \
  "v_mfma_f32_32x32x16_bf16 v[68:83], v[204:207], v[180:183], v[68:83]\n\t" \
  "s_waitcnt lgkmcnt(4)\n\t" \
  "v_mfma_f32_32x32x16_bf16 v[116:131], v[8:11], v[148:151], 0\n\t" \
  "ds_read_b128 v[28:31], v247 offset:12288\n\t" \
  "ds_read_b128 v[32:35], v247 offset:13312\n\t" \
  "s_waitcnt lgkmcnt(5)\n\t" \
  "v_mfma_f32_32x32x16_bf16 v[100:115], v[12:15], v[152:155], v[100:115]\n\t" \
  "v_mul_f32 v230, v230, v132\n\t" \
  "v_mul_f32 v231, v231, v132\n\t" \
  "v_cvt_pk_bf16_f32 v230, v230, v231\n\t" \
  "ds_write_b16 v245, v230 offset:7456\n\t" \
  "ds_write_b16_d16_hi v241, v230 offset:7584\n\t" \
  "ds_read_b128 v[200:203], v246 offset:7456\n\t" \
  "ds_read_b128 v[208:211], v246 offset:7488\n\t" \
  "v_cvt_pk_bf16_f32 v184, v52, v53\n\t" \
  "v_cvt_pk_bf16_f32 v185, v54, v55\n\t" \
  "v_cvt_pk_bf16_f32 v186, v56, v57\n\t" \
  "v_cvt_pk_bf16_f32 v187, v58, v59\n\t" \
  "s_waitcnt lgkmcnt(8)\n\t" \
  "v_mfma_f32_32x32x16_bf16 v[116:131], v[16:19], v[152:155], v[116:131]\n\t" \
  "ds_read_b128 v[4:7], v247 offset:14336\n\t" \
  "ds_read_b128 v[8:11], v247 offset:15360\n\t" \
  "s_waitcnt lgkmcnt(9)\n\t" \
  "v_mfma_f32_32x32x16_bf16 v[100:115], v[20:23], v[156:159], v[100:115]\n\t" \
  "v_cvt_pk_bf16_f32 v188, v60, v61\n\t" \
  "v_cvt_pk_bf16_f32 v189, v62, v63\n\t" \
  "v_cvt_pk_bf16_f32 v190, v64, v65\n\t" \
  "v_cvt_pk_bf16_f32 v191, v66, v67\n\t" \
  "s_waitcnt lgkmcnt(8)\n\t" \
  "v_mfma_f32_32x32x16_bf16 v[116:131], v[24:27], v[156:159], v[116:131]\n\t" \
  "ds_read_b128 v[12:15], v247 offset:16384\n\t" \
  "ds_read_b128 v[16:19], v247 offset:17408\n\t" \
  "s_waitcnt lgkmcnt(9)\n\t" \
  "v_mfma_f32_32x32x16_bf16 v[100:115], v[28:31], v[160:163], v[100:115]\n\t" \
  "v_cvt_pk_bf16_f32 v36, v68, v69\n\t" \
  "v_cvt_pk_bf16_f32 v37, v70, v71\n\t" \
  "v_cvt_pk_bf16_f32 v38, v72, v73\n\t" \
  "v_cvt_pk_bf16_f32 v39, v74, v75\n\t" \
  "s_waitcnt lgkmcnt(5)\n\t" \
  "v_mfma_f32_32x32x16_bf16 v[84:99], v[200:203], v[176:179], v[84:99]\n\t" \
  "v_cvt_pk_bf16_f32 v40, v76, v77\n\t" \
  "v_cvt_pk_bf16_f32 v41, v78, v79\n\t" \
  "v_cvt_pk_bf16_f32 v42, v80, v81\n\t" \
  "v_cvt_pk_bf16_f32 v43, v82, v83\n\t" \
  "v_mfma_f32_32x32x16_bf16 v[116:131], v[32:35], v[160:163], v[116:131]\n\t" \
  "ds_read_b128 v[20:23], v247 offset:18432\n\t" \
  "ds_read_b128 v[24:27], v247 offset:19456\n\t" \
  "s_waitcnt lgkmcnt(6)\n\t" \
  "v_mfma_f32_32x32x16_bf16 v[84:99], v[208:211], v[180:183], v[84:99]\n\t" \
  "s_waitcnt lgkmcnt(5)\n\t" \
  "v_mfma_f32_32x32x16_bf16 v[100:115], v[4:7], v[184:187], v[100:115]\n\t" \
  "ds_read_b128 v[28:31], v247 offset:20480\n\t" \
  "ds_read_b128 v[32:35], v247 offset:21504\n\t" \
  "s_waitcnt lgkmcnt(5)\n\t" \
  "v_mfma_f32_32x32x16_bf16 v[100:115], v[12:15], v[188:191], v[100:115]\n\t" \
  "s_waitcnt lgkmcnt(3)\n\t" \
  "v_mfma_f32_32x32x16_bf16 v[100:115], v[20:23], v[36:39], v[100:115]\n\t" \
  "s_waitcnt lgkmcnt(1)\n\t" \
  "v_mfma_f32_32x32x16_bf16 v[100:115], v[28:31], v[40:43], v[100:115]\n\t" \
  "s_waitcnt lgkmcnt(0)\n\t" \
  "10:\n\t" \
  "v_mov_b32 v212, 0xbfb8aa3b\n\t" \
  "v_mfma_f32_32x32x16_bf16 v[116:131], v[8:11], v[184:187], v[116:131]\n\t" \
  "v_mfma_f32_32x32x16_bf16 v[116:131], v[16:19], v[188:191], v[116:131]\n\t" \
  "s_nop 8\n\t" \
  "v_exp_f32 v192, v100\n\t" \
  "v_exp_f32 v193, v101\n\t" \
  "v_exp_f32 v194, v102\n\t" \
  "v_exp_f32 v195, v103\n\t" \
  "v_lshlrev_b32 v200, 16, v148\n\t" \
  "v_and_b32 v201, 0xffff0000, v148\n\t" \
  "v_lshlrev_b32 v202, 16, v149\n\t" \
  "v_and_b32 v203, 0xffff0000, v149\n\t" \
  "v_mfma_f32_32x32x16_bf16 v[116:131], v[24:27], v[36:39], v[116:131]\n\t" \
  "v_fmaak_f32 v192, v192, v212, 0xbfb8aa3b\n\t" \
  "v_fmaak_f32 v193, v193, v212, 0xbfb8aa3b\n\t" \
  "v_fmaak_f32 v194, v194, v212, 0xbfb8aa3b\n\t" \
  "v_fmaak_f32 v195, v195, v212, 0xbfb8aa3b\n\t" \
  "v_rcp_f32 v192, v192\n\t" \
  "v_rcp_f32 v193, v193\n\t" \
  "v_rcp_f32 v194, v194\n\t" \
  "v_rcp_f32 v195, v195\n\t" \
  "v_mfma_f32_32x32x16_bf16 v[116:131], v[32:35], v[40:43], v[116:131]\n\t" \
  "v_exp_f32 v196, v104\n\t" \
  "v_exp_f32 v197, v105\n\t" \
  "v_exp_f32 v198, v106\n\t" \
  "v_exp_f32 v199, v107\n\t" \
  "v_fmac_f32 v200, v100, v192\n\t" \
  "v_fmac_f32 v201, v101, v193\n\t" \
  "v_fmac_f32 v202, v102, v194\n\t" \
  "v_fmac_f32 v203, v103, v195\n\t" \
  "v_lshlrev_b32 v204, 16, v150\n\t" \
  "v_and_b32 v205, 0xffff0000, v150\n\t" \
  "v_lshlrev_b32 v206, 16, v151\n\t" \
  "v_and_b32 v207, 0xffff0000, v151\n\t" \
  "v_fmaak_f32 v196, v196, v212, 0xbfb8aa3b\n\t" \
  "v_fmaak_f32 v197, v197, v212, 0xbfb8aa3b\n\t" \
  "v_fmaak_f32 v198, v198, v212, 0xbfb8aa3b\n\t" \
  "v_fmaak_f32 v199, v199, v212, 0xbfb8aa3b\n\t" \
  "v_cvt_pk_bf16_f32 v176, v200, v201\n\t" \
  "v_cvt_pk_bf16_f32 v177, v202, v203\n\t" \
  "ds_write_b64 v225, v[176:177] offset:0\n\t" \
  "v_rcp_f32 v196, v196\n\t" \
  "v_rcp_f32 v197, v197\n\t" \
  "v_rcp_f32 v198, v198\n\t" \
  "v_rcp_f32 v199, v199\n\t" \
  "v_exp_f32 v192, v108\n\t" \
  "v_exp_f32 v193, v109\n\t" \
  "v_exp_f32 v194, v110\n\t" \
  "v_exp_f32 v195, v111\n\t" \
  "v_fmac_f32 v204, v104, v196\n\t" \
  "v_fmac_f32 v205, v105, v197\n\t" \
  "v_fmac_f32 v206, v106, v198\n\t" \
  "v_fmac_f32 v207, v107, v199\n\t" \
  "v_lshlrev_b32 v200, 16, v152\n\t" \
  "v_and_b32 v201, 0xffff0000, v152\n\t" \
  "v_lshlrev_b32 v202, 16, v153\n\t" \
  "v_and_b32 v203, 0xffff0000, v153\n\t" \
  "v_fmaak_f32 v192, v192, v212, 0xbfb8aa3b\n\t" \
  "v_fmaak_f32 v193, v193, v212, 0xbfb8aa3b\n\t" \
  "v_fmaak_f32 v194, v194, v212, 0xbfb8aa3b\n\t" \
  "v_fmaak_f32 v195, v195, v212, 0xbfb8aa3b\n\t" \
  "v_cvt_pk_bf16_f32 v178, v204, v205\n\t" \
  "v_cvt_pk_bf16_f32 v179, v206, v207\n\t" \
  "ds_write_b64 v225, v[178:179] offset:16\n\t" \
  "v_rcp_f32 v192, v192\n\t" \
  "v_rcp_f32 v193, v193\n\t" \
  "v_rcp_f32 v194, v194\n\t" \
  "v_rcp_f32 v195, v195\n\t" \
  "v_exp_f32 v196, v112\n\t" \
  "v_exp_f32 v197, v113\n\t" \
  "v_exp_f32 v198, v114\n\t" \
  "v_exp_f32 v199, v115\n\t" \
  "v_fmac_f32 v200, v108, v192\n\t" \
  "v_fmac_f32 v201, v109, v193\n\t" \
  "v_fmac_f32 v202, v110, v194\n\t" \
  "v_fmac_f32 v203, v111, v195\n\t" \
  "v_lshlrev_b32 v204, 16, v154\n\t" \
  "v_and_b32 v205, 0xffff0000, v154\n\t" \
  "v_lshlrev_b32 v206, 16, v155\n\t" \
  "v_and_b32 v207, 0xffff0000, v155\n\t" \
  "v_fmaak_f32 v196, v196, v212, 0xbfb8aa3b\n\t" \
  "v_fmaak_f32 v197, v197, v212, 0xbfb8aa3b\n\t" \
  "v_fmaak_f32 v198, v198, v212, 0xbfb8aa3b\n\t" \
  "v_fmaak_f32 v199, v199, v212, 0xbfb8aa3b\n\t" \
  "v_cvt_pk_bf16_f32 v180, v200, v201\n\t" \
  "v_cvt_pk_bf16_f32 v181, v202, v203\n\t" \
  "ds_write_b64 v225, v[180:181] offset:32\n\t" \
  "v_rcp_f32 v196, v196\n\t" \
  "v_rcp_f32 v197, v197\n\t" \
  "v_rcp_f32 v198, v198\n\t" \
  "v_rcp_f32 v199, v199\n\t" \
  "v_exp_f32 v192, v116\n\t" \
  "v_exp_f32 v193, v117\n\t" \
  "v_exp_f32 v194, v118\n\t" \
  "v_exp_f32 v195, v119\n\t" \
  "v_fmac_f32 v204, v112, v196\n\t" \
  "v_fmac_f32 v205, v113, v197\n\t" \
  "v_fmac_f32 v206, v114, v198\n\t" \
  "v_fmac_f32 v207, v115, v199\n\t" \
  "v_lshlrev_b32 v200, 16, v156\n\t" \
  "v_and_b32 v201, 0xffff0000, v156\n\t" \
  "v_lshlrev_b32 v202, 16, v157\n\t" \
  "v_and_b32 v203, 0xffff0000, v157\n\t" \
  "v_fmaak_f32 v192, v192, v212, 0xbfb8aa3b\n\t" \
  "v_fmaak_f32 v193, v193, v212, 0xbfb8aa3b\n\t" \
  "v_fmaak_f32 v194, v194, v212, 0xbfb8aa3b\n\t" \
  "v_fmaak_f32 v195, v195, v212, 0xbfb8aa3b\n\t" \
  "v_cvt_pk_bf16_f32 v182, v204, v205\n\t" \
  "v_cvt_pk_bf16_f32 v183, v206, v207\n\t" \
  "ds_write_b64 v225, v[182:183] offset:48\n\t" \
  "v_rcp_f32 v192, v192\n\t" \
  "v_rcp_f32 v193, v193\n\t" \
  "v_rcp_f32 v194, v194\n\t" \
  "v_rcp_f32 v195, v195\n\t" \
  "v_exp_f32 v196, v120\n\t" \
  "v_exp_f32 v197, v121\n\t" \
  "v_exp_f32 v198, v122\n\t" \
  "v_exp_f32 v199, v123\n\t" \
  "v_fmac_f32 v200, v116, v192\n\t" \
  "v_fmac_f32 v201, v117, v193\n\t" \
  "v_fmac_f32 v202, v118, v194\n\t" \
  "v_fmac_f32 v203, v119, v195\n\t" \
  "v_lshlrev_b32 v204, 16, v158\n\t" \
  "v_and_b32 v205, 0xffff0000, v158\n\t" \
  "v_lshlrev_b32 v206, 16, v159\n\t" \
  "v_and_b32 v207, 0xffff0000, v159\n\t" \
  "v_fmaak_f32 v196, v196, v212, 0xbfb8aa3b\n\t" \
  "v_fmaak_f32 v197, v197, v212, 0xbfb8aa3b\n\t" \
  "v_fmaak_f32 v198, v198, v212, 0xbfb8aa3b\n\t" \
  "v_fmaak_f32 v199, v199, v212, 0xbfb8aa3b\n\t" \
  "v_cvt_pk_bf16_f32 v176, v200, v201\n\t" \
  "v_cvt_pk_bf16_f32 v177, v202, v203\n\t" \
  "ds_write_b64 v225, v[176:177] offset:64\n\t" \
  "v_rcp_f32 v196, v196\n\t" \
  "v_rcp_f32 v197, v197\n\t" \
  "v_rcp_f32 v198, v198\n\t" \
  "v_rcp_f32 v199, v199\n\t" \
  "v_exp_f32 v192, v124\n\t" \
  "v_exp_f32 v193, v125\n\t" \
  "v_exp_f32 v194, v126\n\t" \
  "v_exp_f32 v195, v127\n\t" \
  "v_fmac_f32 v204, v120, v196\n\t" \
  "v_fmac_f32 v205, v121, v197\n\t" \
  "v_fmac_f32 v206, v122, v198\n\t" \
  "v_fmac_f32 v207, v123, v199\n\t" \
  "v_lshlrev_b32 v200, 16, v160\n\t" \
  "v_and_b32 v201, 0xffff0000, v160\n\t" \
  "v_lshlrev_b32 v202, 16, v161\n\t" \
  "v_and_b32 v203, 0xffff0000, v161\n\t" \
  "v_fmaak_f32 v192, v192, v212, 0xbfb8aa3b\n\t" \
  "v_fmaak_f32 v193, v193, v212, 0xbfb8aa3b\n\t" \
  "v_fmaak_f32 v194, v194, v212, 0xbfb8aa3b\n\t" \
  "v_fmaak_f32 v195, v195, v212, 0xbfb8aa3b\n\t" \
  "v_cvt_pk_bf16_f32 v178, v204, v205\n\t" \
  "v_cvt_pk_bf16_f32 v179, v206, v207\n\t" \
  "ds_write_b64 v225, v[178:179] offset:80\n\t" \
  "v_rcp_f32 v192, v192\n\t" \
  "v_rcp_f32 v193, v193\n\t" \
  "v_rcp_f32 v194, v194\n\t" \
  "v_rcp_f32 v195, v195\n\t" \
  "v_exp_f32 v196, v128\n\t" \
  "v_exp_f32 v197, v129\n\t" \
  "v_exp_f32 v198, v130\n\t" \
  "v_exp_f32 v199, v131\n\t" \
  "v_fmac_f32 v200, v124, v192\n\t" \
  "v_fmac_f32 v201, v125, v193\n\t" \
  "v_fmac_f32 v202, v126, v194\n\t" \
  "v_fmac_f32 v203, v127, v195\n\t" \
  "v_lshlrev_b32 v204, 16, v162\n\t" \
  "v_and_b32 v205, 0xffff0000, v162\n\t" \
  "v_lshlrev_b32 v206, 16, v163\n\t" \
  "v_and_b32 v207, 0xffff0000, v163\n\t" \
  "v_fmaak_f32 v196, v196, v212, 0xbfb8aa3b\n\t" \
  "v_fmaak_f32 v197, v197, v212, 0xbfb8aa3b\n\t" \
  "v_fmaak_f32 v198, v198, v212, 0xbfb8aa3b\n\t" \
  "v_fmaak_f32 v199, v199, v212, 0xbfb8aa3b\n\t" \
  "v_cvt_pk_bf16_f32 v180, v200, v201\n\t" \
  "v_cvt_pk_bf16_f32 v181, v202, v203\n\t" \
  "ds_write_b64 v225, v[180:181] offset:96\n\t" \
  "v_rcp_f32 v196, v196\n\t" \
  "v_rcp_f32 v197, v197\n\t" \
  "v_rcp_f32 v198, v198\n\t" \
  "v_rcp_f32 v199, v199\n\t" \
  "s_nop 0\n\t" \
  "v_fmac_f32 v204, v128, v196\n\t" \
  "v_fmac_f32 v205, v129, v197\n\t" \
  "v_fmac_f32 v206, v130, v198\n\t" \
  "v_fmac_f32 v207, v131, v199\n\t" \
  "v_cvt_pk_bf16_f32 v182, v204, v205\n\t" \
  "v_cvt_pk_bf16_f32 v183, v206, v207\n\t" \
  "ds_write_b64 v225, v[182:183] offset:112\n\t" \
  "9:\n\t" \
  "ds_write_b16 v233, v255 offset:5376\n\t" \
  "s_mov_b32 m0, %[keep]\n\t" \
  "s_setprio 0\n\t" \
  "s_nop 11\n\t" \
  ""
#define LOOP_ASM_PLAIN \
  "s_mov_b32 %[keep], m0\n\t" \
  "s_mov_b64 vcc, %[hsel]\n\t" \
  "ds_read2_b32 v[220:221], v252 offset0:0 offset1:2\n\t" \
  "ds_read2_b32 v[222:223], v252 offset0:4 offset1:6\n\t" \
  "ds_read_b64 v[226:227], v253\n\t" \
  "ds_read_b32 v232, v254\n\t" \
  "ds_read_b128 v[212:215], v247 offset:0\n\t" \
  "v_mov_b32 v176, 0\n\t" \
  "v_mov_b32 v177, 0\n\t" \
  "v_mov_b32 v178, 0\n\t" \
  "v_mov_b32 v179, 0\n\t" \
  "v_mov_b32 v180, 0\n\t" \
  "v_mov_b32 v181, 0\n\t" \
  "v_mov_b32 v182, 0\n\t" \
  "v_mov_b32 v183, 0\n\t" \
  "v_mov_b32 v184, 0\n\t" \
  "v_mov_b32 v185, 0\n\t" \
  "v_mov_b32 v186, 0\n\t" \
  "v_mov_b32 v187, 0\n\t" \
  "v_mov_b32 v188, 0\n\t" \
  "v_mov_b32 v189, 0\n\t" \
  "v_mov_b32 v190, 0\n\t" \
  "v_mov_b32 v191, 0\n\t" \
  "v_mov_b32 v230, 0\n\t" \
  "v_mov_b32 v231, 0\n\t" \
  "v_mov_b32 v208, 0\n\t" \
  "v_mov_b32 v209, 0\n\t" \
  "v_mov_b32 v210, 0\n\t" \
  "v_mov_b32 v211, 0\n\t" \
  "ds_write_b128 v243, v[184:187] offset:0\n\t" \
  "s_nop 0\n\t" \
  "v_mfma_f32_32x32x16_bf16 v[132:147], v[184:187], v[188:191], 0\n\t" \
  "v_add_u32 v252, 0x100, v252\n\t" \
  "v_add_u32 v253, 0x100, v253\n\t" \
  "v_add_u32 v254, 0x100, v254\n\t" \
  "s_waitcnt lgkmcnt(0)\n\t" \
  "v_lshrrev_b32 v232, 20, v232\n\t" \
  "v_add_u32 v233, %[posd], v232\n\t" \
  "ds_read_b128 v[216:219], v233\n\t" \
  "v_add_u32 v233, v240, v232\n\t" \
  "ds_read_b128 v[172:175], v233 offset:32\n\t" \
  "ds_read_b128 v[168:171], v233\n\t" \
  "v_add_u32 v233, v241, v232\n\t" \
  "ds_write_b16_d16_hi v233, v255 offset:5376\n\t" \
  "v_mad_u32_u16 v220, v220, %[c128], v248\n\t" \
  "v_mad_u32_u16 v221, v221, %[c128], v249\n\t" \
  "v_mad_u32_u16 v222, v222, %[c128], v248\n\t" \
  "v_mad_u32_u16 v223, v223, %[c128], v249\n\t" \
  "v_or_b32 v226, v226, v250\n\t" \
  "v_mad_u32_u16 v227, v227, 6, v251\n\t" \
  "s_waitcnt vmcnt(0)\n\t" \
  "ds_read_b128 v[148:151], v234\n\t" \
  "ds_read_b128 v[152:155], v235\n\t" \
  "ds_read_b128 v[156:159], v236\n\t" \
  "ds_read_b128 v[160:163], v237\n\t" \
  "ds_read_b128 v[164:167], v238 offset:4096\n\t" \
  "ds_read_b64 v[224:225], v239 offset:5120\n\t" \
  "s_waitcnt lgkmcnt(0)\n\t" \
  "s_cmp_lt_i32 %[nt], 1\n\t" \
  "s_cbranch_scc1 7f\n\t" \
  "s_mov_b32 m0, %[b2]\n\t" \
  "s_nop 0\n\t" \
  "global_load_lds_dwordx4 v226, %[ea]\n\t" \
  "global_load_lds_dword v227, %[pos] offset:1024\n\t" \
  "s_mov_b32 m0, %[b1]\n\t" \
  "s_nop 0\n\t" \
  "global_load_lds_dwordx4 v220, %[h0]\n\t" \
  "global_load_lds_dwordx4 v221, %[h1] offset:1024\n\t" \
  "global_load_lds_dwordx4 v222, %[h2] offset:2048\n\t" \
  "global_load_lds_dwordx4 v223, %[h3] offset:3072\n\t" \
  "7:\n\t" \
  "ds_read2_b32 v[220:221], v252 offset0:0 offset1:2\n\t" \
  "ds_read2_b32 v[222:223], v252 offset0:4 offset1:6\n\t" \
  "ds_read_b64 v[226:227], v253\n\t" \
  "ds_read_b32 v232, v254\n\t" \
  "v_add_u32 v252, 0x100, v252\n\t" \
  "v_add_u32 v253, 0x100, v253\n\t" \
  "v_add_u32 v254, 0x100, v254\n\t" \
  "v_and_b32 v225, 0xffff0000, v225\n\t" \
  "v_lshlrev_b32 v219, 16, v224\n\t" \
  "v_and_b32 v224, 0xffff0000, v224\n\t" \
  "v_sub_f32 v229, v218, v225\n\t" \
  "v_sub_f32 v216, v216, v219\n\t" \
  "v_sub_f32 v217, v217, v224\n\t" \
  "v_mul_f32 v218, v229, v229\n\t" \
  "v_cndmask_b32 v228, v216, v217, vcc\n\t" \
  "v_fmac_f32 v218, v216, v216\n\t" \
  "v_fmac_f32 v218, v217, v217\n\t" \
  "v_cvt_pk_bf16_f32 v218, v218, v218\n\t" \
  "v_cndmask_b32_sdwa v175, v175, v218, vcc dst_sel:WORD_1 dst_unused:UNUSED_PRESERVE src0_sel:WORD_1 src1_sel:WORD_1\n\t" \
  "v_mfma_f32_32x32x16_bf16 v[100:115], v[4:7], v[148:151], 0\n\t" \
  "v_mfma_f32_32x32x16_bf16 v[100:115], v[12:15], v[152:155], v[100:115]\n\t" \
  "v_mfma_f32_32x32x16_bf16 v[100:115], v[20:23], v[156:159], v[100:115]\n\t" \
  "v_mfma_f32_32x32x16_bf16 v[100:115], v[28:31], v[160:163], v[100:115]\n\t" \
  "v_mfma_f32_32x32x16_bf16 v[100:115], v[36:39], v[168:171], v[100:115]\n\t" \
  "v_mfma_f32_32x32x16_bf16 v[100:115], v[212:215], v[164:167], v[100:115]\n\t" \
  "v_mfma_f32_32x32x16_bf16 v[100:115], v[44:47], v[172:175], v[100:115]\n\t" \
  "s_cmp_eq_u32 %[nt], 0\n\t" \
  "s_nop 10\n\t" \
  "v_mul_f32 v192, 0xbfb8aa3b, v100\n\t" \
  "v_mul_f32 v193, 0xbfb8aa3b, v101\n\t" \
  "v_mul_f32 v194, 0xbfb8aa3b, v102\n\t" \
  "v_mul_f32 v195, 0xbfb8aa3b, v103\n\t" \
  "v_exp_f32 v192, v192\n\t" \
  "v_exp_f32 v193, v193\n\t" \
  "v_exp_f32 v194, v194\n\t" \
  "v_exp_f32 v195, v195\n\t" \
  "s_nop 0\n\t" \
  "v_add_f32 v192, 1.0, v192\n\t" \
  "v_add_f32 v193, 1.0, v193\n\t" \
  "v_add_f32 v194, 1.0, v194\n\t" \
  "v_add_f32 v195, 1.0, v195\n\t" \
  "s_cbranch_scc1 6f\n\t" \
  "s_cmp_lt_i32 %[thr], 0\n\t" \
  "s_cbranch_scc1 1f\n\t" \
  "s_setprio 1\n\t" \
  "1:\n\t" \
  "ds_write_b128 v243, v[188:191] offset:16\n\t" \
  "ds_read_b64_tr_b16 v[200:201], v244 offset:0\n\t" \
  "ds_read_b64_tr_b16 v[202:203], v244 offset:320\n\t" \
  "ds_read_b64_tr_b16 v[204:205], v244 offset:1280\n\t" \
  "ds_read_b64_tr_b16 v[206:207], v244 offset:1600\n\t" \
  "ds_read_b128 v[212:215], v247 offset:1024\n\t" \
  "v_mfma_f32_32x32x16_bf16 v[132:147], v[208:211], v[188:191], v[132:147]\n\t" \
  "v_rcp_f32 v192, v192\n\t" \
  "v_rcp_f32 v193, v193\n\t" \
  "v_rcp_f32 v194, v194\n\t" \
  "v_rcp_f32 v195, v195\n\t" \
  "v_mfma_f32_32x32x16_bf16 v[116:131], v[8:11], v[148:151], 0\n\t" \
  "v_mul_f32 v196, 0xbfb8aa3b, v104\n\t" \
  "v_mul_f32 v197, 0xbfb8aa3b, v105\n\t" \
  "v_mul_f32 v198, 0xbfb8aa3b, v106\n\t" \
  "v_mul_f32 v199, 0xbfb8aa3b, v107\n\t" \
  "v_exp_f32 v196, v196\n\t" \
  "v_exp_f32 v197, v197\n\t" \
  "v_exp_f32 v198, v198\n\t" \
  "v_exp_f32 v199, v199\n\t" \
  "v_mfma_f32_32x32x16_bf16 v[116:131], v[16:19], v[152:155], v[116:131]\n\t" \
  "v_mul_f32 v192, v100, v192\n\t" \
  "v_mul_f32 v193, v101, v193\n\t" \
  "v_mul_f32 v194, v102, v194\n\t" \
  "v_mul_f32 v195, v103, v195\n\t" \
  "v_add_f32 v196, 1.0, v196\n\t" \
  "v_add_f32 v197, 1.0, v197\n\t" \
  "v_add_f32 v198, 1.0, v198\n\t" \
  "v_add_f32 v199, 1.0, v199\n\t" \
  "v_cvt_pk_bf16_f32 v184, v192, v193\n\t" \
  "v_cvt_pk_bf16_f32 v185, v194, v195\n\t" \
  "v_mfma_f32_32x32x16_bf16 v[116:131], v[24:27], v[156:159], v[116:131]\n\t" \
  "v_rcp_f32 v196, v196\n\t" \
  "v_rcp_f32 v197, v197\n\t" \
  "v_rcp_f32 v198, v198\n\t" \
  "v_rcp_f32 v199, v199\n\t" \
  "s_waitcnt lgkmcnt(3)\n\t" \
  "v_mfma_f32_32x32x16_bf16 v[68:83], v[200:203], v[176:179], v[68:83]\n\t" \
  "v_mul_f32 v192, 0xbfb8aa3b, v108\n\t" \
  "v_mul_f32 v193, 0xbfb8aa3b, v109\n\t" \
  "v_mul_f32 v194, 0xbfb8aa3b, v110\n\t" \
  "v_mul_f32 v195, 0xbfb8aa3b, v111\n\t" \
  "v_exp_f32 v192, v192\n\t" \
  "v_exp_f32 v193, v193\n\t" \
  "v_exp_f32 v194, v194\n\t" \
  "v_exp_f32 v195, v195\n\t" \
  "v_mfma_f32_32x32x16_bf16 v[116:131], v[32:35], v[160:163], v[116:131]\n\t" \
  "v_mul_f32 v196, v104, v196\n\t" \
  "v_mul_f32 v197, v105, v197\n\t" \
  "v_mul_f32 v198, v106, v198\n\t" \
  "v_mul_f32 v199, v107, v199\n\t" \
  "v_mul_f32 v230, v230, v132\n\t" \
  "v_mul_f32 v231, v231, v132\n\t" \
  "v_cvt_pk_bf16_f32 v230, v230, v231\n\t" \
  "ds_write_b16 v245, v230 offset:7456\n\t" \
  "ds_write_b16_d16_hi v241, v230 offset:7584\n\t" \
  "ds_read_b128 v[200:203], v246 offset:7456\n\t" \
  "ds_read_b128 v[208:211], v246 offset:7488\n\t" \
  "v_mov_b32 v230, v228\n\t" \
  "v_mov_b32 v231, v229\n\t" \
  "v_add_f32 v192, 1.0, v192\n\t" \
  "v_add_f32 v193, 1.0, v193\n\t" \
  "v_add_f32 v194, 1.0, v194\n\t" \
  "v_add_f32 v195, 1.0, v195\n\t" \
  "v_cvt_pk_bf16_f32 v186, v196, v197\n\t" \
  "v_cvt_pk_bf16_f32 v187, v198, v199\n\t" \
  "v_mfma_f32_32x32x16_bf16 v[116:131], v[40:43], v[168:171], v[116:131]\n\t" \
  "v_rcp_f32 v192, v192\n\t" \
  "v_rcp_f32 v193, v193\n\t" \
  "v_rcp_f32 v194, v194\n\t" \
  "v_rcp_f32 v195, v195\n\t" \
  "s_sub_u32 %[nt], %[nt], 1\n\t" \
  "s_cmp_lg_u32 %[nt], %[thr]\n\t" \
  "s_cbranch_scc1 3f\n\t" \
  "s_setprio 0\n\t" \
  "3:\n\t" \
  "s_waitcnt lgkmcnt(4)\n\t" \
  "v_mfma_f32_32x32x16_bf16 v[116:131], v[212:215], v[164:167], v[116:131]\n\t" \
  "v_mul_f32 v196, 0xbfb8aa3b, v112\n\t" \
  "v_mul_f32 v197, 0xbfb8aa3b, v113\n\t" \
  "v_mul_f32 v198, 0xbfb8aa3b, v114\n\t" \
  "v_mul_f32 v199, 0xbfb8aa3b, v115\n\t" \
  "v_exp_f32 v196, v196\n\t" \
  "v_exp_f32 v197, v197\n\t" \
  "v_exp_f32 v198, v198\n\t" \
  "v_exp_f32 v199, v199\n\t" \
  "v_mfma_f32_32x32x16_bf16 v[116:131], v[48:51], v[172:175], v[116:131]\n\t" \
  "v_mul_f32 v192, v108, v192\n\t" \
  "v_mul_f32 v193, v109, v193\n\t" \
  "v_mul_f32 v194, v110, v194\n\t" \
  "v_mul_f32 v195, v111, v195\n\t" \
  "s_waitcnt vmcnt(0)\n\t" \
  "ds_read_b128 v[148:151], v234\n\t" \
  "ds_read_b128 v[152:155], v235\n\t" \
  "ds_read_b128 v[156:159], v236\n\t" \
  "ds_read_b128 v[160:163], v237\n\t" \
  "ds_read_b128 v[164:167], v238 offset:4096\n\t" \
  "ds_read_b64 v[224:225], v239 offset:5120\n\t" \
  "ds_write_b128 v243, v[184:187] offset:0\n\t" \
  "ds_read_b128 v[212:215], v247 offset:2048\n\t" \
  "v_add_f32 v196, 1.0, v196\n\t" \
  "v_add_f32 v197, 1.0, v197\n\t" \
  "v_add_f32 v198, 1.0, v198\n\t" \
  "v_add_f32 v199, 1.0, v199\n\t" \
  "v_cvt_pk_bf16_f32 v188, v192, v193\n\t" \
  "v_cvt_pk_bf16_f32 v189, v194, v195\n\t" \
  "v_mad_u32_u16 v220, v220, %[c128], v248\n\t" \
  "v_mad_u32_u16 v221, v221, %[c128], v249\n\t" \
  "v_mad_u32_u16 v222, v222, %[c128], v248\n\t" \
  "v_mfma_f32_32x32x16_bf16 v[68:83], v[204:207], v[180:183], v[68:83]\n\t" \
  "v_rcp_f32 v196, v196\n\t" \
  "v_rcp_f32 v197, v197\n\t" \
  "v_rcp_f32 v198, v198\n\t" \
  "v_rcp_f32 v199, v199\n\t" \
  "v_mad_u32_u16 v223, v223, %[c128], v249\n\t" \
  "v_or_b32 v226, v226, v250\n\t" \
  "v_mad_u32_u16 v227, v227, 6, v251\n\t" \
  "s_waitcnt lgkmcnt(9)\n\t" \
  "v_mfma_f32_32x32x16_bf16 v[84:99], v[200:203], v[176:179], v[84:99]\n\t" \
  "v_mul_f32 v192, 0xbfb8aa3b, v116\n\t" \
  "v_mul_f32 v193, 0xbfb8aa3b, v117\n\t" \
  "v_mul_f32 v194, 0xbfb8aa3b, v118\n\t" \
  "v_mul_f32 v195, 0xbfb8aa3b, v119\n\t" \
  "v_exp_f32 v192, v192\n\t" \
  "v_exp_f32 v193, v193\n\t" \
  "v_exp_f32 v194, v194\n\t" \
  "v_exp_f32 v195, v195\n\t" \
  "s_waitcnt lgkmcnt(8)\n\t" \
  "v_mfma_f32_32x32x16_bf16 v[84:99], v[208:211], v[180:183], v[84:99]\n\t" \
  "v_mul_f32 v196, v112, v196\n\t" \
  "v_mul_f32 v197, v113, v197\n\t" \
  "v_mul_f32 v198, v114, v198\n\t" \
  "v_mul_f32 v199, v115, v199\n\t" \
  "s_waitcnt lgkmcnt(2)\n\t" \
  "s_cmp_lt_i32 %[nt], 1\n\t" \
  "s_cbranch_scc1 4f\n\t" \
  "s_mov_b32 m0, %[b2]\n\t" \
  "s_nop 0\n\t" \
  "global_load_lds_dwordx4 v226, %[ea]\n\t" \
  "global_load_lds_dword v227, %[pos] offset:1024\n\t" \
  "s_mov_b32 m0, %[b1]\n\t" \
  "s_nop 0\n\t" \
  "global_load_lds_dwordx4 v220, %[h0]\n\t" \
  "global_load_lds_dwordx4 v221, %[h1] offset:1024\n\t" \
  "global_load_lds_dwordx4 v222, %[h2] offset:2048\n\t" \
  "global_load_lds_dwordx4 v223, %[h3] offset:3072\n\t" \
  "4:\n\t" \
  "v_add_f32 v192, 1.0, v192\n\t" \
  "v_add_f32 v193, 1.0, v193\n\t" \
  "v_add_f32 v194, 1.0, v194\n\t" \
  "v_add_f32 v195, 1.0, v195\n\t" \
  "v_cvt_pk_bf16_f32 v190, v196, v197\n\t" \
  "v_cvt_pk_bf16_f32 v191, v198, v199\n\t" \
  "v_lshrrev_b32 v232, 20, v232\n\t" \
  "v_add_u32 v220, %[posd], v232\n\t" \
  "v_add_u32 v221, v240, v232\n\t" \
  "ds_write_b128 v243, v[188:191] offset:16\n\t" \
  "ds_read_b128 v[176:179], v242 offset:5376\n\t" \
  "ds_read_b128 v[180:183], v242 offset:5408\n\t" \
  "ds_write_b16 v233, v255 offset:5376\n\t" \
  "ds_read_b128 v[216:219], v220\n\t" \
  "ds_read_b128 v[172:175], v221 offset:32\n\t" \
  "ds_read_b128 v[168:171], v221\n\t" \
  "ds_read_b64_tr_b16 v[200:201], v244 offset:0\n\t" \
  "ds_read_b64_tr_b16 v[202:203], v244 offset:320\n\t" \
  "ds_read_b64_tr_b16 v[204:205], v244 offset:1280\n\t" \
  "ds_read_b64_tr_b16 v[206:207], v244 offset:1600\n\t" \
  "ds_read_b128 v[208:211], v247 offset:3072\n\t" \
  "v_add_u32 v233, v241, v232\n\t" \
  "ds_write_b16_d16_hi v233, v255 offset:5376\n\t" \
  "s_waitcnt lgkmcnt(13)\n\t" \
  "v_mfma_f32_32x32x16_bf16 v[132:147], v[212:215], v[184:187], 0\n\t" \
  "v_rcp_f32 v192, v192\n\t" \
  "v_rcp_f32 v193, v193\n\t" \
  "v_rcp_f32 v194, v194\n\t" \
  "v_rcp_f32 v195, v195\n\t" \
  "v_mfma_f32_32x32x16_bf16 v[100:115], v[4:7], v[148:151], 0\n\t" \
  "v_mul_f32 v196, 0xbfb8aa3b, v120\n\t" \
  "v_mul_f32 v197, 0xbfb8aa3b, v121\n\t" \
  "v_mul_f32 v198, 0xbfb8aa3b, v122\n\t" \
  "v_mul_f32 v199, 0xbfb8aa3b, v123\n\t" \
  "v_exp_f32 v196, v196\n\t" \
  "v_exp_f32 v197, v197\n\t" \
  "v_exp_f32 v198, v198\n\t" \
  "v_exp_f32 v199, v199\n\t" \
  "v_mfma_f32_32x32x16_bf16 v[100:115], v[12:15], v[152:155], v[100:115]\n\t" \
  "v_mul_f32 v192, v116, v192\n\t" \
  "v_mul_f32 v193, v117, v193\n\t" \
  "v_mul_f32 v194, v118, v194\n\t" \
  "v_mul_f32 v195, v119, v195\n\t" \
  "v_add_f32 v196, 1.0, v196\n\t" \
  "v_add_f32 v197, 1.0, v197\n\t" \
  "v_add_f32 v198, 1.0, v198\n\t" \
  "v_add_f32 v199, 1.0, v199\n\t" \
  "v_cvt_pk_bf16_f32 v184, v192, v193\n\t" \
  "v_cvt_pk_bf16_f32 v185, v194, v195\n\t" \
  "v_and_b32 v225, 0xffff0000, v225\n\t" \
  "s_waitcnt lgkmcnt(8)\n\t" \
  "v_lshlrev_b32 v219, 16, v224\n\t" \
  "v_and_b32 v224, 0xffff0000, v224\n\t" \
  "v_sub_f32 v229, v218, v225\n\t" \
  "v_sub_f32 v216, v216, v219\n\t" \
  "v_sub_f32 v217, v217, v224\n\t" \
  "v_mfma_f32_32x32x16_bf16 v[100:115], v[20:23], v[156:159], v[100:115]\n\t" \
  "v_rcp_f32 v196, v196\n\t" \
  "v_rcp_f32 v197, v197\n\t" \
  "v_rcp_f32 v198, v198\n\t" \
  "v_rcp_f32 v199, v199\n\t" \
  "v_mul_f32 v218, v229, v229\n\t" \
  "v_cndmask_b32 v228, v216, v217, vcc\n\t" \
  "v_fmac_f32 v218, v216, v216\n\t" \
  "v_fmac_f32 v218, v217, v217\n\t" \
  "v_cvt_pk_bf16_f32 v218, v218, v218\n\t" \
  "s_waitcnt lgkmcnt(7)\n\t" \
  "v_cndmask_b32_sdwa v175, v175, v218, vcc dst_sel:WORD_1 dst_unused:UNUSED_PRESERVE src0_sel:WORD_1 src1_sel:WORD_1\n\t" \
  "s_waitcnt lgkmcnt(1)\n\t" \
  "v_mfma_f32_32x32x16_bf16 v[132:147], v[208:211], v[188:191], v[132:147]\n\t" \
  "v_mul_f32 v192, 0xbfb8aa3b, v124\n\t" \
  "v_mul_f32 v193, 0xbfb8aa3b, v125\n\t" \
  "v_mul_f32 v194, 0xbfb8aa3b, v126\n\t" \
  "v_mul_f32 v195, 0xbfb8aa3b, v127\n\t" \
  "v_exp_f32 v192, v192\n\t" \
  "v_exp_f32 v193, v193\n\t" \
  "v_exp_f32 v194, v194\n\t" \
  "v_exp_f32 v195, v195\n\t" \
  "ds_read_b128 v[212:215], v247 offset:0\n\t" \
  "ds_read2_b32 v[220:221], v252 offset0:0 offset1:2\n\t" \
  "ds_read2_b32 v[222:223], v252 offset0:4 offset1:6\n\t" \
  "ds_read_b64 v[226:227], v253\n\t" \
  "ds_read_b32 v232, v254\n\t" \
  "v_mfma_f32_32x32x16_bf16 v[100:115], v[28:31], v[160:163], v[100:115]\n\t" \
  "v_mul_f32 v196, v120, v196\n\t" \
  "v_mul_f32 v197, v121, v197\n\t" \
  "v_mul_f32 v198, v122, v198\n\t" \
  "v_mul_f32 v199, v123, v199\n\t" \
  "v_add_u32 v252, 0x100, v252\n\t" \
  "v_add_u32 v253, 0x100, v253\n\t" \
  "v_add_u32 v254, 0x100, v254\n\t" \
  "v_add_f32 v192, 1.0, v192\n\t" \
  "v_add_f32 v193, 1.0, v193\n\t" \
  "v_add_f32 v194, 1.0, v194\n\t" \
  "v_add_f32 v195, 1.0, v195\n\t" \
  "v_cvt_pk_bf16_f32 v186, v196, v197\n\t" \
  "v_cvt_pk_bf16_f32 v187, v198, v199\n\t" \
  "v_mfma_f32_32x32x16_bf16 v[100:115], v[36:39], v[168:171], v[100:115]\n\t" \
  "v_rcp_f32 v192, v192\n\t" \
  "v_rcp_f32 v193, v193\n\t" \
  "v_rcp_f32 v194, v194\n\t" \
  "v_rcp_f32 v195, v195\n\t" \
  "v_mfma_f32_32x32x16_bf16 v[100:115], v[44:47], v[172:175], v[100:115]\n\t" \
  "v_mul_f32 v196, 0xbfb8aa3b, v128\n\t" \
  "v_mul_f32 v197, 0xbfb8aa3b, v129\n\t" \
  "v_mul_f32 v198, 0xbfb8aa3b, v130\n\t" \
  "v_mul_f32 v199, 0xbfb8aa3b, v131\n\t" \
  "v_exp_f32 v196, v196\n\t" \
  "v_exp_f32 v197, v197\n\t" \
  "v_exp_f32 v198, v198\n\t" \
  "v_exp_f32 v199, v199\n\t" \
  "s_waitcnt lgkmcnt(4)\n\t" \
  "v_mfma_f32_32x32x16_bf16 v[100:115], v[212:215], v[164:167], v[100:115]\n\t" \
  "v_mul_f32 v192, v124, v192\n\t" \
  "v_mul_f32 v193, v125, v193\n\t" \
  "v_mul_f32 v194, v126, v194\n\t" \
  "v_mul_f32 v195, v127, v195\n\t" \
  "ds_write_b128 v243, v[184:187] offset:0\n\t" \
  "ds_read_b128 v[208:211], v247 offset:5120\n\t" \
  "ds_read_b128 v[212:215], v247 offset:4096\n\t" \
  "v_add_f32 v196, 1.0, v196\n\t" \
  "v_add_f32 v197, 1.0, v197\n\t" \
  "v_add_f32 v198, 1.0, v198\n\t" \
  "v_add_f32 v199, 1.0, v199\n\t" \
  "v_cvt_pk_bf16_f32 v188, v192, v193\n\t" \
  "v_cvt_pk_bf16_f32 v189, v194, v195\n\t" \
  "v_mfma_f32_32x32x16_bf16 v[52:67], v[200:203], v[176:179], v[52:67]\n\t" \
  "v_rcp_f32 v196, v196\n\t" \
  "v_rcp_f32 v197, v197\n\t" \
  "v_rcp_f32 v198, v198\n\t" \
  "v_rcp_f32 v199, v199\n\t" \
  "v_mfma_f32_32x32x16_bf16 v[52:67], v[204:207], v[180:183], v[52:67]\n\t" \
  "v_mul_f32 v192, 0xbfb8aa3b, v100\n\t" \
  "v_mul_f32 v193, 0xbfb8aa3b, v101\n\t" \
  "v_mul_f32 v194, 0xbfb8aa3b, v102\n\t" \
  "v_mul_f32 v195, 0xbfb8aa3b, v103\n\t" \
  "v_exp_f32 v192, v192\n\t" \
  "v_exp_f32 v193, v193\n\t" \
  "v_exp_f32 v194, v194\n\t" \
  "v_exp_f32 v195, v195\n\t" \
  "s_cmp_eq_u32 %[nt], 0\n\t" \
  "s_waitcnt lgkmcnt(0)\n\t" \
  "v_mfma_f32_32x32x16_bf16 v[132:147], v[212:215], v[184:187], v[132:147]\n\t" \
  "v_mul_f32 v196, v128, v196\n\t" \
  "v_mul_f32 v197, v129, v197\n\t" \
  "v_mul_f32 v198, v130, v198\n\t" \
  "v_mul_f32 v199, v131, v199\n\t" \
  "v_add_f32 v192, 1.0, v192\n\t" \
  "v_add_f32 v193, 1.0, v193\n\t" \
  "v_add_f32 v194, 1.0, v194\n\t" \
  "v_add_f32 v195, 1.0, v195\n\t" \
  "v_cvt_pk_bf16_f32 v190, v196, v197\n\t" \
  "v_cvt_pk_bf16_f32 v191, v198, v199\n\t" \
  "s_cbranch_scc0 1b\n\t" \
  "s_cmp_lg_u32 %[last], 0\n\t" \
  "s_cbranch_scc1 8f\n\t" \
  "ds_write_b128 v243, v[188:191] offset:16\n\t" \
  "ds_read_b64_tr_b16 v[200:201], v244 offset:0\n\t" \
  "ds_read_b64_tr_b16 v[202:203], v244 offset:320\n\t" \
  "ds_read_b64_tr_b16 v[204:205], v244 offset:1280\n\t" \
  "ds_read_b64_tr_b16 v[206:207], v244 offset:1600\n\t" \
  "v_mfma_f32_32x32x16_bf16 v[132:147], v[208:211], v[188:191], v[132:147]\n\t" \
  "s_waitcnt lgkmcnt(0)\n\t" \
  "v_mfma_f32_32x32x16_bf16 v[68:83], v[200:203], v[176:179], v[68:83]\n\t" \
  "v_mfma_f32_32x32x16_bf16 v[68:83], v[204:207], v[180:183], v[68:83]\n\t" \
  "s_nop 8\n\t" \
  "v_mul_f32 v230, v230, v132\n\t" \
  "v_mul_f32 v231, v231, v132\n\t" \
  "v_cvt_pk_bf16_f32 v230, v230, v231\n\t" \
  "ds_write_b16 v245, v230 offset:7456\n\t" \
  "ds_write_b16_d16_hi v241, v230 offset:7584\n\t" \
  "ds_read_b128 v[200:203], v246 offset:7456\n\t" \
  "ds_read_b128 v[208:211], v246 offset:7488\n\t" \
  "s_waitcnt lgkmcnt(0)\n\t" \
  "v_mfma_f32_32x32x16_bf16 v[84:99], v[200:203], v[176:179], v[84:99]\n\t" \
  "v_mfma_f32_32x32x16_bf16 v[84:99], v[208:211], v[180:183], v[84:99]\n\t" \
  "s_branch 9f\n\t" \
  "6:\n\t" \
  "s_cmp_lg_u32 %[last], 0\n\t" \
  "s_cbranch_scc0 9f\n\t" \
  "s_waitcnt lgkmcnt(0)\n\t" \
  "v_cndmask_b32 v224, 0, 24, vcc\n\t" \
  "v_xor_b32 v220, v234, v224\n\t" \
  "v_xor_b32 v221, v235, v224\n\t" \
  "v_xor_b32 v222, v236, v224\n\t" \
  "v_xor_b32 v223, v237, v224\n\t" \
  "v_xor_b32 v216, 16, v220\n\t" \
  "v_xor_b32 v217, 16, v221\n\t" \
  "v_xor_b32 v218, 16, v222\n\t" \
  "v_xor_b32 v219, 16, v223\n\t" \
  "ds_read_b64 v[148:149], v220\n\t" \
  "ds_read_b64 v[150:151], v216\n\t" \
  "ds_read_b64 v[152:153], v221\n\t" \
  "ds_read_b64 v[154:155], v217\n\t" \
  "ds_read_b64 v[156:157], v222\n\t" \
  "ds_read_b64 v[158:159], v218\n\t" \
  "ds_read_b64 v[160:161], v223\n\t" \
  "ds_read_b64 v[162:163], v219\n\t" \
  "ds_read_b128 v[4:7], v247 offset:6144\n\t" \
  "ds_read_b128 v[8:11], v247 offset:7168\n\t" \
  "ds_read_b128 v[12:15], v247 offset:8192\n\t" \
  "ds_read_b128 v[16:19], v247 offset:9216\n\t" \
  "ds_read_b128 v[20:23], v247 offset:10240\n\t" \
  "ds_read_b128 v[24:27], v247 offset:11264\n\t" \
  "v_subrev_u32 v225, %[b1], v241\n\t" \
  "v_lshl_add_u32 v225, v225, 5, v243\n\t" \
  "v_sub_u32 v225, v225, v224\n\t" \
  "s_nop 4\n\t" \
  "v_cvt_pk_bf16_f32 v184, v52, v53\n\t" \
  "v_cvt_pk_bf16_f32 v185, v54, v55\n\t" \
  "v_cvt_pk_bf16_f32 v186, v56, v57\n\t" \
  "v_cvt_pk_bf16_f32 v187, v58, v59\n\t" \
  "v_cvt_pk_bf16_f32 v188, v60, v61\n\t" \
  "v_cvt_pk_bf16_f32 v189, v62, v63\n\t" \
  "v_cvt_pk_bf16_f32 v190, v64, v65\n\t" \
  "v_cvt_pk_bf16_f32 v191, v66, v67\n\t" \
  "v_cvt_pk_bf16_f32 v36, v68, v69\n\t" \
  "v_cvt_pk_bf16_f32 v37, v70, v71\n\t" \
  "v_cvt_pk_bf16_f32 v38, v72, v73\n\t" \
  "v_cvt_pk_bf16_f32 v39, v74, v75\n\t" \
  "v_cvt_pk_bf16_f32 v40, v76, v77\n\t" \
  "v_cvt_pk_bf16_f32 v41, v78, v79\n\t" \
  "v_cvt_pk_bf16_f32 v42, v80, v81\n\t" \
  "v_cvt_pk_bf16_f32 v43, v82, v83\n\t" \
  "s_waitcnt lgkmcnt(5)\n\t" \
  "v_mfma_f32_32x32x16_bf16 v[100:115], v[4:7], v[148:151], 0\n\t" \
  "s_waitcnt lgkmcnt(4)\n\t" \
  "v_mfma_f32_32x32x16_bf16 v[116:131], v[8:11], v[148:151], 0\n\t" \
  "ds_read_b128 v[28:31], v247 offset:12288\n\t" \
  "ds_read_b128 v[32:35], v247 offset:13312\n\t" \
  "s_waitcnt lgkmcnt(5)\n\t" \
  "v_mfma_f32_32x32x16_bf16 v[100:115], v[12:15], v[152:155], v[100:115]\n\t" \
  "s_waitcnt lgkmcnt(4)\n\t" \
  "v_mfma_f32_32x32x16_bf16 v[116:131], v[16:19], v[152:155], v[116:131]\n\t" \
  "ds_read_b128 v[4:7], v247 offset:14336\n\t" \
  "ds_read_b128 v[8:11], v247 offset:15360\n\t" \
  "s_waitcnt lgkmcnt(5)\n\t" \
  "v_mfma_f32_32x32x16_bf16 v[100:115], v[20:23], v[156:159], v[100:115]\n\t" \
  "s_waitcnt lgkmcnt(4)\n\t" \
  "v_mfma_f32_32x32x16_bf16 v[116:131], v[24:27], v[156:159], v[116:131]\n\t" \
  "ds_read_b128 v[12:15], v247 offset:16384\n\t" \
  "ds_read_b128 v[16:19], v247 offset:17408\n\t" \
  "s_waitcnt lgkmcnt(5)\n\t" \
  "v_mfma_f32_32x32x16_bf16 v[100:115], v[28:31], v[160:163], v[100:115]\n\t" \
  "s_waitcnt lgkmcnt(4)\n\t" \
  "v_mfma_f32_32x32x16_bf16 v[116:131], v[32:35], v[160:163], v[116:131]\n\t" \
  "ds_read_b128 v[20:23], v247 offset:18432\n\t" \
  "ds_read_b128 v[24:27], v247 offset:19456\n\t" \
  "s_waitcnt lgkmcnt(5)\n\t" \
  "v_mfma_f32_32x32x16_bf16 v[100:115], v[4:7], v[184:187], v[100:115]\n\t" \
  "ds_read_b128 v[28:31], v247 offset:20480\n\t" \
  "ds_read_b128 v[32:35], v247 offset:21504\n\t" \
  "s_waitcnt lgkmcnt(5)\n\t" \
  "v_mfma_f32_32x32x16_bf16 v[100:115], v[12:15], v[188:191], v[100:115]\n\t" \
  "s_waitcnt lgkmcnt(3)\n\t" \
  "v_mfma_f32_32x32x16_bf16 v[100:115], v[20:23], v[36:39], v[100:115]\n\t" \
  "s_waitcnt lgkmcnt(1)\n\t" \
  "v_mfma_f32_32x32x16_bf16 v[100:115], v[28:31], v[40:43], v[100:115]\n\t" \
  "s_waitcnt lgkmcnt(0)\n\t" \
  "s_branch 10f\n\t" \
  "8:\n\t" \
  "ds_write_b128 v243, v[188:191] offset:16\n\t" \
  "ds_read_b64_tr_b16 v[200:201], v244 offset:0\n\t" \
  "ds_read_b64_tr_b16 v[202:203], v244 offset:320\n\t" \
  "ds_read_b64_tr_b16 v[204:205], v244 offset:1280\n\t" \
  "ds_read_b64_tr_b16 v[206:207], v244 offset:1600\n\t" \
  "v_mfma_f32_32x32x16_bf16 v[132:147], v[208:211], v[188:191], v[132:147]\n\t" \
  "v_cndmask_b32 v224, 0, 24, vcc\n\t" \
  "v_xor_b32 v220, v234, v224\n\t" \
  "v_xor_b32 v221, v235, v224\n\t" \
  "v_xor_b32 v222, v236, v224\n\t" \
  "v_xor_b32 v223, v237, v224\n\t" \
  "v_xor_b32 v216, 16, v220\n\t" \
  "v_xor_b32 v217, 16, v221\n\t" \
  "v_xor_b32 v218, 16, v222\n\t" \
  "v_xor_b32 v219, 16, v223\n\t" \
  "ds_read_b64 v[148:149], v220\n\t" \
  "ds_read_b64 v[150:151], v216\n\t" \
  "ds_read_b64 v[152:153], v221\n\t" \
  "ds_read_b64 v[154:155], v217\n\t" \
  "ds_read_b64 v[156:157], v222\n\t" \
  "ds_read_b64 v[158:159], v218\n\t" \
  "ds_read_b64 v[160:161], v223\n\t" \
  "ds_read_b64 v[162:163], v219\n\t" \
  "ds_read_b128 v[4:7], v247 offset:6144\n\t" \
  "ds_read_b128 v[8:11], v247 offset:7168\n\t" \
  "s_waitcnt lgkmcnt(11)\n\t" \
  "ds_read_b128 v[12:15], v247 offset:8192\n\t" \
  "ds_read_b128 v[16:19], v247 offset:9216\n\t" \
  "ds_read_b128 v[20:23], v247 offset:10240\n\t" \
  "ds_read_b128 v[24:27], v247 offset:11264\n\t" \
  "v_subrev_u32 v225, %[b1], v241\n\t" \
  "v_lshl_add_u32 v225, v225, 5, v243\n\t" \
  "v_sub_u32 v225, v225, v224\n\t" \
  "v_mfma_f32_32x32x16_bf16 v[68:83], v[200:203], v[176:179], v[68:83]\n\t" \
  "s_waitcnt lgkmcnt(5)\n\t" \
  "v_mfma_f32_32x32x16_bf16 v[100:115], v[4:7], v[148:151], 0\n\t" \
  "v_mfma_f32_32x32x16_bf16 v[68:83], v[204:207], v[180:183], v[68:83]\n\t" \
  "s_waitcnt lgkmcnt(4)\n\t" \
  "v_mfma_f32_32x32x16_bf16 v[116:131], v[8:11], v[148:151], 0\n\t" \
  "ds_read_b128 v[28:31], v247 offset:12288\n\t" \
  "ds_read_b128 v[32:35], v247 offset:13312\n\t" \
  "s_waitcnt lgkmcnt(5)\n\t" \
  "v_mfma_f32_32x32x16_bf16 v[100:115], v[12:15], v[152:155], v[100:115]\n\t" \
  "v_mul_f32 v230, v230, v132\n\t" \
  "v_mul_f32 v231, v231, v132\n\t" \
  "v_cvt_pk_bf16_f32 v230, v230, v231\n\t" \
  "ds_write_b16 v245, v230 offset:7456\n\t" \
  "ds_write_b16_d16_hi v241, v230 offset:7584\n\t" \
  "ds_read_b128 v[200:203], v246 offset:7456\n\t" \
  "ds_read_b128 v[208:211], v246 offset:7488\n\t" \
  "v_cvt_pk_bf16_f32 v184, v52, v53\n\t" \
  "v_cvt_pk_bf16_f32 v185, v54, v55\n\t" \
  "v_cvt_pk_bf16_f32 v186, v56, v57\n\t" \
  "v_cvt_pk_bf16_f32 v187, v58, v59\n\t" \
  "s_waitcnt lgkmcnt(8)\n\t" \
  "v_mfma_f32_32x32x16_bf16 v[116:131], v[16:19], v[152:155], v[116:131]\n\t" \
  "ds_read_b128 v[4:7], v247 offset:14336\n\t" \
  "ds_read_b128 v[8:11], v247 offset:15360\n\t" \
  "s_waitcnt lgkmcnt(9)\n\t" \
  "v_mfma_f32_32x32x16_bf16 v[100:115], v[20:23], v[156:159], v[100:115]\n\t" \
  "v_cvt_pk_bf16_f32 v188, v60, v61\n\t" \
  "v_cvt_pk_bf16_f32 v189, v62, v63\n\t" \
  "v_cvt_pk_bf16_f32 v190, v64, v65\n\t" \
  "v_cvt_pk_bf16_f32 v191, v66, v67\n\t" \
  "s_waitcnt lgkmcnt(8)\n\t" \
  "v_mfma_f32_32x32x16_bf16 v[116:131], v[24:27], v[156:159], v[116:131]\n\t" \
  "ds_read_b128 v[12:15], v247 offset:16384\n\t" \
  "ds_read_b128 v[16:19], v247 offset:17408\n\t" \
  "s_waitcnt lgkmcnt(9)\n\t" \
  "v_mfma_f32_32x32x16_bf16 v[100:115], v[28:31], v[160:163], v[100:115]\n\t" \
  "v_cvt_pk_bf16_f32 v36, v68, v69\n\t" \
  "v_cvt_pk_bf16_f32 v37, v70, v71\n\t" \
  "v_cvt_pk_bf16_f32 v38, v72, v73\n\t" \
  "v_cvt_pk_bf16_f32 v39, v74, v75\n\t" \
  "s_waitcnt lgkmcnt(5)\n\t" \
  "v_mfma_f32_32x32x16_bf16 v[84:99], v[200:203], v[176:179], v[84:99]\n\t" \
  "v_cvt_pk_bf16_f32 v40, v76, v77\n\t" \
  "v_cvt_pk_bf16_f32 v41, v78, v79\n\t" \
  "v_cvt_pk_bf16_f32 v42, v80, v81\n\t" \
  "v_cvt_pk_bf16_f32 v43, v82, v83\n\t" \
  "v_mfma_f32_32x32x16_bf16 v[116:131], v[32:35], v[160:163], v[116:131]\n\t" \
  "ds_read_b128 v[20:23], v247 offset:18432\n\t" \
  "ds_read_b128 v[24:27], v247 offset:19456\n\t" \
  "s_waitcnt lgkmcnt(6)\n\t" \
  "v_mfma_f32_32x32x16_bf16 v[84:99], v[208:211], v[180:183], v[84:99]\n\t" \
  "s_waitcnt lgkmcnt(5)\n\t" \
  "v_mfma_f32_32x32x16_bf16 v[100:115], v[4:7], v[184:187], v[100:115]\n\t" \
  "ds_read_b128 v[28:31], v247 offset:20480\n\t" \
  "ds_read_b128 v[32:35], v247 offset:21504\n\t" \
  "s_waitcnt lgkmcnt(5)\n\t" \
  "v_mfma_f32_32x32x16_bf16 v[100:115], v[12:15], v[188:191], v[100:115]\n\t" \
  "s_waitcnt lgkmcnt(3)\n\t" \
  "v_mfma_f32_32x32x16_bf16 v[100:115], v[20:23], v[36:39], v[100:115]\n\t" \
  "s_waitcnt lgkmcnt(1)\n\t" \
  "v_mfma_f32_32x32x16_bf16 v[100:115], v[28:31], v[40:43], v[100:115]\n\t" \
  "s_waitcnt lgkmcnt(0)\n\t" \
  "10:\n\t" \
  "v_mov_b32 v212, 0xbfb8aa3b\n\t" \
  "v_mfma_f32_32x32x16_bf16 v[116:131], v[8:11], v[184:187], v[116:131]\n\t" \
  "v_mfma_f32_32x32x16_bf16 v[116:131], v[16:19], v[188:191], v[116:131]\n\t" \
  "s_nop 8\n\t" \
  "v_mul_f32 v192, 0xbfb8aa3b, v100\n\t" \
  "v_mul_f32 v193, 0xbfb8aa3b, v101\n\t" \
  "v_mul_f32 v194, 0xbfb8aa3b, v102\n\t" \
  "v_mul_f32 v195, 0xbfb8aa3b, v103\n\t" \
  "v_exp_f32 v192, v192\n\t" \
  "v_exp_f32 v193, v193\n\t" \
  "v_exp_f32 v194, v194\n\t" \
  "v_exp_f32 v195, v195\n\t" \
  "v_lshlrev_b32 v200, 16, v148\n\t" \
  "v_and_b32 v201, 0xffff0000, v148\n\t" \
  "v_lshlrev_b32 v202, 16, v149\n\t" \
  "v_and_b32 v203, 0xffff0000, v149\n\t" \
  "v_mfma_f32_32x32x16_bf16 v[116:131], v[24:27], v[36:39], v[116:131]\n\t" \
  "v_add_f32 v192, 1.0, v192\n\t" \
  "v_add_f32 v193, 1.0, v193\n\t" \
  "v_add_f32 v194, 1.0, v194\n\t" \
  "v_add_f32 v195, 1.0, v195\n\t" \
  "v_rcp_f32 v192, v192\n\t" \
  "v_rcp_f32 v193, v193\n\t" \
  "v_rcp_f32 v194, v194\n\t" \
  "v_rcp_f32 v195, v195\n\t" \
  "v_mfma_f32_32x32x16_bf16 v[116:131], v[32:35], v[40:43], v[116:131]\n\t" \
  "v_mul_f32 v196, 0xbfb8aa3b, v104\n\t" \
  "v_mul_f32 v197, 0xbfb8aa3b, v105\n\t" \
  "v_mul_f32 v198, 0xbfb8aa3b, v106\n\t" \
  "v_mul_f32 v199, 0xbfb8aa3b, v107\n\t" \
  "v_exp_f32 v196, v196\n\t" \
  "v_exp_f32 v197, v197\n\t" \
  "v_exp_f32 v198, v198\n\t" \
  "v_exp_f32 v199, v199\n\t" \
  "v_fmac_f32 v200, v100, v192\n\t" \
  "v_fmac_f32 v201, v101, v193\n\t" \
  "v_fmac_f32 v202, v102, v194\n\t" \
  "v_fmac_f32 v203, v103, v195\n\t" \
  "v_lshlrev_b32 v204, 16, v150\n\t" \
  "v_and_b32 v205, 0xffff0000, v150\n\t" \
  "v_lshlrev_b32 v206, 16, v151\n\t" \
  "v_and_b32 v207, 0xffff0000, v151\n\t" \
  "v_add_f32 v196, 1.0, v196\n\t" \
  "v_add_f32 v197, 1.0, v197\n\t" \
  "v_add_f32 v198, 1.0, v198\n\t" \
  "v_add_f32 v199, 1.0, v199\n\t" \
  "v_cvt_pk_bf16_f32 v176, v200, v201\n\t" \
  "v_cvt_pk_bf16_f32 v177, v202, v203\n\t" \
  "ds_write_b64 v225, v[176:177] offset:0\n\t" \
  "v_rcp_f32 v196, v196\n\t" \
  "v_rcp_f32 v197, v197\n\t" \
  "v_rcp_f32 v198, v198\n\t" \
  "v_rcp_f32 v199, v199\n\t" \
  "v_mul_f32 v192, 0xbfb8aa3b, v108\n\t" \
  "v_mul_f32 v193, 0xbfb8aa3b, v109\n\t" \
  "v_mul_f32 v194, 0xbfb8aa3b, v110\n\t" \
  "v_mul_f32 v195, 0xbfb8aa3b, v111\n\t" \
  "v_exp_f32 v192, v192\n\t" \
  "v_exp_f32 v193, v193\n\t" \
  "v_exp_f32 v194, v194\n\t" \
  "v_exp_f32 v195, v195\n\t" \
  "v_fmac_f32 v204, v104, v196\n\t" \
  "v_fmac_f32 v205, v105, v197\n\t" \
  "v_fmac_f32 v206, v106, v198\n\t" \
  "v_fmac_f32 v207, v107, v199\n\t" \
  "v_lshlrev_b32 v200, 16, v152\n\t" \
  "v_and_b32 v201, 0xffff0000, v152\n\t" \
  "v_lshlrev_b32 v202, 16, v153\n\t" \
  "v_and_b32 v203, 0xffff0000, v153\n\t" \
  "v_add_f32 v192, 1.0, v192\n\t" \
  "v_add_f32 v193, 1.0, v193\n\t" \
  "v_add_f32 v194, 1.0, v194\n\t" \
  "v_add_f32 v195, 1.0, v195\n\t" \
  "v_cvt_pk_bf16_f32 v178, v204, v205\n\t" \
  "v_cvt_pk_bf16_f32 v179, v206, v207\n\t" \
  "ds_write_b64 v225, v[178:179] offset:16\n\t" \
  "v_rcp_f32 v192, v192\n\t" \
  "v_rcp_f32 v193, v193\n\t" \
  "v_rcp_f32 v194, v194\n\t" \
  "v_rcp_f32 v195, v195\n\t" \
  "v_mul_f32 v196, 0xbfb8aa3b, v112\n\t" \
  "v_mul_f32 v197, 0xbfb8aa3b, v113\n\t" \
  "v_mul_f32 v198, 0xbfb8aa3b, v114\n\t" \
  "v_mul_f32 v199, 0xbfb8aa3b, v115\n\t" \
  "v_exp_f32 v196, v196\n\t" \
  "v_exp_f32 v197, v197\n\t" \
  "v_exp_f32 v198, v198\n\t" \
  "v_exp_f32 v199, v199\n\t" \
  "v_fmac_f32 v200, v108, v192\n\t" \
  "v_fmac_f32 v201, v109, v193\n\t" \
  "v_fmac_f32 v202, v110, v194\n\t" \
  "v_fmac_f32 v203, v111, v195\n\t" \
  "v_lshlrev_b32 v204, 16, v154\n\t" \
  "v_and_b32 v205, 0xffff0000, v154\n\t" \
  "v_lshlrev_b32 v206, 16, v155\n\t" \
  "v_and_b32 v207, 0xffff0000, v155\n\t" \
  "v_add_f32 v196, 1.0, v196\n\t" \
  "v_add_f32 v197, 1.0, v197\n\t" \
  "v_add_f32 v198, 1.0, v198\n\t" \
  "v_add_f32 v199, 1.0, v199\n\t" \
  "v_cvt_pk_bf16_f32 v180, v200, v201\n\t" \
  "v_cvt_pk_bf16_f32 v181, v202, v203\n\t" \
  "ds_write_b64 v225, v[180:181] offset:32\n\t" \
  "v_rcp_f32 v196, v196\n\t" \
  "v_rcp_f32 v197, v197\n\t" \
  "v_rcp_f32 v198, v198\n\t" \
  "v_rcp_f32 v199, v199\n\t" \
  "v_mul_f32 v192, 0xbfb8aa3b, v116\n\t" \
  "v_mul_f32 v193, 0xbfb8aa3b, v117\n\t" \
  "v_mul_f32 v194, 0xbfb8aa3b, v118\n\t" \
  "v_mul_f32 v195, 0xbfb8aa3b, v119\n\t" \
  "v_exp_f32 v192, v192\n\t" \
  "v_exp_f32 v193, v193\n\t" \
  "v_exp_f32 v194, v194\n\t" \
  "v_exp_f32 v195, v195\n\t" \
  "v_fmac_f32 v204, v112, v196\n\t" \
  "v_fmac_f32 v205, v113, v197\n\t" \
  "v_fmac_f32 v206, v114, v198\n\t" \
  "v_fmac_f32 v207, v115, v199\n\t" \
  "v_lshlrev_b32 v200, 16, v156\n\t" \
  "v_and_b32 v201, 0xffff0000, v156\n\t" \
  "v_lshlrev_b32 v202, 16, v157\n\t" \
  "v_and_b32 v203, 0xffff0000, v157\n\t" \
  "v_add_f32 v192, 1.0, v192\n\t" \
  "v_add_f32 v193, 1.0, v193\n\t" \
  "v_add_f32 v194, 1.0, v194\n\t" \
  "v_add_f32 v195, 1.0, v195\n\t" \
  "v_cvt_pk_bf16_f32 v182, v204, v205\n\t" \
  "v_cvt_pk_bf16_f32 v183, v206, v207\n\t" \
  "ds_write_b64 v225, v[182:183] offset:48\n\t" \
  "v_rcp_f32 v192, v192\n\t" \
  "v_rcp_f32 v193, v193\n\t" \
  "v_rcp_f32 v194, v194\n\t" \
  "v_rcp_f32 v195, v195\n\t" \
  "v_mul_f32 v196, 0xbfb8aa3b, v120\n\t" \
  "v_mul_f32 v197, 0xbfb8aa3b, v121\n\t" \
  "v_mul_f32 v198, 0xbfb8aa3b, v122\n\t" \
  "v_mul_f32 v199, 0xbfb8aa3b, v123\n\t" \
  "v_exp_f32 v196, v196\n\t" \
  "v_exp_f32 v197, v197\n\t" \
  "v_exp_f32 v198, v198\n\t" \
  "v_exp_f32 v199, v199\n\t" \
  "v_fmac_f32 v200, v116, v192\n\t" \
  "v_fmac_f32 v201, v117, v193\n\t" \
  "v_fmac_f32 v202, v118, v194\n\t" \
  "v_fmac_f32 v203, v119, v195\n\t" \
  "v_lshlrev_b32 v204, 16, v158\n\t" \
  "v_and_b32 v205, 0xffff0000, v158\n\t" \
  "v_lshlrev_b32 v206, 16, v159\n\t" \
  "v_and_b32 v207, 0xffff0000, v159\n\t" \
  "v_add_f32 v196, 1.0, v196\n\t" \
  "v_add_f32 v197, 1.0, v197\n\t" \
  "v_add_f32 v198, 1.0, v198\n\t" \
  "v_add_f32 v199, 1.0, v199\n\t" \
  "v_cvt_pk_bf16_f32 v176, v200, v201\n\t" \
  "v_cvt_pk_bf16_f32 v177, v202, v203\n\t" \
  "ds_write_b64 v225, v[176:177] offset:64\n\t" \
  "v_rcp_f32 v196, v196\n\t" \
  "v_rcp_f32 v197, v197\n\t" \
  "v_rcp_f32 v198, v198\n\t" \
  "v_rcp_f32 v199, v199\n\t" \
  "v_mul_f32 v192, 0xbfb8aa3b, v124\n\t" \
  "v_mul_f32 v193, 0xbfb8aa3b, v125\n\t" \
  "v_mul_f32 v194, 0xbfb8aa3b, v126\n\t" \
  "v_mul_f32 v195, 0xbfb8aa3b, v127\n\t" \
  "v_exp_f32 v192, v192\n\t" \
  "v_exp_f32 v193, v193\n\t" \
  "v_exp_f32 v194, v194\n\t" \
  "v_exp_f32 v195, v195\n\t" \
  "v_fmac_f32 v204, v120, v196\n\t" \
  "v_fmac_f32 v205, v121, v197\n\t" \
  "v_fmac_f32 v206, v122, v198\n\t" \
  "v_fmac_f32 v207, v123, v199\n\t" \
  "v_lshlrev_b32 v200, 16, v160\n\t" \
  "v_and_b32 v201, 0xffff0000, v160\n\t" \
  "v_lshlrev_b32 v202, 16, v161\n\t" \
  "v_and_b32 v203, 0xffff0000, v161\n\t" \
  "v_add_f32 v192, 1.0, v192\n\t" \
  "v_add_f32 v193, 1.0, v193\n\t" \
  "v_add_f32 v194, 1.0, v194\n\t" \
  "v_add_f32 v195, 1.0, v195\n\t" \
  "v_cvt_pk_bf16_f32 v178, v204, v205\n\t" \
  "v_cvt_pk_bf16_f32 v179, v206, v207\n\t" \
  "ds_write_b64 v225, v[178:179] offset:80\n\t" \
  "v_rcp_f32 v192, v192\n\t" \
  "v_rcp_f32 v193, v193\n\t" \
  "v_rcp_f32 v194, v194\n\t" \
  "v_rcp_f32 v195, v195\n\t" \
  "v_mul_f32 v196, 0xbfb8aa3b, v128\n\t" \
  "v_mul_f32 v197, 0xbfb8aa3b, v129\n\t" \
  "v_mul_f32 v198, 0xbfb8aa3b, v130\n\t" \
  "v_mul_f32 v199, 0xbfb8aa3b, v131\n\t" \
  "v_exp_f32 v196, v196\n\t" \
  "v_exp_f32 v197, v197\n\t" \
  "v_exp_f32 v198, v198\n\t" \
  "v_exp_f32 v199, v199\n\t" \
  "v_fmac_f32 v200, v124, v192\n\t" \
  "v_fmac_f32 v201, v125, v193\n\t" \
  "v_fmac_f32 v202, v126, v194\n\t" \
  "v_fmac_f32 v203, v127, v195\n\t" \
  "v_lshlrev_b32 v204, 16, v162\n\t" \
  "v_and_b32 v205, 0xffff0000, v162\n\t" \
  "v_lshlrev_b32 v206, 16, v163\n\t" \
  "v_and_b32 v207, 0xffff0000, v163\n\t" \
  "v_add_f32 v196, 1.0, v196\n\t" \
  "v_add_f32 v197, 1.0, v197\n\t" \
  "v_add_f32 v198, 1.0, v198\n\t" \
  "v_add_f32 v199, 1.0, v199\n\t" \
  "v_cvt_pk_bf16_f32 v180, v200, v201\n\t" \
  "v_cvt_pk_bf16_f32 v181, v202, v203\n\t" \
  "ds_write_b64 v225, v[180:181] offset:96\n\t" \
  "v_rcp_f32 v196, v196\n\t" \
  "v_rcp_f32 v197, v197\n\t" \
  "v_rcp_f32 v198, v198\n\t" \
  "v_rcp_f32 v199, v199\n\t" \
  "s_nop 0\n\t" \
  "v_fmac_f32 v204, v128, v196\n\t" \
  "v_fmac_f32 v205, v129, v197\n\t" \
  "v_fmac_f32 v206, v130, v198\n\t" \
  "v_fmac_f32 v207, v131, v199\n\t" \
  "v_cvt_pk_bf16_f32 v182, v204, v205\n\t" \
  "v_cvt_pk_bf16_f32 v183, v206, v207\n\t" \
  "ds_write_b64 v225, v[182:183] offset:112\n\t" \
  "9:\n\t" \
  "ds_write_b16 v233, v255 offset:5376\n\t" \
  "s_mov_b32 m0, %[keep]\n\t" \
  "s_setprio 0\n\t" \
  "s_nop 11\n\t" \
  ""
#define LOOP_ASM LOOP_ASM_FOLD

DI u32x4 tr_frag(unsigned p, int half) {
  const s16x4 lo = __builtin_amdgcn_ds_read_tr16_b64_v4i16((lds_s16x4_p)p);
  const s16x4 hi = __builtin_amdgcn_ds_read_tr16_b64_v4i16((lds_s16x4_p)(p + half));
  return __builtin_bit_cast(u32x4, __builtin_shufflevector(lo, hi, 0, 1, 2, 3, 4, 5, 6, 7));
}
DI u32x4 fold(u32x4 v) {
#pragma unroll
  for (int i = 0; i < 4; i++) v[i] = pk_bf16(bf2f(v[i] & 0xffffu) * -LOG2E, __uint_as_float(v[i] & 0xffff0000u) * -LOG2E);
  return v;
}

__global__ __launch_bounds__(512, 2) void egnn_kernel(const char* __restrict__ hmat, const char* __restrict__ pos,
                                                      const char* __restrict__ ea, const int* __restrict__ src,
                                                      const int* __restrict__ dst, const char* __restrict__ we,
                                                      const char* __restrict__ wx, const char* __restrict__ wh,
                                                      char* __restrict__ ws, uint16_t* __restrict__ out, int N, int E STAMP_ARG) {
  const int w = __builtin_amdgcn_readfirstlane(threadIdx.x >> 6);
  const int bkt = (blockIdx.x & 7) * 32 + (blockIdx.x >> 3);
  const int n0 = bkt * NB;
  STAMP(0);
  unsigned wl = L_WAVE + w * WSZ, mi = L_MI + w * MISZ;
  const auto rs_tab = __builtin_amdgcn_make_buffer_rsrc((void*)(ws + WS_TAB), 0, NBK * NBK * 8, 0x00020000);
  const auto rs_rec = __builtin_amdgcn_make_buffer_rsrc((void*)(ws + WS_REC), 0, NBK * CH * 8, 0x00020000);
  const auto rs_h = __builtin_amdgcn_make_buffer_rsrc((void*)hmat, 0, N * 128, 0x00020000);
  const auto rs_ea = __builtin_amdgcn_make_buffer_rsrc((void*)ea, 0, E * 32, 0x00020000);
  unsigned tag_lo, tag_hi;

  {
    const int lane = lane_id(), tid = w * 64 + lane, r = lane & 31, h = lane >> 5;
    const int e0 = bkt * CH;
    int ne = max(E - e0, 0);
    asm("" : "+s"(ne));
    ne = min(ne, CH);
    const auto rs_d = __builtin_amdgcn_make_buffer_rsrc((void*)(dst + e0), 0, ne * 4, 0x00020000);
    const auto rs_s = __builtin_amdgcn_make_buffer_rsrc((void*)(src + e0), 0, ne * 4, 0x00020000);
    unsigned d[7], sv[7], wdv0, wdv1;
    u32x2 ep, xa, xb;
#pragma unroll
    for (int i = 0; i < 7; i++)
      asm volatile("buffer_load_dword %0, %2, %3, %5 offen nt\n\tbuffer_load_dword %1, %2, %4, %5 offen nt"
                   : "=&v"(d[i]), "=&v"(sv[i]) : "v"(tid * 4), "s"(rs_d), "s"(rs_s), "s"(i * 2048) : "memory");
    LDS(unsigned, L_CNT + tid * 4) = 0;
    barrier_lds();
    asm volatile("global_load_dwordx2 %0, %5, %6 sc1\n\tglobal_load_ushort %1, %7, %8\n\tglobal_load_ushort %2, %7, %8 offset:64\n\t"
                 "global_load_dwordx2 %3, %9, %10\n\tglobal_load_dwordx2 %4, %9, %10 offset:16"
                 : "=&v"(ep), "=&v"(wdv0), "=&v"(wdv1), "=&v"(xa), "=&v"(xb)
                 : "v"(0), "s"(ws + WS_EPOCH), "v"(r * 2), "s"(we + 128 * 128), "v"(h * 8), "s"(wx + ((w + 2) & 3) * 32) : "memory");
    {
      const int j = lane >> 3, jl = (j & 3) * 128 + (lane & 7) * 16, jh = (j >> 2) * 128;
      const char* const h0 = wh + (w < 4 ? 16 * w : 64 + 32 * ((w - 4) >> 1) + 4 * (w & 1)) * 128;
      const char* const e0p = we + (w < 4 ? 64 + 16 * w : 16 * (w - 4)) * 128;
      const unsigned oh = jl + jh * (w < 4 ? 8 : 16), oe = jl + jh * (w < 4 ? 4 : 8);
      const unsigned cc0 = ((lane & 7) ^ (lane >> 4)) * 16, cc1 = ((lane & 7) ^ (4 + (lane >> 4))) * 16;
      const int nr = n0 + w + (lane & 56);
      unsigned keep;
      asm volatile("s_mov_b32 %0, m0\n\ts_mov_b32 m0, %16\n\ts_nop 0\n\t"
                   "global_load_lds_dwordx4 %1, %9\n\tglobal_load_lds_dwordx4 %1, %10 offset:1024\n\t"
                   "global_load_lds_dwordx4 %2, %11 offset:2048\n\tglobal_load_lds_dwordx4 %2, %12 offset:3072\n\t"
                   "s_add_u32 m0, %16, 0x1000\n\ts_nop 0\n\tglobal_load_lds_dwordx4 %3, %13\n\t"
                   "s_mov_b32 m0, %17\n\ts_nop 0\n\tglobal_load_lds_dwordx4 %4, %14\n\t"
                   "s_add_u32 m0, %17, 0x400\n\ts_nop 0\n\tglobal_load_lds_dwordx4 %5, %14\n\t"
                   "s_add_u32 m0, %17, 0x800\n\ts_nop 0\n\tglobal_load_lds_dwordx4 %6, %14\n\t"
                   "s_add_u32 m0, %17, 0xc00\n\ts_nop 0\n\tglobal_load_lds_dwordx4 %7, %14\n\t"
                   "s_add_u32 m0, %17, 0x1000\n\ts_nop 0\n\tglobal_load_lds_dword %8, %15\n\ts_mov_b32 m0, %0"
                   : "=&s"(keep)
                   : "v"(oh), "v"(oe), "v"(w < 2 ? (lane >> 2) * 128 + (lane & 3) * 16 : 0),
                     "v"(min(nr, N - 1) * 128 + cc0), "v"(min(nr + 64, N - 1) * 128 + cc1), "v"(min(nr + 128, N - 1) * 128 + cc0),
                     "v"(min(n0 + w + 192, N - 1) * 128 + cc1), "v"(mul24(min(n0 + w + min(lane >> 1, 24) * 8, N - 1), 6) + (lane & 1) * 2),
                     "s"(h0), "s"(h0 + (w < 4 ? 4 : 8) * 128 - 1024), "s"(e0p - 2048), "s"(e0p + (w < 4 ? 8 : 4) * 128 - 3072),
                     "s"(we + 129 * 128 + (w & 1) * 64), "s"(hmat), "s"(pos), "s"(wl), "s"(mi)
                   : "memory", "scc");
    }
    asm volatile("s_waitcnt vmcnt(15)" : "+v"(d[0]), "+v"(d[1]), "+v"(d[2]), "+v"(d[3]), "+v"(d[4]), "+v"(d[5]), "+v"(d[6]),
                 "+v"(sv[0]), "+v"(sv[1]), "+v"(sv[2]), "+v"(sv[3]), "+v"(sv[4]), "+v"(sv[5]), "+v"(sv[6]) :: "memory");
    STAMP(10);
    unsigned ent;
    auto sort = [&](auto NS_, auto FULL_) __attribute__((always_inline)) {
      constexpr int NS = decltype(NS_)::value;
      constexpr bool FULL = decltype(FULL_)::value;
      unsigned bk[NS], rank[NS];
      u32x2 rec[NS];
#pragma unroll
      for (int i = 0; i < NS; i++) {
        const int el = i * 512 + tid;
        const unsigned b = mul24(d[i], 85599u) >> 24;
        const unsigned dl = d[i] - mul24(b, NB);
        bk[i] = (FULL || el < ne ? b : NBK + lane) * 4;
        rank[i] = __hip_atomic_fetch_add(&LDS(unsigned, L_CNT + bk[i]), 8u, __ATOMIC_RELAXED, __HIP_MEMORY_SCOPE_WORKGROUP);
        rec[i][0] = (unsigned)(e0 + el) << 5;
        rec[i][1] = ((mul24(dl >> 3, 1272u) + dl) << 16) | sv[i] | 0x80000u;
      }
      LDS(unsigned, L_BASE + (NBK + lane) * 4) = 0x40000000u;
      barrier_lds();
      const u32x4 cv = LDS(u32x4, L_CNT + lane * 16);
      const unsigned sum = cv[0] + cv[1] + cv[2] + cv[3];
      const unsigned ex = wave_scan_incl(sum) - sum + L_REC;
      const u32x4 bv = {ex, ex + cv[0], ex + cv[0] + cv[1], ex + cv[0] + cv[1] + cv[2]};
      LDS(u32x4, L_BASE + lane * 16) = bv;
      unsigned bs[NS];
#pragma unroll
      for (int i = 0; i < NS; i++) bs[i] = LDS(unsigned, L_BASE + bk[i]);
      ent = ((LDS(unsigned, L_BASE + (tid & 255) * 4) - L_REC) >> 3) | (LDS(unsigned, L_CNT + (tid & 255) * 4) << 9);
      const unsigned lim = L_REC + 8 * SEG + lane * 8;
#pragma unroll
      for (int i = 0; i < NS; i++) LDS(u32x2, FULL ? bs[i] + rank[i] : min(bs[i] + rank[i], lim)) = rec[i];
      barrier_lds();
    };
    if (ne == CH && w != 0) sort(std::integral_constant<int, 6>(), std::true_type());
    else sort(std::integral_constant<int, 7>(), std::false_type());
    LDS(unsigned, L_CNT + tid * 4) = 0;
    STAMP(11);
    {
      const auto rs_out = __builtin_amdgcn_make_buffer_rsrc((void*)(ws + WS_REC + (size_t)bkt * (CH * 8)), 0, ((ne + 1) >> 1) * 16, 0x00020000);
#pragma unroll
      for (int i = 0; i < 4; i++)
        __builtin_amdgcn_raw_buffer_store_b128(LDS(u32x4, L_REC + (i * 512 + tid) * 16), rs_out, (i * 512 + tid) * 16, 0, 16);
    }
    asm volatile("s_waitcnt vmcnt(4)" : "+v"(ep), "+v"(wdv0), "+v"(wdv1), "+v"(xa), "+v"(xb) :: "memory");
    {
      const unsigned tp = wl + (8 * h + ((lane >> 2) & 3)) * 128 + ((lane >> 4) & 1) * 32 + (lane & 3) * 8;
      u32x4 f[2], g[2];
#pragma unroll
      for (int t = 0; t < 2; t++) { f[t] = tr_frag(tp + 64 * t, 512); g[t] = tr_frag(tp + 2048 + 64 * t, 512); }
#pragma unroll
      for (int t = 0; t < 2; t++) {
        if (w < 4) { f[t] = fold(f[t]); g[t] = fold(g[t]); }
        LDS(u32x4, L_WH + (2 * w + t) * 1024 + lane * 16) = f[t];
        LDS(u32x4, wl + 2048 + t * 1024 + lane * 16) = g[t];
      }
      u32x4 x;
      if (w < 2) x = fold(tr_frag(wl + W_STE + (8 * h + ((lane >> 2) & 3)) * 64 + ((lane >> 4) & 1) * 32 + (lane & 3) * 8, 256));
      else { const unsigned m = (r & 27) == 0 ? ~0u : 0u; x = (u32x4){xa[0] & m, xa[1] & m, xb[0] & m, xb[1] & m}; }
      if (w < 6) LDS(u32x4, L_WJ + w * 1024 + lane * 16) = x;
    }
    const unsigned long long e1 = (((unsigned long long)ep[1] << 32) | ep[0]) + 1;
    tag_lo = __builtin_amdgcn_readfirstlane((unsigned)e1 & 0xffu);
    tag_hi = __builtin_amdgcn_readfirstlane((unsigned)(e1 >> 8));
    const u32x2 g = {ent | (tag_lo << 24), tag_hi};
    asm volatile("s_waitcnt vmcnt(0) lgkmcnt(0)\n\ts_barrier" ::: "memory");
    if (tid < NBK) __builtin_amdgcn_raw_buffer_store_b64(g, rs_tab, tid * (NBK * 8) + bkt * 8, 0, 16);
    STAMP(12);
    {
      u32x2 hb[8];
#pragma unroll
      for (int i = 0; i < 8; i++) hb[i] = LDS(u32x2, mi + r * 128 + ((i ^ ((r >> 1) & 7)) * 16) + h * 8);
#pragma unroll
      for (int t = 0; t < 2; t++) {
        f32x16 P;
#pragma unroll
        for (int i = 0; i < 16; i++) P[i] = 0.f;
#pragma unroll
        for (int s = 0; s < 4; s++) {
          const u32x4 a = {hb[2 * s][0], hb[2 * s][1], hb[2 * s + 1][0], hb[2 * s + 1][1]};
          P = MFMA(as_bf(a), LDS(bf16x8, L_WAVE + (4 + s) * WSZ + 2048 + t * 1024 + lane * 16), P);
        }
        if (h) P[15] = bf2f(t ? wdv1 : wdv0);
#pragma unroll
        for (int i = 0; i < 16; i++) P[i] *= -LOG2E;
#pragma unroll
        for (int s = 0; s < 2; s++) {
          const u32x4 a = {pk_bf16(P[8 * s], P[8 * s + 1]), pk_bf16(P[8 * s + 2], P[8 * s + 3]),
                           pk_bf16(P[8 * s + 4], P[8 * s + 5]), pk_bf16(P[8 * s + 6], P[8 * s + 7])};
          LDS(u32x4, mi + (2 * s + t) * 1024 + lane * 16) = a;
        }
      }
    }
    if (tid >= 256 && tid < 256 + 26 * 4) {
      const int t = tid - 256, row = t >> 2, sx = (t >> 1) & 1, hh = t & 1;
      const int x = row < DUMMY ? row - 16 * sx - 4 * hh : -1;
      u32x4 v;
      v[0] = x == 0 ? 0x3f80u : x == 1 ? 0x3f800000u : 0u;  v[1] = x == 2 ? 0x3f80u : x == 3 ? 0x3f800000u : 0u;
      v[2] = x == 8 ? 0x3f80u : x == 9 ? 0x3f800000u : 0u;  v[3] = x == 10 ? 0x3f80u : x == 11 ? 0x3f800000u : 0u;
      LDS(u32x4, L_LUT + row * 80 + (t & 3) * 16) = v;
    }
    {
      const u32x4 z4 = {0u, 0u, 0u, 0u};
#pragma unroll
      for (int i = 0; i < 3; i++) { const int o = i * 1024 + lane * 16; if (o < 26 * 80 + 256) LDS(u32x4, wl + W_SI + o) = z4; }
      const u32x2 pg = LDS(u32x2, mi + 4096 + r * 8);
      const f32x4 pv = {bf2f(pg[0] & 0xffffu), __uint_as_float(pg[0] & 0xffff0000u), __uint_as_float(pg[1] & 0xffff0000u), 0.f};
      if (lane < DUMMY) LDS(f32x4, wl + W_SI + lane * 80 + 64) = pv;
    }
    STAMP(14);
  }

  f32x16 Y0, Y1, Yp;
#pragma unroll
  for (int i = 0; i < 16; i++) { Y0[i] = 0.f; Y1[i] = 0.f; Yp[i] = 0.f; }
  u32x32 W2; u32x16 PA;
  int jbase = 0, part = -1, grp = w, gother = w;

  auto round = [&](bool first) __attribute__((always_inline)) -> bool {
    const int lane = lane_id(), tid = w * 64 + lane, r = lane & 31, h = lane >> 5;
    const unsigned zr = opaque(0);
    if (!first) { LDS(unsigned, L_CNT + tid * 4) = zr; barrier_lds(); }
    LDS(unsigned, L_BASE + lane * 4) = 0x40000000u;
    unsigned tb[8];
    {
      u32x2 t;
      for (int spin = 0;; spin++) {
        asm volatile("buffer_load_dwordx2 %0, %1, %2, %3 offen sc1\n\ts_waitcnt vmcnt(0)"
                     : "=&v"(t) : "v"(w * 256 + r * 8), "s"(rs_tab), "s"(bkt * (NBK * 8)) : "memory");
        if (__builtin_amdgcn_ballot_w64(t[1] != tag_hi || (t[0] >> 24) != tag_lo) == 0) break;
        if (spin > (1 << 20)) { t[0] = 0; break; }
        __builtin_amdgcn_s_sleep(POLL_SLEEP);
      }
      LDS(unsigned, L_WAVE + w * WSZ + W_STE + r * 4) = t[0];
      const u32x4 t0 = LDS(u32x4, L_WAVE + w * WSZ + W_STE + (lane >> 4) * 32), t1 = LDS(u32x4, L_WAVE + w * WSZ + W_STE + (lane >> 4) * 32 + 16);
#pragma unroll
      for (int p = 0; p < 4; p++) { tb[p] = t0[p]; tb[4 + p] = t1[p]; }
    }
    STAMP(8);
    u32x2 rc[10];
    unsigned lmax = 0, nov = 0;
    bool big;
    {
      const unsigned jj = lane & (LG - 1), j = jbase + jj, o0 = mul24((unsigned)tid / LG, 8 * CH * 8) + j * 8, oob = 0x80000000u + zr;
      unsigned off[8], len[8];
#pragma unroll
      for (int p = 0; p < 8; p++) {
        len[p] = (tb[p] >> 12) & 0xfffu;
        if (part >= 0 && part != 2 * p + (w & 1)) len[p] = 0;
        off[p] = o0 + (tb[p] & 0xfffu) * 8;
        const unsigned a = j < len[p] ? off[p] : oob;
        asm volatile("buffer_load_dwordx2 %0, %1, %2, %3 offen sc1" : "=v"(rc[p]) : "v"(a), "s"(rs_rec), "s"(p * CH * 8) : "memory");
      }
      asm volatile("" : "+v"(len[0]), "+v"(len[1]), "+v"(len[2]), "+v"(len[3]), "+v"(len[4]), "+v"(len[5]), "+v"(len[6]), "+v"(len[7]));
      unsigned a9 = oob;
#pragma unroll
      for (int p = 0; p < 8; p++) {
        const unsigned ov = (unsigned)min(max((int)len[p] - (jbase + LG), 0), LG);
        a9 = jj - nov < ov ? off[p] - nov * 8 + (p * CH * 8 + LG * 8) : a9;
        nov += ov;
      }
      asm volatile("buffer_load_dwordx2 %0, %1, %2, 0 offen sc1" : "=v"(rc[8]) : "v"(a9), "s"(rs_rec) : "memory");
#pragma unroll
      for (int p = 0; p < 8; p++) lmax = max(lmax, tb[p] & 0xfff000u);
      rc[9] = (u32x2){zr, zr};
      big = __builtin_amdgcn_ballot_w64(nov > LG) != 0;
      if (big) {
        unsigned a10 = oob, s1 = 0;
#pragma unroll
        for (int p = 0; p < 8; p++) {
          const unsigned ov = (unsigned)min(max((int)len[p] - (jbase + LG), 0), LG);
          a10 = jj + LG - s1 < ov ? off[p] - s1 * 8 + (p * CH * 8 + LG * 16) : a10;
          s1 += ov;
        }
        asm volatile("buffer_load_dwordx2 %0, %1, %2, 0 offen sc1" : "+v"(rc[9]) : "v"(a10), "s"(rs_rec) : "memory");
      }
    }
    unsigned sink = zr;
    if (HPF) {
      const unsigned a = lane < HPF ? (((blockIdx.x >> 3) * 8 + w) * HPF + lane) * 128 : 0x80000000u + zr;
      asm volatile("buffer_load_dword %0, %1, %2, 0 offen" : "+{v3}"(sink) : "v"(a), "s"(rs_h) : "memory");
    }
    STAMP(7);
    if (first) {
#pragma unroll
      for (int i = 0; i < 8; i++) {
        const u32x4 v = LDS(u32x4, L_WAVE + (i >> 1) * WSZ + 2048 + (i & 1) * 1024 + lane * 16);
#pragma unroll
        for (int j = 0; j < 4; j++) W2[4 * i + j] = v[j];
      }
    }
    STAMP(13);
    if ((lmax >> 12) > (unsigned)jbase + 2 * LG || nov > 2 * LG) __hip_atomic_fetch_or(&LDS(unsigned, L_FLAG), nov > 2 * LG ? 3u : 1u, __ATOMIC_RELAXED, __HIP_MEMORY_SCOPE_WORKGROUP);
    unsigned mk1 = (lane & 7) >= 1 ? ~zr : zr, mk2 = (lane & 7) >= 2 ? ~zr : zr, sg0 = L_REC + (lane >> 3) * SEG;
    unsigned lim = L_REC + 8 * SEG + lane * 8, own = n0 + min(8 * (r & 3) + (r >> 2), 24) * 8;
    unsigned ce = ((lane & 1) ^ ((lane >> 4) & 1)) * 16, cp = (lane & 1) * 2;
    unsigned cc0 = ((lane & 7) ^ (lane >> 4)) * 16, cc1 = ((lane & 7) ^ (4 + (lane >> 4))) * 16;
    unsigned q_s4 = L_REC + (lane >> 3) * 32 + 4;
    unsigned q_r2 = L_REC + (4 * ((lane >> 1) & 7) + (lane >> 4)) * 8;
    unsigned q_r1 = L_REC + (4 * (r & 7) + (r >> 3)) * 8 + 4;
    asm volatile("" : "+v"(mk1), "+v"(mk2), "+v"(sg0), "+v"(lim), "+v"(own), "+v"(ce), "+v"(cp), "+v"(cc0), "+v"(cc1), "+v"(q_s4),
                 "+v"(q_r2), "+v"(q_r1));
    asm volatile("s_waitcnt vmcnt(%10)" : "+v"(rc[0]), "+v"(rc[1]), "+v"(rc[2]), "+v"(rc[3]), "+v"(rc[4]), "+v"(rc[5]), "+v"(rc[6]),
                 "+v"(rc[7]), "+v"(rc[8]), "+v"(rc[9]) : "i"(HPF ? 1 : 0) : "memory");
    STAMP(1);
    unsigned rk[10], ky[10];
#pragma unroll
    for (int p = 0; p < 10; p++) {
      if (p == 9 && !big) { ky[p] = 0; rk[p] = 0; break; }
      ky[p] = max((rc[p][1] >> 9) & 0x7fcu, (unsigned)lane * 4);
      rk[p] = __hip_atomic_fetch_add(&LDS(unsigned, L_CNT + ky[p]), 8u + zr, __ATOMIC_RELAXED, __HIP_MEMORY_SCOPE_WORKGROUP);
    }
    if (EPF) {
#pragma unroll
      for (int p = 0; p < 9; p++) {
        const unsigned a = (ky[p] & 0x47cu) - 0x400u < (unsigned)EPF * 4 ? rc[p][0] : 0x80000000u + zr;
        asm volatile("buffer_load_dword %0, %1, %2, 0 offen" : "+{v3}"(sink) : "v"(a), "s"(rs_ea) : "memory");
      }
    }
    barrier_lds();
    STAMP(2);
    unsigned g;
    const unsigned flagv = LDS(unsigned, L_FLAG);
    {
      const u32x4 cv = LDS(u32x4, L_CNT + 1024 + lane * 16);
      const unsigned sum = cv[0] + cv[1] + cv[2] + cv[3];
      g = sum + (__builtin_amdgcn_update_dpp(0, (int)sum, 0x111, 0xf, 0xf, true) & mk1);
      g += __builtin_amdgcn_update_dpp(0, (int)g, 0x112, 0xf, 0xf, true) & mk2;
      g += __builtin_amdgcn_update_dpp(0, (int)g, 0x114, 0xf, 0xa, false);
      const unsigned ex = g - sum + sg0;
      const u32x4 bv = {ex, ex + cv[0], ex + cv[0] + cv[1], ex + cv[0] + cv[1] + cv[2]};
      LDS(u32x4, L_BASE + 1024 + lane * 16) = bv;
    }
    unsigned bs[9];
#pragma unroll
    for (int p = 0; p < 9; p++) bs[p] = LDS(unsigned, L_BASE + ky[p]);
    const unsigned flag = __builtin_amdgcn_readfirstlane(flagv);
    const bool again = flag & 1;
    const bool over = (flag & 2) || __builtin_amdgcn_ballot_w64(g > (unsigned)(SEG - 256)) != 0;
    if (!over) {
#pragma unroll
      for (int p = 0; p < 9; p++) LDS(u32x2, min(bs[p] + rk[p], lim)) = rc[p];
      if (big) LDS(u32x2, min(LDS(unsigned, L_BASE + ky[9]) + rk[9], lim)) = rc[9];
    }
    if (first) {
      const unsigned key = (g << 3) | (lane >> 3);
      unsigned ks[8], rank = 0;
#pragma unroll
      for (int k = 0; k < 8; k++) ks[k] = __builtin_amdgcn_readlane(key, 8 * k + 7);
#pragma unroll
      for (int k = 0; k < 8; k++) rank += ks[k] > key;
      const unsigned long long sel = 0x8080808080808080ull;
      grp = __builtin_ctzll(__builtin_amdgcn_ballot_w64(rank == (unsigned)(w < 4 ? w : 11 - w)) & sel) >> 3;
      gother = __builtin_ctzll(__builtin_amdgcn_ballot_w64(rank == (unsigned)(w < 4 ? 7 - w : w - 4)) & sel) >> 3;
      wl = L_WAVE + grp * WSZ; mi = L_MI + grp * MISZ;
    }
    unsigned segn = over ? 0u : (unsigned)__builtin_amdgcn_readlane(g, 8 * grp + 7);
    const unsigned seg0 = grp * SEG;
    const int nt = (segn + 255) >> 8;
    {
      const u32x2 pad = {zr, DUMMY * (80u << 20)}, row = {zr, min(own + grp, (unsigned)N - 1) | (DUMMY * (80u << 20))};
      LDS(u32x2, L_REC + seg0 + segn + r * 8) = pad;
      LDS(u32x2, L_REC + seg0 + nt * 256 + r * 8) = row;
    }
    barrier_lds();
    STAMP(3);

    const unsigned o_s4 = q_s4 + seg0, o_r2 = q_r2 + seg0, o_r1 = q_r1 + seg0;
    {
      unsigned ns[4];
#pragma unroll
      for (int i = 0; i < 4; i++) ns[i] = LDS(uint16_t, o_s4 + i * 8);
      const u32x2 nr2 = LDS(u32x2, o_r2);
      dma_tile(hmat, ea, pos, ns[0] * 128 + cc0, ns[1] * 128 + cc1, ns[2] * 128 + cc0, ns[3] * 128 + cc1,
               nr2[0] | ce, (nr2[1] & 0xffffu) * 6 + cp, wl);
    }
    if (first) {
#pragma unroll
      for (int i = 0; i < 4; i++) {
        const u32x4 v = LDS(u32x4, mi + i * 1024 + lane * 16);
#pragma unroll
        for (int j = 0; j < 4; j++) PA[4 * i + j] = v[j];
      }
      if (bkt == 0 && tid == 0) {
        const u32x2 e1 = {tag_lo | (tag_hi << 8), tag_hi >> 24};
        asm volatile("global_store_dwordx2 %0, %1, %2 sc1" :: "v"(zr), "v"(e1), "s"(ws + WS_EPOCH) : "memory");
      }
    }
    const unsigned nother = (__builtin_amdgcn_readlane(g, 8 * gother + 7) + 255) >> 8;
    const bool last_round = !over && !again && (part < 0 || part == 15);
    STAMP(4);

    {
      u32x16 A0; u32x8 A1;
      A0[0] = zr; A0[1] = zr;
#pragma unroll
      for (int s = 0; s < 4; s++) A0[2 + s] = wl + W_STH + r * 128 + (((2 * s + h) ^ ((r >> 1) & 7)) * 16);
      A0[6] = wl + r * 32 + ((h ^ ((r >> 3) & 1)) * 16);
      A0[7] = wl + r * 8;
      A0[8] = L_LUT + h * 16;
      A0[9] = wl + r * 2;
      A0[10] = wl + min(r, DUMMY) * 80 + h * 16;
      A0[11] = mi + r * MIL + h * 32;
      A0[12] = mi + (8 * h + ((lane >> 2) & 3)) * MIL + (4 * ((lane >> 4) & 1) + (lane & 3)) * 8;
      A0[13] = wl + h * 64 + r * 2;
      A0[14] = wl + min(r, 3) * 64 + h * 16;
      A0[15] = L_WJ + lane * 16;
      A1[0] = cc0; A1[1] = cc1; A1[2] = ce; A1[3] = cp;
      A1[4] = o_s4 + 256; A1[5] = o_r2 + 256; A1[6] = o_r1;
      A1[7] = 0x3f800000u + zr;
      unsigned cnt = nt, keep;
#define PRIO_A 14
#define PRIO_B 7
      unsigned keep2;
      const int lastv = __builtin_amdgcn_readfirstlane(opaque(last_round ? 1 : 0));
      const int thr = __builtin_amdgcn_readfirstlane(w >= 4 ? max((int)(nother * PRIO_A - cnt * PRIO_B) >> 5, 0) : -1);
      asm volatile(LOOP_ASM
                   : "+{v[52:67]}"(Y0), "+{v[68:83]}"(Y1), "+{v[84:99]}"(Yp), "+{v[232:247]}"(A0), "+{v[248:255]}"(A1), "+{v3}"(sink),
                     [nt] "+s"(cnt), [keep] "=&s"(keep), [keep2] "=&s"(keep2)
                   : "{v[4:35]}"(W2), "{v[36:51]}"(PA),
                     [h0] "s"(hmat), [h1] "s"(hmat - 1024), [h2] "s"(hmat - 2048), [h3] "s"(hmat - 3072), [ea] "s"(ea), [pos] "s"(pos - 1024),
                     [b1] "s"(wl), [b2] "s"(wl + W_STE), [posd] "s"(wl + W_SI + 64), [c128] "s"(128u), [thr] "s"(thr), [last] "s"(lastv),
                     [hsel] "s"(0xffffffff00000000ull), [rsea] "s"(rs_ea)
                   : "memory", "scc", "vcc", "v1", CLOBBER_V);
    }
    if (over) part = 0;
    else if (part >= 0 && part < 15) part++;
    else { part = -1; jbase += 2 * LG; }
    return last_round;
  };
  if (!round(true)) {
    do {
      wait_vm0();
      barrier_lds();
      jbase = __builtin_amdgcn_readfirstlane(jbase); part = __builtin_amdgcn_readfirstlane(part);
    } while (!round(false));
  }
  STAMP(5);

  {
    const int lane = lane_id(), r = lane & 31, h = lane >> 5;
    Yp[0] *= -LN2; Yp[1] *= -LN2; Yp[2] *= -LN2;
    const auto rs_o = __builtin_amdgcn_make_buffer_rsrc((void*)out, 0, N * 128, 0x00020000);
#pragma unroll
    for (int i = 0; i < 4; i++) {
      const int q = 8 * i + (lane >> 3), dl = q * 8 + grp;
      const u32x4 v = LDS(u32x4, mi + q * MIP + (lane & 7) * 16);
      if (dl < NB) __builtin_amdgcn_raw_buffer_store_b128(v, rs_o, (n0 + dl) * 128 + (lane & 7) * 16, 0, OUT_AUX);
    }
    const int dl = r * 8 + grp;
    if (h == 0 && dl < NB && n0 + dl < N) {
      const f32x4 pd = LDS(f32x4, wl + W_SI + r * 80 + 64);
      uint16_t* po = out + (size_t)N * 64 + (size_t)(n0 + dl) * 3;
      const unsigned xy = pk_bf16(pd[0] + Yp[0], pd[1] + Yp[1]), zz = pk_bf16(pd[2] + Yp[2], 0.f);
      po[0] = (uint16_t)xy; po[1] = (uint16_t)(xy >> 16); po[2] = (uint16_t)zz;
    }
  }
  STAMP(6);
}

}

#define STAMP_PASS1
#define STAMP_PASS2

extern "C" void kernel_launch(void* const* d_in, const int* in_sizes, int n_in, void* d_out, int out_size,
                              void* d_ws, size_t ws_size, hipStream_t stream) {
  const int N = in_sizes[0] / 64, E = in_sizes[3];
  if (N > NBK * NB || E > NBK * CH || N < 1 || E < 1) return;
  (void)hipFuncSetAttribute((const void*)egnn_kernel, hipFuncAttributeMaxDynamicSharedMemorySize, L_TOTAL);
  egnn_kernel<<<NBK, 512, L_TOTAL, stream>>>((const char*)d_in[0], (const char*)d_in[1], (const char*)d_in[2], (const int*)d_in[3],
                                             (const int*)d_in[4], (const char*)d_in[5], (const char*)d_in[6], (const char*)d_in[7],
                                             (char*)d_ws, (uint16_t*)d_out, N, E STAMP_PASS2);
}
```

```cpp
#include <hip/hip_runtime.h>
#include <stdint.h>
#include <type_traits>

#define FOLD_LOG2E 1

namespace {

typedef __bf16 bf16x8 __attribute__((ext_vector_type(8)));
typedef __bf16 bf16x2 __attribute__((ext_vector_type(2)));
typedef float f32x16 __attribute__((ext_vector_type(16)));
typedef float f32x4 __attribute__((ext_vector_type(4)));
typedef unsigned u32x4 __attribute__((ext_vector_type(4)));
typedef unsigned u32x2 __attribute__((ext_vector_type(2)));
typedef unsigned u32x8 __attribute__((ext_vector_type(8)));
typedef unsigned u32x16 __attribute__((ext_vector_type(16)));
typedef unsigned u32x32 __attribute__((ext_vector_type(32)));
typedef short s16x4 __attribute__((ext_vector_type(4)));
typedef __attribute__((address_space(3))) s16x4* lds_s16x4_p;

#define DI __device__ __forceinline__
#define MFMA(a, b, c) __builtin_amdgcn_mfma_f32_32x32x16_bf16((a), (b), (c), 0, 0, 0)

#define POLL_SLEEP 2
#define EPF 2
#define OUT_AUX 16
constexpr int NBK = 256;
constexpr int LG = 8;
constexpr int NB = 196;
constexpr int CH = 3136;
constexpr float LOG2E = 1.4426950408889634f;
constexpr float LN2 = 0.6931471805599453f;

constexpr size_t WS_EPOCH = 0;
constexpr size_t WS_TAB = 256;
constexpr size_t WS_REC = WS_TAB + (size_t)NBK * NBK * 8;
constexpr int CHS = (CH + NBK) * 8;

constexpr int W_STH = 0, W_STE = 4096, W_STP = 5120, W_SI = 5376, W_TI = W_SI + 26 * 80, WSZ = W_TI + 256;
constexpr int DUMMY = 25;
constexpr int MIP = 144, MISZ = 32 * MIP;
constexpr int MIL = 80;
constexpr int SEG = 17 * 256;
constexpr int L_CNT = 0;
constexpr int L_BASE = L_CNT + 2048;
constexpr int L_FLAG = L_CNT + 512;
constexpr int L_REC = L_BASE + 2048;
constexpr int L_LUT = L_REC + 8 * SEG + 512;
constexpr int L_WAVE = L_LUT + 26 * 80;
constexpr int L_MI = L_WAVE + 8 * WSZ;
constexpr int L_WJ = L_MI + 8 * MISZ;
constexpr int L_WH = L_WJ + 6 * 1024;
constexpr int L_TOTAL = L_WH + 16 * 1024;
static_assert(L_TOTAL <= 163840, "LDS");
static_assert(WSZ % 16 == 0 && L_MI % 16 == 0 && L_LUT % 16 == 0, "align");

#pragma clang diagnostic ignored "-Wint-to-pointer-cast"
#define LDS(T, off) (*(__attribute__((address_space(3))) T*)(unsigned)(off))
DI unsigned mul24(unsigned a, unsigned b) { return (unsigned)__umul24(a, b); }
DI float bf2f(unsigned lo16) { return __uint_as_float(lo16 << 16); }
DI unsigned pk_bf16(float a, float b) { bf16x2 v = {(__bf16)a, (__bf16)b}; return __builtin_bit_cast(unsigned, v); }
DI bf16x8 as_bf(u32x4 v) { return __builtin_bit_cast(bf16x8, v); }
DI int opaque(int x) { asm volatile("" : "+v"(x)); return x; }
DI int lane_id() {
  int x; asm volatile("v_mbcnt_lo_u32_b32 %0, -1, 0\n\tv_mbcnt_hi_u32_b32 %0, -1, %0" : "=v"(x)); return x;
}
DI void wait_vm0() { asm volatile("s_waitcnt vmcnt(0)" ::: "memory"); }
DI void wait_lgkm0() { asm volatile("s_waitcnt lgkmcnt(0)" ::: "memory"); }
DI void barrier_lds() { asm volatile("s_waitcnt lgkmcnt(0)\n\ts_barrier" ::: "memory"); }
DI void dma16(const void* g, unsigned off, unsigned l) {
  unsigned keep;
  asm volatile("s_mov_b32 %0, m0\n\ts_mov_b32 m0, %3\n\ts_nop 0\n\tglobal_load_lds_dwordx4 %1, %2\n\ts_mov_b32 m0, %0"
               : "=&s"(keep) : "v"(off), "s"(g), "s"(l) : "memory");
}
DI void dma4(const void* g, unsigned off, unsigned l) {
  unsigned keep;
  asm volatile("s_mov_b32 %0, m0\n\ts_mov_b32 m0, %3\n\ts_nop 0\n\tglobal_load_lds_dword %1, %2\n\ts_mov_b32 m0, %0"
               : "=&s"(keep) : "v"(off), "s"(g), "s"(l) : "memory");
}
DI void dma_tile(const void* hm, const void* ea, const void* ps, unsigned o0, unsigned o1, unsigned o2, unsigned o3,
                 unsigned oe, unsigned op, unsigned l) {
  unsigned keep;
  asm volatile("s_mov_b32 %0, m0\n\t"
               "s_add_u32 m0, %10, 0x1000\n\ts_nop 0\n\tglobal_load_lds_dwordx4 %5, %8\n\t"
               "s_mov_b32 m0, %10\n\ts_nop 0\n\tglobal_load_lds_dwordx4 %1, %7\n\t"
               "s_add_u32 m0, %10, 0x400\n\ts_nop 0\n\tglobal_load_lds_dwordx4 %2, %7\n\t"
               "s_add_u32 m0, %10, 0x800\n\ts_nop 0\n\tglobal_load_lds_dwordx4 %3, %7\n\t"
               "s_add_u32 m0, %10, 0xc00\n\ts_nop 0\n\tglobal_load_lds_dwordx4 %4, %7\n\t"
               "s_add_u32 m0, %10, 0x1400\n\ts_nop 0\n\tglobal_load_lds_dword %6, %9\n\t"
               "s_mov_b32 m0, %0"
               : "=&s"(keep) : "v"(o0), "v"(o1), "v"(o2), "v"(o3), "v"(oe), "v"(op), "s"(hm), "s"(ea), "s"(ps), "s"(l)
               : "memory", "scc");
}
template <int CTRL, int ROWMASK> DI unsigned dpp_add(unsigned x) {
  return x + (unsigned)__builtin_amdgcn_update_dpp(0, (int)x, CTRL, ROWMASK, 0xf, false);
}
DI unsigned wave_scan_incl(unsigned x) {
  x = dpp_add<0x111, 0xf>(x); x = dpp_add<0x112, 0xf>(x); x = dpp_add<0x114, 0xf>(x); x = dpp_add<0x118, 0xf>(x);
  x = dpp_add<0x142, 0xa>(x); x = dpp_add<0x143, 0xc>(x);
  return x;
}
#define STAMP(i)
#define STAMP_ARG

#define CLOBBER_V "v100", "v101", "v102", "v103", "v104", "v105", "v106", "v107", "v108", "v109", "v110", "v111", "v112", "v113", "v114", "v115", "v116", "v117", "v118", "v119", "v120", "v121", "v122", "v123", "v124", "v125", "v126", "v127", "v128", "v129", "v130", "v131", "v132", "v133", "v134", "v135", "v136", "v137", "v138", "v139", "v140", "v141", "v142", "v143", "v144", "v145", "v146", "v147", "v148", "v149", "v150", "v151", "v152", "v153", "v154", "v155", "v156", "v157", "v158", "v159", "v160", "v161", "v162", "v163", "v164", "v165", "v166", "v167", "v168", "v169", "v170", "v171", "v172", "v173", "v174", "v175", "v176", "v177", "v178", "v179", "v180", "v181", "v182", "v183", "v184", "v185", "v186", "v187", "v188", "v189", "v190", "v191", "v192", "v193", "v194", "v195", "v196", "v197", "v198", "v199", "v200", "v201", "v202", "v203", "v204", "v205", "v206", "v207", "v208", "v209", "v210", "v211", "v212", "v213", "v214", "v215", "v216", "v217", "v218", "v219", "v220", "v221", "v222", "v223", "v224", "v225", "v226", "v227", "v228", "v229", "v230", "v231"
#define LOOP_ASM_FOLD \
  "s_mov_b32 %[keep], m0\n\t" \
  "s_mov_b64 vcc, %[hsel]\n\t" \
  "ds_read2_b32 v[220:221], v252 offset0:0 offset1:2\n\t" \
  "ds_read2_b32 v[222:223], v252 offset0:4 offset1:6\n\t" \
  "ds_read_b64 v[226:227], v253\n\t" \
  "ds_read_b32 v232, v254\n\t" \
  "ds_read_b128 v[212:215], v247 offset:0\n\t" \
  "v_mov_b32 v176, 0\n\t" \
  "v_mov_b32 v177, 0\n\t" \
  "v_mov_b32 v178, 0\n\t" \
  "v_mov_b32 v179, 0\n\t" \
  "v_mov_b32 v180, 0\n\t" \
  "v_mov_b32 v181, 0\n\t" \
  "v_mov_b32 v182, 0\n\t" \
  "v_mov_b32 v183, 0\n\t" \
  "v_mov_b32 v184, 0\n\t" \
  "v_mov_b32 v185, 0\n\t" \
  "v_mov_b32 v186, 0\n\t" \
  "v_mov_b32 v187, 0\n\t" \
  "v_mov_b32 v188, 0\n\t" \
  "v_mov_b32 v189, 0\n\t" \
  "v_mov_b32 v190, 0\n\t" \
  "v_mov_b32 v191, 0\n\t" \
  "v_mov_b32 v230, 0\n\t" \
  "v_mov_b32 v231, 0\n\t" \
  "v_mov_b32 v208, 0\n\t" \
  "v_mov_b32 v209, 0\n\t" \
  "v_mov_b32 v210, 0\n\t" \
  "v_mov_b32 v211, 0\n\t" \
  "ds_write_b128 v243, v[184:187] offset:0\n\t" \
  "s_nop 0\n\t" \
  "v_mfma_f32_32x32x16_bf16 v[132:147], v[184:187], v[188:191], 0\n\t" \
  "v_add_u32 v252, 0x100, v252\n\t" \
  "v_add_u32 v253, 0x100, v253\n\t" \
  "v_add_u32 v254, 0x100, v254\n\t" \
  "s_waitcnt lgkmcnt(0)\n\t" \
  "v_lshrrev_b32 v232, 20, v232\n\t" \
  "v_add_u32 v233, %[posd], v232\n\t" \
  "ds_read_b128 v[216:219], v233\n\t" \
  "v_add_u32 v233, v240, v232\n\t" \
  "ds_read_b128 v[172:175], v233 offset:32\n\t" \
  "ds_read_b128 v[168:171], v233\n\t" \
  "v_add_u32 v233, v241, v232\n\t" \
  "ds_write_b16_d16_hi v233, v255 offset:5376\n\t" \
  "v_mad_u32_u16 v220, v220, %[c128], v248\n\t" \
  "v_mad_u32_u16 v221, v221, %[c128], v249\n\t" \
  "v_mad_u32_u16 v222, v222, %[c128], v248\n\t" \
  "v_mad_u32_u16 v223, v223, %[c128], v249\n\t" \
  "v_or_b32 v226, v226, v250\n\t" \
  "v_mad_u32_u16 v227, v227, 6, v251\n\t" \
  "s_waitcnt vmcnt(0)\n\t" \
  "ds_read_b128 v[148:151], v234\n\t" \
  "ds_read_b128 v[152:155], v235\n\t" \
  "ds_read_b128 v[156:159], v236\n\t" \
  "ds_read_b128 v[160:163], v237\n\t" \
  "ds_read_b128 v[164:167], v238 offset:4096\n\t" \
  "ds_read_b64 v[224:225], v239 offset:5120\n\t" \
  "s_waitcnt lgkmcnt(0)\n\t" \
  "s_cmp_lt_i32 %[nt], 1\n\t" \
  "s_cbranch_scc1 7f\n\t" \
  "s_mov_b32 m0, %[b2]\n\t" \
  "s_nop 0\n\t" \
  "global_load_lds_dwordx4 v226, %[ea]\n\t" \
  "global_load_lds_dword v227, %[pos] offset:1024\n\t" \
  "s_mov_b32 m0, %[b1]\n\t" \
  "s_nop 0\n\t" \
  "global_load_lds_dwordx4 v220, %[h0]\n\t" \
  "global_load_lds_dwordx4 v221, %[h1] offset:1024\n\t" \
  "global_load_lds_dwordx4 v222, %[h2] offset:2048\n\t" \
  "global_load_lds_dwordx4 v223, %[h3] offset:3072\n\t" \
  "7:\n\t" \
  "ds_read2_b32 v[220:221], v252 offset0:0 offset1:2\n\t" \
  "ds_read2_b32 v[222:223], v252 offset0:4 offset1:6\n\t" \
  "ds_read_b64 v[226:227], v253\n\t" \
  "ds_read_b32 v232, v254\n\t" \
  "v_add_u32 v252, 0x100, v252\n\t" \
  "v_add_u32 v253, 0x100, v253\n\t" \
  "v_add_u32 v254, 0x100, v254\n\t" \
  "v_and_b32 v225, 0xffff0000, v225\n\t" \
  "v_lshlrev_b32 v219, 16, v224\n\t" \
  "v_and_b32 v224, 0xffff0000, v224\n\t" \
  "v_sub_f32 v229, v218, v225\n\t" \
  "v_sub_f32 v216, v216, v219\n\t" \
  "v_sub_f32 v217, v217, v224\n\t" \
  "v_mul_f32 v218, v229, v229\n\t" \
  "v_cndmask_b32 v228, v216, v217, vcc\n\t" \
  "v_fmac_f32 v218, v216, v216\n\t" \
  "v_fmac_f32 v218, v217, v217\n\t" \
  "v_cvt_pk_bf16_f32 v218, v218, v218\n\t" \
  "v_cndmask_b32_sdwa v175, v175, v218, vcc dst_sel:WORD_1 dst_unused:UNUSED_PRESERVE src0_sel:WORD_1 src1_sel:WORD_1\n\t" \
  "v_mfma_f32_32x32x16_bf16 v[100:115], v[4:7], v[148:151], 0\n\t" \
  "v_mfma_f32_32x32x16_bf16 v[100:115], v[12:15], v[152:155], v[100:115]\n\t" \
  "v_mfma_f32_32x32x16_bf16 v[100:115], v[20:23], v[156:159], v[100:115]\n\t" \
  "v_mfma_f32_32x32x16_bf16 v[100:115], v[28:31], v[160:163], v[100:115]\n\t" \
  "v_mfma_f32_32x32x16_bf16 v[100:115], v[36:39], v[168:171], v[100:115]\n\t" \
  "v_mfma_f32_32x32x16_bf16 v[100:115], v[212:215], v[164:167], v[100:115]\n\t" \
  "v_mfma_f32_32x32x16_bf16 v[100:115], v[44:47], v[172:175], v[100:115]\n\t" \
  "s_cmp_eq_u32 %[nt], 0\n\t" \
  "s_nop 10\n\t" \
  "v_exp_f32 v192, v100\n\t" \
  "v_exp_f32 v193, v101\n\t" \
  "v_exp_f32 v194, v102\n\t" \
  "v_exp_f32 v195, v103\n\t" \
  "s_nop 0\n\t" \
  "v_add_f32 v192, 1.0, v192\n\t" \
  "v_add_f32 v193, 1.0, v193\n\t" \
  "v_add_f32 v194, 1.0, v194\n\t" \
  "v_add_f32 v195, 1.0, v195\n\t" \
  "s_cbranch_scc1 6f\n\t" \
  "s_cmp_lt_i32 %[thr], 0\n\t" \
  "s_cbranch_scc1 1f\n\t" \
  "s_setprio 1\n\t" \
  "1:\n\t" \
  "ds_write_b128 v243, v[188:191] offset:16\n\t" \
  "ds_read_b64_tr_b16 v[200:201], v244 offset:0\n\t" \
  "ds_read_b64_tr_b16 v[202:203], v244 offset:320\n\t" \
  "ds_read_b64_tr_b16 v[204:205], v244 offset:1280\n\t" \
  "ds_read_b64_tr_b16 v[206:207], v244 offset:1600\n\t" \
  "ds_read_b128 v[212:215], v247 offset:1024\n\t" \
  "v_mfma_f32_32x32x16_bf16 v[132:147], v[208:211], v[188:191], v[132:147]\n\t" \
  "v_rcp_f32 v192, v192\n\t" \
  "v_rcp_f32 v193, v193\n\t" \
  "v_rcp_f32 v194, v194\n\t" \
  "v_rcp_f32 v195, v195\n\t" \
  "v_mfma_f32_32x32x16_bf16 v[116:131], v[8:11], v[148:151], 0\n\t" \
  "v_exp_f32 v196, v104\n\t" \
  "v_exp_f32 v197, v105\n\t" \
  "v_exp_f32 v198, v106\n\t" \
  "v_exp_f32 v199, v107\n\t" \
  "v_mfma_f32_32x32x16_bf16 v[116:131], v[16:19], v[152:155], v[116:131]\n\t" \
  "v_mul_f32 v192, v100, v192\n\t" \
  "v_mul_f32 v193, v101, v193\n\t" \
  "v_mul_f32 v194, v102, v194\n\t" \
  "v_mul_f32 v195, v103, v195\n\t" \
  "v_add_f32 v196, 1.0, v196\n\t" \
  "v_add_f32 v197, 1.0, v197\n\t" \
  "v_add_f32 v198, 1.0, v198\n\t" \
  "v_add_f32 v199, 1.0, v199\n\t" \
  "v_cvt_pk_bf16_f32 v184, v192, v193\n\t" \
  "v_cvt_pk_bf16_f32 v185, v194, v195\n\t" \
  "v_mfma_f32_32x32x16_bf16 v[116:131], v[24:27], v[156:159], v[116:131]\n\t" \
  "v_rcp_f32 v196, v196\n\t" \
  "v_rcp_f32 v197, v197\n\t" \
  "v_rcp_f32 v198, v198\n\t" \
  "v_rcp_f32 v199, v199\n\t" \
  "s_waitcnt lgkmcnt(3)\n\t" \
  "v_mfma_f32_32x32x16_bf16 v[68:83], v[200:203], v[176:179], v[68:83]\n\t" \
  "v_exp_f32 v192, v108\n\t" \
  "v_exp_f32 v193, v109\n\t" \
  "v_exp_f32 v194, v110\n\t" \
  "v_exp_f32 v195, v111\n\t" \
  "v_mfma_f32_32x32x16_bf16 v[116:131], v[32:35], v[160:163], v[116:131]\n\t" \
  "v_mul_f32 v196, v104, v196\n\t" \
  "v_mul_f32 v197, v105, v197\n\t" \
  "v_mul_f32 v198, v106, v198\n\t" \
  "v_mul_f32 v199, v107, v199\n\t" \
  "v_mul_f32 v230, v230, v132\n\t" \
  "v_mul_f32 v231, v231, v132\n\t" \
  "v_cvt_pk_bf16_f32 v230, v230, v231\n\t" \
  "ds_write_b16 v245, v230 offset:7456\n\t" \
  "ds_write_b16_d16_hi v241, v230 offset:7584\n\t" \
  "ds_read_b128 v[200:203], v246 offset:7456\n\t" \
  "ds_read_b128 v[208:211], v246 offset:7488\n\t" \
  "v_mov_b32 v230, v228\n\t" \
  "v_mov_b32 v231, v229\n\t" \
  "v_add_f32 v192, 1.0, v192\n\t" \
  "v_add_f32 v193, 1.0, v193\n\t" \
  "v_add_f32 v194, 1.0, v194\n\t" \
  "v_add_f32 v195, 1.0, v195\n\t" \
  "v_cvt_pk_bf16_f32 v186, v196, v197\n\t" \
  "v_cvt_pk_bf16_f32 v187, v198, v199\n\t" \
  "v_mfma_f32_32x32x16_bf16 v[116:131], v[40:43], v[168:171], v[116:131]\n\t" \
  "v_rcp_f32 v192, v192\n\t" \
  "v_rcp_f32 v193, v193\n\t" \
  "v_rcp_f32 v194, v194\n\t" \
  "v_rcp_f32 v195, v195\n\t" \
  "s_sub_u32 %[nt], %[nt], 1\n\t" \
  "s_cmp_lg_u32 %[nt], %[thr]\n\t" \
  "s_cbranch_scc1 3f\n\t" \
  "s_setprio 0\n\t" \
  "3:\n\t" \
  "s_waitcnt lgkmcnt(4)\n\t" \
  "v_mfma_f32_32x32x16_bf16 v[116:131], v[212:215], v[164:167], v[116:131]\n\t" \
  "v_exp_f32 v196, v112\n\t" \
  "v_exp_f32 v197, v113\n\t" \
  "v_exp_f32 v198, v114\n\t" \
  "v_exp_f32 v199, v115\n\t" \
  "v_mfma_f32_32x32x16_bf16 v[116:131], v[48:51], v[172:175], v[116:131]\n\t" \
  "v_mul_f32 v192, v108, v192\n\t" \
  "v_mul_f32 v193, v109, v193\n\t" \
  "v_mul_f32 v194, v110, v194\n\t" \
  "v_mul_f32 v195, v111, v195\n\t" \
  "s_waitcnt vmcnt(0)\n\t" \
  "ds_read_b128 v[148:151], v234\n\t" \
  "ds_read_b128 v[152:155], v235\n\t" \
  "ds_read_b128 v[156:159], v236\n\t" \
  "ds_read_b128 v[160:163], v237\n\t" \
  "ds_read_b128 v[164:167], v238 offset:4096\n\t" \
  "ds_read_b64 v[224:225], v239 offset:5120\n\t" \
  "ds_write_b128 v243, v[184:187] offset:0\n\t" \
  "ds_read_b128 v[212:215], v247 offset:2048\n\t" \
  "v_add_f32 v196, 1.0, v196\n\t" \
  "v_add_f32 v197, 1.0, v197\n\t" \
  "v_add_f32 v198, 1.0, v198\n\t" \
  "v_add_f32 v199, 1.0, v199\n\t" \
  "v_cvt_pk_bf16_f32 v188, v192, v193\n\t" \
  "v_cvt_pk_bf16_f32 v189, v194, v195\n\t" \
  "v_mad_u32_u16 v220, v220, %[c128], v248\n\t" \
  "v_mad_u32_u16 v221, v221, %[c128], v249\n\t" \
  "v_mad_u32_u16 v222, v222, %[c128], v248\n\t" \
  "v_mfma_f32_32x32x16_bf16 v[68:83], v[204:207], v[180:183], v[68:83]\n\t" \
  "v_rcp_f32 v196, v196\n\t" \
  "v_rcp_f32 v197, v197\n\t" \
  "v_rcp_f32 v198, v198\n\t" \
  "v_rcp_f32 v199, v199\n\t" \
  "v_mad_u32_u16 v223, v223, %[c128], v249\n\t" \
  "v_or_b32 v226, v226, v250\n\t" \
  "v_mad_u32_u16 v227, v227, 6, v251\n\t" \
  "s_waitcnt lgkmcnt(9)\n\t" \
  "v_mfma_f32_32x32x16_bf16 v[84:99], v[200:203], v[176:179], v[84:99]\n\t" \
  "v_exp_f32 v192, v116\n\t" \
  "v_exp_f32 v193, v117\n\t" \
  "v_exp_f32 v194, v118\n\t" \
  "v_exp_f32 v195, v119\n\t" \
  "s_waitcnt lgkmcnt(8)\n\t" \
  "v_mfma_f32_32x32x16_bf16 v[84:99], v[208:211], v[180:183], v[84:99]\n\t" \
  "v_mul_f32 v196, v112, v196\n\t" \
  "v_mul_f32 v197, v113, v197\n\t" \
  "v_mul_f32 v198, v114, v198\n\t" \
  "v_mul_f32 v199, v115, v199\n\t" \
  "s_waitcnt lgkmcnt(2)\n\t" \
  "s_cmp_lt_i32 %[nt], 1\n\t" \
  "s_cbranch_scc1 4f\n\t" \
  "s_mov_b32 m0, %[b2]\n\t" \
  "s_nop 0\n\t" \
  "global_load_lds_dwordx4 v226, %[ea]\n\t" \
  "global_load_lds_dword v227, %[pos] offset:1024\n\t" \
  "s_mov_b32 m0, %[b1]\n\t" \
  "s_nop 0\n\t" \
  "global_load_lds_dwordx4 v220, %[h0]\n\t" \
  "global_load_lds_dwordx4 v221, %[h1] offset:1024\n\t" \
  "global_load_lds_dwordx4 v222, %[h2] offset:2048\n\t" \
  "global_load_lds_dwordx4 v223, %[h3] offset:3072\n\t" \
  "4:\n\t" \
  "v_add_f32 v192, 1.0, v192\n\t" \
  "v_add_f32 v193, 1.0, v193\n\t" \
  "v_add_f32 v194, 1.0, v194\n\t" \
  "v_add_f32 v195, 1.0, v195\n\t" \
  "v_cvt_pk_bf16_f32 v190, v196, v197\n\t" \
  "v_cvt_pk_bf16_f32 v191, v198, v199\n\t" \
  "v_lshrrev_b32 v232, 20, v232\n\t" \
  "v_add_u32 v220, %[posd], v232\n\t" \
  "v_add_u32 v221, v240, v232\n\t" \
  "ds_write_b128 v243, v[188:191] offset:16\n\t" \
  "ds_read_b128 v[176:179], v242 offset:5376\n\t" \
  "ds_read_b128 v[180:183], v242 offset:5408\n\t" \
  "ds_write_b16 v233, v255 offset:5376\n\t" \
  "ds_read_b128 v[216:219], v220\n\t" \
  "ds_read_b128 v[172:175], v221 offset:32\n\t" \
  "ds_read_b128 v[168:171], v221\n\t" \
  "ds_read_b64_tr_b16 v[200:201], v244 offset:0\n\t" \
  "ds_read_b64_tr_b16 v[202:203], v244 offset:320\n\t" \
  "ds_read_b64_tr_b16 v[204:205], v244 offset:1280\n\t" \
  "ds_read_b64_tr_b16 v[206:207], v244 offset:1600\n\t" \
  "ds_read_b128 v[208:211], v247 offset:3072\n\t" \
  "v_add_u32 v233, v241, v232\n\t" \
  "ds_write_b16_d16_hi v233, v255 offset:5376\n\t" \
  "s_waitcnt lgkmcnt(13)\n\t" \
  "v_mfma_f32_32x32x16_bf16 v[132:147], v[212:215], v[184:187], 0\n\t" \
  "v_rcp_f32 v192, v192\n\t" \
  "v_rcp_f32 v193, v193\n\t" \
  "v_rcp_f32 v194, v194\n\t" \
  "v_rcp_f32 v195, v195\n\t" \
  "v_mfma_f32_32x32x16_bf16 v[100:115], v[4:7], v[148:151], 0\n\t" \
  "v_exp_f32 v196, v120\n\t" \
  "v_exp_f32 v197, v121\n\t" \
  "v_exp_f32 v198, v122\n\t" \
  "v_exp_f32 v199, v123\n\t" \
  "v_mfma_f32_32x32x16_bf16 v[100:115], v[12:15], v[152:155], v[100:115]\n\t" \
  "v_mul_f32 v192, v116, v192\n\t" \
  "v_mul_f32 v193, v117, v193\n\t" \
  "v_mul_f32 v194, v118, v194\n\t" \
  "v_mul_f32 v195, v119, v195\n\t" \
  "v_add_f32 v196, 1.0, v196\n\t" \
  "v_add_f32 v197, 1.0, v197\n\t" \
  "v_add_f32 v198, 1.0, v198\n\t" \
  "v_add_f32 v199, 1.0, v199\n\t" \
  "v_cvt_pk_bf16_f32 v184, v192, v193\n\t" \
  "v_cvt_pk_bf16_f32 v185, v194, v195\n\t" \
  "v_and_b32 v225, 0xffff0000, v225\n\t" \
  "s_waitcnt lgkmcnt(8)\n\t" \
  "v_lshlrev_b32 v219, 16, v224\n\t" \
  "v_and_b32 v224, 0xffff0000, v224\n\t" \
  "v_sub_f32 v229, v218, v225\n\t" \
  "v_sub_f32 v216, v216, v219\n\t" \
  "v_sub_f32 v217, v217, v224\n\t" \
  "v_mfma_f32_32x32x16_bf16 v[100:115], v[20:23], v[156:159], v[100:115]\n\t" \
  "v_rcp_f32 v196, v196\n\t" \
  "v_rcp_f32 v197, v197\n\t" \
  "v_rcp_f32 v198, v198\n\t" \
  "v_rcp_f32 v199, v199\n\t" \
  "v_mul_f32 v218, v229, v229\n\t" \
  "v_cndmask_b32 v228, v216, v217, vcc\n\t" \
  "v_fmac_f32 v218, v216, v216\n\t" \
  "v_fmac_f32 v218, v217, v217\n\t" \
  "v_cvt_pk_bf16_f32 v218, v218, v218\n\t" \
  "s_waitcnt lgkmcnt(7)\n\t" \
  "v_cndmask_b32_sdwa v175, v175, v218, vcc dst_sel:WORD_1 dst_unused:UNUSED_PRESERVE src0_sel:WORD_1 src1_sel:WORD_1\n\t" \
  "s_waitcnt lgkmcnt(1)\n\t" \
  "v_mfma_f32_32x32x16_bf16 v[132:147], v[208:211], v[188:191], v[132:147]\n\t" \
  "v_exp_f32 v192, v124\n\t" \
  "v_exp_f32 v193, v125\n\t" \
  "v_exp_f32 v194, v126\n\t" \
  "v_exp_f32 v195, v127\n\t" \
  "ds_read_b128 v[212:215], v247 offset:0\n\t" \
  "ds_read2_b32 v[220:221], v252 offset0:0 offset1:2\n\t" \
  "ds_read2_b32 v[222:223], v252 offset0:4 offset1:6\n\t" \
  "ds_read_b64 v[226:227], v253\n\t" \
  "ds_read_b32 v232, v254\n\t" \
  "v_mfma_f32_32x32x16_bf16 v[100:115], v[28:31], v[160:163], v[100:115]\n\t" \
  "v_mul_f32 v196, v120, v196\n\t" \
  "v_mul_f32 v197, v121, v197\n\t" \
  "v_mul_f32 v198, v122, v198\n\t" \
  "v_mul_f32 v199, v123, v199\n\t" \
  "v_add_u32 v252, 0x100, v252\n\t" \
  "v_add_u32 v253, 0x100, v253\n\t" \
  "v_add_u32 v254, 0x100, v254\n\t" \
  "v_add_f32 v192, 1.0, v192\n\t" \
  "v_add_f32 v193, 1.0, v193\n\t" \
  "v_add_f32 v194, 1.0, v194\n\t" \
  "v_add_f32 v195, 1.0, v195\n\t" \
  "v_cvt_pk_bf16_f32 v186, v196, v197\n\t" \
  "v_cvt_pk_bf16_f32 v187, v198, v199\n\t" \
  "v_mfma_f32_32x32x16_bf16 v[100:115], v[36:39], v[168:171], v[100:115]\n\t" \
  "v_rcp_f32 v192, v192\n\t" \
  "v_rcp_f32 v193, v193\n\t" \
  "v_rcp_f32 v194, v194\n\t" \
  "v_rcp_f32 v195, v195\n\t" \
  "v_mfma_f32_32x32x16_bf16 v[100:115], v[44:47], v[172:175], v[100:115]\n\t" \
  "v_exp_f32 v196, v128\n\t" \
  "v_exp_f32 v197, v129\n\t" \
  "v_exp_f32 v198, v130\n\t" \
  "v_exp_f32 v199, v131\n\t" \
  "s_waitcnt lgkmcnt(4)\n\t" \
  "v_mfma_f32_32x32x16_bf16 v[100:115], v[212:215], v[164:167], v[100:115]\n\t" \
  "v_mul_f32 v192, v124, v192\n\t" \
  "v_mul_f32 v193, v125, v193\n\t" \
  "v_mul_f32 v194, v126, v194\n\t" \
  "v_mul_f32 v195, v127, v195\n\t" \
  "ds_write_b128 v243, v[184:187] offset:0\n\t" \
  "ds_read_b128 v[208:211], v247 offset:5120\n\t" \
  "ds_read_b128 v[212:215], v247 offset:4096\n\t" \
  "v_add_f32 v196, 1.0, v196\n\t" \
  "v_add_f32 v197, 1.0, v197\n\t" \
  "v_add_f32 v198, 1.0, v198\n\t" \
  "v_add_f32 v199, 1.0, v199\n\t" \
  "v_cvt_pk_bf16_f32 v188, v192, v193\n\t" \
  "v_cvt_pk_bf16_f32 v189, v194, v195\n\t" \
  "v_mfma_f32_32x32x16_bf16 v[52:67], v[200:203], v[176:179], v[52:67]\n\t" \
  "v_rcp_f32 v196, v196\n\t" \
  "v_rcp_f32 v197, v197\n\t" \
  "v_rcp_f32 v198, v198\n\t" \
  "v_rcp_f32 v199, v199\n\t" \
  "v_mfma_f32_32x32x16_bf16 v[52:67], v[204:207], v[180:183], v[52:67]\n\t" \
  "v_exp_f32 v192, v100\n\t" \
  "v_exp_f32 v193, v101\n\t" \
  "v_exp_f32 v194, v102\n\t" \
  "v_exp_f32 v195, v103\n\t" \
  "s_cmp_eq_u32 %[nt], 0\n\t" \
  "s_waitcnt lgkmcnt(0)\n\t" \
  "v_mfma_f32_32x32x16_bf16 v[132:147], v[212:215], v[184:187], v[132:147]\n\t" \
  "v_mul_f32 v196, v128, v196\n\t" \
  "v_mul_f32 v197, v129, v197\n\t" \
  "v_mul_f32 v198, v130, v198\n\t" \
  "v_mul_f32 v199, v131, v199\n\t" \
  "v_add_f32 v192, 1.0, v192\n\t" \
  "v_add_f32 v193, 1.0, v193\n\t" \
  "v_add_f32 v194, 1.0, v194\n\t" \
  "v_add_f32 v195, 1.0, v195\n\t" \
  "v_cvt_pk_bf16_f32 v190, v196, v197\n\t" \
  "v_cvt_pk_bf16_f32 v191, v198, v199\n\t" \
  "s_cbranch_scc0 1b\n\t" \
  "s_cmp_lg_u32 %[last], 0\n\t" \
  "s_cbranch_scc1 8f\n\t" \
  "ds_write_b128 v243, v[188:191] offset:16\n\t" \
  "ds_read_b64_tr_b16 v[200:201], v244 offset:0\n\t" \
  "ds_read_b64_tr_b16 v[202:203], v244 offset:320\n\t" \
  "ds_read_b64_tr_b16 v[204:205], v244 offset:1280\n\t" \
  "ds_read_b64_tr_b16 v[206:207], v244 offset:1600\n\t" \
  "v_mfma_f32_32x32x16_bf16 v[132:147], v[208:211], v[188:191], v[132:147]\n\t" \
  "s_waitcnt lgkmcnt(0)\n\t" \
  "v_mfma_f32_32x32x16_bf16 v[68:83], v[200:203], v[176:179], v[68:83]\n\t" \
  "v_mfma_f32_32x32x16_bf16 v[68:83], v[204:207], v[180:183], v[68:83]\n\t" \
  "s_nop 8\n\t" \
  "v_mul_f32 v230, v230, v132\n\t" \
  "v_mul_f32 v231, v231, v132\n\t" \
  "v_cvt_pk_bf16_f32 v230, v230, v231\n\t" \
  "ds_write_b16 v245, v230 offset:7456\n\t" \
  "ds_write_b16_d16_hi v241, v230 offset:7584\n\t" \
  "ds_read_b128 v[200:203], v246 offset:7456\n\t" \
  "ds_read_b128 v[208:211], v246 offset:7488\n\t" \
  "s_waitcnt lgkmcnt(0)\n\t" \
  "v_mfma_f32_32x32x16_bf16 v[84:99], v[200:203], v[176:179], v[84:99]\n\t" \
  "v_mfma_f32_32x32x16_bf16 v[84:99], v[208:211], v[180:183], v[84:99]\n\t" \
  "s_branch 9f\n\t" \
  "6:\n\t" \
  "s_cmp_lg_u32 %[last], 0\n\t" \
  "s_cbranch_scc0 9f\n\t" \
  "s_waitcnt lgkmcnt(0)\n\t" \
  "v_cndmask_b32 v224, 0, 24, vcc\n\t" \
  "v_xor_b32 v220, v234, v224\n\t" \
  "v_xor_b32 v221, v235, v224\n\t" \
  "v_xor_b32 v222, v236, v224\n\t" \
  "v_xor_b32 v223, v237, v224\n\t" \
  "v_xor_b32 v216, 16, v220\n\t" \
  "v_xor_b32 v217, 16, v221\n\t" \
  "v_xor_b32 v218, 16, v222\n\t" \
  "v_xor_b32 v219, 16, v223\n\t" \
  "ds_read_b64 v[148:149], v220\n\t" \
  "ds_read_b64 v[150:151], v216\n\t" \
  "ds_read_b64 v[152:153], v221\n\t" \
  "ds_read_b64 v[154:155], v217\n\t" \
  "ds_read_b64 v[156:157], v222\n\t" \
  "ds_read_b64 v[158:159], v218\n\t" \
  "ds_read_b64 v[160:161], v223\n\t" \
  "ds_read_b64 v[162:163], v219\n\t" \
  "ds_read_b128 v[4:7], v247 offset:6144\n\t" \
  "ds_read_b128 v[8:11], v247 offset:7168\n\t" \
  "ds_read_b128 v[12:15], v247 offset:8192\n\t" \
  "ds_read_b128 v[16:19], v247 offset:9216\n\t" \
  "ds_read_b128 v[20:23], v247 offset:10240\n\t" \
  "ds_read_b128 v[24:27], v247 offset:11264\n\t" \
  "v_subrev_u32 v225, %[b1], v241\n\t" \
  "v_lshl_add_u32 v225, v225, 5, v243\n\t" \
  "v_sub_u32 v225, v225, v224\n\t" \
  "s_nop 4\n\t" \
  "v_cvt_pk_bf16_f32 v184, v52, v53\n\t" \
  "v_cvt_pk_bf16_f32 v185, v54, v55\n\t" \
  "v_cvt_pk_bf16_f32 v186, v56, v57\n\t" \
  "v_cvt_pk_bf16_f32 v187, v58, v59\n\t" \
  "v_cvt_pk_bf16_f32 v188, v60, v61\n\t" \
  "v_cvt_pk_bf16_f32 v189, v62, v63\n\t" \
  "v_cvt_pk_bf16_f32 v190, v64, v65\n\t" \
  "v_cvt_pk_bf16_f32 v191, v66, v67\n\t" \
  "v_cvt_pk_bf16_f32 v36, v68, v69\n\t" \
  "v_cvt_pk_bf16_f32 v37, v70, v71\n\t" \
  "v_cvt_pk_bf16_f32 v38, v72, v73\n\t" \
  "v_cvt_pk_bf16_f32 v39, v74, v75\n\t" \
  "v_cvt_pk_bf16_f32 v40, v76, v77\n\t" \
  "v_cvt_pk_bf16_f32 v41, v78, v79\n\t" \
  "v_cvt_pk_bf16_f32 v42, v80, v81\n\t" \
  "v_cvt_pk_bf16_f32 v43, v82, v83\n\t" \
  "s_waitcnt lgkmcnt(5)\n\t" \
  "v_mfma_f32_32x32x16_bf16 v[100:115], v[4:7], v[148:151], 0\n\t" \
  "s_waitcnt lgkmcnt(4)\n\t" \
  "v_mfma_f32_32x32x16_bf16 v[116:131], v[8:11], v[148:151], 0\n\t" \
  "ds_read_b128 v[28:31], v247 offset:12288\n\t" \
  "ds_read_b128 v[32:35], v247 offset:13312\n\t" \
  "s_waitcnt lgkmcnt(5)\n\t" \
  "v_mfma_f32_32x32x16_bf16 v[100:115], v[12:15], v[152:155], v[100:115]\n\t" \
  "s_waitcnt lgkmcnt(4)\n\t" \
  "v_mfma_f32_32x32x16_bf16 v[116:131], v[16:19], v[152:155], v[116:131]\n\t" \
  "ds_read_b128 v[4:7], v247 offset:14336\n\t" \
  "ds_read_b128 v[8:11], v247 offset:15360\n\t" \
  "s_waitcnt lgkmcnt(5)\n\t" \
  "v_mfma_f32_32x32x16_bf16 v[100:115], v[20:23], v[156:159], v[100:115]\n\t" \
  "s_waitcnt lgkmcnt(4)\n\t" \
  "v_mfma_f32_32x32x16_bf16 v[116:131], v[24:27], v[156:159], v[116:131]\n\t" \
  "ds_read_b128 v[12:15], v247 offset:16384\n\t" \
  "ds_read_b128 v[16:19], v247 offset:17408\n\t" \
  "s_waitcnt lgkmcnt(5)\n\t" \
  "v_mfma_f32_32x32x16_bf16 v[100:115], v[28:31], v[160:163], v[100:115]\n\t" \
  "s_waitcnt lgkmcnt(4)\n\t" \
  "v_mfma_f32_32x32x16_bf16 v[116:131], v[32:35], v[160:163], v[116:131]\n\t" \
  "ds_read_b128 v[20:23], v247 offset:18432\n\t" \
  "ds_read_b128 v[24:27], v247 offset:19456\n\t" \
  "s_waitcnt lgkmcnt(5)\n\t" \
  "v_mfma_f32_32x32x16_bf16 v[100:115], v[4:7], v[184:187], v[100:115]\n\t" \
  "ds_read_b128 v[28:31], v247 offset:20480\n\t" \
  "ds_read_b128 v[32:35], v247 offset:21504\n\t" \
  "s_waitcnt lgkmcnt(5)\n\t" \
  "v_mfma_f32_32x32x16_bf16 v[100:115], v[12:15], v[188:191], v[100:115]\n\t" \
  "s_waitcnt lgkmcnt(3)\n\t" \
  "v_mfma_f32_32x32x16_bf16 v[100:115], v[20:23], v[36:39], v[100:115]\n\t" \
  "s_waitcnt lgkmcnt(1)\n\t" \
  "v_mfma_f32_32x32x16_bf16 v[100:115], v[28:31], v[40:43], v[100:115]\n\t" \
  "s_waitcnt lgkmcnt(0)\n\t" \
  "s_branch 10f\n\t" \
  "8:\n\t" \
  "ds_write_b128 v243, v[188:191] offset:16\n\t" \
  "ds_read_b64_tr_b16 v[200:201], v244 offset:0\n\t" \
  "ds_read_b64_tr_b16 v[202:203], v244 offset:320\n\t" \
  "ds_read_b64_tr_b16 v[204:205], v244 offset:1280\n\t" \
  "ds_read_b64_tr_b16 v[206:207], v244 offset:1600\n\t" \
  "v_mfma_f32_32x32x16_bf16 v[132:147], v[208:211], v[188:191], v[132:147]\n\t" \
  "v_cndmask_b32 v224, 0, 24, vcc\n\t" \
  "v_xor_b32 v220, v234, v224\n\t" \
  "v_xor_b32 v221, v235, v224\n\t" \
  "v_xor_b32 v222, v236, v224\n\t" \
  "v_xor_b32 v223, v237, v224\n\t" \
  "v_xor_b32 v216, 16, v220\n\t" \
  "v_xor_b32 v217, 16, v221\n\t" \
  "v_xor_b32 v218, 16, v222\n\t" \
  "v_xor_b32 v219, 16, v223\n\t" \
  "ds_read_b64 v[148:149], v220\n\t" \
  "ds_read_b64 v[150:151], v216\n\t" \
  "ds_read_b64 v[152:153], v221\n\t" \
  "ds_read_b64 v[154:155], v217\n\t" \
  "ds_read_b64 v[156:157], v222\n\t" \
  "ds_read_b64 v[158:159], v218\n\t" \
  "ds_read_b64 v[160:161], v223\n\t" \
  "ds_read_b64 v[162:163], v219\n\t" \
  "ds_read_b128 v[4:7], v247 offset:6144\n\t" \
  "ds_read_b128 v[8:11], v247 offset:7168\n\t" \
  "s_waitcnt lgkmcnt(11)\n\t" \
  "ds_read_b128 v[12:15], v247 offset:8192\n\t" \
  "ds_read_b128 v[16:19], v247 offset:9216\n\t" \
  "ds_read_b128 v[20:23], v247 offset:10240\n\t" \
  "ds_read_b128 v[24:27], v247 offset:11264\n\t" \
  "v_subrev_u32 v225, %[b1], v241\n\t" \
  "v_lshl_add_u32 v225, v225, 5, v243\n\t" \
  "v_sub_u32 v225, v225, v224\n\t" \
  "v_mfma_f32_32x32x16_bf16 v[68:83], v[200:203], v[176:179], v[68:83]\n\t" \
  "s_waitcnt lgkmcnt(5)\n\t" \
  "v_mfma_f32_32x32x16_bf16 v[100:115], v[4:7], v[148:151], 0\n\t" \
  "v_mfma_f32_32x32x16_bf16 v[68:83], v[204:207], v[180:183], v[68:83]\n\t" \
  "s_waitcnt lgkmcnt(4)\n\t" \
  "v_mfma_f32_32x32x16_bf16 v[116:131], v[8:11], v[148:151], 0\n\t" \
  "ds_read_b128 v[28:31], v247 offset:12288\n\t" \
  "ds_read_b128 v[32:35], v247 offset:13312\n\t" \
  "s_waitcnt lgkmcnt(5)\n\t" \
  "v_mfma_f32_32x32x16_bf16 v[100:115], v[12:15], v[152:155], v[100:115]\n\t" \
  "v_mul_f32 v230, v230, v132\n\t" \
  "v_mul_f32 v231, v231, v132\n\t" \
  "v_cvt_pk_bf16_f32 v230, v230, v231\n\t" \
  "ds_write_b16 v245, v230 offset:7456\n\t" \
  "ds_write_b16_d16_hi v241, v230 offset:7584\n\t" \
  "ds_read_b128 v[200:203], v246 offset:7456\n\t" \
  "ds_read_b128 v[208:211], v246 offset:7488\n\t" \
  "v_cvt_pk_bf16_f32 v184, v52, v53\n\t" \
  "v_cvt_pk_bf16_f32 v185, v54, v55\n\t" \
  "v_cvt_pk_bf16_f32 v186, v56, v57\n\t" \
  "v_cvt_pk_bf16_f32 v187, v58, v59\n\t" \
  "s_waitcnt lgkmcnt(8)\n\t" \
  "v_mfma_f32_32x32x16_bf16 v[116:131], v[16:19], v[152:155], v[116:131]\n\t" \
  "ds_read_b128 v[4:7], v247 offset:14336\n\t" \
  "ds_read_b128 v[8:11], v247 offset:15360\n\t" \
  "s_waitcnt lgkmcnt(9)\n\t" \
  "v_mfma_f32_32x32x16_bf16 v[100:115], v[20:23], v[156:159], v[100:115]\n\t" \
  "v_cvt_pk_bf16_f32 v188, v60, v61\n\t" \
  "v_cvt_pk_bf16_f32 v189, v62, v63\n\t" \
  "v_cvt_pk_bf16_f32 v190, v64, v65\n\t" \
  "v_cvt_pk_bf16_f32 v191, v66, v67\n\t" \
  "s_waitcnt lgkmcnt(8)\n\t" \
  "v_mfma_f32_32x32x16_bf16 v[116:131], v[24:27], v[156:159], v[116:131]\n\t" \
  "ds_read_b128 v[12:15], v247 offset:16384\n\t" \
  "ds_read_b128 v[16:19], v247 offset:17408\n\t" \
  "s_waitcnt lgkmcnt(9)\n\t" \
  "v_mfma_f32_32x32x16_bf16 v[100:115], v[28:31], v[160:163], v[100:115]\n\t" \
  "v_cvt_pk_bf16_f32 v36, v68, v69\n\t" \
  "v_cvt_pk_bf16_f32 v37, v70, v71\n\t" \
  "v_cvt_pk_bf16_f32 v38, v72, v73\n\t" \
  "v_cvt_pk_bf16_f32 v39, v74, v75\n\t" \
  "s_waitcnt lgkmcnt(5)\n\t" \
  "v_mfma_f32_32x32x16_bf16 v[84:99], v[200:203], v[176:179], v[84:99]\n\t" \
  "v_cvt_pk_bf16_f32 v40, v76, v77\n\t" \
  "v_cvt_pk_bf16_f32 v41, v78, v79\n\t" \
  "v_cvt_pk_bf16_f32 v42, v80, v81\n\t" \
  "v_cvt_pk_bf16_f32 v43, v82, v83\n\t" \
  "v_mfma_f32_32x32x16_bf16 v[116:131], v[32:35], v[160:163], v[116:131]\n\t" \
  "ds_read_b128 v[20:23], v247 offset:18432\n\t" \
  "ds_read_b128 v[24:27], v247 offset:19456\n\t" \
  "s_waitcnt lgkmcnt(6)\n\t" \
  "v_mfma_f32_32x32x16_bf16 v[84:99], v[208:211], v[180:183], v[84:99]\n\t" \
  "s_waitcnt lgkmcnt(5)\n\t" \
  "v_mfma_f32_32x32x16_bf16 v[100:115], v[4:7], v[184:187], v[100:115]\n\t" \
  "ds_read_b128 v[28:31], v247 offset:20480\n\t" \
  "ds_read_b128 v[32:35], v247 offset:21504\n\t" \
  "s_waitcnt lgkmcnt(5)\n\t" \
  "v_mfma_f32_32x32x16_bf16 v[100:115], v[12:15], v[188:191], v[100:115]\n\t" \
  "s_waitcnt lgkmcnt(3)\n\t" \
  "v_mfma_f32_32x32x16_bf16 v[100:115], v[20:23], v[36:39], v[100:115]\n\t" \
  "s_waitcnt lgkmcnt(1)\n\t" \
  "v_mfma_f32_32x32x16_bf16 v[100:115], v[28:31], v[40:43], v[100:115]\n\t" \
  "s_waitcnt lgkmcnt(0)\n\t" \
  "10:\n\t" \
  "v_mov_b32 v212, 0xbfb8aa3b\n\t" \
  "v_mfma_f32_32x32x16_bf16 v[116:131], v[8:11], v[184:187], v[116:131]\n\t" \
  "v_mfma_f32_32x32x16_bf16 v[116:131], v[16:19], v[188:191], v[116:131]\n\t" \
  "s_nop 8\n\t" \
  "v_exp_f32 v192, v100\n\t" \
  "v_exp_f32 v193, v101\n\t" \
  "v_exp_f32 v194, v102\n\t" \
  "v_exp_f32 v195, v103\n\t" \
  "v_lshlrev_b32 v200, 16, v148\n\t" \
  "v_and_b32 v201, 0xffff0000, v148\n\t" \
  "v_lshlrev_b32 v202, 16, v149\n\t" \
  "v_and_b32 v203, 0xffff0000, v149\n\t" \
  "v_mfma_f32_32x32x16_bf16 v[116:131], v[24:27], v[36:39], v[116:131]\n\t" \
  "v_fmaak_f32 v192, v192, v212, 0xbfb8aa3b\n\t" \
  "v_fmaak_f32 v193, v193, v212, 0xbfb8aa3b\n\t" \
  "v_fmaak_f32 v194, v194, v212, 0xbfb8aa3b\n\t" \
  "v_fmaak_f32 v195, v195, v212, 0xbfb8aa3b\n\t" \
  "v_rcp_f32 v192, v192\n\t" \
  "v_rcp_f32 v193, v193\n\t" \
  "v_rcp_f32 v194, v194\n\t" \
  "v_rcp_f32 v195, v195\n\t" \
  "v_mfma_f32_32x32x16_bf16 v[116:131], v[32:35], v[40:43], v[116:131]\n\t" \
  "v_exp_f32 v196, v104\n\t" \
  "v_exp_f32 v197, v105\n\t" \
  "v_exp_f32 v198, v106\n\t" \
  "v_exp_f32 v199, v107\n\t" \
  "v_fmac_f32 v200, v100, v192\n\t" \
  "v_fmac_f32 v201, v101, v193\n\t" \
  "v_fmac_f32 v202, v102, v194\n\t" \
  "v_fmac_f32 v203, v103, v195\n\t" \
  "v_lshlrev_b32 v204, 16, v150\n\t" \
  "v_and_b32 v205, 0xffff0000, v150\n\t" \
  "v_lshlrev_b32 v206, 16, v151\n\t" \
  "v_and_b32 v207, 0xffff0000, v151\n\t" \
  "v_fmaak_f32 v196, v196, v212, 0xbfb8aa3b\n\t" \
  "v_fmaak_f32 v197, v197, v212, 0xbfb8aa3b\n\t" \
  "v_fmaak_f32 v198, v198, v212, 0xbfb8aa3b\n\t" \
  "v_fmaak_f32 v199, v199, v212, 0xbfb8aa3b\n\t" \
  "v_cvt_pk_bf16_f32 v176, v200, v201\n\t" \
  "v_cvt_pk_bf16_f32 v177, v202, v203\n\t" \
  "ds_write_b64 v225, v[176:177] offset:0\n\t" \
  "v_rcp_f32 v196, v196\n\t" \
  "v_rcp_f32 v197, v197\n\t" \
  "v_rcp_f32 v198, v198\n\t" \
  "v_rcp_f32 v199, v199\n\t" \
  "v_exp_f32 v192, v108\n\t" \
  "v_exp_f32 v193, v109\n\t" \
  "v_exp_f32 v194, v110\n\t" \
  "v_exp_f32 v195, v111\n\t" \
  "v_fmac_f32 v204, v104, v196\n\t" \
  "v_fmac_f32 v205, v105, v197\n\t" \
  "v_fmac_f32 v206, v106, v198\n\t" \
  "v_fmac_f32 v207, v107, v199\n\t" \
  "v_lshlrev_b32 v200, 16, v152\n\t" \
  "v_and_b32 v201, 0xffff0000, v152\n\t" \
  "v_lshlrev_b32 v202, 16, v153\n\t" \
  "v_and_b32 v203, 0xffff0000, v153\n\t" \
  "v_fmaak_f32 v192, v192, v212, 0xbfb8aa3b\n\t" \
  "v_fmaak_f32 v193, v193, v212, 0xbfb8aa3b\n\t" \
  "v_fmaak_f32 v194, v194, v212, 0xbfb8aa3b\n\t" \
  "v_fmaak_f32 v195, v195, v212, 0xbfb8aa3b\n\t" \
  "v_cvt_pk_bf16_f32 v178, v204, v205\n\t" \
  "v_cvt_pk_bf16_f32 v179, v206, v207\n\t" \
  "ds_write_b64 v225, v[178:179] offset:16\n\t" \
  "v_rcp_f32 v192, v192\n\t" \
  "v_rcp_f32 v193, v193\n\t" \
  "v_rcp_f32 v194, v194\n\t" \
  "v_rcp_f32 v195, v195\n\t" \
  "v_exp_f32 v196, v112\n\t" \
  "v_exp_f32 v197, v113\n\t" \
  "v_exp_f32 v198, v114\n\t" \
  "v_exp_f32 v199, v115\n\t" \
  "v_fmac_f32 v200, v108, v192\n\t" \
  "v_fmac_f32 v201, v109, v193\n\t" \
  "v_fmac_f32 v202, v110, v194\n\t" \
  "v_fmac_f32 v203, v111, v195\n\t" \
  "v_lshlrev_b32 v204, 16, v154\n\t" \
  "v_and_b32 v205, 0xffff0000, v154\n\t" \
  "v_lshlrev_b32 v206, 16, v155\n\t" \
  "v_and_b32 v207, 0xffff0000, v155\n\t" \
  "v_fmaak_f32 v196, v196, v212, 0xbfb8aa3b\n\t" \
  "v_fmaak_f32 v197, v197, v212, 0xbfb8aa3b\n\t" \
  "v_fmaak_f32 v198, v198, v212, 0xbfb8aa3b\n\t" \
  "v_fmaak_f32 v199, v199, v212, 0xbfb8aa3b\n\t" \
  "v_cvt_pk_bf16_f32 v180, v200, v201\n\t" \
  "v_cvt_pk_bf16_f32 v181, v202, v203\n\t" \
  "ds_write_b64 v225, v[180:181] offset:32\n\t" \
  "v_rcp_f32 v196, v196\n\t" \
  "v_rcp_f32 v197, v197\n\t" \
  "v_rcp_f32 v198, v198\n\t" \
  "v_rcp_f32 v199, v199\n\t" \
  "v_exp_f32 v192, v116\n\t" \
  "v_exp_f32 v193, v117\n\t" \
  "v_exp_f32 v194, v118\n\t" \
  "v_exp_f32 v195, v119\n\t" \
  "v_fmac_f32 v204, v112, v196\n\t" \
  "v_fmac_f32 v205, v113, v197\n\t" \
  "v_fmac_f32 v206, v114, v198\n\t" \
  "v_fmac_f32 v207, v115, v199\n\t" \
  "v_lshlrev_b32 v200, 16, v156\n\t" \
  "v_and_b32 v201, 0xffff0000, v156\n\t" \
  "v_lshlrev_b32 v202, 16, v157\n\t" \
  "v_and_b32 v203, 0xffff0000, v157\n\t" \
  "v_fmaak_f32 v192, v192, v212, 0xbfb8aa3b\n\t" \
  "v_fmaak_f32 v193, v193, v212, 0xbfb8aa3b\n\t" \
  "v_fmaak_f32 v194, v194, v212, 0xbfb8aa3b\n\t" \
  "v_fmaak_f32 v195, v195, v212, 0xbfb8aa3b\n\t" \
  "v_cvt_pk_bf16_f32 v182, v204, v205\n\t" \
  "v_cvt_pk_bf16_f32 v183, v206, v207\n\t" \
  "ds_write_b64 v225, v[182:183] offset:48\n\t" \
  "v_rcp_f32 v192, v192\n\t" \
  "v_rcp_f32 v193, v193\n\t" \
  "v_rcp_f32 v194, v194\n\t" \
  "v_rcp_f32 v195, v195\n\t" \
  "v_exp_f32 v196, v120\n\t" \
  "v_exp_f32 v197, v121\n\t" \
  "v_exp_f32 v198, v122\n\t" \
  "v_exp_f32 v199, v123\n\t" \
  "v_fmac_f32 v200, v116, v192\n\t" \
  "v_fmac_f32 v201, v117, v193\n\t" \
  "v_fmac_f32 v202, v118, v194\n\t" \
  "v_fmac_f32 v203, v119, v195\n\t" \
  "v_lshlrev_b32 v204, 16, v158\n\t" \
  "v_and_b32 v205, 0xffff0000, v158\n\t" \
  "v_lshlrev_b32 v206, 16, v159\n\t" \
  "v_and_b32 v207, 0xffff0000, v159\n\t" \
  "v_fmaak_f32 v196, v196, v212, 0xbfb8aa3b\n\t" \
  "v_fmaak_f32 v197, v197, v212, 0xbfb8aa3b\n\t" \
  "v_fmaak_f32 v198, v198, v212, 0xbfb8aa3b\n\t" \
  "v_fmaak_f32 v199, v199, v212, 0xbfb8aa3b\n\t" \
  "v_cvt_pk_bf16_f32 v176, v200, v201\n\t" \
  "v_cvt_pk_bf16_f32 v177, v202, v203\n\t" \
  "ds_write_b64 v225, v[176:177] offset:64\n\t" \
  "v_rcp_f32 v196, v196\n\t" \
  "v_rcp_f32 v197, v197\n\t" \
  "v_rcp_f32 v198, v198\n\t" \
  "v_rcp_f32 v199, v199\n\t" \
  "v_exp_f32 v192, v124\n\t" \
  "v_exp_f32 v193, v125\n\t" \
  "v_exp_f32 v194, v126\n\t" \
  "v_exp_f32 v195, v127\n\t" \
  "v_fmac_f32 v204, v120, v196\n\t" \
  "v_fmac_f32 v205, v121, v197\n\t" \
  "v_fmac_f32 v206, v122, v198\n\t" \
  "v_fmac_f32 v207, v123, v199\n\t" \
  "v_lshlrev_b32 v200, 16, v160\n\t" \
  "v_and_b32 v201, 0xffff0000, v160\n\t" \
  "v_lshlrev_b32 v202, 16, v161\n\t" \
  "v_and_b32 v203, 0xffff0000, v161\n\t" \
  "v_fmaak_f32 v192, v192, v212, 0xbfb8aa3b\n\t" \
  "v_fmaak_f32 v193, v193, v212, 0xbfb8aa3b\n\t" \
  "v_fmaak_f32 v194, v194, v212, 0xbfb8aa3b\n\t" \
  "v_fmaak_f32 v195, v195, v212, 0xbfb8aa3b\n\t" \
  "v_cvt_pk_bf16_f32 v178, v204, v205\n\t" \
  "v_cvt_pk_bf16_f32 v179, v206, v207\n\t" \
  "ds_write_b64 v225, v[178:179] offset:80\n\t" \
  "v_rcp_f32 v192, v192\n\t" \
  "v_rcp_f32 v193, v193\n\t" \
  "v_rcp_f32 v194, v194\n\t" \
  "v_rcp_f32 v195, v195\n\t" \
  "v_exp_f32 v196, v128\n\t" \
  "v_exp_f32 v197, v129\n\t" \
  "v_exp_f32 v198, v130\n\t" \
  "v_exp_f32 v199, v131\n\t" \
  "v_fmac_f32 v200, v124, v192\n\t" \
  "v_fmac_f32 v201, v125, v193\n\t" \
  "v_fmac_f32 v202, v126, v194\n\t" \
  "v_fmac_f32 v203, v127, v195\n\t" \
  "v_lshlrev_b32 v204, 16, v162\n\t" \
  "v_and_b32 v205, 0xffff0000, v162\n\t" \
  "v_lshlrev_b32 v206, 16, v163\n\t" \
  "v_and_b32 v207, 0xffff0000, v163\n\t" \
  "v_fmaak_f32 v196, v196, v212, 0xbfb8aa3b\n\t" \
  "v_fmaak_f32 v197, v197, v212, 0xbfb8aa3b\n\t" \
  "v_fmaak_f32 v198, v198, v212, 0xbfb8aa3b\n\t" \
  "v_fmaak_f32 v199, v199, v212, 0xbfb8aa3b\n\t" \
  "v_cvt_pk_bf16_f32 v180, v200, v201\n\t" \
  "v_cvt_pk_bf16_f32 v181, v202, v203\n\t" \
  "ds_write_b64 v225, v[180:181] offset:96\n\t" \
  "v_rcp_f32 v196, v196\n\t" \
  "v_rcp_f32 v197, v197\n\t" \
  "v_rcp_f32 v198, v198\n\t" \
  "v_rcp_f32 v199, v199\n\t" \
  "s_nop 0\n\t" \
  "v_fmac_f32 v204, v128, v196\n\t" \
  "v_fmac_f32 v205, v129, v197\n\t" \
  "v_fmac_f32 v206, v130, v198\n\t" \
  "v_fmac_f32 v207, v131, v199\n\t" \
  "v_cvt_pk_bf16_f32 v182, v204, v205\n\t" \
  "v_cvt_pk_bf16_f32 v183, v206, v207\n\t" \
  "ds_write_b64 v225, v[182:183] offset:112\n\t" \
  "9:\n\t" \
  "ds_write_b16 v233, v255 offset:5376\n\t" \
  "s_mov_b32 m0, %[keep]\n\t" \
  "s_setprio 0\n\t" \
  "s_nop 11\n\t" \
  ""
#define LOOP_ASM_PLAIN \
  "s_mov_b32 %[keep], m0\n\t" \
  "s_mov_b64 vcc, %[hsel]\n\t" \
  "ds_read2_b32 v[220:221], v252 offset0:0 offset1:2\n\t" \
  "ds_read2_b32 v[222:223], v252 offset0:4 offset1:6\n\t" \
  "ds_read_b64 v[226:227], v253\n\t" \
  "ds_read_b32 v232, v254\n\t" \
  "ds_read_b128 v[212:215], v247 offset:0\n\t" \
  "v_mov_b32 v176, 0\n\t" \
  "v_mov_b32 v177, 0\n\t" \
  "v_mov_b32 v178, 0\n\t" \
  "v_mov_b32 v179, 0\n\t" \
  "v_mov_b32 v180, 0\n\t" \
  "v_mov_b32 v181, 0\n\t" \
  "v_mov_b32 v182, 0\n\t" \
  "v_mov_b32 v183, 0\n\t" \
  "v_mov_b32 v184, 0\n\t" \
  "v_mov_b32 v185, 0\n\t" \
  "v_mov_b32 v186, 0\n\t" \
  "v_mov_b32 v187, 0\n\t" \
  "v_mov_b32 v188, 0\n\t" \
  "v_mov_b32 v189, 0\n\t" \
  "v_mov_b32 v190, 0\n\t" \
  "v_mov_b32 v191, 0\n\t" \
  "v_mov_b32 v230, 0\n\t" \
  "v_mov_b32 v231, 0\n\t" \
  "v_mov_b32 v208, 0\n\t" \
  "v_mov_b32 v209, 0\n\t" \
  "v_mov_b32 v210, 0\n\t" \
  "v_mov_b32 v211, 0\n\t" \
  "ds_write_b128 v243, v[184:187] offset:0\n\t" \
  "s_nop 0\n\t" \
  "v_mfma_f32_32x32x16_bf16 v[132:147], v[184:187], v[188:191], 0\n\t" \
  "v_add_u32 v252, 0x100, v252\n\t" \
  "v_add_u32 v253, 0x100, v253\n\t" \
  "v_add_u32 v254, 0x100, v254\n\t" \
  "s_waitcnt lgkmcnt(0)\n\t" \
  "v_lshrrev_b32 v232, 20, v232\n\t" \
  "v_add_u32 v233, %[posd], v232\n\t" \
  "ds_read_b128 v[216:219], v233\n\t" \
  "v_add_u32 v233, v240, v232\n\t" \
  "ds_read_b128 v[172:175], v233 offset:32\n\t" \
  "ds_read_b128 v[168:171], v233\n\t" \
  "v_add_u32 v233, v241, v232\n\t" \
  "ds_write_b16_d16_hi v233, v255 offset:5376\n\t" \
  "v_mad_u32_u16 v220, v220, %[c128], v248\n\t" \
  "v_mad_u32_u16 v221, v221, %[c128], v249\n\t" \
  "v_mad_u32_u16 v222, v222, %[c128], v248\n\t" \
  "v_mad_u32_u16 v223, v223, %[c128], v249\n\t" \
  "v_or_b32 v226, v226, v250\n\t" \
  "v_mad_u32_u16 v227, v227, 6, v251\n\t" \
  "s_waitcnt vmcnt(0)\n\t" \
  "ds_read_b128 v[148:151], v234\n\t" \
  "ds_read_b128 v[152:155], v235\n\t" \
  "ds_read_b128 v[156:159], v236\n\t" \
  "ds_read_b128 v[160:163], v237\n\t" \
  "ds_read_b128 v[164:167], v238 offset:4096\n\t" \
  "ds_read_b64 v[224:225], v239 offset:5120\n\t" \
  "s_waitcnt lgkmcnt(0)\n\t" \
  "s_cmp_lt_i32 %[nt], 1\n\t" \
  "s_cbranch_scc1 7f\n\t" \
  "s_mov_b32 m0, %[b2]\n\t" \
  "s_nop 0\n\t" \
  "global_load_lds_dwordx4 v226, %[ea]\n\t" \
  "global_load_lds_dword v227, %[pos] offset:1024\n\t" \
  "s_mov_b32 m0, %[b1]\n\t" \
  "s_nop 0\n\t" \
  "global_load_lds_dwordx4 v220, %[h0]\n\t" \
  "global_load_lds_dwordx4 v221, %[h1] offset:1024\n\t" \
  "global_load_lds_dwordx4 v222, %[h2] offset:2048\n\t" \
  "global_load_lds_dwordx4 v223, %[h3] offset:3072\n\t" \
  "7:\n\t" \
  "ds_read2_b32 v[220:221], v252 offset0:0 offset1:2\n\t" \
  "ds_read2_b32 v[222:223], v252 offset0:4 offset1:6\n\t" \
  "ds_read_b64 v[226:227], v253\n\t" \
  "ds_read_b32 v232, v254\n\t" \
  "v_add_u32 v252, 0x100, v252\n\t" \
  "v_add_u32 v253, 0x100, v253\n\t" \
  "v_add_u32 v254, 0x100, v254\n\t" \
  "v_and_b32 v225, 0xffff0000, v225\n\t" \
  "v_lshlrev_b32 v219, 16, v224\n\t" \
  "v_and_b32 v224, 0xffff0000, v224\n\t" \
  "v_sub_f32 v229, v218, v225\n\t" \
  "v_sub_f32 v216, v216, v219\n\t" \
  "v_sub_f32 v217, v217, v224\n\t" \
  "v_mul_f32 v218, v229, v229\n\t" \
  "v_cndmask_b32 v228, v216, v217, vcc\n\t" \
  "v_fmac_f32 v218, v216, v216\n\t" \
  "v_fmac_f32 v218, v217, v217\n\t" \
  "v_cvt_pk_bf16_f32 v218, v218, v218\n\t" \
  "v_cndmask_b32_sdwa v175, v175, v218, vcc dst_sel:WORD_1 dst_unused:UNUSED_PRESERVE src0_sel:WORD_1 src1_sel:WORD_1\n\t" \
  "v_mfma_f32_32x32x16_bf16 v[100:115], v[4:7], v[148:151], 0\n\t" \
  "v_mfma_f32_32x32x16_bf16 v[100:115], v[12:15], v[152:155], v[100:115]\n\t" \
  "v_mfma_f32_32x32x16_bf16 v[100:115], v[20:23], v[156:159], v[100:115]\n\t" \
  "v_mfma_f32_32x32x16_bf16 v[100:115], v[28:31], v[160:163], v[100:115]\n\t" \
  "v_mfma_f32_32x32x16_bf16 v[100:115], v[36:39], v[168:171], v[100:115]\n\t" \
  "v_mfma_f32_32x32x16_bf16 v[100:115], v[212:215], v[164:167], v[100:115]\n\t" \
  "v_mfma_f32_32x32x16_bf16 v[100:115], v[44:47], v[172:175], v[100:115]\n\t" \
  "s_cmp_eq_u32 %[nt], 0\n\t" \
  "s_nop 10\n\t" \
  "v_mul_f32 v192, 0xbfb8aa3b, v100\n\t" \
  "v_mul_f32 v193, 0xbfb8aa3b, v101\n\t" \
  "v_mul_f32 v194, 0xbfb8aa3b, v102\n\t" \
  "v_mul_f32 v195, 0xbfb8aa3b, v103\n\t" \
  "v_exp_f32 v192, v192\n\t" \
  "v_exp_f32 v193, v193\n\t" \
  "v_exp_f32 v194, v194\n\t" \
  "v_exp_f32 v195, v195\n\t" \
  "s_nop 0\n\t" \
  "v_add_f32 v192, 1.0, v192\n\t" \
  "v_add_f32 v193, 1.0, v193\n\t" \
  "v_add_f32 v194, 1.0, v194\n\t" \
  "v_add_f32 v195, 1.0, v195\n\t" \
  "s_cbranch_scc1 6f\n\t" \
  "s_cmp_lt_i32 %[thr], 0\n\t" \
  "s_cbranch_scc1 1f\n\t" \
  "s_setprio 1\n\t" \
  "1:\n\t" \
  "ds_write_b128 v243, v[188:191] offset:16\n\t" \
  "ds_read_b64_tr_b16 v[200:201], v244 offset:0\n\t" \
  "ds_read_b64_tr_b16 v[202:203], v244 offset:320\n\t" \
  "ds_read_b64_tr_b16 v[204:205], v244 offset:1280\n\t" \
  "ds_read_b64_tr_b16 v[206:207], v244 offset:1600\n\t" \
  "ds_read_b128 v[212:215], v247 offset:1024\n\t" \
  "v_mfma_f32_32x32x16_bf16 v[132:147], v[208:211], v[188:191], v[132:147]\n\t" \
  "v_rcp_f32 v192, v192\n\t" \
  "v_rcp_f32 v193, v193\n\t" \
  "v_rcp_f32 v194, v194\n\t" \
  "v_rcp_f32 v195, v195\n\t" \
  "v_mfma_f32_32x32x16_bf16 v[116:131], v[8:11], v[148:151], 0\n\t" \
  "v_mul_f32 v196, 0xbfb8aa3b, v104\n\t" \
  "v_mul_f32 v197, 0xbfb8aa3b, v105\n\t" \
  "v_mul_f32 v198, 0xbfb8aa3b, v106\n\t" \
  "v_mul_f32 v199, 0xbfb8aa3b, v107\n\t" \
  "v_exp_f32 v196, v196\n\t" \
  "v_exp_f32 v197, v197\n\t" \
  "v_exp_f32 v198, v198\n\t" \
  "v_exp_f32 v199, v199\n\t" \
  "v_mfma_f32_32x32x16_bf16 v[116:131], v[16:19], v[152:155], v[116:131]\n\t" \
  "v_mul_f32 v192, v100, v192\n\t" \
  "v_mul_f32 v193, v101, v193\n\t" \
  "v_mul_f32 v194, v102, v194\n\t" \
  "v_mul_f32 v195, v103, v195\n\t" \
  "v_add_f32 v196, 1.0, v196\n\t" \
  "v_add_f32 v197, 1.0, v197\n\t" \
  "v_add_f32 v198, 1.0, v198\n\t" \
  "v_add_f32 v199, 1.0, v199\n\t" \
  "v_cvt_pk_bf16_f32 v184, v192, v193\n\t" \
  "v_cvt_pk_bf16_f32 v185, v194, v195\n\t" \
  "v_mfma_f32_32x32x16_bf16 v[116:131], v[24:27], v[156:159], v[116:131]\n\t" \
  "v_rcp_f32 v196, v196\n\t" \
  "v_rcp_f32 v197, v197\n\t" \
  "v_rcp_f32 v198, v198\n\t" \
  "v_rcp_f32 v199, v199\n\t" \
  "s_waitcnt lgkmcnt(3)\n\t" \
  "v_mfma_f32_32x32x16_bf16 v[68:83], v[200:203], v[176:179], v[68:83]\n\t" \
  "v_mul_f32 v192, 0xbfb8aa3b, v108\n\t" \
  "v_mul_f32 v193, 0xbfb8aa3b, v109\n\t" \
  "v_mul_f32 v194, 0xbfb8aa3b, v110\n\t" \
  "v_mul_f32 v195, 0xbfb8aa3b, v111\n\t" \
  "v_exp_f32 v192, v192\n\t" \
  "v_exp_f32 v193, v193\n\t" \
  "v_exp_f32 v194, v194\n\t" \
  "v_exp_f32 v195, v195\n\t" \
  "v_mfma_f32_32x32x16_bf16 v[116:131], v[32:35], v[160:163], v[116:131]\n\t" \
  "v_mul_f32 v196, v104, v196\n\t" \
  "v_mul_f32 v197, v105, v197\n\t" \
  "v_mul_f32 v198, v106, v198\n\t" \
  "v_mul_f32 v199, v107, v199\n\t" \
  "v_mul_f32 v230, v230, v132\n\t" \
  "v_mul_f32 v231, v231, v132\n\t" \
  "v_cvt_pk_bf16_f32 v230, v230, v231\n\t" \
  "ds_write_b16 v245, v230 offset:7456\n\t" \
  "ds_write_b16_d16_hi v241, v230 offset:7584\n\t" \
  "ds_read_b128 v[200:203], v246 offset:7456\n\t" \
  "ds_read_b128 v[208:211], v246 offset:7488\n\t" \
  "v_mov_b32 v230, v228\n\t" \
  "v_mov_b32 v231, v229\n\t" \
  "v_add_f32 v192, 1.0, v192\n\t" \
  "v_add_f32 v193, 1.0, v193\n\t" \
  "v_add_f32 v194, 1.0, v194\n\t" \
  "v_add_f32 v195, 1.0, v195\n\t" \
  "v_cvt_pk_bf16_f32 v186, v196, v197\n\t" \
  "v_cvt_pk_bf16_f32 v187, v198, v199\n\t" \
  "v_mfma_f32_32x32x16_bf16 v[116:131], v[40:43], v[168:171], v[116:131]\n\t" \
  "v_rcp_f32 v192, v192\n\t" \
  "v_rcp_f32 v193, v193\n\t" \
  "v_rcp_f32 v194, v194\n\t" \
  "v_rcp_f32 v195, v195\n\t" \
  "s_sub_u32 %[nt], %[nt], 1\n\t" \
  "s_cmp_lg_u32 %[nt], %[thr]\n\t" \
  "s_cbranch_scc1 3f\n\t" \
  "s_setprio 0\n\t" \
  "3:\n\t" \
  "s_waitcnt lgkmcnt(4)\n\t" \
  "v_mfma_f32_32x32x16_bf16 v[116:131], v[212:215], v[164:167], v[116:131]\n\t" \
  "v_mul_f32 v196, 0xbfb8aa3b, v112\n\t" \
  "v_mul_f32 v197, 0xbfb8aa3b, v113\n\t" \
  "v_mul_f32 v198, 0xbfb8aa3b, v114\n\t" \
  "v_mul_f32 v199, 0xbfb8aa3b, v115\n\t" \
  "v_exp_f32 v196, v196\n\t" \
  "v_exp_f32 v197, v197\n\t" \
  "v_exp_f32 v198, v198\n\t" \
  "v_exp_f32 v199, v199\n\t" \
  "v_mfma_f32_32x32x16_bf16 v[116:131], v[48:51], v[172:175], v[116:131]\n\t" \
  "v_mul_f32 v192, v108, v192\n\t" \
  "v_mul_f32 v193, v109, v193\n\t" \
  "v_mul_f32 v194, v110, v194\n\t" \
  "v_mul_f32 v195, v111, v195\n\t" \
  "s_waitcnt vmcnt(0)\n\t" \
  "ds_read_b128 v[148:151], v234\n\t" \
  "ds_read_b128 v[152:155], v235\n\t" \
  "ds_read_b128 v[156:159], v236\n\t" \
  "ds_read_b128 v[160:163], v237\n\t" \
  "ds_read_b128 v[164:167], v238 offset:4096\n\t" \
  "ds_read_b64 v[224:225], v239 offset:5120\n\t" \
  "ds_write_b128 v243, v[184:187] offset:0\n\t" \
  "ds_read_b128 v[212:215], v247 offset:2048\n\t" \
  "v_add_f32 v196, 1.0, v196\n\t" \
  "v_add_f32 v197, 1.0, v197\n\t" \
  "v_add_f32 v198, 1.0, v198\n\t" \
  "v_add_f32 v199, 1.0, v199\n\t" \
  "v_cvt_pk_bf16_f32 v188, v192, v193\n\t" \
  "v_cvt_pk_bf16_f32 v189, v194, v195\n\t" \
  "v_mad_u32_u16 v220, v220, %[c128], v248\n\t" \
  "v_mad_u32_u16 v221, v221, %[c128], v249\n\t" \
  "v_mad_u32_u16 v222, v222, %[c128], v248\n\t" \
  "v_mfma_f32_32x32x16_bf16 v[68:83], v[204:207], v[180:183], v[68:83]\n\t" \
  "v_rcp_f32 v196, v196\n\t" \
  "v_rcp_f32 v197, v197\n\t" \
  "v_rcp_f32 v198, v198\n\t" \
  "v_rcp_f32 v199, v199\n\t" \
  "v_mad_u32_u16 v223, v223, %[c128], v249\n\t" \
  "v_or_b32 v226, v226, v250\n\t" \
  "v_mad_u32_u16 v227, v227, 6, v251\n\t" \
  "s_waitcnt lgkmcnt(9)\n\t" \
  "v_mfma_f32_32x32x16_bf16 v[84:99], v[200:203], v[176:179], v[84:99]\n\t" \
  "v_mul_f32 v192, 0xbfb8aa3b, v116\n\t" \
  "v_mul_f32 v193, 0xbfb8aa3b, v117\n\t" \
  "v_mul_f32 v194, 0xbfb8aa3b, v118\n\t" \
  "v_mul_f32 v195, 0xbfb8aa3b, v119\n\t" \
  "v_exp_f32 v192, v192\n\t" \
  "v_exp_f32 v193, v193\n\t" \
  "v_exp_f32 v194, v194\n\t" \
  "v_exp_f32 v195, v195\n\t" \
  "s_waitcnt lgkmcnt(8)\n\t" \
  "v_mfma_f32_32x32x16_bf16 v[84:99], v[208:211], v[180:183], v[84:99]\n\t" \
  "v_mul_f32 v196, v112, v196\n\t" \
  "v_mul_f32 v197, v113, v197\n\t" \
  "v_mul_f32 v198, v114, v198\n\t" \
  "v_mul_f32 v199, v115, v199\n\t" \
  "s_waitcnt lgkmcnt(2)\n\t" \
  "s_cmp_lt_i32 %[nt], 1\n\t" \
  "s_cbranch_scc1 4f\n\t" \
  "s_mov_b32 m0, %[b2]\n\t" \
  "s_nop 0\n\t" \
  "global_load_lds_dwordx4 v226, %[ea]\n\t" \
  "global_load_lds_dword v227, %[pos] offset:1024\n\t" \
  "s_mov_b32 m0, %[b1]\n\t" \
  "s_nop 0\n\t" \
  "global_load_lds_dwordx4 v220, %[h0]\n\t" \
  "global_load_lds_dwordx4 v221, %[h1] offset:1024\n\t" \
  "global_load_lds_dwordx4 v222, %[h2] offset:2048\n\t" \
  "global_load_lds_dwordx4 v223, %[h3] offset:3072\n\t" \
  "4:\n\t" \
  "v_add_f32 v192, 1.0, v192\n\t" \
  "v_add_f32 v193, 1.0, v193\n\t" \
  "v_add_f32 v194, 1.0, v194\n\t" \
  "v_add_f32 v195, 1.0, v195\n\t" \
  "v_cvt_pk_bf16_f32 v190, v196, v197\n\t" \
  "v_cvt_pk_bf16_f32 v191, v198, v199\n\t" \
  "v_lshrrev_b32 v232, 20, v232\n\t" \
  "v_add_u32 v220, %[posd], v232\n\t" \
  "v_add_u32 v221, v240, v232\n\t" \
  "ds_write_b128 v243, v[188:191] offset:16\n\t" \
  "ds_read_b128 v[176:179], v242 offset:5376\n\t" \
  "ds_read_b128 v[180:183], v242 offset:5408\n\t" \
  "ds_write_b16 v233, v255 offset:5376\n\t" \
  "ds_read_b128 v[216:219], v220\n\t" \
  "ds_read_b128 v[172:175], v221 offset:32\n\t" \
  "ds_read_b128 v[168:171], v221\n\t" \
  "ds_read_b64_tr_b16 v[200:201], v244 offset:0\n\t" \
  "ds_read_b64_tr_b16 v[202:203], v244 offset:320\n\t" \
  "ds_read_b64_tr_b16 v[204:205], v244 offset:1280\n\t" \
  "ds_read_b64_tr_b16 v[206:207], v244 offset:1600\n\t" \
  "ds_read_b128 v[208:211], v247 offset:3072\n\t" \
  "v_add_u32 v233, v241, v232\n\t" \
  "ds_write_b16_d16_hi v233, v255 offset:5376\n\t" \
  "s_waitcnt lgkmcnt(13)\n\t" \
  "v_mfma_f32_32x32x16_bf16 v[132:147], v[212:215], v[184:187], 0\n\t" \
  "v_rcp_f32 v192, v192\n\t" \
  "v_rcp_f32 v193, v193\n\t" \
  "v_rcp_f32 v194, v194\n\t" \
  "v_rcp_f32 v195, v195\n\t" \
  "v_mfma_f32_32x32x16_bf16 v[100:115], v[4:7], v[148:151], 0\n\t" \
  "v_mul_f32 v196, 0xbfb8aa3b, v120\n\t" \
  "v_mul_f32 v197, 0xbfb8aa3b, v121\n\t" \
  "v_mul_f32 v198, 0xbfb8aa3b, v122\n\t" \
  "v_mul_f32 v199, 0xbfb8aa3b, v123\n\t" \
  "v_exp_f32 v196, v196\n\t" \
  "v_exp_f32 v197, v197\n\t" \
  "v_exp_f32 v198, v198\n\t" \
  "v_exp_f32 v199, v199\n\t" \
  "v_mfma_f32_32x32x16_bf16 v[100:115], v[12:15], v[152:155], v[100:115]\n\t" \
  "v_mul_f32 v192, v116, v192\n\t" \
  "v_mul_f32 v193, v117, v193\n\t" \
  "v_mul_f32 v194, v118, v194\n\t" \
  "v_mul_f32 v195, v119, v195\n\t" \
  "v_add_f32 v196, 1.0, v196\n\t" \
  "v_add_f32 v197, 1.0, v197\n\t" \
  "v_add_f32 v198, 1.0, v198\n\t" \
  "v_add_f32 v199, 1.0, v199\n\t" \
  "v_cvt_pk_bf16_f32 v184, v192, v193\n\t" \
  "v_cvt_pk_bf16_f32 v185, v194, v195\n\t" \
  "v_and_b32 v225, 0xffff0000, v225\n\t" \
  "s_waitcnt lgkmcnt(8)\n\t" \
  "v_lshlrev_b32 v219, 16, v224\n\t" \
  "v_and_b32 v224, 0xffff0000, v224\n\t" \
  "v_sub_f32 v229, v218, v225\n\t" \
  "v_sub_f32 v216, v216, v219\n\t" \
  "v_sub_f32 v217, v217, v224\n\t" \
  "v_mfma_f32_32x32x16_bf16 v[100:115], v[20:23], v[156:159], v[100:115]\n\t" \
  "v_rcp_f32 v196, v196\n\t" \
  "v_rcp_f32 v197, v197\n\t" \
  "v_rcp_f32 v198, v198\n\t" \
  "v_rcp_f32 v199, v199\n\t" \
  "v_mul_f32 v218, v229, v229\n\t" \
  "v_cndmask_b32 v228, v216, v217, vcc\n\t" \
  "v_fmac_f32 v218, v216, v216\n\t" \
  "v_fmac_f32 v218, v217, v217\n\t" \
  "v_cvt_pk_bf16_f32 v218, v218, v218\n\t" \
  "s_waitcnt lgkmcnt(7)\n\t" \
  "v_cndmask_b32_sdwa v175, v175, v218, vcc dst_sel:WORD_1 dst_unused:UNUSED_PRESERVE src0_sel:WORD_1 src1_sel:WORD_1\n\t" \
  "s_waitcnt lgkmcnt(1)\n\t" \
  "v_mfma_f32_32x32x16_bf16 v[132:147], v[208:211], v[188:191], v[132:147]\n\t" \
  "v_mul_f32 v192, 0xbfb8aa3b, v124\n\t" \
  "v_mul_f32 v193, 0xbfb8aa3b, v125\n\t" \
  "v_mul_f32 v194, 0xbfb8aa3b, v126\n\t" \
  "v_mul_f32 v195, 0xbfb8aa3b, v127\n\t" \
  "v_exp_f32 v192, v192\n\t" \
  "v_exp_f32 v193, v193\n\t" \
  "v_exp_f32 v194, v194\n\t" \
  "v_exp_f32 v195, v195\n\t" \
  "ds_read_b128 v[212:215], v247 offset:0\n\t" \
  "ds_read2_b32 v[220:221], v252 offset0:0 offset1:2\n\t" \
  "ds_read2_b32 v[222:223], v252 offset0:4 offset1:6\n\t" \
  "ds_read_b64 v[226:227], v253\n\t" \
  "ds_read_b32 v232, v254\n\t" \
  "v_mfma_f32_32x32x16_bf16 v[100:115], v[28:31], v[160:163], v[100:115]\n\t" \
  "v_mul_f32 v196, v120, v196\n\t" \
  "v_mul_f32 v197, v121, v197\n\t" \
  "v_mul_f32 v198, v122, v198\n\t" \
  "v_mul_f32 v199, v123, v199\n\t" \
  "v_add_u32 v252, 0x100, v252\n\t" \
  "v_add_u32 v253, 0x100, v253\n\t" \
  "v_add_u32 v254, 0x100, v254\n\t" \
  "v_add_f32 v192, 1.0, v192\n\t" \
  "v_add_f32 v193, 1.0, v193\n\t" \
  "v_add_f32 v194, 1.0, v194\n\t" \
  "v_add_f32 v195, 1.0, v195\n\t" \
  "v_cvt_pk_bf16_f32 v186, v196, v197\n\t" \
  "v_cvt_pk_bf16_f32 v187, v198, v199\n\t" \
  "v_mfma_f32_32x32x16_bf16 v[100:115], v[36:39], v[168:171], v[100:115]\n\t" \
  "v_rcp_f32 v192, v192\n\t" \
  "v_rcp_f32 v193, v193\n\t" \
  "v_rcp_f32 v194, v194\n\t" \
  "v_rcp_f32 v195, v195\n\t" \
  "v_mfma_f32_32x32x16_bf16 v[100:115], v[44:47], v[172:175], v[100:115]\n\t" \
  "v_mul_f32 v196, 0xbfb8aa3b, v128\n\t" \
  "v_mul_f32 v197, 0xbfb8aa3b, v129\n\t" \
  "v_mul_f32 v198, 0xbfb8aa3b, v130\n\t" \
  "v_mul_f32 v199, 0xbfb8aa3b, v131\n\t" \
  "v_exp_f32 v196, v196\n\t" \
  "v_exp_f32 v197, v197\n\t" \
  "v_exp_f32 v198, v198\n\t" \
  "v_exp_f32 v199, v199\n\t" \
  "s_waitcnt lgkmcnt(4)\n\t" \
  "v_mfma_f32_32x32x16_bf16 v[100:115], v[212:215], v[164:167], v[100:115]\n\t" \
  "v_mul_f32 v192, v124, v192\n\t" \
  "v_mul_f32 v193, v125, v193\n\t" \
  "v_mul_f32 v194, v126, v194\n\t" \
  "v_mul_f32 v195, v127, v195\n\t" \
  "ds_write_b128 v243, v[184:187] offset:0\n\t" \
  "ds_read_b128 v[208:211], v247 offset:5120\n\t" \
  "ds_read_b128 v[212:215], v247 offset:4096\n\t" \
  "v_add_f32 v196, 1.0, v196\n\t" \
  "v_add_f32 v197, 1.0, v197\n\t" \
  "v_add_f32 v198, 1.0, v198\n\t" \
  "v_add_f32 v199, 1.0, v199\n\t" \
  "v_cvt_pk_bf16_f32 v188, v192, v193\n\t" \
  "v_cvt_pk_bf16_f32 v189, v194, v195\n\t" \
  "v_mfma_f32_32x32x16_bf16 v[52:67], v[200:203], v[176:179], v[52:67]\n\t" \
  "v_rcp_f32 v196, v196\n\t" \
  "v_rcp_f32 v197, v197\n\t" \
  "v_rcp_f32 v198, v198\n\t" \
  "v_rcp_f32 v199, v199\n\t" \
  "v_mfma_f32_32x32x16_bf16 v[52:67], v[204:207], v[180:183], v[52:67]\n\t" \
  "v_mul_f32 v192, 0xbfb8aa3b, v100\n\t" \
  "v_mul_f32 v193, 0xbfb8aa3b, v101\n\t" \
  "v_mul_f32 v194, 0xbfb8aa3b, v102\n\t" \
  "v_mul_f32 v195, 0xbfb8aa3b, v103\n\t" \
  "v_exp_f32 v192, v192\n\t" \
  "v_exp_f32 v193, v193\n\t" \
  "v_exp_f32 v194, v194\n\t" \
  "v_exp_f32 v195, v195\n\t" \
  "s_cmp_eq_u32 %[nt], 0\n\t" \
  "s_waitcnt lgkmcnt(0)\n\t" \
  "v_mfma_f32_32x32x16_bf16 v[132:147], v[212:215], v[184:187], v[132:147]\n\t" \
  "v_mul_f32 v196, v128, v196\n\t" \
  "v_mul_f32 v197, v129, v197\n\t" \
  "v_mul_f32 v198, v130, v198\n\t" \
  "v_mul_f32 v199, v131, v199\n\t" \
  "v_add_f32 v192, 1.0, v192\n\t" \
  "v_add_f32 v193, 1.0, v193\n\t" \
  "v_add_f32 v194, 1.0, v194\n\t" \
  "v_add_f32 v195, 1.0, v195\n\t" \
  "v_cvt_pk_bf16_f32 v190, v196, v197\n\t" \
  "v_cvt_pk_bf16_f32 v191, v198, v199\n\t" \
  "s_cbranch_scc0 1b\n\t" \
  "s_cmp_lg_u32 %[last], 0\n\t" \
  "s_cbranch_scc1 8f\n\t" \
  "ds_write_b128 v243, v[188:191] offset:16\n\t" \
  "ds_read_b64_tr_b16 v[200:201], v244 offset:0\n\t" \
  "ds_read_b64_tr_b16 v[202:203], v244 offset:320\n\t" \
  "ds_read_b64_tr_b16 v[204:205], v244 offset:1280\n\t" \
  "ds_read_b64_tr_b16 v[206:207], v244 offset:1600\n\t" \
  "v_mfma_f32_32x32x16_bf16 v[132:147], v[208:211], v[188:191], v[132:147]\n\t" \
  "s_waitcnt lgkmcnt(0)\n\t" \
  "v_mfma_f32_32x32x16_bf16 v[68:83], v[200:203], v[176:179], v[68:83]\n\t" \
  "v_mfma_f32_32x32x16_bf16 v[68:83], v[204:207], v[180:183], v[68:83]\n\t" \
  "s_nop 8\n\t" \
  "v_mul_f32 v230, v230, v132\n\t" \
  "v_mul_f32 v231, v231, v132\n\t" \
  "v_cvt_pk_bf16_f32 v230, v230, v231\n\t" \
  "ds_write_b16 v245, v230 offset:7456\n\t" \
  "ds_write_b16_d16_hi v241, v230 offset:7584\n\t" \
  "ds_read_b128 v[200:203], v246 offset:7456\n\t" \
  "ds_read_b128 v[208:211], v246 offset:7488\n\t" \
  "s_waitcnt lgkmcnt(0)\n\t" \
  "v_mfma_f32_32x32x16_bf16 v[84:99], v[200:203], v[176:179], v[84:99]\n\t" \
  "v_mfma_f32_32x32x16_bf16 v[84:99], v[208:211], v[180:183], v[84:99]\n\t" \
  "s_branch 9f\n\t" \
  "6:\n\t" \
  "s_cmp_lg_u32 %[last], 0\n\t" \
  "s_cbranch_scc0 9f\n\t" \
  "s_waitcnt lgkmcnt(0)\n\t" \
  "v_cndmask_b32 v224, 0, 24, vcc\n\t" \
  "v_xor_b32 v220, v234, v224\n\t" \
  "v_xor_b32 v221, v235, v224\n\t" \
  "v_xor_b32 v222, v236, v224\n\t" \
  "v_xor_b32 v223, v237, v224\n\t" \
  "v_xor_b32 v216, 16, v220\n\t" \
  "v_xor_b32 v217, 16, v221\n\t" \
  "v_xor_b32 v218, 16, v222\n\t" \
  "v_xor_b32 v219, 16, v223\n\t" \
  "ds_read_b64 v[148:149], v220\n\t" \
  "ds_read_b64 v[150:151], v216\n\t" \
  "ds_read_b64 v[152:153], v221\n\t" \
  "ds_read_b64 v[154:155], v217\n\t" \
  "ds_read_b64 v[156:157], v222\n\t" \
  "ds_read_b64 v[158:159], v218\n\t" \
  "ds_read_b64 v[160:161], v223\n\t" \
  "ds_read_b64 v[162:163], v219\n\t" \
  "ds_read_b128 v[4:7], v247 offset:6144\n\t" \
  "ds_read_b128 v[8:11], v247 offset:7168\n\t" \
  "ds_read_b128 v[12:15], v247 offset:8192\n\t" \
  "ds_read_b128 v[16:19], v247 offset:9216\n\t" \
  "ds_read_b128 v[20:23], v247 offset:10240\n\t" \
  "ds_read_b128 v[24:27], v247 offset:11264\n\t" \
  "v_subrev_u32 v225, %[b1], v241\n\t" \
  "v_lshl_add_u32 v225, v225, 5, v243\n\t" \
  "v_sub_u32 v225, v225, v224\n\t" \
  "s_nop 4\n\t" \
  "v_cvt_pk_bf16_f32 v184, v52, v53\n\t" \
  "v_cvt_pk_bf16_f32 v185, v54, v55\n\t" \
  "v_cvt_pk_bf16_f32 v186, v56, v57\n\t" \
  "v_cvt_pk_bf16_f32 v187, v58, v59\n\t" \
  "v_cvt_pk_bf16_f32 v188, v60, v61\n\t" \
  "v_cvt_pk_bf16_f32 v189, v62, v63\n\t" \
  "v_cvt_pk_bf16_f32 v190, v64, v65\n\t" \
  "v_cvt_pk_bf16_f32 v191, v66, v67\n\t" \
  "v_cvt_pk_bf16_f32 v36, v68, v69\n\t" \
  "v_cvt_pk_bf16_f32 v37, v70, v71\n\t" \
  "v_cvt_pk_bf16_f32 v38, v72, v73\n\t" \
  "v_cvt_pk_bf16_f32 v39, v74, v75\n\t" \
  "v_cvt_pk_bf16_f32 v40, v76, v77\n\t" \
  "v_cvt_pk_bf16_f32 v41, v78, v79\n\t" \
  "v_cvt_pk_bf16_f32 v42, v80, v81\n\t" \
  "v_cvt_pk_bf16_f32 v43, v82, v83\n\t" \
  "s_waitcnt lgkmcnt(5)\n\t" \
  "v_mfma_f32_32x32x16_bf16 v[100:115], v[4:7], v[148:151], 0\n\t" \
  "s_waitcnt lgkmcnt(4)\n\t" \
  "v_mfma_f32_32x32x16_bf16 v[116:131], v[8:11], v[148:151], 0\n\t" \
  "ds_read_b128 v[28:31], v247 offset:12288\n\t" \
  "ds_read_b128 v[32:35], v247 offset:13312\n\t" \
  "s_waitcnt lgkmcnt(5)\n\t" \
  "v_mfma_f32_32x32x16_bf16 v[100:115], v[12:15], v[152:155], v[100:115]\n\t" \
  "s_waitcnt lgkmcnt(4)\n\t" \
  "v_mfma_f32_32x32x16_bf16 v[116:131], v[16:19], v[152:155], v[116:131]\n\t" \
  "ds_read_b128 v[4:7], v247 offset:14336\n\t" \
  "ds_read_b128 v[8:11], v247 offset:15360\n\t" \
  "s_waitcnt lgkmcnt(5)\n\t" \
  "v_mfma_f32_32x32x16_bf16 v[100:115], v[20:23], v[156:159], v[100:115]\n\t" \
  "s_waitcnt lgkmcnt(4)\n\t" \
  "v_mfma_f32_32x32x16_bf16 v[116:131], v[24:27], v[156:159], v[116:131]\n\t" \
  "ds_read_b128 v[12:15], v247 offset:16384\n\t" \
  "ds_read_b128 v[16:19], v247 offset:17408\n\t" \
  "s_waitcnt lgkmcnt(5)\n\t" \
  "v_mfma_f32_32x32x16_bf16 v[100:115], v[28:31], v[160:163], v[100:115]\n\t" \
  "s_waitcnt lgkmcnt(4)\n\t" \
  "v_mfma_f32_32x32x16_bf16 v[116:131], v[32:35], v[160:163], v[116:131]\n\t" \
  "ds_read_b128 v[20:23], v247 offset:18432\n\t" \
  "ds_read_b128 v[24:27], v247 offset:19456\n\t" \
  "s_waitcnt lgkmcnt(5)\n\t" \
  "v_mfma_f32_32x32x16_bf16 v[100:115], v[4:7], v[184:187], v[100:115]\n\t" \
  "ds_read_b128 v[28:31], v247 offset:20480\n\t" \
  "ds_read_b128 v[32:35], v247 offset:21504\n\t" \
  "s_waitcnt lgkmcnt(5)\n\t" \
  "v_mfma_f32_32x32x16_bf16 v[100:115], v[12:15], v[188:191], v[100:115]\n\t" \
  "s_waitcnt lgkmcnt(3)\n\t" \
  "v_mfma_f32_32x32x16_bf16 v[100:115], v[20:23], v[36:39], v[100:115]\n\t" \
  "s_waitcnt lgkmcnt(1)\n\t" \
  "v_mfma_f32_32x32x16_bf16 v[100:115], v[28:31], v[40:43], v[100:115]\n\t" \
  "s_waitcnt lgkmcnt(0)\n\t" \
  "s_branch 10f\n\t" \
  "8:\n\t" \
  "ds_write_b128 v243, v[188:191] offset:16\n\t" \
  "ds_read_b64_tr_b16 v[200:201], v244 offset:0\n\t" \
  "ds_read_b64_tr_b16 v[202:203], v244 offset:320\n\t" \
  "ds_read_b64_tr_b16 v[204:205], v244 offset:1280\n\t" \
  "ds_read_b64_tr_b16 v[206:207], v244 offset:1600\n\t" \
  "v_mfma_f32_32x32x16_bf16 v[132:147], v[208:211], v[188:191], v[132:147]\n\t" \
  "v_cndmask_b32 v224, 0, 24, vcc\n\t" \
  "v_xor_b32 v220, v234, v224\n\t" \
  "v_xor_b32 v221, v235, v224\n\t" \
  "v_xor_b32 v222, v236, v224\n\t" \
  "v_xor_b32 v223, v237, v224\n\t" \
  "v_xor_b32 v216, 16, v220\n\t" \
  "v_xor_b32 v217, 16, v221\n\t" \
  "v_xor_b32 v218, 16, v222\n\t" \
  "v_xor_b32 v219, 16, v223\n\t" \
  "ds_read_b64 v[148:149], v220\n\t" \
  "ds_read_b64 v[150:151], v216\n\t" \
  "ds_read_b64 v[152:153], v221\n\t" \
  "ds_read_b64 v[154:155], v217\n\t" \
  "ds_read_b64 v[156:157], v222\n\t" \
  "ds_read_b64 v[158:159], v218\n\t" \
  "ds_read_b64 v[160:161], v223\n\t" \
  "ds_read_b64 v[162:163], v219\n\t" \
  "ds_read_b128 v[4:7], v247 offset:6144\n\t" \
  "ds_read_b128 v[8:11], v247 offset:7168\n\t" \
  "s_waitcnt lgkmcnt(11)\n\t" \
  "ds_read_b128 v[12:15], v247 offset:8192\n\t" \
  "ds_read_b128 v[16:19], v247 offset:9216\n\t" \
  "ds_read_b128 v[20:23], v247 offset:10240\n\t" \
  "ds_read_b128 v[24:27], v247 offset:11264\n\t" \
  "v_subrev_u32 v225, %[b1], v241\n\t" \
  "v_lshl_add_u32 v225, v225, 5, v243\n\t" \
  "v_sub_u32 v225, v225, v224\n\t" \
  "v_mfma_f32_32x32x16_bf16 v[68:83], v[200:203], v[176:179], v[68:83]\n\t" \
  "s_waitcnt lgkmcnt(5)\n\t" \
  "v_mfma_f32_32x32x16_bf16 v[100:115], v[4:7], v[148:151], 0\n\t" \
  "v_mfma_f32_32x32x16_bf16 v[68:83], v[204:207], v[180:183], v[68:83]\n\t" \
  "s_waitcnt lgkmcnt(4)\n\t" \
  "v_mfma_f32_32x32x16_bf16 v[116:131], v[8:11], v[148:151], 0\n\t" \
  "ds_read_b128 v[28:31], v247 offset:12288\n\t" \
  "ds_read_b128 v[32:35], v247 offset:13312\n\t" \
  "s_waitcnt lgkmcnt(5)\n\t" \
  "v_mfma_f32_32x32x16_bf16 v[100:115], v[12:15], v[152:155], v[100:115]\n\t" \
  "v_mul_f32 v230, v230, v132\n\t" \
  "v_mul_f32 v231, v231, v132\n\t" \
  "v_cvt_pk_bf16_f32 v230, v230, v231\n\t" \
  "ds_write_b16 v245, v230 offset:7456\n\t" \
  "ds_write_b16_d16_hi v241, v230 offset:7584\n\t" \
  "ds_read_b128 v[200:203], v246 offset:7456\n\t" \
  "ds_read_b128 v[208:211], v246 offset:7488\n\t" \
  "v_cvt_pk_bf16_f32 v184, v52, v53\n\t" \
  "v_cvt_pk_bf16_f32 v185, v54, v55\n\t" \
  "v_cvt_pk_bf16_f32 v186, v56, v57\n\t" \
  "v_cvt_pk_bf16_f32 v187, v58, v59\n\t" \
  "s_waitcnt lgkmcnt(8)\n\t" \
  "v_mfma_f32_32x32x16_bf16 v[116:131], v[16:19], v[152:155], v[116:131]\n\t" \
  "ds_read_b128 v[4:7], v247 offset:14336\n\t" \
  "ds_read_b128 v[8:11], v247 offset:15360\n\t" \
  "s_waitcnt lgkmcnt(9)\n\t" \
  "v_mfma_f32_32x32x16_bf16 v[100:115], v[20:23], v[156:159], v[100:115]\n\t" \
  "v_cvt_pk_bf16_f32 v188, v60, v61\n\t" \
  "v_cvt_pk_bf16_f32 v189, v62, v63\n\t" \
  "v_cvt_pk_bf16_f32 v190, v64, v65\n\t" \
  "v_cvt_pk_bf16_f32 v191, v66, v67\n\t" \
  "s_waitcnt lgkmcnt(8)\n\t" \
  "v_mfma_f32_32x32x16_bf16 v[116:131], v[24:27], v[156:159], v[116:131]\n\t" \
  "ds_read_b128 v[12:15], v247 offset:16384\n\t" \
  "ds_read_b128 v[16:19], v247 offset:17408\n\t" \
  "s_waitcnt lgkmcnt(9)\n\t" \
  "v_mfma_f32_32x32x16_bf16 v[100:115], v[28:31], v[160:163], v[100:115]\n\t" \
  "v_cvt_pk_bf16_f32 v36, v68, v69\n\t" \
  "v_cvt_pk_bf16_f32 v37, v70, v71\n\t" \
  "v_cvt_pk_bf16_f32 v38, v72, v73\n\t" \
  "v_cvt_pk_bf16_f32 v39, v74, v75\n\t" \
  "s_waitcnt lgkmcnt(5)\n\t" \
  "v_mfma_f32_32x32x16_bf16 v[84:99], v[200:203], v[176:179], v[84:99]\n\t" \
  "v_cvt_pk_bf16_f32 v40, v76, v77\n\t" \
  "v_cvt_pk_bf16_f32 v41, v78, v79\n\t" \
  "v_cvt_pk_bf16_f32 v42, v80, v81\n\t" \
  "v_cvt_pk_bf16_f32 v43, v82, v83\n\t" \
  "v_mfma_f32_32x32x16_bf16 v[116:131], v[32:35], v[160:163], v[116:131]\n\t" \
  "ds_read_b128 v[20:23], v247 offset:18432\n\t" \
  "ds_read_b128 v[24:27], v247 offset:19456\n\t" \
  "s_waitcnt lgkmcnt(6)\n\t" \
  "v_mfma_f32_32x32x16_bf16 v[84:99], v[208:211], v[180:183], v[84:99]\n\t" \
  "s_waitcnt lgkmcnt(5)\n\t" \
  "v_mfma_f32_32x32x16_bf16 v[100:115], v[4:7], v[184:187], v[100:115]\n\t" \
  "ds_read_b128 v[28:31], v247 offset:20480\n\t" \
  "ds_read_b128 v[32:35], v247 offset:21504\n\t" \
  "s_waitcnt lgkmcnt(5)\n\t" \
  "v_mfma_f32_32x32x16_bf16 v[100:115], v[12:15], v[188:191], v[100:115]\n\t" \
  "s_waitcnt lgkmcnt(3)\n\t" \
  "v_mfma_f32_32x32x16_bf16 v[100:115], v[20:23], v[36:39], v[100:115]\n\t" \
  "s_waitcnt lgkmcnt(1)\n\t" \
  "v_mfma_f32_32x32x16_bf16 v[100:115], v[28:31], v[40:43], v[100:115]\n\t" \
  "s_waitcnt lgkmcnt(0)\n\t" \
  "10:\n\t" \
  "v_mov_b32 v212, 0xbfb8aa3b\n\t" \
  "v_mfma_f32_32x32x16_bf16 v[116:131], v[8:11], v[184:187], v[116:131]\n\t" \
  "v_mfma_f32_32x32x16_bf16 v[116:131], v[16:19], v[188:191], v[116:131]\n\t" \
  "s_nop 8\n\t" \
  "v_mul_f32 v192, 0xbfb8aa3b, v100\n\t" \
  "v_mul_f32 v193, 0xbfb8aa3b, v101\n\t" \
  "v_mul_f32 v194, 0xbfb8aa3b, v102\n\t" \
  "v_mul_f32 v195, 0xbfb8aa3b, v103\n\t" \
  "v_exp_f32 v192, v192\n\t" \
  "v_exp_f32 v193, v193\n\t" \
  "v_exp_f32 v194, v194\n\t" \
  "v_exp_f32 v195, v195\n\t" \
  "v_lshlrev_b32 v200, 16, v148\n\t" \
  "v_and_b32 v201, 0xffff0000, v148\n\t" \
  "v_lshlrev_b32 v202, 16, v149\n\t" \
  "v_and_b32 v203, 0xffff0000, v149\n\t" \
  "v_mfma_f32_32x32x16_bf16 v[116:131], v[24:27], v[36:39], v[116:131]\n\t" \
  "v_add_f32 v192, 1.0, v192\n\t" \
  "v_add_f32 v193, 1.0, v193\n\t" \
  "v_add_f32 v194, 1.0, v194\n\t" \
  "v_add_f32 v195, 1.0, v195\n\t" \
  "v_rcp_f32 v192, v192\n\t" \
  "v_rcp_f32 v193, v193\n\t" \
  "v_rcp_f32 v194, v194\n\t" \
  "v_rcp_f32 v195, v195\n\t" \
  "v_mfma_f32_32x32x16_bf16 v[116:131], v[32:35], v[40:43], v[116:131]\n\t" \
  "v_mul_f32 v196, 0xbfb8aa3b, v104\n\t" \
  "v_mul_f32 v197, 0xbfb8aa3b, v105\n\t" \
  "v_mul_f32 v198, 0xbfb8aa3b, v106\n\t" \
  "v_mul_f32 v199, 0xbfb8aa3b, v107\n\t" \
  "v_exp_f32 v196, v196\n\t" \
  "v_exp_f32 v197, v197\n\t" \
  "v_exp_f32 v198, v198\n\t" \
  "v_exp_f32 v199, v199\n\t" \
  "v_fmac_f32 v200, v100, v192\n\t" \
  "v_fmac_f32 v201, v101, v193\n\t" \
  "v_fmac_f32 v202, v102, v194\n\t" \
  "v_fmac_f32 v203, v103, v195\n\t" \
  "v_lshlrev_b32 v204, 16, v150\n\t" \
  "v_and_b32 v205, 0xffff0000, v150\n\t" \
  "v_lshlrev_b32 v206, 16, v151\n\t" \
  "v_and_b32 v207, 0xffff0000, v151\n\t" \
  "v_add_f32 v196, 1.0, v196\n\t" \
  "v_add_f32 v197, 1.0, v197\n\t" \
  "v_add_f32 v198, 1.0, v198\n\t" \
  "v_add_f32 v199, 1.0, v199\n\t" \
  "v_cvt_pk_bf16_f32 v176, v200, v201\n\t" \
  "v_cvt_pk_bf16_f32 v177, v202, v203\n\t" \
  "ds_write_b64 v225, v[176:177] offset:0\n\t" \
  "v_rcp_f32 v196, v196\n\t" \
  "v_rcp_f32 v197, v197\n\t" \
  "v_rcp_f32 v198, v198\n\t" \
  "v_rcp_f32 v199, v199\n\t" \
  "v_mul_f32 v192, 0xbfb8aa3b, v108\n\t" \
  "v_mul_f32 v193, 0xbfb8aa3b, v109\n\t" \
  "v_mul_f32 v194, 0xbfb8aa3b, v110\n\t" \
  "v_mul_f32 v195, 0xbfb8aa3b, v111\n\t" \
  "v_exp_f32 v192, v192\n\t" \
  "v_exp_f32 v193, v193\n\t" \
  "v_exp_f32 v194, v194\n\t" \
  "v_exp_f32 v195, v195\n\t" \
  "v_fmac_f32 v204, v104, v196\n\t" \
  "v_fmac_f32 v205, v105, v197\n\t" \
  "v_fmac_f32 v206, v106, v198\n\t" \
  "v_fmac_f32 v207, v107, v199\n\t" \
  "v_lshlrev_b32 v200, 16, v152\n\t" \
  "v_and_b32 v201, 0xffff0000, v152\n\t" \
  "v_lshlrev_b32 v202, 16, v153\n\t" \
  "v_and_b32 v203, 0xffff0000, v153\n\t" \
  "v_add_f32 v192, 1.0, v192\n\t" \
  "v_add_f32 v193, 1.0, v193\n\t" \
  "v_add_f32 v194, 1.0, v194\n\t" \
  "v_add_f32 v195, 1.0, v195\n\t" \
  "v_cvt_pk_bf16_f32 v178, v204, v205\n\t" \
  "v_cvt_pk_bf16_f32 v179, v206, v207\n\t" \
  "ds_write_b64 v225, v[178:179] offset:16\n\t" \
  "v_rcp_f32 v192, v192\n\t" \
  "v_rcp_f32 v193, v193\n\t" \
  "v_rcp_f32 v194, v194\n\t" \
  "v_rcp_f32 v195, v195\n\t" \
  "v_mul_f32 v196, 0xbfb8aa3b, v112\n\t" \
  "v_mul_f32 v197, 0xbfb8aa3b, v113\n\t" \
  "v_mul_f32 v198, 0xbfb8aa3b, v114\n\t" \
  "v_mul_f32 v199, 0xbfb8aa3b, v115\n\t" \
  "v_exp_f32 v196, v196\n\t" \
  "v_exp_f32 v197, v197\n\t" \
  "v_exp_f32 v198, v198\n\t" \
  "v_exp_f32 v199, v199\n\t" \
  "v_fmac_f32 v200, v108, v192\n\t" \
  "v_fmac_f32 v201, v109, v193\n\t" \
  "v_fmac_f32 v202, v110, v194\n\t" \
  "v_fmac_f32 v203, v111, v195\n\t" \
  "v_lshlrev_b32 v204, 16, v154\n\t" \
  "v_and_b32 v205, 0xffff0000, v154\n\t" \
  "v_lshlrev_b32 v206, 16, v155\n\t" \
  "v_and_b32 v207, 0xffff0000, v155\n\t" \
  "v_add_f32 v196, 1.0, v196\n\t" \
  "v_add_f32 v197, 1.0, v197\n\t" \
  "v_add_f32 v198, 1.0, v198\n\t" \
  "v_add_f32 v199, 1.0, v199\n\t" \
  "v_cvt_pk_bf16_f32 v180, v200, v201\n\t" \
  "v_cvt_pk_bf16_f32 v181, v202, v203\n\t" \
  "ds_write_b64 v225, v[180:181] offset:32\n\t" \
  "v_rcp_f32 v196, v196\n\t" \
  "v_rcp_f32 v197, v197\n\t" \
  "v_rcp_f32 v198, v198\n\t" \
  "v_rcp_f32 v199, v199\n\t" \
  "v_mul_f32 v192, 0xbfb8aa3b, v116\n\t" \
  "v_mul_f32 v193, 0xbfb8aa3b, v117\n\t" \
  "v_mul_f32 v194, 0xbfb8aa3b, v118\n\t" \
  "v_mul_f32 v195, 0xbfb8aa3b, v119\n\t" \
  "v_exp_f32 v192, v192\n\t" \
  "v_exp_f32 v193, v193\n\t" \
  "v_exp_f32 v194, v194\n\t" \
  "v_exp_f32 v195, v195\n\t" \
  "v_fmac_f32 v204, v112, v196\n\t" \
  "v_fmac_f32 v205, v113, v197\n\t" \
  "v_fmac_f32 v206, v114, v198\n\t" \
  "v_fmac_f32 v207, v115, v199\n\t" \
  "v_lshlrev_b32 v200, 16, v156\n\t" \
  "v_and_b32 v201, 0xffff0000, v156\n\t" \
  "v_lshlrev_b32 v202, 16, v157\n\t" \
  "v_and_b32 v203, 0xffff0000, v157\n\t" \
  "v_add_f32 v192, 1.0, v192\n\t" \
  "v_add_f32 v193, 1.0, v193\n\t" \
  "v_add_f32 v194, 1.0, v194\n\t" \
  "v_add_f32 v195, 1.0, v195\n\t" \
  "v_cvt_pk_bf16_f32 v182, v204, v205\n\t" \
  "v_cvt_pk_bf16_f32 v183, v206, v207\n\t" \
  "ds_write_b64 v225, v[182:183] offset:48\n\t" \
  "v_rcp_f32 v192, v192\n\t" \
  "v_rcp_f32 v193, v193\n\t" \
  "v_rcp_f32 v194, v194\n\t" \
  "v_rcp_f32 v195, v195\n\t" \
  "v_mul_f32 v196, 0xbfb8aa3b, v120\n\t" \
  "v_mul_f32 v197, 0xbfb8aa3b, v121\n\t" \
  "v_mul_f32 v198, 0xbfb8aa3b, v122\n\t" \
  "v_mul_f32 v199, 0xbfb8aa3b, v123\n\t" \
  "v_exp_f32 v196, v196\n\t" \
  "v_exp_f32 v197, v197\n\t" \
  "v_exp_f32 v198, v198\n\t" \
  "v_exp_f32 v199, v199\n\t" \
  "v_fmac_f32 v200, v116, v192\n\t" \
  "v_fmac_f32 v201, v117, v193\n\t" \
  "v_fmac_f32 v202, v118, v194\n\t" \
  "v_fmac_f32 v203, v119, v195\n\t" \
  "v_lshlrev_b32 v204, 16, v158\n\t" \
  "v_and_b32 v205, 0xffff0000, v158\n\t" \
  "v_lshlrev_b32 v206, 16, v159\n\t" \
  "v_and_b32 v207, 0xffff0000, v159\n\t" \
  "v_add_f32 v196, 1.0, v196\n\t" \
  "v_add_f32 v197, 1.0, v197\n\t" \
  "v_add_f32 v198, 1.0, v198\n\t" \
  "v_add_f32 v199, 1.0, v199\n\t" \
  "v_cvt_pk_bf16_f32 v176, v200, v201\n\t" \
  "v_cvt_pk_bf16_f32 v177, v202, v203\n\t" \
  "ds_write_b64 v225, v[176:177] offset:64\n\t" \
  "v_rcp_f32 v196, v196\n\t" \
  "v_rcp_f32 v197, v197\n\t" \
  "v_rcp_f32 v198, v198\n\t" \
  "v_rcp_f32 v199, v199\n\t" \
  "v_mul_f32 v192, 0xbfb8aa3b, v124\n\t" \
  "v_mul_f32 v193, 0xbfb8aa3b, v125\n\t" \
  "v_mul_f32 v194, 0xbfb8aa3b, v126\n\t" \
  "v_mul_f32 v195, 0xbfb8aa3b, v127\n\t" \
  "v_exp_f32 v192, v192\n\t" \
  "v_exp_f32 v193, v193\n\t" \
  "v_exp_f32 v194, v194\n\t" \
  "v_exp_f32 v195, v195\n\t" \
  "v_fmac_f32 v204, v120, v196\n\t" \
  "v_fmac_f32 v205, v121, v197\n\t" \
  "v_fmac_f32 v206, v122, v198\n\t" \
  "v_fmac_f32 v207, v123, v199\n\t" \
  "v_lshlrev_b32 v200, 16, v160\n\t" \
  "v_and_b32 v201, 0xffff0000, v160\n\t" \
  "v_lshlrev_b32 v202, 16, v161\n\t" \
  "v_and_b32 v203, 0xffff0000, v161\n\t" \
  "v_add_f32 v192, 1.0, v192\n\t" \
  "v_add_f32 v193, 1.0, v193\n\t" \
  "v_add_f32 v194, 1.0, v194\n\t" \
  "v_add_f32 v195, 1.0, v195\n\t" \
  "v_cvt_pk_bf16_f32 v178, v204, v205\n\t" \
  "v_cvt_pk_bf16_f32 v179, v206, v207\n\t" \
  "ds_write_b64 v225, v[178:179] offset:80\n\t" \
  "v_rcp_f32 v192, v192\n\t" \
  "v_rcp_f32 v193, v193\n\t" \
  "v_rcp_f32 v194, v194\n\t" \
  "v_rcp_f32 v195, v195\n\t" \
  "v_mul_f32 v196, 0xbfb8aa3b, v128\n\t" \
  "v_mul_f32 v197, 0xbfb8aa3b, v129\n\t" \
  "v_mul_f32 v198, 0xbfb8aa3b, v130\n\t" \
  "v_mul_f32 v199, 0xbfb8aa3b, v131\n\t" \
  "v_exp_f32 v196, v196\n\t" \
  "v_exp_f32 v197, v197\n\t" \
  "v_exp_f32 v198, v198\n\t" \
  "v_exp_f32 v199, v199\n\t" \
  "v_fmac_f32 v200, v124, v192\n\t" \
  "v_fmac_f32 v201, v125, v193\n\t" \
  "v_fmac_f32 v202, v126, v194\n\t" \
  "v_fmac_f32 v203, v127, v195\n\t" \
  "v_lshlrev_b32 v204, 16, v162\n\t" \
  "v_and_b32 v205, 0xffff0000, v162\n\t" \
  "v_lshlrev_b32 v206, 16, v163\n\t" \
  "v_and_b32 v207, 0xffff0000, v163\n\t" \
  "v_add_f32 v196, 1.0, v196\n\t" \
  "v_add_f32 v197, 1.0, v197\n\t" \
  "v_add_f32 v198, 1.0, v198\n\t" \
  "v_add_f32 v199, 1.0, v199\n\t" \
  "v_cvt_pk_bf16_f32 v180, v200, v201\n\t" \
  "v_cvt_pk_bf16_f32 v181, v202, v203\n\t" \
  "ds_write_b64 v225, v[180:181] offset:96\n\t" \
  "v_rcp_f32 v196, v196\n\t" \
  "v_rcp_f32 v197, v197\n\t" \
  "v_rcp_f32 v198, v198\n\t" \
  "v_rcp_f32 v199, v199\n\t" \
  "s_nop 0\n\t" \
  "v_fmac_f32 v204, v128, v196\n\t" \
  "v_fmac_f32 v205, v129, v197\n\t" \
  "v_fmac_f32 v206, v130, v198\n\t" \
  "v_fmac_f32 v207, v131, v199\n\t" \
  "v_cvt_pk_bf16_f32 v182, v204, v205\n\t" \
  "v_cvt_pk_bf16_f32 v183, v206, v207\n\t" \
  "ds_write_b64 v225, v[182:183] offset:112\n\t" \
  "9:\n\t" \
  "ds_write_b16 v233, v255 offset:5376\n\t" \
  "s_mov_b32 m0, %[keep]\n\t" \
  "s_setprio 0\n\t" \
  "s_nop 11\n\t" \
  ""
#define LOOP_ASM LOOP_ASM_FOLD

DI u32x4 tr_frag(unsigned p, int half) {
  const s16x4 lo = __builtin_amdgcn_ds_read_tr16_b64_v4i16((lds_s16x4_p)p);
  const s16x4 hi = __builtin_amdgcn_ds_read_tr16_b64_v4i16((lds_s16x4_p)(p + half));
  return __builtin_bit_cast(u32x4, __builtin_shufflevector(lo, hi, 0, 1, 2, 3, 4, 5, 6, 7));
}
DI u32x4 fold(u32x4 v) {
#pragma unroll
  for (int i = 0; i < 4; i++) v[i] = pk_bf16(bf2f(v[i] & 0xffffu) * -LOG2E, __uint_as_float(v[i] & 0xffff0000u) * -LOG2E);
  return v;
}

__global__ __launch_bounds__(512, 2) void egnn_kernel(const char* __restrict__ hmat, const char* __restrict__ pos,
                                                      const char* __restrict__ ea, const int* __restrict__ src,
                                                      const int* __restrict__ dst, const char* __restrict__ we,
                                                      const char* __restrict__ wx, const char* __restrict__ wh,
                                                      char* __restrict__ ws, uint16_t* __restrict__ out, int N, int E STAMP_ARG) {
  const int w = __builtin_amdgcn_readfirstlane(threadIdx.x >> 6);
  const int bkt = (blockIdx.x & 7) * 32 + (blockIdx.x >> 3);
  const int n0 = bkt * NB;
  STAMP(0);
  unsigned wl = L_WAVE + w * WSZ, mi = L_MI + w * MISZ;
  const auto rs_tab = __builtin_amdgcn_make_buffer_rsrc((void*)(ws + WS_TAB), 0, NBK * NBK * 8, 0x00020000);
  const auto rs_rec = __builtin_amdgcn_make_buffer_rsrc((void*)(ws + WS_REC), 0, NBK * CHS, 0x00020000);
  const auto rs_ea = __builtin_amdgcn_make_buffer_rsrc((void*)ea, 0, E * 32, 0x00020000);
  unsigned tag_lo, tag_hi;
  unsigned wdv0, wdv1;

  {
    const int lane = lane_id(), tid = w * 64 + lane, r = lane & 31, h = lane >> 5;
    const int e0 = bkt * CH;
    int ne = max(E - e0, 0);
    asm("" : "+s"(ne));
    ne = min(ne, CH);
    const auto rs_d = __builtin_amdgcn_make_buffer_rsrc((void*)(dst + e0), 0, ne * 4, 0x00020000);
    const auto rs_s = __builtin_amdgcn_make_buffer_rsrc((void*)(src + e0), 0, ne * 4, 0x00020000);
    unsigned d[7], sv[7];
    u32x2 xa, xb;
    unsigned long long ep;
    asm volatile("s_load_dwordx2 %0, %1, 0x0" : "=s"(ep) : "s"(ws + WS_EPOCH) : "memory");
#pragma unroll
    for (int i = 0; i < 7; i++)
      asm volatile("buffer_load_dword %0, %2, %3, %5 offen nt\n\tbuffer_load_dword %1, %2, %4, %5 offen nt"
                   : "=&v"(d[i]), "=&v"(sv[i]) : "v"(tid * 4), "s"(rs_d), "s"(rs_s), "s"(i * 2048) : "memory");
    LDS(unsigned, L_CNT + tid * 4) = 0;
    asm volatile("s_waitcnt lgkmcnt(0)\n\ts_barrier" : "+s"(ep) :: "memory");
    asm volatile("global_load_ushort %0, %4, %5\n\tglobal_load_ushort %1, %4, %5 offset:64\n\t"
                 "global_load_dwordx2 %2, %6, %7\n\tglobal_load_dwordx2 %3, %6, %7 offset:16"
                 : "=&v"(wdv0), "=&v"(wdv1), "=&v"(xa), "=&v"(xb)
                 : "v"(r * 2), "s"(we + 128 * 128), "v"(h * 8), "s"(wx + ((w + 2) & 3) * 32) : "memory");
    {
      const int j = lane >> 3, jl = (j & 3) * 128 + (lane & 7) * 16, jh = (j >> 2) * 128;
      const char* const h0 = wh + (w < 4 ? 16 * w : 64 + 32 * ((w - 4) >> 1) + 4 * (w & 1)) * 128;
      const char* const e0p = we + (w < 4 ? 64 + 16 * w : 16 * (w - 4)) * 128;
      const unsigned oh = jl + jh * (w < 4 ? 8 : 16), oe = jl + jh * (w < 4 ? 4 : 8);
      unsigned keep;
      asm volatile("s_mov_b32 %0, m0\n\ts_mov_b32 m0, %9\n\ts_nop 0\n\t"
                   "global_load_lds_dwordx4 %1, %4\n\tglobal_load_lds_dwordx4 %1, %5 offset:1024\n\t"
                   "global_load_lds_dwordx4 %2, %6 offset:2048\n\tglobal_load_lds_dwordx4 %2, %7 offset:3072\n\t"
                   "s_add_u32 m0, %9, 0x1000\n\ts_nop 0\n\tglobal_load_lds_dwordx4 %3, %8\n\ts_mov_b32 m0, %0"
                   : "=&s"(keep)
                   : "v"(oh), "v"(oe), "v"(w < 2 ? (lane >> 2) * 128 + (lane & 3) * 16 : 0),
                     "s"(h0), "s"(h0 + (w < 4 ? 4 : 8) * 128 - 1024), "s"(e0p - 2048), "s"(e0p + (w < 4 ? 8 : 4) * 128 - 3072),
                     "s"(we + 129 * 128 + (w & 1) * 64), "s"(wl)
                   : "memory", "scc");
    }
    unsigned ra[5], keep;
    {
      const unsigned cc0 = ((lane & 7) ^ (lane >> 4)) * 16, cc1 = ((lane & 7) ^ (4 + (lane >> 4))) * 16;
      const int nr = n0 + w + (lane & 56);
      ra[0] = min(nr, N - 1) * 128 + cc0; ra[1] = min(nr + 64, N - 1) * 128 + cc1; ra[2] = min(nr + 128, N - 1) * 128 + cc0;
      ra[3] = min(n0 + w + 192, N - 1) * 128 + cc1; ra[4] = mul24(min(n0 + w + min(lane >> 1, 24) * 8, N - 1), 6) + (lane & 1) * 2;
      asm volatile("" : "+v"(ra[0]), "+v"(ra[1]), "+v"(ra[2]), "+v"(ra[3]), "+v"(ra[4]));
    }
    asm volatile("s_waitcnt vmcnt(9)" : "+v"(d[0]), "+v"(d[1]), "+v"(d[2]), "+v"(d[3]), "+v"(d[4]), "+v"(d[5]), "+v"(d[6]),
                 "+v"(sv[0]), "+v"(sv[1]), "+v"(sv[2]), "+v"(sv[3]), "+v"(sv[4]), "+v"(sv[5]), "+v"(sv[6]) :: "memory");
    STAMP(10);
    {
      asm volatile("s_mov_b32 %0, m0\n\t"
                   "s_mov_b32 m0, %8\n\ts_nop 0\n\tglobal_load_lds_dwordx4 %1, %6\n\t"
                   "s_add_u32 m0, %8, 0x400\n\ts_nop 0\n\tglobal_load_lds_dwordx4 %2, %6\n\t"
                   "s_add_u32 m0, %8, 0x800\n\ts_nop 0\n\tglobal_load_lds_dwordx4 %3, %6\n\t"
                   "s_add_u32 m0, %8, 0xc00\n\ts_nop 0\n\tglobal_load_lds_dwordx4 %4, %6\n\t"
                   "s_add_u32 m0, %8, 0x1000\n\ts_nop 0\n\tglobal_load_lds_dword %5, %7\n\ts_mov_b32 m0, %0"
                   : "=&s"(keep)
                   : "v"(ra[0]), "v"(ra[1]), "v"(ra[2]), "v"(ra[3]), "v"(ra[4]), "s"(hmat), "s"(pos), "s"(mi)
                   : "memory", "scc");
    }
    unsigned ent, total;
    auto sort = [&](auto NS_, auto FULL_) __attribute__((always_inline)) {
      constexpr int NS = decltype(NS_)::value;
      constexpr bool FULL = decltype(FULL_)::value;
      unsigned bk[NS], rank[NS];
      u32x2 rec[NS];
#pragma unroll
      for (int i = 0; i < NS; i++) {
        const int el = i * 512 + tid;
        const unsigned b = mul24(d[i], 85599u) >> 24;
        const unsigned dl = d[i] - mul24(b, NB);
        bk[i] = (FULL || el < ne ? b : NBK + lane) * 4;
        rank[i] = __hip_atomic_fetch_add(&LDS(unsigned, L_CNT + bk[i]), 8u, __ATOMIC_RELAXED, __HIP_MEMORY_SCOPE_WORKGROUP);
        rec[i][0] = el;
        rec[i][1] = ((mul24(dl >> 3, 1272u) + dl) << 16) | sv[i] | 0x80000u;
      }
      LDS(unsigned, L_BASE + (NBK + lane) * 4) = 0x40000000u;
      barrier_lds();
      u32x4 cv = LDS(u32x4, L_CNT + lane * 16);
#pragma unroll
      for (int i = 0; i < 4; i++) cv[i] = (cv[i] + 8) & ~15u;
      const unsigned sum = cv[0] + cv[1] + cv[2] + cv[3], incl = wave_scan_incl(sum);
      const unsigned ex = incl - sum + L_REC;
      total = __builtin_amdgcn_readlane(incl, 63);
      const u32x4 bv = {ex, ex + cv[0], ex + cv[0] + cv[1], ex + cv[0] + cv[1] + cv[2]};
      LDS(u32x4, L_BASE + lane * 16) = bv;
      unsigned bs[NS];
#pragma unroll
      for (int i = 0; i < NS; i++) bs[i] = LDS(unsigned, L_BASE + bk[i]);
      ent = ((LDS(unsigned, L_BASE + (tid & 255) * 4) - L_REC) >> 4) | (LDS(unsigned, L_CNT + (tid & 255) * 4) << 9);
      const unsigned lim = L_REC + 8 * SEG + lane * 8;
#pragma unroll
      for (int i = 0; i < NS; i++) LDS(u32x2, FULL ? bs[i] + rank[i] : min(bs[i] + rank[i], lim)) = rec[i];
      barrier_lds();
    };
    if (ne == CH && w != 0) sort(std::integral_constant<int, 6>(), std::true_type());
    else sort(std::integral_constant<int, 7>(), std::false_type());
    LDS(unsigned, L_CNT + tid * 4) = 0;
    STAMP(11);
    tag_lo = (unsigned)(ep + 1) & 0xffu;
    tag_hi = (unsigned)((ep + 1) >> 8);
    {
      const u32x2 g = {ent | (tag_lo << 24), tag_hi};
      __builtin_amdgcn_raw_buffer_store_b64(g, rs_tab, tid < NBK ? bkt * (NBK * 8) + tid * 8 : 0x80000000u, 0, 16);
      const auto rs_out = __builtin_amdgcn_make_buffer_rsrc((void*)(ws + WS_REC + (size_t)bkt * CHS), 0, total, 0x00020000);
#pragma unroll
      for (int i = 0; i < 4; i++) {
        const u32x4 v = LDS(u32x4, L_REC + (i * 512 + tid) * 16);
        const u32x4 o = {v[1], v[3], v[0] | ((v[2] & 0xfffu) << 12) | (tag_lo << 24), tag_hi};
        __builtin_amdgcn_raw_buffer_store_b128(o, rs_out, (i * 512 + tid) * 16, 0, 16);
      }
    }
    STAMP(12);
    asm volatile("s_waitcnt vmcnt(10)" : "+v"(wdv0), "+v"(wdv1), "+v"(xa), "+v"(xb) :: "memory");
    {
      const unsigned tp = wl + (8 * h + ((lane >> 2) & 3)) * 128 + ((lane >> 4) & 1) * 32 + (lane & 3) * 8;
      u32x4 f[2], g[2];
#pragma unroll
      for (int t = 0; t < 2; t++) { f[t] = tr_frag(tp + 64 * t, 512); g[t] = tr_frag(tp + 2048 + 64 * t, 512); }
#pragma unroll
      for (int t = 0; t < 2; t++) {
        if (w < 4) { f[t] = fold(f[t]); g[t] = fold(g[t]); }
        LDS(u32x4, L_WH + (2 * w + t) * 1024 + lane * 16) = f[t];
        LDS(u32x4, wl + 2048 + t * 1024 + lane * 16) = g[t];
      }
      u32x4 x;
      if (w < 2) x = fold(tr_frag(wl + W_STE + (8 * h + ((lane >> 2) & 3)) * 64 + ((lane >> 4) & 1) * 32 + (lane & 3) * 8, 256));
      else { const unsigned m = (r & 27) == 0 ? ~0u : 0u; x = (u32x4){xa[0] & m, xa[1] & m, xb[0] & m, xb[1] & m}; }
      if (w < 6) LDS(u32x4, L_WJ + w * 1024 + lane * 16) = x;
    }
    if (tid >= 256 && tid < 256 + 26 * 4) {
      const int t = tid - 256, row = t >> 2, sx = (t >> 1) & 1, hh = t & 1;
      const int x = row < DUMMY ? row - 16 * sx - 4 * hh : -1;
      u32x4 v;
      v[0] = x == 0 ? 0x3f80u : x == 1 ? 0x3f800000u : 0u;  v[1] = x == 2 ? 0x3f80u : x == 3 ? 0x3f800000u : 0u;
      v[2] = x == 8 ? 0x3f80u : x == 9 ? 0x3f800000u : 0u;  v[3] = x == 10 ? 0x3f80u : x == 11 ? 0x3f800000u : 0u;
      LDS(u32x4, L_LUT + row * 80 + (t & 3) * 16) = v;
    }
    {
      const u32x4 z4 = {0u, 0u, 0u, 0u};
#pragma unroll
      for (int i = 0; i < 3; i++) { const int o = i * 1024 + lane * 16; if (o < 26 * 80 + 256) LDS(u32x4, wl + W_SI + o) = z4; }
    }
    barrier_lds();
    STAMP(14);
  }

  f32x16 Y0, Y1, Yp;
#pragma unroll
  for (int i = 0; i < 16; i++) { Y0[i] = 0.f; Y1[i] = 0.f; Yp[i] = 0.f; }
  u32x32 W2; u32x16 PA;
  int jbase = 0, part = -1, grp = w, gother = w;

  auto round = [&](bool first) __attribute__((always_inline)) -> bool {
    const int lane = lane_id(), tid = w * 64 + lane, r = lane & 31, h = lane >> 5;
    const unsigned zr = opaque(0);
    if (!first) { LDS(unsigned, L_CNT + tid * 4) = zr; barrier_lds(); }
    LDS(unsigned, L_BASE + lane * 4) = 0x40000000u;
    unsigned tb[4];
    {
      u32x2 t;
      for (int spin = 0;; spin++) {
        asm volatile("buffer_load_dwordx2 %0, %1, %2, %3 offen sc1\n\ts_waitcnt vmcnt(0)"
                     : "=&v"(t) : "v"((w * 32 + r) * (NBK * 8)), "s"(rs_tab), "s"(bkt * 8) : "memory");
        if (__builtin_amdgcn_ballot_w64(t[1] != tag_hi || (t[0] >> 24) != tag_lo) == 0) break;
        if (spin > (1 << 20)) { t[0] = 0; break; }
        __builtin_amdgcn_s_sleep(POLL_SLEEP);
      }
      LDS(unsigned, L_WAVE + w * WSZ + W_STE + r * 4) = t[0];
      const u32x4 t0 = LDS(u32x4, L_WAVE + w * WSZ + W_STE + (lane >> 3) * 16);
#pragma unroll
      for (int p = 0; p < 4; p++) tb[p] = t0[p];
    }
    STAMP(8);
    u32x4 gr[6];
    unsigned ad[6], left[6], eb[6];
    unsigned lmax = 0, nov = 0;
    bool big;
    {
      const unsigned jj = lane & (LG - 1), c0 = (unsigned)tid / LG * 4, oob = 0x80000000u + zr;
      const unsigned o0 = mul24(c0, CHS) + (jbase / 2 + jj) * 16, eb0 = mul24(c0, CH);
      unsigned off[4], len[4];
#pragma unroll
      for (int p = 0; p < 4; p++) {
        len[p] = (tb[p] >> 12) & 0xfffu;
        if (part >= 0 && part != 4 * p + (w & 3)) len[p] = 0;
        off[p] = o0 + (tb[p] & 0xfffu) * 16 + p * CHS;
        left[p] = len[p] - (jbase + 2 * jj);
        ad[p] = (int)left[p] > 0 ? off[p] : oob;
        eb[p] = eb0 + p * CH;
        asm volatile("buffer_load_dwordx4 %0, %1, %2, 0 offen sc1" : "=v"(gr[p]) : "v"(ad[p]), "s"(rs_rec) : "memory");
        lmax = max(lmax, tb[p] & 0xfff000u);
      }
      asm volatile("" : "+v"(len[0]), "+v"(len[1]), "+v"(len[2]), "+v"(len[3]));
      ad[4] = oob; ad[5] = oob; left[4] = zr; left[5] = zr; eb[4] = zr; eb[5] = zr;
      unsigned ov[4], s1 = 0;
#pragma unroll
      for (int p = 0; p < 4; p++) {
        ov[p] = (unsigned)min(max(((int)len[p] - (jbase + 2 * LG) + 1) >> 1, 0), LG);
        const bool sel = jj - nov < ov[p];
        ad[4] = sel ? off[p] + (LG - nov) * 16 : ad[4];
        left[4] = sel ? len[p] - (jbase + 2 * LG) - 2 * (jj - nov) : left[4];
        eb[4] = sel ? eb0 + p * CH : eb[4];
        nov += ov[p];
      }
      asm volatile("buffer_load_dwordx4 %0, %1, %2, 0 offen sc1" : "=v"(gr[4]) : "v"(ad[4]), "s"(rs_rec) : "memory");
      gr[5] = (u32x4){zr, zr, zr, zr};
      big = __builtin_amdgcn_ballot_w64(nov > LG) != 0;
      if (big) {
#pragma unroll
        for (int p = 0; p < 4; p++) {
          const bool sel = jj + LG - s1 < ov[p];
          ad[5] = sel ? off[p] + (2 * LG - s1) * 16 : ad[5];
          left[5] = sel ? len[p] - (jbase + 2 * LG) - 2 * (jj + LG - s1) : left[5];
          eb[5] = sel ? eb0 + p * CH : eb[5];
          s1 += ov[p];
        }
        asm volatile("buffer_load_dwordx4 %0, %1, %2, 0 offen sc1" : "+v"(gr[5]) : "v"(ad[5]), "s"(rs_rec) : "memory");
      }
    }
    unsigned sink = zr;
    STAMP(7);
    u32x4 w1f[8];
    if (first) {
#pragma unroll
      for (int i = 0; i < 8; i++) w1f[i] = LDS(u32x4, L_WAVE + (4 + (i >> 1)) * WSZ + 2048 + (i & 1) * 1024 + lane * 16);
    if (first) {
#pragma unroll
      for (int i = 0; i < 8; i++) {
        const u32x4 v = LDS(u32x4, L_WAVE + (i >> 1) * WSZ + 2048 + (i & 1) * 1024 + lane * 16);
#pragma unroll
        for (int j = 0; j < 4; j++) W2[4 * i + j] = v[j];
      }
    }
    }
    STAMP(13);
    if ((lmax >> 12) > (unsigned)jbase + 4 * LG || nov > 2 * LG) __hip_atomic_fetch_or(&LDS(unsigned, L_FLAG), nov > 2 * LG ? 3u : 1u, __ATOMIC_RELAXED, __HIP_MEMORY_SCOPE_WORKGROUP);
    unsigned mk1 = (lane & 7) >= 1 ? ~zr : zr, mk2 = (lane & 7) >= 2 ? ~zr : zr, sg0 = L_REC + (lane >> 3) * SEG;
    unsigned lim = L_REC + 8 * SEG + lane * 8, own = n0 + min(8 * (r & 3) + (r >> 2), 24) * 8;
    unsigned ce = ((lane & 1) ^ ((lane >> 4) & 1)) * 16, cp = (lane & 1) * 2;
    unsigned cc0 = ((lane & 7) ^ (lane >> 4)) * 16, cc1 = ((lane & 7) ^ (4 + (lane >> 4))) * 16;
    unsigned q_s4 = L_REC + (lane >> 3) * 32 + 4;
    unsigned q_r2 = L_REC + (4 * ((lane >> 1) & 7) + (lane >> 4)) * 8;
    unsigned q_r1 = L_REC + (4 * (r & 7) + (r >> 3)) * 8 + 4;
    asm volatile("" : "+v"(mk1), "+v"(mk2), "+v"(sg0), "+v"(lim), "+v"(own), "+v"(ce), "+v"(cp), "+v"(cc0), "+v"(cc1), "+v"(q_s4),
                 "+v"(q_r2), "+v"(q_r1));
    asm volatile("s_waitcnt vmcnt(0)" : "+v"(gr[0]), "+v"(gr[1]), "+v"(gr[2]), "+v"(gr[3]), "+v"(gr[4]), "+v"(gr[5]) :: "memory");
    for (int spin = 0;; spin++) {
      bool bad = false;
#pragma unroll
      for (int p = 0; p < 6; p++) bad |= ((int)left[p] > 0) & ((gr[p][3] != tag_hi) | ((gr[p][2] >> 24) != tag_lo));
      if (__builtin_amdgcn_ballot_w64(bad) == 0 || spin > (1 << 20)) break;
      asm volatile("buffer_load_dwordx4 %0, %6, %12, 0 offen sc1\n\tbuffer_load_dwordx4 %1, %7, %12, 0 offen sc1\n\t"
                   "buffer_load_dwordx4 %2, %8, %12, 0 offen sc1\n\tbuffer_load_dwordx4 %3, %9, %12, 0 offen sc1\n\t"
                   "buffer_load_dwordx4 %4, %10, %12, 0 offen sc1\n\tbuffer_load_dwordx4 %5, %11, %12, 0 offen sc1\n\ts_waitcnt vmcnt(0)"
                   : "=&v"(gr[0]), "=&v"(gr[1]), "=&v"(gr[2]), "=&v"(gr[3]), "=&v"(gr[4]), "=&v"(gr[5])
                   : "v"(ad[0]), "v"(ad[1]), "v"(ad[2]), "v"(ad[3]), "v"(ad[4]), "v"(ad[5]), "s"(rs_rec) : "memory");
    }
    STAMP(1);
    u32x2 rc[12];
#pragma unroll
    for (int p = 0; p < 6; p++) {
      rc[2 * p] = (u32x2){(eb[p] + (gr[p][2] & 0xfffu)) << 5, (int)left[p] > 0 ? gr[p][0] : 0u};
      rc[2 * p + 1] = (u32x2){(eb[p] + ((gr[p][2] >> 12) & 0xfffu)) << 5, (int)left[p] > 1 ? gr[p][1] : 0u};
    }
    unsigned rk[12], ky[12];
#pragma unroll
    for (int p = 0; p < 12; p++) {
      if (p == 10 && !big) { ky[10] = ky[11] = 0; rk[10] = rk[11] = 0; break; }
      ky[p] = max((rc[p][1] >> 9) & 0x7fcu, (unsigned)lane * 4);
      rk[p] = __hip_atomic_fetch_add(&LDS(unsigned, L_CNT + ky[p]), 8u + zr, __ATOMIC_RELAXED, __HIP_MEMORY_SCOPE_WORKGROUP);
    }
    if (EPF) {
#pragma unroll
      for (int p = 0; p < 10; p++) {
        const unsigned a = (ky[p] & 0x47cu) - 0x400u < (unsigned)EPF * 4 ? rc[p][0] : 0x80000000u + zr;
        asm volatile("buffer_load_dword %0, %1, %2, 0 offen" : "+{v3}"(sink) : "v"(a), "s"(rs_ea) : "memory");
      }
    }
    barrier_lds();
    STAMP(2);
    unsigned g;
    const unsigned flagv = LDS(unsigned, L_FLAG);
    {
      const u32x4 cv = LDS(u32x4, L_CNT + 1024 + lane * 16);
      const unsigned sum = cv[0] + cv[1] + cv[2] + cv[3];
      g = sum + (__builtin_amdgcn_update_dpp(0, (int)sum, 0x111, 0xf, 0xf, true) & mk1);
      g += __builtin_amdgcn_update_dpp(0, (int)g, 0x112, 0xf, 0xf, true) & mk2;
      g += __builtin_amdgcn_update_dpp(0, (int)g, 0x114, 0xf, 0xa, false);
      const unsigned ex = g - sum + sg0;
      const u32x4 bv = {ex, ex + cv[0], ex + cv[0] + cv[1], ex + cv[0] + cv[1] + cv[2]};
      LDS(u32x4, L_BASE + 1024 + lane * 16) = bv;
    }
    unsigned bs[10];
#pragma unroll
    for (int p = 0; p < 10; p++) bs[p] = LDS(unsigned, L_BASE + ky[p]);
    const unsigned flag = __builtin_amdgcn_readfirstlane(flagv);
    const bool again = flag & 1;
    const bool over = (flag & 2) || __builtin_amdgcn_ballot_w64(g > (unsigned)(SEG - 256)) != 0;
    if (!over) {
#pragma unroll
      for (int p = 0; p < 10; p++) LDS(u32x2, min(bs[p] + rk[p], lim)) = rc[p];
      if (big) {
        LDS(u32x2, min(LDS(unsigned, L_BASE + ky[10]) + rk[10], lim)) = rc[10];
        LDS(u32x2, min(LDS(unsigned, L_BASE + ky[11]) + rk[11], lim)) = rc[11];
      }
    }
    if (first) {
      const unsigned key = (g << 3) | (lane >> 3);
      unsigned ks[8], rank = 0;
#pragma unroll
      for (int k = 0; k < 8; k++) ks[k] = __builtin_amdgcn_readlane(key, 8 * k + 7);
#pragma unroll
      for (int k = 0; k < 8; k++) rank += ks[k] > key;
      const unsigned long long sel = 0x8080808080808080ull;
      grp = __builtin_ctzll(__builtin_amdgcn_ballot_w64(rank == (unsigned)(w < 4 ? w : 11 - w)) & sel) >> 3;
      gother = __builtin_ctzll(__builtin_amdgcn_ballot_w64(rank == (unsigned)(w < 4 ? 7 - w : w - 4)) & sel) >> 3;
      wl = L_WAVE + grp * WSZ; mi = L_MI + grp * MISZ;
    }
    unsigned segn = over ? 0u : (unsigned)__builtin_amdgcn_readlane(g, 8 * grp + 7);
    const unsigned seg0 = grp * SEG;
    const int nt = (segn + 255) >> 8;
    {
      const u32x2 pad = {zr, DUMMY * (80u << 20)}, row = {zr, min(own + grp, (unsigned)N - 1) | (DUMMY * (80u << 20))};
      LDS(u32x2, L_REC + seg0 + segn + r * 8) = pad;
      LDS(u32x2, L_REC + seg0 + nt * 256 + r * 8) = row;
    }
    barrier_lds();
    STAMP(3);

    const unsigned o_s4 = q_s4 + seg0, o_r2 = q_r2 + seg0, o_r1 = q_r1 + seg0;
    {
      unsigned ns[4];
#pragma unroll
      for (int i = 0; i < 4; i++) ns[i] = LDS(uint16_t, o_s4 + i * 8);
      const u32x2 nr2 = LDS(u32x2, o_r2);
      dma_tile(hmat, ea, pos, ns[0] * 128 + cc0, ns[1] * 128 + cc1, ns[2] * 128 + cc0, ns[3] * 128 + cc1,
               nr2[0] | ce, (nr2[1] & 0xffffu) * 6 + cp, wl);
    }
    if (first) {
      u32x2 hb[8];
#pragma unroll
      for (int i = 0; i < 8; i++) hb[i] = LDS(u32x2, mi + r * 128 + ((i ^ ((r >> 1) & 7)) * 16) + h * 8);
      const u32x2 pg = LDS(u32x2, mi + 4096 + r * 8);
#pragma unroll
      for (int t = 0; t < 2; t++) {
        f32x16 P;
#pragma unroll
        for (int i = 0; i < 16; i++) P[i] = 0.f;
#pragma unroll
        for (int s = 0; s < 4; s++) {
          const u32x4 a = {hb[2 * s][0], hb[2 * s][1], hb[2 * s + 1][0], hb[2 * s + 1][1]};
          P = MFMA(as_bf(a), as_bf(w1f[2 * s + t]), P);
        }
        if (h) P[15] = bf2f(t ? wdv1 : wdv0);
#pragma unroll
        for (int i = 0; i < 16; i++) P[i] *= -LOG2E;
#pragma unroll
        for (int s = 0; s < 2; s++)
#pragma unroll
          for (int j = 0; j < 4; j++) PA[4 * (2 * s + t) + j] = pk_bf16(P[8 * s + 2 * j], P[8 * s + 2 * j + 1]);
      }
      const f32x4 pv = {bf2f(pg[0] & 0xffffu), __uint_as_float(pg[0] & 0xffff0000u), __uint_as_float(pg[1] & 0xffff0000u), 0.f};
      if (lane < DUMMY) LDS(f32x4, wl + W_SI + lane * 80 + 64) = pv;
      if (bkt == 0 && tid == 0) {
        const u32x2 e1 = {tag_lo | (tag_hi << 8), tag_hi >> 24};
        asm volatile("global_store_dwordx2 %0, %1, %2 sc1" :: "v"(zr), "v"(e1), "s"(ws + WS_EPOCH) : "memory");
      }
    }
    const unsigned nother = (__builtin_amdgcn_readlane(g, 8 * gother + 7) + 255) >> 8;
    const bool last_round = !over && !again && (part < 0 || part == 15);
    STAMP(4);

    {
      u32x16 A0; u32x8 A1;
      A0[0] = zr; A0[1] = zr;
#pragma unroll
      for (int s = 0; s < 4; s++) A0[2 + s] = wl + W_STH + r * 128 + (((2 * s + h) ^ ((r >> 1) & 7)) * 16);
      A0[6] = wl + r * 32 + ((h ^ ((r >> 3) & 1)) * 16);
      A0[7] = wl + r * 8;
      A0[8] = L_LUT + h * 16;
      A0[9] = wl + r * 2;
      A0[10] = wl + min(r, DUMMY) * 80 + h * 16;
      A0[11] = mi + r * MIL + h * 32;
      A0[12] = mi + (8 * h + ((lane >> 2) & 3)) * MIL + (4 * ((lane >> 4) & 1) + (lane & 3)) * 8;
      A0[13] = wl + h * 64 + r * 2;
      A0[14] = wl + min(r, 3) * 64 + h * 16;
      A0[15] = L_WJ + lane * 16;
      A1[0] = cc0; A1[1] = cc1; A1[2] = ce; A1[3] = cp;
      A1[4] = o_s4 + 256; A1[5] = o_r2 + 256; A1[6] = o_r1;
      A1[7] = 0x3f800000u + zr;
      unsigned cnt = nt, keep;
#define PRIO_A 14
#define PRIO_B 7
      unsigned keep2;
      const int lastv = __builtin_amdgcn_readfirstlane(opaque(last_round ? 1 : 0));
      const int thr = __builtin_amdgcn_readfirstlane(w >= 4 ? max((int)(nother * PRIO_A - cnt * PRIO_B) >> 5, 0) : -1);
      asm volatile(LOOP_ASM
                   : "+{v[52:67]}"(Y0), "+{v[68:83]}"(Y1), "+{v[84:99]}"(Yp), "+{v[232:247]}"(A0), "+{v[248:255]}"(A1), "+{v3}"(sink),
                     [nt] "+s"(cnt), [keep] "=&s"(keep), [keep2] "=&s"(keep2)
                   : "{v[4:35]}"(W2), "{v[36:51]}"(PA),
                     [h0] "s"(hmat), [h1] "s"(hmat - 1024), [h2] "s"(hmat - 2048), [h3] "s"(hmat - 3072), [ea] "s"(ea), [pos] "s"(pos - 1024),
                     [b1] "s"(wl), [b2] "s"(wl + W_STE), [posd] "s"(wl + W_SI + 64), [c128] "s"(128u), [thr] "s"(thr), [last] "s"(lastv),
                     [hsel] "s"(0xffffffff00000000ull), [rsea] "s"(rs_ea)
                   : "memory", "scc", "vcc", "v1", CLOBBER_V);
    }
    if (over) part = 0;
    else if (part >= 0 && part < 15) part++;
    else { part = -1; jbase += 4 * LG; }
    return last_round;
  };
  if (!round(true)) {
    do {
      wait_vm0();
      barrier_lds();
      jbase = __builtin_amdgcn_readfirstlane(jbase); part = __builtin_amdgcn_readfirstlane(part);
    } while (!round(false));
  }
  STAMP(5);

  {
    const int lane = lane_id(), r = lane & 31, h = lane >> 5;
    Yp[0] *= -LN2; Yp[1] *= -LN2; Yp[2] *= -LN2;
    const auto rs_o = __builtin_amdgcn_make_buffer_rsrc((void*)out, 0, N * 128, 0x00020000);
#pragma unroll
    for (int i = 0; i < 4; i++) {
      const int q = 8 * i + (lane >> 3), dl = q * 8 + grp;
      const u32x4 v = LDS(u32x4, mi + q * MIP + (lane & 7) * 16);
      if (dl < NB) __builtin_amdgcn_raw_buffer_store_b128(v, rs_o, (n0 + dl) * 128 + (lane & 7) * 16, 0, OUT_AUX);
    }
    const int dl = r * 8 + grp;
    if (h == 0 && dl < NB && n0 + dl < N) {
      const f32x4 pd = LDS(f32x4, wl + W_SI + r * 80 + 64);
      uint16_t* po = out + (size_t)N * 64 + (size_t)(n0 + dl) * 3;
      const unsigned xy = pk_bf16(pd[0] + Yp[0], pd[1] + Yp[1]), zz = pk_bf16(pd[2] + Yp[2], 0.f);
      po[0] = (uint16_t)xy; po[1] = (uint16_t)(xy >> 16); po[2] = (uint16_t)zz;
    }
  }
  STAMP(6);
}

}

#define STAMP_PASS1
#define STAMP_PASS2

extern "C" void kernel_launch(void* const* d_in, const int* in_sizes, int n_in, void* d_out, int out_size,
                              void* d_ws, size_t ws_size, hipStream_t stream) {
  const int N = in_sizes[0] / 64, E = in_sizes[3];
  if (N > NBK * NB || E > NBK * CH || N < 1 || E < 1) return;
  (void)hipFuncSetAttribute((const void*)egnn_kernel, hipFuncAttributeMaxDynamicSharedMemorySize, L_TOTAL);
  egnn_kernel<<<NBK, 512, L_TOTAL, stream>>>((const char*)d_in[0], (const char*)d_in[1], (const char*)d_in[2], (const int*)d_in[3],
                                             (const int*)d_in[4], (const char*)d_in[5], (const char*)d_in[6], (const char*)d_in[7],
                                             (char*)d_ws, (uint16_t*)d_out, N, E STAMP_PASS2);
}
```

```cpp
#include <hip/hip_runtime.h>
#include <stdint.h>
#include <type_traits>

#define FOLD_LOG2E 1

namespace {

typedef __bf16 bf16x8 __attribute__((ext_vector_type(8)));
typedef __bf16 bf16x2 __attribute__((ext_vector_type(2)));
typedef float f32x16 __attribute__((ext_vector_type(16)));
typedef float f32x4 __attribute__((ext_vector_type(4)));
typedef unsigned u32x4 __attribute__((ext_vector_type(4)));
typedef unsigned u32x2 __attribute__((ext_vector_type(2)));
typedef unsigned u32x8 __attribute__((ext_vector_type(8)));
typedef unsigned u32x16 __attribute__((ext_vector_type(16)));
typedef unsigned u32x32 __attribute__((ext_vector_type(32)));
typedef short s16x4 __attribute__((ext_vector_type(4)));
typedef __attribute__((address_space(3))) s16x4* lds_s16x4_p;

#define DI __device__ __forceinline__
#define MFMA(a, b, c) __builtin_amdgcn_mfma_f32_32x32x16_bf16((a), (b), (c), 0, 0, 0)

#define POLL_SLEEP 2
#define EPF 2
#define OUT_AUX 16
constexpr int NBK = 256;
constexpr int LG = 8;
constexpr int NB = 196;
constexpr int CH = 3136;
constexpr float LOG2E = 1.4426950408889634f;
constexpr float LN2 = 0.6931471805599453f;

constexpr size_t WS_EPOCH = 0;
constexpr size_t WS_TAB = 256;
constexpr size_t WS_REC = WS_TAB + (size_t)NBK * NBK * 8;
constexpr int CHS = (CH + NBK) * 8;

constexpr int W_STH = 0, W_STE = 4096, W_STP = 5120, W_SI = 5376, W_TI = W_SI + 26 * 80, WSZ = W_TI + 256;
constexpr int DUMMY = 25;
constexpr int MIP = 144, MISZ = 32 * MIP;
constexpr int MIL = 80;
constexpr int SEG = 17 * 256;
constexpr int L_CNT = 0;
constexpr int L_BASE = L_CNT + 2048;
constexpr int L_FLAG = L_CNT + 512;
constexpr int L_REC = L_BASE + 2048;
constexpr int L_LUT = L_REC + 8 * SEG + 512;
constexpr int L_WAVE = L_LUT + 26 * 80;
constexpr int L_MI = L_WAVE + 8 * WSZ;
constexpr int L_WJ = L_MI + 8 * MISZ;
constexpr int L_WH = L_WJ + 6 * 1024;
constexpr int L_TOTAL = L_WH + 16 * 1024;
static_assert(L_TOTAL <= 163840, "LDS");
static_assert(WSZ % 16 == 0 && L_MI % 16 == 0 && L_LUT % 16 == 0, "align");

#pragma clang diagnostic ignored "-Wint-to-pointer-cast"
#define LDS(T, off) (*(__attribute__((address_space(3))) T*)(unsigned)(off))
DI unsigned mul24(unsigned a, unsigned b) { return (unsigned)__umul24(a, b); }
DI float bf2f(unsigned lo16) { return __uint_as_float(lo16 << 16); }
DI unsigned pk_bf16(float a, float b) { bf16x2 v = {(__bf16)a, (__bf16)b}; return __builtin_bit_cast(unsigned, v); }
DI bf16x8 as_bf(u32x4 v) { return __builtin_bit_cast(bf16x8, v); }
DI int opaque(int x) { asm volatile("" : "+v"(x)); return x; }
DI int lane_id() {
  int x; asm volatile("v_mbcnt_lo_u32_b32 %0, -1, 0\n\tv_mbcnt_hi_u32_b32 %0, -1, %0" : "=v"(x)); return x;
}
DI void wait_vm0() { asm volatile("s_waitcnt vmcnt(0)" ::: "memory"); }
DI void wait_lgkm0() { asm volatile("s_waitcnt lgkmcnt(0)" ::: "memory"); }
DI void barrier_lds() { asm volatile("s_waitcnt lgkmcnt(0)\n\ts_barrier" ::: "memory"); }
DI void dma16(const void* g, unsigned off, unsigned l) {
  unsigned keep;
  asm volatile("s_mov_b32 %0, m0\n\ts_mov_b32 m0, %3\n\ts_nop 0\n\tglobal_load_lds_dwordx4 %1, %2\n\ts_mov_b32 m0, %0"
               : "=&s"(keep) : "v"(off), "s"(g), "s"(l) : "memory");
}
DI void dma4(const void* g, unsigned off, unsigned l) {
  unsigned keep;
  asm volatile("s_mov_b32 %0, m0\n\ts_mov_b32 m0, %3\n\ts_nop 0\n\tglobal_load_lds_dword %1, %2\n\ts_mov_b32 m0, %0"
               : "=&s"(keep) : "v"(off), "s"(g), "s"(l) : "memory");
}
DI void dma_tile(const void* hm, const void* ea, const void* ps, unsigned o0, unsigned o1, unsigned o2, unsigned o3,
                 unsigned oe, unsigned op, unsigned l) {
  unsigned keep;
  asm volatile("s_mov_b32 %0, m0\n\t"
               "s_add_u32 m0, %10, 0x1000\n\ts_nop 0\n\tglobal_load_lds_dwordx4 %5, %8\n\t"
               "s_mov_b32 m0, %10\n\ts_nop 0\n\tglobal_load_lds_dwordx4 %1, %7\n\t"
               "s_add_u32 m0, %10, 0x400\n\ts_nop 0\n\tglobal_load_lds_dwordx4 %2, %7\n\t"
               "s_add_u32 m0, %10, 0x800\n\ts_nop 0\n\tglobal_load_lds_dwordx4 %3, %7\n\t"
               "s_add_u32 m0, %10, 0xc00\n\ts_nop 0\n\tglobal_load_lds_dwordx4 %4, %7\n\t"
               "s_add_u32 m0, %10, 0x1400\n\ts_nop 0\n\tglobal_load_lds_dword %6, %9\n\t"
               "s_mov_b32 m0, %0"
               : "=&s"(keep) : "v"(o0), "v"(o1), "v"(o2), "v"(o3), "v"(oe), "v"(op), "s"(hm), "s"(ea), "s"(ps), "s"(l)
               : "memory", "scc");
}
template <int CTRL, int ROWMASK> DI unsigned dpp_add(unsigned x) {
  return x + (unsigned)__builtin_amdgcn_update_dpp(0, (int)x, CTRL, ROWMASK, 0xf, false);
}
DI unsigned wave_scan_incl(unsigned x) {
  x = dpp_add<0x111, 0xf>(x); x = dpp_add<0x112, 0xf>(x); x = dpp_add<0x114, 0xf>(x); x = dpp_add<0x118, 0xf>(x);
  x = dpp_add<0x142, 0xa>(x); x = dpp_add<0x143, 0xc>(x);
  return x;
}
#define STAMP(i)
#define STAMP_ARG

#define CLOBBER_V "v100", "v101", "v102", "v103", "v104", "v105", "v106", "v107", "v108", "v109", "v110", "v111", "v112", "v113", "v114", "v115", "v116", "v117", "v118", "v119", "v120", "v121", "v122", "v123", "v124", "v125", "v126", "v127", "v128", "v129", "v130", "v131", "v132", "v133", "v134", "v135", "v148", "v149", "v150", "v151", "v152", "v153", "v154", "v155", "v156", "v157", "v158", "v159", "v160", "v161", "v162", "v163", "v164", "v165", "v166", "v167", "v168", "v169", "v170", "v171", "v172", "v173", "v174", "v175", "v176", "v177", "v178", "v179", "v180", "v181", "v182", "v183", "v184", "v185", "v186", "v187", "v188", "v189", "v190", "v191", "v192", "v193", "v194", "v195", "v196", "v197", "v198", "v199", "v200", "v201", "v202", "v203", "v204", "v205", "v206", "v207", "v208", "v209", "v210", "v211", "v212", "v213", "v214", "v215", "v216", "v217", "v218", "v219", "v220", "v221", "v222", "v223", "v224", "v225", "v226", "v227", "v228", "v229", "v230", "v231"
#define LOOP_ASM_FOLD \
  "s_mov_b32 %[keep], m0\n\t" \
  "s_mov_b64 vcc, %[hsel]\n\t" \
  "ds_read2_b32 v[220:221], v252 offset0:0 offset1:2\n\t" \
  "ds_read2_b32 v[222:223], v252 offset0:4 offset1:6\n\t" \
  "ds_read_b64 v[226:227], v253\n\t" \
  "ds_read_b32 v232, v254\n\t" \
  "v_mov_b32 v176, 0\n\t" \
  "v_mov_b32 v177, 0\n\t" \
  "v_mov_b32 v178, 0\n\t" \
  "v_mov_b32 v179, 0\n\t" \
  "v_mov_b32 v180, 0\n\t" \
  "v_mov_b32 v181, 0\n\t" \
  "v_mov_b32 v182, 0\n\t" \
  "v_mov_b32 v183, 0\n\t" \
  "v_mov_b32 v184, 0\n\t" \
  "v_mov_b32 v185, 0\n\t" \
  "v_mov_b32 v186, 0\n\t" \
  "v_mov_b32 v187, 0\n\t" \
  "v_mov_b32 v188, 0\n\t" \
  "v_mov_b32 v189, 0\n\t" \
  "v_mov_b32 v190, 0\n\t" \
  "v_mov_b32 v191, 0\n\t" \
  "v_mov_b32 v230, 0\n\t" \
  "v_mov_b32 v231, 0\n\t" \
  "v_mov_b32 v208, 0\n\t" \
  "v_mov_b32 v209, 0\n\t" \
  "v_mov_b32 v210, 0\n\t" \
  "v_mov_b32 v211, 0\n\t" \
  "ds_write_b128 v243, v[184:187] offset:0\n\t" \
  "s_nop 0\n\t" \
  "v_mfma_f32_16x16x32_bf16 v[132:135], v[184:187], v[188:191], 0\n\t" \
  "v_add_u32 v252, 0x100, v252\n\t" \
  "v_add_u32 v253, 0x100, v253\n\t" \
  "v_add_u32 v254, 0x100, v254\n\t" \
  "s_waitcnt lgkmcnt(0)\n\t" \
  "v_lshrrev_b32 v232, 20, v232\n\t" \
  "v_add_u32 v233, %[posd], v232\n\t" \
  "ds_read_b128 v[216:219], v233\n\t" \
  "v_add_u32 v233, v240, v232\n\t" \
  "ds_read_b128 v[172:175], v233 offset:32\n\t" \
  "ds_read_b128 v[168:171], v233\n\t" \
  "v_add_u32 v233, v241, v232\n\t" \
  "ds_write_b16_d16_hi v233, v255 offset:5376\n\t" \
  "v_mad_u32_u16 v220, v220, %[c128], v248\n\t" \
  "v_mad_u32_u16 v221, v221, %[c128], v249\n\t" \
  "v_mad_u32_u16 v222, v222, %[c128], v248\n\t" \
  "v_mad_u32_u16 v223, v223, %[c128], v249\n\t" \
  "v_or_b32 v226, v226, v250\n\t" \
  "v_mad_u32_u16 v227, v227, 6, v251\n\t" \
  "s_waitcnt vmcnt(0)\n\t" \
  "ds_read_b128 v[148:151], v234\n\t" \
  "ds_read_b128 v[152:155], v235\n\t" \
  "ds_read_b128 v[156:159], v236\n\t" \
  "ds_read_b128 v[160:163], v237\n\t" \
  "ds_read_b128 v[164:167], v238 offset:4096\n\t" \
  "ds_read_b64 v[224:225], v239 offset:5120\n\t" \
  "s_waitcnt lgkmcnt(0)\n\t" \
  "s_cmp_lt_i32 %[nt], 1\n\t" \
  "s_cbranch_scc1 7f\n\t" \
  "s_mov_b32 m0, %[b2]\n\t" \
  "s_nop 0\n\t" \
  "global_load_lds_dwordx4 v226, %[ea]\n\t" \
  "global_load_lds_dword v227, %[pos] offset:1024\n\t" \
  "s_mov_b32 m0, %[b1]\n\t" \
  "s_nop 0\n\t" \
  "global_load_lds_dwordx4 v220, %[h0]\n\t" \
  "global_load_lds_dwordx4 v221, %[h1] offset:1024\n\t" \
  "global_load_lds_dwordx4 v222, %[h2] offset:2048\n\t" \
  "global_load_lds_dwordx4 v223, %[h3] offset:3072\n\t" \
  "7:\n\t" \
  "ds_read2_b32 v[220:221], v252 offset0:0 offset1:2\n\t" \
  "ds_read2_b32 v[222:223], v252 offset0:4 offset1:6\n\t" \
  "ds_read_b64 v[226:227], v253\n\t" \
  "ds_read_b32 v232, v254\n\t" \
  "v_add_u32 v252, 0x100, v252\n\t" \
  "v_add_u32 v253, 0x100, v253\n\t" \
  "v_add_u32 v254, 0x100, v254\n\t" \
  "v_and_b32 v225, 0xffff0000, v225\n\t" \
  "v_lshlrev_b32 v219, 16, v224\n\t" \
  "v_and_b32 v224, 0xffff0000, v224\n\t" \
  "v_sub_f32 v229, v218, v225\n\t" \
  "v_sub_f32 v216, v216, v219\n\t" \
  "v_sub_f32 v217, v217, v224\n\t" \
  "v_mul_f32 v218, v229, v229\n\t" \
  "v_cndmask_b32 v228, v216, v217, vcc\n\t" \
  "v_fmac_f32 v218, v216, v216\n\t" \
  "v_fmac_f32 v218, v217, v217\n\t" \
  "v_cvt_pk_bf16_f32 v218, v218, v218\n\t" \
  "v_cndmask_b32_sdwa v175, v175, v218, vcc dst_sel:WORD_1 dst_unused:UNUSED_PRESERVE src0_sel:WORD_1 src1_sel:WORD_1\n\t" \
  "v_mfma_f32_32x32x16_bf16 v[100:115], v[4:7], v[148:151], 0\n\t" \
  "v_mfma_f32_32x32x16_bf16 v[100:115], v[12:15], v[152:155], v[100:115]\n\t" \
  "v_mfma_f32_32x32x16_bf16 v[100:115], v[20:23], v[156:159], v[100:115]\n\t" \
  "v_mfma_f32_32x32x16_bf16 v[100:115], v[28:31], v[160:163], v[100:115]\n\t" \
  "v_mfma_f32_32x32x16_bf16 v[100:115], v[36:39], v[168:171], v[100:115]\n\t" \
  "v_mfma_f32_32x32x16_bf16 v[100:115], v[88:91], v[164:167], v[100:115]\n\t" \
  "v_mfma_f32_32x32x16_bf16 v[100:115], v[44:47], v[172:175], v[100:115]\n\t" \
  "s_cmp_eq_u32 %[nt], 0\n\t" \
  "s_nop 10\n\t" \
  "v_exp_f32 v192, v100\n\t" \
  "v_exp_f32 v193, v101\n\t" \
  "v_exp_f32 v194, v102\n\t" \
  "v_exp_f32 v195, v103\n\t" \
  "s_nop 0\n\t" \
  "v_add_f32 v192, 1.0, v192\n\t" \
  "v_add_f32 v193, 1.0, v193\n\t" \
  "v_add_f32 v194, 1.0, v194\n\t" \
  "v_add_f32 v195, 1.0, v195\n\t" \
  "s_cbranch_scc1 6f\n\t" \
  "s_cmp_lt_i32 %[thr], 0\n\t" \
  "s_cbranch_scc1 1f\n\t" \
  "s_setprio 1\n\t" \
  "1:\n\t" \
  "ds_write_b128 v243, v[188:191] offset:16\n\t" \
  "ds_read_b64_tr_b16 v[200:201], v244 offset:0\n\t" \
  "ds_read_b64_tr_b16 v[202:203], v244 offset:320\n\t" \
  "ds_read_b64_tr_b16 v[204:205], v244 offset:1280\n\t" \
  "ds_read_b64_tr_b16 v[206:207], v244 offset:1600\n\t" \
  "v_mfma_f32_16x16x32_bf16 v[132:135], v[144:147], v[188:191], v[132:135]\n\t" \
  "v_rcp_f32 v192, v192\n\t" \
  "v_rcp_f32 v193, v193\n\t" \
  "v_rcp_f32 v194, v194\n\t" \
  "v_rcp_f32 v195, v195\n\t" \
  "v_mfma_f32_32x32x16_bf16 v[116:131], v[8:11], v[148:151], 0\n\t" \
  "v_exp_f32 v196, v104\n\t" \
  "v_exp_f32 v197, v105\n\t" \
  "v_exp_f32 v198, v106\n\t" \
  "v_exp_f32 v199, v107\n\t" \
  "v_mfma_f32_32x32x16_bf16 v[116:131], v[16:19], v[152:155], v[116:131]\n\t" \
  "v_mul_f32 v192, v100, v192\n\t" \
  "v_mul_f32 v193, v101, v193\n\t" \
  "v_mul_f32 v194, v102, v194\n\t" \
  "v_mul_f32 v195, v103, v195\n\t" \
  "v_add_f32 v196, 1.0, v196\n\t" \
  "v_add_f32 v197, 1.0, v197\n\t" \
  "v_add_f32 v198, 1.0, v198\n\t" \
  "v_add_f32 v199, 1.0, v199\n\t" \
  "v_cvt_pk_bf16_f32 v184, v192, v193\n\t" \
  "v_cvt_pk_bf16_f32 v185, v194, v195\n\t" \
  "v_mfma_f32_32x32x16_bf16 v[116:131], v[24:27], v[156:159], v[116:131]\n\t" \
  "v_rcp_f32 v196, v196\n\t" \
  "v_rcp_f32 v197, v197\n\t" \
  "v_rcp_f32 v198, v198\n\t" \
  "v_rcp_f32 v199, v199\n\t" \
  "s_waitcnt lgkmcnt(2)\n\t" \
  "v_mfma_f32_32x32x16_bf16 v[68:83], v[200:203], v[176:179], v[68:83]\n\t" \
  "v_exp_f32 v192, v108\n\t" \
  "v_exp_f32 v193, v109\n\t" \
  "v_exp_f32 v194, v110\n\t" \
  "v_exp_f32 v195, v111\n\t" \
  "v_mfma_f32_32x32x16_bf16 v[116:131], v[32:35], v[160:163], v[116:131]\n\t" \
  "v_mul_f32 v196, v104, v196\n\t" \
  "v_mul_f32 v197, v105, v197\n\t" \
  "v_mul_f32 v198, v106, v198\n\t" \
  "v_mul_f32 v199, v107, v199\n\t" \
  "v_mul_f32 v230, v230, v132\n\t" \
  "v_mul_f32 v231, v231, v132\n\t" \
  "v_cvt_pk_bf16_f32 v230, v230, v231\n\t" \
  "ds_write_b16 v245, v230 offset:7456\n\t" \
  "ds_write_b16_d16_hi v241, v230 offset:7584\n\t" \
  "ds_read_b128 v[200:203], v246 offset:7456\n\t" \
  "ds_read_b128 v[208:211], v246 offset:7488\n\t" \
  "v_mov_b32 v230, v228\n\t" \
  "v_mov_b32 v231, v229\n\t" \
  "v_add_f32 v192, 1.0, v192\n\t" \
  "v_add_f32 v193, 1.0, v193\n\t" \
  "v_add_f32 v194, 1.0, v194\n\t" \
  "v_add_f32 v195, 1.0, v195\n\t" \
  "v_cvt_pk_bf16_f32 v186, v196, v197\n\t" \
  "v_cvt_pk_bf16_f32 v187, v198, v199\n\t" \
  "v_mfma_f32_32x32x16_bf16 v[116:131], v[40:43], v[168:171], v[116:131]\n\t" \
  "v_rcp_f32 v192, v192\n\t" \
  "v_rcp_f32 v193, v193\n\t" \
  "v_rcp_f32 v194, v194\n\t" \
  "v_rcp_f32 v195, v195\n\t" \
  "s_sub_u32 %[nt], %[nt], 1\n\t" \
  "s_cmp_lg_u32 %[nt], %[thr]\n\t" \
  "s_cbranch_scc1 3f\n\t" \
  "s_setprio 0\n\t" \
  "3:\n\t" \
  "v_mfma_f32_32x32x16_bf16 v[116:131], v[92:95], v[164:167], v[116:131]\n\t" \
  "v_exp_f32 v196, v112\n\t" \
  "v_exp_f32 v197, v113\n\t" \
  "v_exp_f32 v198, v114\n\t" \
  "v_exp_f32 v199, v115\n\t" \
  "v_mfma_f32_32x32x16_bf16 v[116:131], v[48:51], v[172:175], v[116:131]\n\t" \
  "v_mul_f32 v192, v108, v192\n\t" \
  "v_mul_f32 v193, v109, v193\n\t" \
  "v_mul_f32 v194, v110, v194\n\t" \
  "v_mul_f32 v195, v111, v195\n\t" \
  "s_waitcnt vmcnt(0)\n\t" \
  "ds_read_b128 v[148:151], v234\n\t" \
  "ds_read_b128 v[152:155], v235\n\t" \
  "ds_read_b128 v[156:159], v236\n\t" \
  "ds_read_b128 v[160:163], v237\n\t" \
  "ds_read_b128 v[164:167], v238 offset:4096\n\t" \
  "ds_read_b64 v[224:225], v239 offset:5120\n\t" \
  "ds_write_b128 v243, v[184:187] offset:0\n\t" \
  "v_add_f32 v196, 1.0, v196\n\t" \
  "v_add_f32 v197, 1.0, v197\n\t" \
  "v_add_f32 v198, 1.0, v198\n\t" \
  "v_add_f32 v199, 1.0, v199\n\t" \
  "v_cvt_pk_bf16_f32 v188, v192, v193\n\t" \
  "v_cvt_pk_bf16_f32 v189, v194, v195\n\t" \
  "v_mad_u32_u16 v220, v220, %[c128], v248\n\t" \
  "v_mad_u32_u16 v221, v221, %[c128], v249\n\t" \
  "v_mad_u32_u16 v222, v222, %[c128], v248\n\t" \
  "s_waitcnt lgkmcnt(11)\n\t" \
  "v_mfma_f32_32x32x16_bf16 v[68:83], v[204:207], v[180:183], v[68:83]\n\t" \
  "v_rcp_f32 v196, v196\n\t" \
  "v_rcp_f32 v197, v197\n\t" \
  "v_rcp_f32 v198, v198\n\t" \
  "v_rcp_f32 v199, v199\n\t" \
  "v_mad_u32_u16 v223, v223, %[c128], v249\n\t" \
  "v_or_b32 v226, v226, v250\n\t" \
  "v_mad_u32_u16 v227, v227, 6, v251\n\t" \
  "s_waitcnt lgkmcnt(8)\n\t" \
  "v_mfma_f32_16x16x32_bf16 v[84:87], v[200:203], v[176:179], v[84:87]\n\t" \
  "v_exp_f32 v192, v116\n\t" \
  "v_exp_f32 v193, v117\n\t" \
  "v_exp_f32 v194, v118\n\t" \
  "v_exp_f32 v195, v119\n\t" \
  "s_waitcnt lgkmcnt(7)\n\t" \
  "v_mfma_f32_16x16x32_bf16 v[84:87], v[208:211], v[180:183], v[84:87]\n\t" \
  "v_mul_f32 v196, v112, v196\n\t" \
  "v_mul_f32 v197, v113, v197\n\t" \
  "v_mul_f32 v198, v114, v198\n\t" \
  "v_mul_f32 v199, v115, v199\n\t" \
  "s_waitcnt lgkmcnt(1)\n\t" \
  "s_cmp_lt_i32 %[nt], 1\n\t" \
  "s_cbranch_scc1 4f\n\t" \
  "s_mov_b32 m0, %[b2]\n\t" \
  "s_nop 0\n\t" \
  "global_load_lds_dwordx4 v226, %[ea]\n\t" \
  "global_load_lds_dword v227, %[pos] offset:1024\n\t" \
  "s_mov_b32 m0, %[b1]\n\t" \
  "s_nop 0\n\t" \
  "global_load_lds_dwordx4 v220, %[h0]\n\t" \
  "global_load_lds_dwordx4 v221, %[h1] offset:1024\n\t" \
  "global_load_lds_dwordx4 v222, %[h2] offset:2048\n\t" \
  "global_load_lds_dwordx4 v223, %[h3] offset:3072\n\t" \
  "4:\n\t" \
  "v_add_f32 v192, 1.0, v192\n\t" \
  "v_add_f32 v193, 1.0, v193\n\t" \
  "v_add_f32 v194, 1.0, v194\n\t" \
  "v_add_f32 v195, 1.0, v195\n\t" \
  "v_cvt_pk_bf16_f32 v190, v196, v197\n\t" \
  "v_cvt_pk_bf16_f32 v191, v198, v199\n\t" \
  "v_lshrrev_b32 v232, 20, v232\n\t" \
  "v_add_u32 v220, %[posd], v232\n\t" \
  "v_add_u32 v221, v240, v232\n\t" \
  "ds_write_b128 v243, v[188:191] offset:16\n\t" \
  "ds_read_b128 v[176:179], v242 offset:5376\n\t" \
  "ds_read_b128 v[180:183], v242 offset:5408\n\t" \
  "ds_write_b16 v233, v255 offset:5376\n\t" \
  "ds_read_b128 v[216:219], v220\n\t" \
  "ds_read_b128 v[172:175], v221 offset:32\n\t" \
  "ds_read_b128 v[168:171], v221\n\t" \
  "ds_read_b64_tr_b16 v[200:201], v244 offset:0\n\t" \
  "ds_read_b64_tr_b16 v[202:203], v244 offset:320\n\t" \
  "ds_read_b64_tr_b16 v[204:205], v244 offset:1280\n\t" \
  "ds_read_b64_tr_b16 v[206:207], v244 offset:1600\n\t" \
  "v_add_u32 v233, v241, v232\n\t" \
  "ds_write_b16_d16_hi v233, v255 offset:5376\n\t" \
  "v_mfma_f32_16x16x32_bf16 v[132:135], v[96:99], v[184:187], 0\n\t" \
  "v_rcp_f32 v192, v192\n\t" \
  "v_rcp_f32 v193, v193\n\t" \
  "v_rcp_f32 v194, v194\n\t" \
  "v_rcp_f32 v195, v195\n\t" \
  "v_mfma_f32_32x32x16_bf16 v[100:115], v[4:7], v[148:151], 0\n\t" \
  "v_exp_f32 v196, v120\n\t" \
  "v_exp_f32 v197, v121\n\t" \
  "v_exp_f32 v198, v122\n\t" \
  "v_exp_f32 v199, v123\n\t" \
  "v_mfma_f32_32x32x16_bf16 v[100:115], v[12:15], v[152:155], v[100:115]\n\t" \
  "v_mul_f32 v192, v116, v192\n\t" \
  "v_mul_f32 v193, v117, v193\n\t" \
  "v_mul_f32 v194, v118, v194\n\t" \
  "v_mul_f32 v195, v119, v195\n\t" \
  "v_add_f32 v196, 1.0, v196\n\t" \
  "v_add_f32 v197, 1.0, v197\n\t" \
  "v_add_f32 v198, 1.0, v198\n\t" \
  "v_add_f32 v199, 1.0, v199\n\t" \
  "v_cvt_pk_bf16_f32 v184, v192, v193\n\t" \
  "v_cvt_pk_bf16_f32 v185, v194, v195\n\t" \
  "v_and_b32 v225, 0xffff0000, v225\n\t" \
  "s_waitcnt lgkmcnt(7)\n\t" \
  "v_lshlrev_b32 v219, 16, v224\n\t" \
  "v_and_b32 v224, 0xffff0000, v224\n\t" \
  "v_sub_f32 v229, v218, v225\n\t" \
  "v_sub_f32 v216, v216, v219\n\t" \
  "v_sub_f32 v217, v217, v224\n\t" \
  "v_mfma_f32_32x32x16_bf16 v[100:115], v[20:23], v[156:159], v[100:115]\n\t" \
  "v_rcp_f32 v196, v196\n\t" \
  "v_rcp_f32 v197, v197\n\t" \
  "v_rcp_f32 v198, v198\n\t" \
  "v_rcp_f32 v199, v199\n\t" \
  "v_mul_f32 v218, v229, v229\n\t" \
  "v_cndmask_b32 v228, v216, v217, vcc\n\t" \
  "v_fmac_f32 v218, v216, v216\n\t" \
  "v_fmac_f32 v218, v217, v217\n\t" \
  "v_cvt_pk_bf16_f32 v218, v218, v218\n\t" \
  "s_waitcnt lgkmcnt(6)\n\t" \
  "v_cndmask_b32_sdwa v175, v175, v218, vcc dst_sel:WORD_1 dst_unused:UNUSED_PRESERVE src0_sel:WORD_1 src1_sel:WORD_1\n\t" \
  "v_mfma_f32_16x16x32_bf16 v[132:135], v[136:139], v[188:191], v[132:135]\n\t" \
  "v_exp_f32 v192, v124\n\t" \
  "v_exp_f32 v193, v125\n\t" \
  "v_exp_f32 v194, v126\n\t" \
  "v_exp_f32 v195, v127\n\t" \
  "ds_read2_b32 v[220:221], v252 offset0:0 offset1:2\n\t" \
  "ds_read2_b32 v[222:223], v252 offset0:4 offset1:6\n\t" \
  "ds_read_b64 v[226:227], v253\n\t" \
  "ds_read_b32 v232, v254\n\t" \
  "v_mfma_f32_32x32x16_bf16 v[100:115], v[28:31], v[160:163], v[100:115]\n\t" \
  "v_mul_f32 v196, v120, v196\n\t" \
  "v_mul_f32 v197, v121, v197\n\t" \
  "v_mul_f32 v198, v122, v198\n\t" \
  "v_mul_f32 v199, v123, v199\n\t" \
  "v_add_u32 v252, 0x100, v252\n\t" \
  "v_add_u32 v253, 0x100, v253\n\t" \
  "v_add_u32 v254, 0x100, v254\n\t" \
  "v_add_f32 v192, 1.0, v192\n\t" \
  "v_add_f32 v193, 1.0, v193\n\t" \
  "v_add_f32 v194, 1.0, v194\n\t" \
  "v_add_f32 v195, 1.0, v195\n\t" \
  "v_cvt_pk_bf16_f32 v186, v196, v197\n\t" \
  "v_cvt_pk_bf16_f32 v187, v198, v199\n\t" \
  "s_waitcnt lgkmcnt(9)\n\t" \
  "v_mfma_f32_32x32x16_bf16 v[100:115], v[36:39], v[168:171], v[100:115]\n\t" \
  "v_rcp_f32 v192, v192\n\t" \
  "v_rcp_f32 v193, v193\n\t" \
  "v_rcp_f32 v194, v194\n\t" \
  "v_rcp_f32 v195, v195\n\t" \
  "v_mfma_f32_32x32x16_bf16 v[100:115], v[44:47], v[172:175], v[100:115]\n\t" \
  "v_exp_f32 v196, v128\n\t" \
  "v_exp_f32 v197, v129\n\t" \
  "v_exp_f32 v198, v130\n\t" \
  "v_exp_f32 v199, v131\n\t" \
  "v_mfma_f32_32x32x16_bf16 v[100:115], v[88:91], v[164:167], v[100:115]\n\t" \
  "v_mul_f32 v192, v124, v192\n\t" \
  "v_mul_f32 v193, v125, v193\n\t" \
  "v_mul_f32 v194, v126, v194\n\t" \
  "v_mul_f32 v195, v127, v195\n\t" \
  "ds_write_b128 v243, v[184:187] offset:0\n\t" \
  "v_add_f32 v196, 1.0, v196\n\t" \
  "v_add_f32 v197, 1.0, v197\n\t" \
  "v_add_f32 v198, 1.0, v198\n\t" \
  "v_add_f32 v199, 1.0, v199\n\t" \
  "v_cvt_pk_bf16_f32 v188, v192, v193\n\t" \
  "v_cvt_pk_bf16_f32 v189, v194, v195\n\t" \
  "s_waitcnt lgkmcnt(8)\n\t" \
  "v_mfma_f32_32x32x16_bf16 v[52:67], v[200:203], v[176:179], v[52:67]\n\t" \
  "v_rcp_f32 v196, v196\n\t" \
  "v_rcp_f32 v197, v197\n\t" \
  "v_rcp_f32 v198, v198\n\t" \
  "v_rcp_f32 v199, v199\n\t" \
  "s_waitcnt lgkmcnt(6)\n\t" \
  "v_mfma_f32_32x32x16_bf16 v[52:67], v[204:207], v[180:183], v[52:67]\n\t" \
  "v_exp_f32 v192, v100\n\t" \
  "v_exp_f32 v193, v101\n\t" \
  "v_exp_f32 v194, v102\n\t" \
  "v_exp_f32 v195, v103\n\t" \
  "s_cmp_eq_u32 %[nt], 0\n\t" \
  "v_mfma_f32_16x16x32_bf16 v[132:135], v[140:143], v[184:187], v[132:135]\n\t" \
  "v_mul_f32 v196, v128, v196\n\t" \
  "v_mul_f32 v197, v129, v197\n\t" \
  "v_mul_f32 v198, v130, v198\n\t" \
  "v_mul_f32 v199, v131, v199\n\t" \
  "v_add_f32 v192, 1.0, v192\n\t" \
  "v_add_f32 v193, 1.0, v193\n\t" \
  "v_add_f32 v194, 1.0, v194\n\t" \
  "v_add_f32 v195, 1.0, v195\n\t" \
  "v_cvt_pk_bf16_f32 v190, v196, v197\n\t" \
  "v_cvt_pk_bf16_f32 v191, v198, v199\n\t" \
  "s_cbranch_scc0 1b\n\t" \
  "s_waitcnt lgkmcnt(0)\n\t" \
  "s_cmp_lg_u32 %[last], 0\n\t" \
  "s_cbranch_scc1 8f\n\t" \
  "ds_write_b128 v243, v[188:191] offset:16\n\t" \
  "ds_read_b64_tr_b16 v[200:201], v244 offset:0\n\t" \
  "ds_read_b64_tr_b16 v[202:203], v244 offset:320\n\t" \
  "ds_read_b64_tr_b16 v[204:205], v244 offset:1280\n\t" \
  "ds_read_b64_tr_b16 v[206:207], v244 offset:1600\n\t" \
  "v_mfma_f32_16x16x32_bf16 v[132:135], v[144:147], v[188:191], v[132:135]\n\t" \
  "s_waitcnt lgkmcnt(0)\n\t" \
  "v_mfma_f32_32x32x16_bf16 v[68:83], v[200:203], v[176:179], v[68:83]\n\t" \
  "v_mfma_f32_32x32x16_bf16 v[68:83], v[204:207], v[180:183], v[68:83]\n\t" \
  "s_nop 8\n\t" \
  "v_mul_f32 v230, v230, v132\n\t" \
  "v_mul_f32 v231, v231, v132\n\t" \
  "v_cvt_pk_bf16_f32 v230, v230, v231\n\t" \
  "ds_write_b16 v245, v230 offset:7456\n\t" \
  "ds_write_b16_d16_hi v241, v230 offset:7584\n\t" \
  "ds_read_b128 v[200:203], v246 offset:7456\n\t" \
  "ds_read_b128 v[208:211], v246 offset:7488\n\t" \
  "s_waitcnt lgkmcnt(0)\n\t" \
  "v_mfma_f32_16x16x32_bf16 v[84:87], v[200:203], v[176:179], v[84:87]\n\t" \
  "v_mfma_f32_16x16x32_bf16 v[84:87], v[208:211], v[180:183], v[84:87]\n\t" \
  "s_branch 9f\n\t" \
  "6:\n\t" \
  "s_cmp_lg_u32 %[last], 0\n\t" \
  "s_cbranch_scc0 9f\n\t" \
  "s_waitcnt lgkmcnt(0)\n\t" \
  "v_cndmask_b32 v224, 0, 24, vcc\n\t" \
  "v_xor_b32 v220, v234, v224\n\t" \
  "v_xor_b32 v221, v235, v224\n\t" \
  "v_xor_b32 v222, v236, v224\n\t" \
  "v_xor_b32 v223, v237, v224\n\t" \
  "v_xor_b32 v216, 16, v220\n\t" \
  "v_xor_b32 v217, 16, v221\n\t" \
  "v_xor_b32 v218, 16, v222\n\t" \
  "v_xor_b32 v219, 16, v223\n\t" \
  "ds_read_b64 v[148:149], v220\n\t" \
  "ds_read_b64 v[150:151], v216\n\t" \
  "ds_read_b64 v[152:153], v221\n\t" \
  "ds_read_b64 v[154:155], v217\n\t" \
  "ds_read_b64 v[156:157], v222\n\t" \
  "ds_read_b64 v[158:159], v218\n\t" \
  "ds_read_b64 v[160:161], v223\n\t" \
  "ds_read_b64 v[162:163], v219\n\t" \
  "ds_read_b128 v[4:7], v247 offset:6144\n\t" \
  "ds_read_b128 v[8:11], v247 offset:7168\n\t" \
  "ds_read_b128 v[12:15], v247 offset:8192\n\t" \
  "ds_read_b128 v[16:19], v247 offset:9216\n\t" \
  "ds_read_b128 v[20:23], v247 offset:10240\n\t" \
  "ds_read_b128 v[24:27], v247 offset:11264\n\t" \
  "v_subrev_u32 v225, %[b1], v241\n\t" \
  "v_lshl_add_u32 v225, v225, 5, v243\n\t" \
  "v_sub_u32 v225, v225, v224\n\t" \
  "s_nop 4\n\t" \
  "v_cvt_pk_bf16_f32 v184, v52, v53\n\t" \
  "v_cvt_pk_bf16_f32 v185, v54, v55\n\t" \
  "v_cvt_pk_bf16_f32 v186, v56, v57\n\t" \
  "v_cvt_pk_bf16_f32 v187, v58, v59\n\t" \
  "v_cvt_pk_bf16_f32 v188, v60, v61\n\t" \
  "v_cvt_pk_bf16_f32 v189, v62, v63\n\t" \
  "v_cvt_pk_bf16_f32 v190, v64, v65\n\t" \
  "v_cvt_pk_bf16_f32 v191, v66, v67\n\t" \
  "v_cvt_pk_bf16_f32 v36, v68, v69\n\t" \
  "v_cvt_pk_bf16_f32 v37, v70, v71\n\t" \
  "v_cvt_pk_bf16_f32 v38, v72, v73\n\t" \
  "v_cvt_pk_bf16_f32 v39, v74, v75\n\t" \
  "v_cvt_pk_bf16_f32 v40, v76, v77\n\t" \
  "v_cvt_pk_bf16_f32 v41, v78, v79\n\t" \
  "v_cvt_pk_bf16_f32 v42, v80, v81\n\t" \
  "v_cvt_pk_bf16_f32 v43, v82, v83\n\t" \
  "s_waitcnt lgkmcnt(5)\n\t" \
  "v_mfma_f32_32x32x16_bf16 v[100:115], v[4:7], v[148:151], 0\n\t" \
  "s_waitcnt lgkmcnt(4)\n\t" \
  "v_mfma_f32_32x32x16_bf16 v[116:131], v[8:11], v[148:151], 0\n\t" \
  "ds_read_b128 v[28:31], v247 offset:12288\n\t" \
  "ds_read_b128 v[32:35], v247 offset:13312\n\t" \
  "s_waitcnt lgkmcnt(5)\n\t" \
  "v_mfma_f32_32x32x16_bf16 v[100:115], v[12:15], v[152:155], v[100:115]\n\t" \
  "s_waitcnt lgkmcnt(4)\n\t" \
  "v_mfma_f32_32x32x16_bf16 v[116:131], v[16:19], v[152:155], v[116:131]\n\t" \
  "ds_read_b128 v[4:7], v247 offset:14336\n\t" \
  "ds_read_b128 v[8:11], v247 offset:15360\n\t" \
  "s_waitcnt lgkmcnt(5)\n\t" \
  "v_mfma_f32_32x32x16_bf16 v[100:115], v[20:23], v[156:159], v[100:115]\n\t" \
  "s_waitcnt lgkmcnt(4)\n\t" \
  "v_mfma_f32_32x32x16_bf16 v[116:131], v[24:27], v[156:159], v[116:131]\n\t" \
  "ds_read_b128 v[12:15], v247 offset:16384\n\t" \
  "ds_read_b128 v[16:19], v247 offset:17408\n\t" \
  "s_waitcnt lgkmcnt(5)\n\t" \
  "v_mfma_f32_32x32x16_bf16 v[100:115], v[28:31], v[160:163], v[100:115]\n\t" \
  "s_waitcnt lgkmcnt(4)\n\t" \
  "v_mfma_f32_32x32x16_bf16 v[116:131], v[32:35], v[160:163], v[116:131]\n\t" \
  "ds_read_b128 v[20:23], v247 offset:18432\n\t" \
  "ds_read_b128 v[24:27], v247 offset:19456\n\t" \
  "s_waitcnt lgkmcnt(5)\n\t" \
  "v_mfma_f32_32x32x16_bf16 v[100:115], v[4:7], v[184:187], v[100:115]\n\t" \
  "ds_read_b128 v[28:31], v247 offset:20480\n\t" \
  "ds_read_b128 v[32:35], v247 offset:21504\n\t" \
  "s_waitcnt lgkmcnt(5)\n\t" \
  "v_mfma_f32_32x32x16_bf16 v[100:115], v[12:15], v[188:191], v[100:115]\n\t" \
  "s_waitcnt lgkmcnt(3)\n\t" \
  "v_mfma_f32_32x32x16_bf16 v[100:115], v[20:23], v[36:39], v[100:115]\n\t" \
  "s_waitcnt lgkmcnt(1)\n\t" \
  "v_mfma_f32_32x32x16_bf16 v[100:115], v[28:31], v[40:43], v[100:115]\n\t" \
  "s_waitcnt lgkmcnt(0)\n\t" \
  "s_branch 10f\n\t" \
  "8:\n\t" \
  "ds_write_b128 v243, v[188:191] offset:16\n\t" \
  "ds_read_b64_tr_b16 v[200:201], v244 offset:0\n\t" \
  "ds_read_b64_tr_b16 v[202:203], v244 offset:320\n\t" \
  "ds_read_b64_tr_b16 v[204:205], v244 offset:1280\n\t" \
  "ds_read_b64_tr_b16 v[206:207], v244 offset:1600\n\t" \
  "v_mfma_f32_16x16x32_bf16 v[132:135], v[144:147], v[188:191], v[132:135]\n\t" \
  "v_cndmask_b32 v224, 0, 24, vcc\n\t" \
  "v_xor_b32 v220, v234, v224\n\t" \
  "v_xor_b32 v221, v235, v224\n\t" \
  "v_xor_b32 v222, v236, v224\n\t" \
  "v_xor_b32 v223, v237, v224\n\t" \
  "v_xor_b32 v216, 16, v220\n\t" \
  "v_xor_b32 v217, 16, v221\n\t" \
  "v_xor_b32 v218, 16, v222\n\t" \
  "v_xor_b32 v219, 16, v223\n\t" \
  "ds_read_b64 v[148:149], v220\n\t" \
  "ds_read_b64 v[150:151], v216\n\t" \
  "ds_read_b64 v[152:153], v221\n\t" \
  "ds_read_b64 v[154:155], v217\n\t" \
  "ds_read_b64 v[156:157], v222\n\t" \
  "ds_read_b64 v[158:159], v218\n\t" \
  "ds_read_b64 v[160:161], v223\n\t" \
  "ds_read_b64 v[162:163], v219\n\t" \
  "ds_read_b128 v[4:7], v247 offset:6144\n\t" \
  "ds_read_b128 v[8:11], v247 offset:7168\n\t" \
  "s_waitcnt lgkmcnt(11)\n\t" \
  "ds_read_b128 v[12:15], v247 offset:8192\n\t" \
  "ds_read_b128 v[16:19], v247 offset:9216\n\t" \
  "ds_read_b128 v[20:23], v247 offset:10240\n\t" \
  "ds_read_b128 v[24:27], v247 offset:11264\n\t" \
  "v_subrev_u32 v225, %[b1], v241\n\t" \
  "v_lshl_add_u32 v225, v225, 5, v243\n\t" \
  "v_sub_u32 v225, v225, v224\n\t" \
  "v_mfma_f32_32x32x16_bf16 v[68:83], v[200:203], v[176:179], v[68:83]\n\t" \
  "s_waitcnt lgkmcnt(5)\n\t" \
  "v_mfma_f32_32x32x16_bf16 v[100:115], v[4:7], v[148:151], 0\n\t" \
  "v_mfma_f32_32x32x16_bf16 v[68:83], v[204:207], v[180:183], v[68:83]\n\t" \
  "s_waitcnt lgkmcnt(4)\n\t" \
  "v_mfma_f32_32x32x16_bf16 v[116:131], v[8:11], v[148:151], 0\n\t" \
  "ds_read_b128 v[28:31], v247 offset:12288\n\t" \
  "ds_read_b128 v[32:35], v247 offset:13312\n\t" \
  "s_waitcnt lgkmcnt(5)\n\t" \
  "v_mfma_f32_32x32x16_bf16 v[100:115], v[12:15], v[152:155], v[100:115]\n\t" \
  "v_mul_f32 v230, v230, v132\n\t" \
  "v_mul_f32 v231, v231, v132\n\t" \
  "v_cvt_pk_bf16_f32 v230, v230, v231\n\t" \
  "ds_write_b16 v245, v230 offset:7456\n\t" \
  "ds_write_b16_d16_hi v241, v230 offset:7584\n\t" \
  "ds_read_b128 v[200:203], v246 offset:7456\n\t" \
  "ds_read_b128 v[208:211], v246 offset:7488\n\t" \
  "v_cvt_pk_bf16_f32 v184, v52, v53\n\t" \
  "v_cvt_pk_bf16_f32 v185, v54, v55\n\t" \
  "v_cvt_pk_bf16_f32 v186, v56, v57\n\t" \
  "v_cvt_pk_bf16_f32 v187, v58, v59\n\t" \
  "s_waitcnt lgkmcnt(8)\n\t" \
  "v_mfma_f32_32x32x16_bf16 v[116:131], v[16:19], v[152:155], v[116:131]\n\t" \
  "ds_read_b128 v[4:7], v247 offset:14336\n\t" \
  "ds_read_b128 v[8:11], v247 offset:15360\n\t" \
  "s_waitcnt lgkmcnt(9)\n\t" \
  "v_mfma_f32_32x32x16_bf16 v[100:115], v[20:23], v[156:159], v[100:115]\n\t" \
  "v_cvt_pk_bf16_f32 v188, v60, v61\n\t" \
  "v_cvt_pk_bf16_f32 v189, v62, v63\n\t" \
  "v_cvt_pk_bf16_f32 v190, v64, v65\n\t" \
  "v_cvt_pk_bf16_f32 v191, v66, v67\n\t" \
  "s_waitcnt lgkmcnt(8)\n\t" \
  "v_mfma_f32_32x32x16_bf16 v[116:131], v[24:27], v[156:159], v[116:131]\n\t" \
  "ds_read_b128 v[12:15], v247 offset:16384\n\t" \
  "ds_read_b128 v[16:19], v247 offset:17408\n\t" \
  "s_waitcnt lgkmcnt(9)\n\t" \
  "v_mfma_f32_32x32x16_bf16 v[100:115], v[28:31], v[160:163], v[100:115]\n\t" \
  "v_cvt_pk_bf16_f32 v36, v68, v69\n\t" \
  "v_cvt_pk_bf16_f32 v37, v70, v71\n\t" \
  "v_cvt_pk_bf16_f32 v38, v72, v73\n\t" \
  "v_cvt_pk_bf16_f32 v39, v74, v75\n\t" \
  "s_waitcnt lgkmcnt(5)\n\t" \
  "v_mfma_f32_16x16x32_bf16 v[84:87], v[200:203], v[176:179], v[84:87]\n\t" \
  "v_cvt_pk_bf16_f32 v40, v76, v77\n\t" \
  "v_cvt_pk_bf16_f32 v41, v78, v79\n\t" \
  "v_cvt_pk_bf16_f32 v42, v80, v81\n\t" \
  "v_cvt_pk_bf16_f32 v43, v82, v83\n\t" \
  "v_mfma_f32_32x32x16_bf16 v[116:131], v[32:35], v[160:163], v[116:131]\n\t" \
  "ds_read_b128 v[20:23], v247 offset:18432\n\t" \
  "ds_read_b128 v[24:27], v247 offset:19456\n\t" \
  "s_waitcnt lgkmcnt(6)\n\t" \
  "v_mfma_f32_16x16x32_bf16 v[84:87], v[208:211], v[180:183], v[84:87]\n\t" \
  "s_waitcnt lgkmcnt(5)\n\t" \
  "v_mfma_f32_32x32x16_bf16 v[100:115], v[4:7], v[184:187], v[100:115]\n\t" \
  "ds_read_b128 v[28:31], v247 offset:20480\n\t" \
  "ds_read_b128 v[32:35], v247 offset:21504\n\t" \
  "s_waitcnt lgkmcnt(5)\n\t" \
  "v_mfma_f32_32x32x16_bf16 v[100:115], v[12:15], v[188:191], v[100:115]\n\t" \
  "s_waitcnt lgkmcnt(3)\n\t" \
  "v_mfma_f32_32x32x16_bf16 v[100:115], v[20:23], v[36:39], v[100:115]\n\t" \
  "s_waitcnt lgkmcnt(1)\n\t" \
  "v_mfma_f32_32x32x16_bf16 v[100:115], v[28:31], v[40:43], v[100:115]\n\t" \
  "s_waitcnt lgkmcnt(0)\n\t" \
  "10:\n\t" \
  "v_mov_b32 v212, 0xbfb8aa3b\n\t" \
  "v_mfma_f32_32x32x16_bf16 v[116:131], v[8:11], v[184:187], v[116:131]\n\t" \
  "v_mfma_f32_32x32x16_bf16 v[116:131], v[16:19], v[188:191], v[116:131]\n\t" \
  "s_nop 8\n\t" \
  "v_exp_f32 v192, v100\n\t" \
  "v_exp_f32 v193, v101\n\t" \
  "v_exp_f32 v194, v102\n\t" \
  "v_exp_f32 v195, v103\n\t" \
  "v_lshlrev_b32 v200, 16, v148\n\t" \
  "v_and_b32 v201, 0xffff0000, v148\n\t" \
  "v_lshlrev_b32 v202, 16, v149\n\t" \
  "v_and_b32 v203, 0xffff0000, v149\n\t" \
  "v_mfma_f32_32x32x16_bf16 v[116:131], v[24:27], v[36:39], v[116:131]\n\t" \
  "v_fmaak_f32 v192, v192, v212, 0xbfb8aa3b\n\t" \
  "v_fmaak_f32 v193, v193, v212, 0xbfb8aa3b\n\t" \
  "v_fmaak_f32 v194, v194, v212, 0xbfb8aa3b\n\t" \
  "v_fmaak_f32 v195, v195, v212, 0xbfb8aa3b\n\t" \
  "v_rcp_f32 v192, v192\n\t" \
  "v_rcp_f32 v193, v193\n\t" \
  "v_rcp_f32 v194, v194\n\t" \
  "v_rcp_f32 v195, v195\n\t" \
  "v_mfma_f32_32x32x16_bf16 v[116:131], v[32:35], v[40:43], v[116:131]\n\t" \
  "v_exp_f32 v196, v104\n\t" \
  "v_exp_f32 v197, v105\n\t" \
  "v_exp_f32 v198, v106\n\t" \
  "v_exp_f32 v199, v107\n\t" \
  "v_fmac_f32 v200, v100, v192\n\t" \
  "v_fmac_f32 v201, v101, v193\n\t" \
  "v_fmac_f32 v202, v102, v194\n\t" \
  "v_fmac_f32 v203, v103, v195\n\t" \
  "v_lshlrev_b32 v204, 16, v150\n\t" \
  "v_and_b32 v205, 0xffff0000, v150\n\t" \
  "v_lshlrev_b32 v206, 16, v151\n\t" \
  "v_and_b32 v207, 0xffff0000, v151\n\t" \
  "v_fmaak_f32 v196, v196, v212, 0xbfb8aa3b\n\t" \
  "v_fmaak_f32 v197, v197, v212, 0xbfb8aa3b\n\t" \
  "v_fmaak_f32 v198, v198, v212, 0xbfb8aa3b\n\t" \
  "v_fmaak_f32 v199, v199, v212, 0xbfb8aa3b\n\t" \
  "v_cvt_pk_bf16_f32 v176, v200, v201\n\t" \
  "v_cvt_pk_bf16_f32 v177, v202, v203\n\t" \
  "ds_write_b64 v225, v[176:177] offset:0\n\t" \
  "v_rcp_f32 v196, v196\n\t" \
  "v_rcp_f32 v197, v197\n\t" \
  "v_rcp_f32 v198, v198\n\t" \
  "v_rcp_f32 v199, v199\n\t" \
  "v_exp_f32 v192, v108\n\t" \
  "v_exp_f32 v193, v109\n\t" \
  "v_exp_f32 v194, v110\n\t" \
  "v_exp_f32 v195, v111\n\t" \
  "v_fmac_f32 v204, v104, v196\n\t" \
  "v_fmac_f32 v205, v105, v197\n\t" \
  "v_fmac_f32 v206, v106, v198\n\t" \
  "v_fmac_f32 v207, v107, v199\n\t" \
  "v_lshlrev_b32 v200, 16, v152\n\t" \
  "v_and_b32 v201, 0xffff0000, v152\n\t" \
  "v_lshlrev_b32 v202, 16, v153\n\t" \
  "v_and_b32 v203, 0xffff0000, v153\n\t" \
  "v_fmaak_f32 v192, v192, v212, 0xbfb8aa3b\n\t" \
  "v_fmaak_f32 v193, v193, v212, 0xbfb8aa3b\n\t" \
  "v_fmaak_f32 v194, v194, v212, 0xbfb8aa3b\n\t" \
  "v_fmaak_f32 v195, v195, v212, 0xbfb8aa3b\n\t" \
  "v_cvt_pk_bf16_f32 v178, v204, v205\n\t" \
  "v_cvt_pk_bf16_f32 v179, v206, v207\n\t" \
  "ds_write_b64 v225, v[178:179] offset:16\n\t" \
  "v_rcp_f32 v192, v192\n\t" \
  "v_rcp_f32 v193, v193\n\t" \
  "v_rcp_f32 v194, v194\n\t" \
  "v_rcp_f32 v195, v195\n\t" \
  "v_exp_f32 v196, v112\n\t" \
  "v_exp_f32 v197, v113\n\t" \
  "v_exp_f32 v198, v114\n\t" \
  "v_exp_f32 v199, v115\n\t" \
  "v_fmac_f32 v200, v108, v192\n\t" \
  "v_fmac_f32 v201, v109, v193\n\t" \
  "v_fmac_f32 v202, v110, v194\n\t" \
  "v_fmac_f32 v203, v111, v195\n\t" \
  "v_lshlrev_b32 v204, 16, v154\n\t" \
  "v_and_b32 v205, 0xffff0000, v154\n\t" \
  "v_lshlrev_b32 v206, 16, v155\n\t" \
  "v_and_b32 v207, 0xffff0000, v155\n\t" \
  "v_fmaak_f32 v196, v196, v212, 0xbfb8aa3b\n\t" \
  "v_fmaak_f32 v197, v197, v212, 0xbfb8aa3b\n\t" \
  "v_fmaak_f32 v198, v198, v212, 0xbfb8aa3b\n\t" \
  "v_fmaak_f32 v199, v199, v212, 0xbfb8aa3b\n\t" \
  "v_cvt_pk_bf16_f32 v180, v200, v201\n\t" \
  "v_cvt_pk_bf16_f32 v181, v202, v203\n\t" \
  "ds_write_b64 v225, v[180:181] offset:32\n\t" \
  "v_rcp_f32 v196, v196\n\t" \
  "v_rcp_f32 v197, v197\n\t" \
  "v_rcp_f32 v198, v198\n\t" \
  "v_rcp_f32 v199, v199\n\t" \
  "v_exp_f32 v192, v116\n\t" \
  "v_exp_f32 v193, v117\n\t" \
  "v_exp_f32 v194, v118\n\t" \
  "v_exp_f32 v195, v119\n\t" \
  "v_fmac_f32 v204, v112, v196\n\t" \
  "v_fmac_f32 v205, v113, v197\n\t" \
  "v_fmac_f32 v206, v114, v198\n\t" \
  "v_fmac_f32 v207, v115, v199\n\t" \
  "v_lshlrev_b32 v200, 16, v156\n\t" \
  "v_and_b32 v201, 0xffff0000, v156\n\t" \
  "v_lshlrev_b32 v202, 16, v157\n\t" \
  "v_and_b32 v203, 0xffff0000, v157\n\t" \
  "v_fmaak_f32 v192, v192, v212, 0xbfb8aa3b\n\t" \
  "v_fmaak_f32 v193, v193, v212, 0xbfb8aa3b\n\t" \
  "v_fmaak_f32 v194, v194, v212, 0xbfb8aa3b\n\t" \
  "v_fmaak_f32 v195, v195, v212, 0xbfb8aa3b\n\t" \
  "v_cvt_pk_bf16_f32 v182, v204, v205\n\t" \
  "v_cvt_pk_bf16_f32 v183, v206, v207\n\t" \
  "ds_write_b64 v225, v[182:183] offset:48\n\t" \
  "v_rcp_f32 v192, v192\n\t" \
  "v_rcp_f32 v193, v193\n\t" \
  "v_rcp_f32 v194, v194\n\t" \
  "v_rcp_f32 v195, v195\n\t" \
  "v_exp_f32 v196, v120\n\t" \
  "v_exp_f32 v197, v121\n\t" \
  "v_exp_f32 v198, v122\n\t" \
  "v_exp_f32 v199, v123\n\t" \
  "v_fmac_f32 v200, v116, v192\n\t" \
  "v_fmac_f32 v201, v117, v193\n\t" \
  "v_fmac_f32 v202, v118, v194\n\t" \
  "v_fmac_f32 v203, v119, v195\n\t" \
  "v_lshlrev_b32 v204, 16, v158\n\t" \
  "v_and_b32 v205, 0xffff0000, v158\n\t" \
  "v_lshlrev_b32 v206, 16, v159\n\t" \
  "v_and_b32 v207, 0xffff0000, v159\n\t" \
  "v_fmaak_f32 v196, v196, v212, 0xbfb8aa3b\n\t" \
  "v_fmaak_f32 v197, v197, v212, 0xbfb8aa3b\n\t" \
  "v_fmaak_f32 v198, v198, v212, 0xbfb8aa3b\n\t" \
  "v_fmaak_f32 v199, v199, v212, 0xbfb8aa3b\n\t" \
  "v_cvt_pk_bf16_f32 v176, v200, v201\n\t" \
  "v_cvt_pk_bf16_f32 v177, v202, v203\n\t" \
  "ds_write_b64 v225, v[176:177] offset:64\n\t" \
  "v_rcp_f32 v196, v196\n\t" \
  "v_rcp_f32 v197, v197\n\t" \
  "v_rcp_f32 v198, v198\n\t" \
  "v_rcp_f32 v199, v199\n\t" \
  "v_exp_f32 v192, v124\n\t" \
  "v_exp_f32 v193, v125\n\t" \
  "v_exp_f32 v194, v126\n\t" \
  "v_exp_f32 v195, v127\n\t" \
  "v_fmac_f32 v204, v120, v196\n\t" \
  "v_fmac_f32 v205, v121, v197\n\t" \
  "v_fmac_f32 v206, v122, v198\n\t" \
  "v_fmac_f32 v207, v123, v199\n\t" \
  "v_lshlrev_b32 v200, 16, v160\n\t" \
  "v_and_b32 v201, 0xffff0000, v160\n\t" \
  "v_lshlrev_b32 v202, 16, v161\n\t" \
  "v_and_b32 v203, 0xffff0000, v161\n\t" \
  "v_fmaak_f32 v192, v192, v212, 0xbfb8aa3b\n\t" \
  "v_fmaak_f32 v193, v193, v212, 0xbfb8aa3b\n\t" \
  "v_fmaak_f32 v194, v194, v212, 0xbfb8aa3b\n\t" \
  "v_fmaak_f32 v195, v195, v212, 0xbfb8aa3b\n\t" \
  "v_cvt_pk_bf16_f32 v178, v204, v205\n\t" \
  "v_cvt_pk_bf16_f32 v179, v206, v207\n\t" \
  "ds_write_b64 v225, v[178:179] offset:80\n\t" \
  "v_rcp_f32 v192, v192\n\t" \
  "v_rcp_f32 v193, v193\n\t" \
  "v_rcp_f32 v194, v194\n\t" \
  "v_rcp_f32 v195, v195\n\t" \
  "v_exp_f32 v196, v128\n\t" \
  "v_exp_f32 v197, v129\n\t" \
  "v_exp_f32 v198, v130\n\t" \
  "v_exp_f32 v199, v131\n\t" \
  "v_fmac_f32 v200, v124, v192\n\t" \
  "v_fmac_f32 v201, v125, v193\n\t" \
  "v_fmac_f32 v202, v126, v194\n\t" \
  "v_fmac_f32 v203, v127, v195\n\t" \
  "v_lshlrev_b32 v204, 16, v162\n\t" \
  "v_and_b32 v205, 0xffff0000, v162\n\t" \
  "v_lshlrev_b32 v206, 16, v163\n\t" \
  "v_and_b32 v207, 0xffff0000, v163\n\t" \
  "v_fmaak_f32 v196, v196, v212, 0xbfb8aa3b\n\t" \
  "v_fmaak_f32 v197, v197, v212, 0xbfb8aa3b\n\t" \
  "v_fmaak_f32 v198, v198, v212, 0xbfb8aa3b\n\t" \
  "v_fmaak_f32 v199, v199, v212, 0xbfb8aa3b\n\t" \
  "v_cvt_pk_bf16_f32 v180, v200, v201\n\t" \
  "v_cvt_pk_bf16_f32 v181, v202, v203\n\t" \
  "ds_write_b64 v225, v[180:181] offset:96\n\t" \
  "v_rcp_f32 v196, v196\n\t" \
  "v_rcp_f32 v197, v197\n\t" \
  "v_rcp_f32 v198, v198\n\t" \
  "v_rcp_f32 v199, v199\n\t" \
  "s_nop 0\n\t" \
  "v_fmac_f32 v204, v128, v196\n\t" \
  "v_fmac_f32 v205, v129, v197\n\t" \
  "v_fmac_f32 v206, v130, v198\n\t" \
  "v_fmac_f32 v207, v131, v199\n\t" \
  "v_cvt_pk_bf16_f32 v182, v204, v205\n\t" \
  "v_cvt_pk_bf16_f32 v183, v206, v207\n\t" \
  "ds_write_b64 v225, v[182:183] offset:112\n\t" \
  "9:\n\t" \
  "ds_write_b16 v233, v255 offset:5376\n\t" \
  "s_mov_b32 m0, %[keep]\n\t" \
  "s_setprio 0\n\t" \
  "s_nop 11\n\t" \
  ""
#define LOOP_ASM_PLAIN \
  "s_mov_b32 %[keep], m0\n\t" \
  "s_mov_b64 vcc, %[hsel]\n\t" \
  "ds_read2_b32 v[220:221], v252 offset0:0 offset1:2\n\t" \
  "ds_read2_b32 v[222:223], v252 offset0:4 offset1:6\n\t" \
  "ds_read_b64 v[226:227], v253\n\t" \
  "ds_read_b32 v232, v254\n\t" \
  "v_mov_b32 v176, 0\n\t" \
  "v_mov_b32 v177, 0\n\t" \
  "v_mov_b32 v178, 0\n\t" \
  "v_mov_b32 v179, 0\n\t" \
  "v_mov_b32 v180, 0\n\t" \
  "v_mov_b32 v181, 0\n\t" \
  "v_mov_b32 v182, 0\n\t" \
  "v_mov_b32 v183, 0\n\t" \
  "v_mov_b32 v184, 0\n\t" \
  "v_mov_b32 v185, 0\n\t" \
  "v_mov_b32 v186, 0\n\t" \
  "v_mov_b32 v187, 0\n\t" \
  "v_mov_b32 v188, 0\n\t" \
  "v_mov_b32 v189, 0\n\t" \
  "v_mov_b32 v190, 0\n\t" \
  "v_mov_b32 v191, 0\n\t" \
  "v_mov_b32 v230, 0\n\t" \
  "v_mov_b32 v231, 0\n\t" \
  "v_mov_b32 v208, 0\n\t" \
  "v_mov_b32 v209, 0\n\t" \
  "v_mov_b32 v210, 0\n\t" \
  "v_mov_b32 v211, 0\n\t" \
  "ds_write_b128 v243, v[184:187] offset:0\n\t" \
  "s_nop 0\n\t" \
  "v_mfma_f32_16x16x32_bf16 v[132:135], v[184:187], v[188:191], 0\n\t" \
  "v_add_u32 v252, 0x100, v252\n\t" \
  "v_add_u32 v253, 0x100, v253\n\t" \
  "v_add_u32 v254, 0x100, v254\n\t" \
  "s_waitcnt lgkmcnt(0)\n\t" \
  "v_lshrrev_b32 v232, 20, v232\n\t" \
  "v_add_u32 v233, %[posd], v232\n\t" \
  "ds_read_b128 v[216:219], v233\n\t" \
  "v_add_u32 v233, v240, v232\n\t" \
  "ds_read_b128 v[172:175], v233 offset:32\n\t" \
  "ds_read_b128 v[168:171], v233\n\t" \
  "v_add_u32 v233, v241, v232\n\t" \
  "ds_write_b16_d16_hi v233, v255 offset:5376\n\t" \
  "v_mad_u32_u16 v220, v220, %[c128], v248\n\t" \
  "v_mad_u32_u16 v221, v221, %[c128], v249\n\t" \
  "v_mad_u32_u16 v222, v222, %[c128], v248\n\t" \
  "v_mad_u32_u16 v223, v223, %[c128], v249\n\t" \
  "v_or_b32 v226, v226, v250\n\t" \
  "v_mad_u32_u16 v227, v227, 6, v251\n\t" \
  "s_waitcnt vmcnt(0)\n\t" \
  "ds_read_b128 v[148:151], v234\n\t" \
  "ds_read_b128 v[152:155], v235\n\t" \
  "ds_read_b128 v[156:159], v236\n\t" \
  "ds_read_b128 v[160:163], v237\n\t" \
  "ds_read_b128 v[164:167], v238 offset:4096\n\t" \
  "ds_read_b64 v[224:225], v239 offset:5120\n\t" \
  "s_waitcnt lgkmcnt(0)\n\t" \
  "s_cmp_lt_i32 %[nt], 1\n\t" \
  "s_cbranch_scc1 7f\n\t" \
  "s_mov_b32 m0, %[b2]\n\t" \
  "s_nop 0\n\t" \
  "global_load_lds_dwordx4 v226, %[ea]\n\t" \
  "global_load_lds_dword v227, %[pos] offset:1024\n\t" \
  "s_mov_b32 m0, %[b1]\n\t" \
  "s_nop 0\n\t" \
  "global_load_lds_dwordx4 v220, %[h0]\n\t" \
  "global_load_lds_dwordx4 v221, %[h1] offset:1024\n\t" \
  "global_load_lds_dwordx4 v222, %[h2] offset:2048\n\t" \
  "global_load_lds_dwordx4 v223, %[h3] offset:3072\n\t" \
  "7:\n\t" \
  "ds_read2_b32 v[220:221], v252 offset0:0 offset1:2\n\t" \
  "ds_read2_b32 v[222:223], v252 offset0:4 offset1:6\n\t" \
  "ds_read_b64 v[226:227], v253\n\t" \
  "ds_read_b32 v232, v254\n\t" \
  "v_add_u32 v252, 0x100, v252\n\t" \
  "v_add_u32 v253, 0x100, v253\n\t" \
  "v_add_u32 v254, 0x100, v254\n\t" \
  "v_and_b32 v225, 0xffff0000, v225\n\t" \
  "v_lshlrev_b32 v219, 16, v224\n\t" \
  "v_and_b32 v224, 0xffff0000, v224\n\t" \
  "v_sub_f32 v229, v218, v225\n\t" \
  "v_sub_f32 v216, v216, v219\n\t" \
  "v_sub_f32 v217, v217, v224\n\t" \
  "v_mul_f32 v218, v229, v229\n\t" \
  "v_cndmask_b32 v228, v216, v217, vcc\n\t" \
  "v_fmac_f32 v218, v216, v216\n\t" \
  "v_fmac_f32 v218, v217, v217\n\t" \
  "v_cvt_pk_bf16_f32 v218, v218, v218\n\t" \
  "v_cndmask_b32_sdwa v175, v175, v218, vcc dst_sel:WORD_1 dst_unused:UNUSED_PRESERVE src0_sel:WORD_1 src1_sel:WORD_1\n\t" \
  "v_mfma_f32_32x32x16_bf16 v[100:115], v[4:7], v[148:151], 0\n\t" \
  "v_mfma_f32_32x32x16_bf16 v[100:115], v[12:15], v[152:155], v[100:115]\n\t" \
  "v_mfma_f32_32x32x16_bf16 v[100:115], v[20:23], v[156:159], v[100:115]\n\t" \
  "v_mfma_f32_32x32x16_bf16 v[100:115], v[28:31], v[160:163], v[100:115]\n\t" \
  "v_mfma_f32_32x32x16_bf16 v[100:115], v[36:39], v[168:171], v[100:115]\n\t" \
  "v_mfma_f32_32x32x16_bf16 v[100:115], v[88:91], v[164:167], v[100:115]\n\t" \
  "v_mfma_f32_32x32x16_bf16 v[100:115], v[44:47], v[172:175], v[100:115]\n\t" \
  "s_cmp_eq_u32 %[nt], 0\n\t" \
  "s_nop 10\n\t" \
  "v_mul_f32 v192, 0xbfb8aa3b, v100\n\t" \
  "v_mul_f32 v193, 0xbfb8aa3b, v101\n\t" \
  "v_mul_f32 v194, 0xbfb8aa3b, v102\n\t" \
  "v_mul_f32 v195, 0xbfb8aa3b, v103\n\t" \
  "v_exp_f32 v192, v192\n\t" \
  "v_exp_f32 v193, v193\n\t" \
  "v_exp_f32 v194, v194\n\t" \
  "v_exp_f32 v195, v195\n\t" \
  "s_nop 0\n\t" \
  "v_add_f32 v192, 1.0, v192\n\t" \
  "v_add_f32 v193, 1.0, v193\n\t" \
  "v_add_f32 v194, 1.0, v194\n\t" \
  "v_add_f32 v195, 1.0, v195\n\t" \
  "s_cbranch_scc1 6f\n\t" \
  "s_cmp_lt_i32 %[thr], 0\n\t" \
  "s_cbranch_scc1 1f\n\t" \
  "s_setprio 1\n\t" \
  "1:\n\t" \
  "ds_write_b128 v243, v[188:191] offset:16\n\t" \
  "ds_read_b64_tr_b16 v[200:201], v244 offset:0\n\t" \
  "ds_read_b64_tr_b16 v[202:203], v244 offset:320\n\t" \
  "ds_read_b64_tr_b16 v[204:205], v244 offset:1280\n\t" \
  "ds_read_b64_tr_b16 v[206:207], v244 offset:1600\n\t" \
  "v_mfma_f32_16x16x32_bf16 v[132:135], v[144:147], v[188:191], v[132:135]\n\t" \
  "v_rcp_f32 v192, v192\n\t" \
  "v_rcp_f32 v193, v193\n\t" \
  "v_rcp_f32 v194, v194\n\t" \
  "v_rcp_f32 v195, v195\n\t" \
  "v_mfma_f32_32x32x16_bf16 v[116:131], v[8:11], v[148:151], 0\n\t" \
  "v_mul_f32 v196, 0xbfb8aa3b, v104\n\t" \
  "v_mul_f32 v197, 0xbfb8aa3b, v105\n\t" \
  "v_mul_f32 v198, 0xbfb8aa3b, v106\n\t" \
  "v_mul_f32 v199, 0xbfb8aa3b, v107\n\t" \
  "v_exp_f32 v196, v196\n\t" \
  "v_exp_f32 v197, v197\n\t" \
  "v_exp_f32 v198, v198\n\t" \
  "v_exp_f32 v199, v199\n\t" \
  "v_mfma_f32_32x32x16_bf16 v[116:131], v[16:19], v[152:155], v[116:131]\n\t" \
  "v_mul_f32 v192, v100, v192\n\t" \
  "v_mul_f32 v193, v101, v193\n\t" \
  "v_mul_f32 v194, v102, v194\n\t" \
  "v_mul_f32 v195, v103, v195\n\t" \
  "v_add_f32 v196, 1.0, v196\n\t" \
  "v_add_f32 v197, 1.0, v197\n\t" \
  "v_add_f32 v198, 1.0, v198\n\t" \
  "v_add_f32 v199, 1.0, v199\n\t" \
  "v_cvt_pk_bf16_f32 v184, v192, v193\n\t" \
  "v_cvt_pk_bf16_f32 v185, v194, v195\n\t" \
  "v_mfma_f32_32x32x16_bf16 v[116:131], v[24:27], v[156:159], v[116:131]\n\t" \
  "v_rcp_f32 v196, v196\n\t" \
  "v_rcp_f32 v197, v197\n\t" \
  "v_rcp_f32 v198, v198\n\t" \
  "v_rcp_f32 v199, v199\n\t" \
  "s_waitcnt lgkmcnt(2)\n\t" \
  "v_mfma_f32_32x32x16_bf16 v[68:83], v[200:203], v[176:179], v[68:83]\n\t" \
  "v_mul_f32 v192, 0xbfb8aa3b, v108\n\t" \
  "v_mul_f32 v193, 0xbfb8aa3b, v109\n\t" \
  "v_mul_f32 v194, 0xbfb8aa3b, v110\n\t" \
  "v_mul_f32 v195, 0xbfb8aa3b, v111\n\t" \
  "v_exp_f32 v192, v192\n\t" \
  "v_exp_f32 v193, v193\n\t" \
  "v_exp_f32 v194, v194\n\t" \
  "v_exp_f32 v195, v195\n\t" \
  "v_mfma_f32_32x32x16_bf16 v[116:131], v[32:35], v[160:163], v[116:131]\n\t" \
  "v_mul_f32 v196, v104, v196\n\t" \
  "v_mul_f32 v197, v105, v197\n\t" \
  "v_mul_f32 v198, v106, v198\n\t" \
  "v_mul_f32 v199, v107, v199\n\t" \
  "v_mul_f32 v230, v230, v132\n\t" \
  "v_mul_f32 v231, v231, v132\n\t" \
  "v_cvt_pk_bf16_f32 v230, v230, v231\n\t" \
  "ds_write_b16 v245, v230 offset:7456\n\t" \
  "ds_write_b16_d16_hi v241, v230 offset:7584\n\t" \
  "ds_read_b128 v[200:203], v246 offset:7456\n\t" \
  "ds_read_b128 v[208:211], v246 offset:7488\n\t" \
  "v_mov_b32 v230, v228\n\t" \
  "v_mov_b32 v231, v229\n\t" \
  "v_add_f32 v192, 1.0, v192\n\t" \
  "v_add_f32 v193, 1.0, v193\n\t" \
  "v_add_f32 v194, 1.0, v194\n\t" \
  "v_add_f32 v195, 1.0, v195\n\t" \
  "v_cvt_pk_bf16_f32 v186, v196, v197\n\t" \
  "v_cvt_pk_bf16_f32 v187, v198, v199\n\t" \
  "v_mfma_f32_32x32x16_bf16 v[116:131], v[40:43], v[168:171], v[116:131]\n\t" \
  "v_rcp_f32 v192, v192\n\t" \
  "v_rcp_f32 v193, v193\n\t" \
  "v_rcp_f32 v194, v194\n\t" \
  "v_rcp_f32 v195, v195\n\t" \
  "s_sub_u32 %[nt], %[nt], 1\n\t" \
  "s_cmp_lg_u32 %[nt], %[thr]\n\t" \
  "s_cbranch_scc1 3f\n\t" \
  "s_setprio 0\n\t" \
  "3:\n\t" \
  "v_mfma_f32_32x32x16_bf16 v[116:131], v[92:95], v[164:167], v[116:131]\n\t" \
  "v_mul_f32 v196, 0xbfb8aa3b, v112\n\t" \
  "v_mul_f32 v197, 0xbfb8aa3b, v113\n\t" \
  "v_mul_f32 v198, 0xbfb8aa3b, v114\n\t" \
  "v_mul_f32 v199, 0xbfb8aa3b, v115\n\t" \
  "v_exp_f32 v196, v196\n\t" \
  "v_exp_f32 v197, v197\n\t" \
  "v_exp_f32 v198, v198\n\t" \
  "v_exp_f32 v199, v199\n\t" \
  "v_mfma_f32_32x32x16_bf16 v[116:131], v[48:51], v[172:175], v[116:131]\n\t" \
  "v_mul_f32 v192, v108, v192\n\t" \
  "v_mul_f32 v193, v109, v193\n\t" \
  "v_mul_f32 v194, v110, v194\n\t" \
  "v_mul_f32 v195, v111, v195\n\t" \
  "s_waitcnt vmcnt(0)\n\t" \
  "ds_read_b128 v[148:151], v234\n\t" \
  "ds_read_b128 v[152:155], v235\n\t" \
  "ds_read_b128 v[156:159], v236\n\t" \
  "ds_read_b128 v[160:163], v237\n\t" \
  "ds_read_b128 v[164:167], v238 offset:4096\n\t" \
  "ds_read_b64 v[224:225], v239 offset:5120\n\t" \
  "ds_write_b128 v243, v[184:187] offset:0\n\t" \
  "v_add_f32 v196, 1.0, v196\n\t" \
  "v_add_f32 v197, 1.0, v197\n\t" \
  "v_add_f32 v198, 1.0, v198\n\t" \
  "v_add_f32 v199, 1.0, v199\n\t" \
  "v_cvt_pk_bf16_f32 v188, v192, v193\n\t" \
  "v_cvt_pk_bf16_f32 v189, v194, v195\n\t" \
  "v_mad_u32_u16 v220, v220, %[c128], v248\n\t" \
  "v_mad_u32_u16 v221, v221, %[c128], v249\n\t" \
  "v_mad_u32_u16 v222, v222, %[c128], v248\n\t" \
  "s_waitcnt lgkmcnt(11)\n\t" \
  "v_mfma_f32_32x32x16_bf16 v[68:83], v[204:207], v[180:183], v[68:83]\n\t" \
  "v_rcp_f32 v196, v196\n\t" \
  "v_rcp_f32 v197, v197\n\t" \
  "v_rcp_f32 v198, v198\n\t" \
  "v_rcp_f32 v199, v199\n\t" \
  "v_mad_u32_u16 v223, v223, %[c128], v249\n\t" \
  "v_or_b32 v226, v226, v250\n\t" \
  "v_mad_u32_u16 v227, v227, 6, v251\n\t" \
  "s_waitcnt lgkmcnt(8)\n\t" \
  "v_mfma_f32_16x16x32_bf16 v[84:87], v[200:203], v[176:179], v[84:87]\n\t" \
  "v_mul_f32 v192, 0xbfb8aa3b, v116\n\t" \
  "v_mul_f32 v193, 0xbfb8aa3b, v117\n\t" \
  "v_mul_f32 v194, 0xbfb8aa3b, v118\n\t" \
  "v_mul_f32 v195, 0xbfb8aa3b, v119\n\t" \
  "v_exp_f32 v192, v192\n\t" \
  "v_exp_f32 v193, v193\n\t" \
  "v_exp_f32 v194, v194\n\t" \
  "v_exp_f32 v195, v195\n\t" \
  "s_waitcnt lgkmcnt(7)\n\t" \
  "v_mfma_f32_16x16x32_bf16 v[84:87], v[208:211], v[180:183], v[84:87]\n\t" \
  "v_mul_f32 v196, v112, v196\n\t" \
  "v_mul_f32 v197, v113, v197\n\t" \
  "v_mul_f32 v198, v114, v198\n\t" \
  "v_mul_f32 v199, v115, v199\n\t" \
  "s_waitcnt lgkmcnt(1)\n\t" \
  "s_cmp_lt_i32 %[nt], 1\n\t" \
  "s_cbranch_scc1 4f\n\t" \
  "s_mov_b32 m0, %[b2]\n\t" \
  "s_nop 0\n\t" \
  "global_load_lds_dwordx4 v226, %[ea]\n\t" \
  "global_load_lds_dword v227, %[pos] offset:1024\n\t" \
  "s_mov_b32 m0, %[b1]\n\t" \
  "s_nop 0\n\t" \
  "global_load_lds_dwordx4 v220, %[h0]\n\t" \
  "global_load_lds_dwordx4 v221, %[h1] offset:1024\n\t" \
  "global_load_lds_dwordx4 v222, %[h2] offset:2048\n\t" \
  "global_load_lds_dwordx4 v223, %[h3] offset:3072\n\t" \
  "4:\n\t" \
  "v_add_f32 v192, 1.0, v192\n\t" \
  "v_add_f32 v193, 1.0, v193\n\t" \
  "v_add_f32 v194, 1.0, v194\n\t" \
  "v_add_f32 v195, 1.0, v195\n\t" \
  "v_cvt_pk_bf16_f32 v190, v196, v197\n\t" \
  "v_cvt_pk_bf16_f32 v191, v198, v199\n\t" \
  "v_lshrrev_b32 v232, 20, v232\n\t" \
  "v_add_u32 v220, %[posd], v232\n\t" \
  "v_add_u32 v221, v240, v232\n\t" \
  "ds_write_b128 v243, v[188:191] offset:16\n\t" \
  "ds_read_b128 v[176:179], v242 offset:5376\n\t" \
  "ds_read_b128 v[180:183], v242 offset:5408\n\t" \
  "ds_write_b16 v233, v255 offset:5376\n\t" \
  "ds_read_b128 v[216:219], v220\n\t" \
  "ds_read_b128 v[172:175], v221 offset:32\n\t" \
  "ds_read_b128 v[168:171], v221\n\t" \
  "ds_read_b64_tr_b16 v[200:201], v244 offset:0\n\t" \
  "ds_read_b64_tr_b16 v[202:203], v244 offset:320\n\t" \
  "ds_read_b64_tr_b16 v[204:205], v244 offset:1280\n\t" \
  "ds_read_b64_tr_b16 v[206:207], v244 offset:1600\n\t" \
  "v_add_u32 v233, v241, v232\n\t" \
  "ds_write_b16_d16_hi v233, v255 offset:5376\n\t" \
  "v_mfma_f32_16x16x32_bf16 v[132:135], v[96:99], v[184:187], 0\n\t" \
  "v_rcp_f32 v192, v192\n\t" \
  "v_rcp_f32 v193, v193\n\t" \
  "v_rcp_f32 v194, v194\n\t" \
  "v_rcp_f32 v195, v195\n\t" \
  "v_mfma_f32_32x32x16_bf16 v[100:115], v[4:7], v[148:151], 0\n\t" \
  "v_mul_f32 v196, 0xbfb8aa3b, v120\n\t" \
  "v_mul_f32 v197, 0xbfb8aa3b, v121\n\t" \
  "v_mul_f32 v198, 0xbfb8aa3b, v122\n\t" \
  "v_mul_f32 v199, 0xbfb8aa3b, v123\n\t" \
  "v_exp_f32 v196, v196\n\t" \
  "v_exp_f32 v197, v197\n\t" \
  "v_exp_f32 v198, v198\n\t" \
  "v_exp_f32 v199, v199\n\t" \
  "v_mfma_f32_32x32x16_bf16 v[100:115], v[12:15], v[152:155], v[100:115]\n\t" \
  "v_mul_f32 v192, v116, v192\n\t" \
  "v_mul_f32 v193, v117, v193\n\t" \
  "v_mul_f32 v194, v118, v194\n\t" \
  "v_mul_f32 v195, v119, v195\n\t" \
  "v_add_f32 v196, 1.0, v196\n\t" \
  "v_add_f32 v197, 1.0, v197\n\t" \
  "v_add_f32 v198, 1.0, v198\n\t" \
  "v_add_f32 v199, 1.0, v199\n\t" \
  "v_cvt_pk_bf16_f32 v184, v192, v193\n\t" \
  "v_cvt_pk_bf16_f32 v185, v194, v195\n\t" \
  "v_and_b32 v225, 0xffff0000, v225\n\t" \
  "s_waitcnt lgkmcnt(7)\n\t" \
  "v_lshlrev_b32 v219, 16, v224\n\t" \
  "v_and_b32 v224, 0xffff0000, v224\n\t" \
  "v_sub_f32 v229, v218, v225\n\t" \
  "v_sub_f32 v216, v216, v219\n\t" \
  "v_sub_f32 v217, v217, v224\n\t" \
  "v_mfma_f32_32x32x16_bf16 v[100:115], v[20:23], v[156:159], v[100:115]\n\t" \
  "v_rcp_f32 v196, v196\n\t" \
  "v_rcp_f32 v197, v197\n\t" \
  "v_rcp_f32 v198, v198\n\t" \
  "v_rcp_f32 v199, v199\n\t" \
  "v_mul_f32 v218, v229, v229\n\t" \
  "v_cndmask_b32 v228, v216, v217, vcc\n\t" \
  "v_fmac_f32 v218, v216, v216\n\t" \
  "v_fmac_f32 v218, v217, v217\n\t" \
  "v_cvt_pk_bf16_f32 v218, v218, v218\n\t" \
  "s_waitcnt lgkmcnt(6)\n\t" \
  "v_cndmask_b32_sdwa v175, v175, v218, vcc dst_sel:WORD_1 dst_unused:UNUSED_PRESERVE src0_sel:WORD_1 src1_sel:WORD_1\n\t" \
  "v_mfma_f32_16x16x32_bf16 v[132:135], v[136:139], v[188:191], v[132:135]\n\t" \
  "v_mul_f32 v192, 0xbfb8aa3b, v124\n\t" \
  "v_mul_f32 v193, 0xbfb8aa3b, v125\n\t" \
  "v_mul_f32 v194, 0xbfb8aa3b, v126\n\t" \
  "v_mul_f32 v195, 0xbfb8aa3b, v127\n\t" \
  "v_exp_f32 v192, v192\n\t" \
  "v_exp_f32 v193, v193\n\t" \
  "v_exp_f32 v194, v194\n\t" \
  "v_exp_f32 v195, v195\n\t" \
  "ds_read2_b32 v[220:221], v252 offset0:0 offset1:2\n\t" \
  "ds_read2_b32 v[222:223], v252 offset0:4 offset1:6\n\t" \
  "ds_read_b64 v[226:227], v253\n\t" \
  "ds_read_b32 v232, v254\n\t" \
  "v_mfma_f32_32x32x16_bf16 v[100:115], v[28:31], v[160:163], v[100:115]\n\t" \
  "v_mul_f32 v196, v120, v196\n\t" \
  "v_mul_f32 v197, v121, v197\n\t" \
  "v_mul_f32 v198, v122, v198\n\t" \
  "v_mul_f32 v199, v123, v199\n\t" \
  "v_add_u32 v252, 0x100, v252\n\t" \
  "v_add_u32 v253, 0x100, v253\n\t" \
  "v_add_u32 v254, 0x100, v254\n\t" \
  "v_add_f32 v192, 1.0, v192\n\t" \
  "v_add_f32 v193, 1.0, v193\n\t" \
  "v_add_f32 v194, 1.0, v194\n\t" \
  "v_add_f32 v195, 1.0, v195\n\t" \
  "v_cvt_pk_bf16_f32 v186, v196, v197\n\t" \
  "v_cvt_pk_bf16_f32 v187, v198, v199\n\t" \
  "s_waitcnt lgkmcnt(9)\n\t" \
  "v_mfma_f32_32x32x16_bf16 v[100:115], v[36:39], v[168:171], v[100:115]\n\t" \
  "v_rcp_f32 v192, v192\n\t" \
  "v_rcp_f32 v193, v193\n\t" \
  "v_rcp_f32 v194, v194\n\t" \
  "v_rcp_f32 v195, v195\n\t" \
  "v_mfma_f32_32x32x16_bf16 v[100:115], v[44:47], v[172:175], v[100:115]\n\t" \
  "v_mul_f32 v196, 0xbfb8aa3b, v128\n\t" \
  "v_mul_f32 v197, 0xbfb8aa3b, v129\n\t" \
  "v_mul_f32 v198, 0xbfb8aa3b, v130\n\t" \
  "v_mul_f32 v199, 0xbfb8aa3b, v131\n\t" \
  "v_exp_f32 v196, v196\n\t" \
  "v_exp_f32 v197, v197\n\t" \
  "v_exp_f32 v198, v198\n\t" \
  "v_exp_f32 v199, v199\n\t" \
  "v_mfma_f32_32x32x16_bf16 v[100:115], v[88:91], v[164:167], v[100:115]\n\t" \
  "v_mul_f32 v192, v124, v192\n\t" \
  "v_mul_f32 v193, v125, v193\n\t" \
  "v_mul_f32 v194, v126, v194\n\t" \
  "v_mul_f32 v195, v127, v195\n\t" \
  "ds_write_b128 v243, v[184:187] offset:0\n\t" \
  "v_add_f32 v196, 1.0, v196\n\t" \
  "v_add_f32 v197, 1.0, v197\n\t" \
  "v_add_f32 v198, 1.0, v198\n\t" \
  "v_add_f32 v199, 1.0, v199\n\t" \
  "v_cvt_pk_bf16_f32 v188, v192, v193\n\t" \
  "v_cvt_pk_bf16_f32 v189, v194, v195\n\t" \
  "s_waitcnt lgkmcnt(8)\n\t" \
  "v_mfma_f32_32x32x16_bf16 v[52:67], v[200:203], v[176:179], v[52:67]\n\t" \
  "v_rcp_f32 v196, v196\n\t" \
  "v_rcp_f32 v197, v197\n\t" \
  "v_rcp_f32 v198, v198\n\t" \
  "v_rcp_f32 v199, v199\n\t" \
  "s_waitcnt lgkmcnt(6)\n\t" \
  "v_mfma_f32_32x32x16_bf16 v[52:67], v[204:207], v[180:183], v[52:67]\n\t" \
  "v_mul_f32 v192, 0xbfb8aa3b, v100\n\t" \
  "v_mul_f32 v193, 0xbfb8aa3b, v101\n\t" \
  "v_mul_f32 v194, 0xbfb8aa3b, v102\n\t" \
  "v_mul_f32 v195, 0xbfb8aa3b, v103\n\t" \
  "v_exp_f32 v192, v192\n\t" \
  "v_exp_f32 v193, v193\n\t" \
  "v_exp_f32 v194, v194\n\t" \
  "v_exp_f32 v195, v195\n\t" \
  "s_cmp_eq_u32 %[nt], 0\n\t" \
  "v_mfma_f32_16x16x32_bf16 v[132:135], v[140:143], v[184:187], v[132:135]\n\t" \
  "v_mul_f32 v196, v128, v196\n\t" \
  "v_mul_f32 v197, v129, v197\n\t" \
  "v_mul_f32 v198, v130, v198\n\t" \
  "v_mul_f32 v199, v131, v199\n\t" \
  "v_add_f32 v192, 1.0, v192\n\t" \
  "v_add_f32 v193, 1.0, v193\n\t" \
  "v_add_f32 v194, 1.0, v194\n\t" \
  "v_add_f32 v195, 1.0, v195\n\t" \
  "v_cvt_pk_bf16_f32 v190, v196, v197\n\t" \
  "v_cvt_pk_bf16_f32 v191, v198, v199\n\t" \
  "s_cbranch_scc0 1b\n\t" \
  "s_waitcnt lgkmcnt(0)\n\t" \
  "s_cmp_lg_u32 %[last], 0\n\t" \
  "s_cbranch_scc1 8f\n\t" \
  "ds_write_b128 v243, v[188:191] offset:16\n\t" \
  "ds_read_b64_tr_b16 v[200:201], v244 offset:0\n\t" \
  "ds_read_b64_tr_b16 v[202:203], v244 offset:320\n\t" \
  "ds_read_b64_tr_b16 v[204:205], v244 offset:1280\n\t" \
  "ds_read_b64_tr_b16 v[206:207], v244 offset:1600\n\t" \
  "v_mfma_f32_16x16x32_bf16 v[132:135], v[144:147], v[188:191], v[132:135]\n\t" \
  "s_waitcnt lgkmcnt(0)\n\t" \
  "v_mfma_f32_32x32x16_bf16 v[68:83], v[200:203], v[176:179], v[68:83]\n\t" \
  "v_mfma_f32_32x32x16_bf16 v[68:83], v[204:207], v[180:183], v[68:83]\n\t" \
  "s_nop 8\n\t" \
  "v_mul_f32 v230, v230, v132\n\t" \
  "v_mul_f32 v231, v231, v132\n\t" \
  "v_cvt_pk_bf16_f32 v230, v230, v231\n\t" \
  "ds_write_b16 v245, v230 offset:7456\n\t" \
  "ds_write_b16_d16_hi v241, v230 offset:7584\n\t" \
  "ds_read_b128 v[200:203], v246 offset:7456\n\t" \
  "ds_read_b128 v[208:211], v246 offset:7488\n\t" \
  "s_waitcnt lgkmcnt(0)\n\t" \
  "v_mfma_f32_16x16x32_bf16 v[84:87], v[200:203], v[176:179], v[84:87]\n\t" \
  "v_mfma_f32_16x16x32_bf16 v[84:87], v[208:211], v[180:183], v[84:87]\n\t" \
  "s_branch 9f\n\t" \
  "6:\n\t" \
  "s_cmp_lg_u32 %[last], 0\n\t" \
  "s_cbranch_scc0 9f\n\t" \
  "s_waitcnt lgkmcnt(0)\n\t" \
  "v_cndmask_b32 v224, 0, 24, vcc\n\t" \
  "v_xor_b32 v220, v234, v224\n\t" \
  "v_xor_b32 v221, v235, v224\n\t" \
  "v_xor_b32 v222, v236, v224\n\t" \
  "v_xor_b32 v223, v237, v224\n\t" \
  "v_xor_b32 v216, 16, v220\n\t" \
  "v_xor_b32 v217, 16, v221\n\t" \
  "v_xor_b32 v218, 16, v222\n\t" \
  "v_xor_b32 v219, 16, v223\n\t" \
  "ds_read_b64 v[148:149], v220\n\t" \
  "ds_read_b64 v[150:151], v216\n\t" \
  "ds_read_b64 v[152:153], v221\n\t" \
  "ds_read_b64 v[154:155], v217\n\t" \
  "ds_read_b64 v[156:157], v222\n\t" \
  "ds_read_b64 v[158:159], v218\n\t" \
  "ds_read_b64 v[160:161], v223\n\t" \
  "ds_read_b64 v[162:163], v219\n\t" \
  "ds_read_b128 v[4:7], v247 offset:6144\n\t" \
  "ds_read_b128 v[8:11], v247 offset:7168\n\t" \
  "ds_read_b128 v[12:15], v247 offset:8192\n\t" \
  "ds_read_b128 v[16:19], v247 offset:9216\n\t" \
  "ds_read_b128 v[20:23], v247 offset:10240\n\t" \
  "ds_read_b128 v[24:27], v247 offset:11264\n\t" \
  "v_subrev_u32 v225, %[b1], v241\n\t" \
  "v_lshl_add_u32 v225, v225, 5, v243\n\t" \
  "v_sub_u32 v225, v225, v224\n\t" \
  "s_nop 4\n\t" \
  "v_cvt_pk_bf16_f32 v184, v52, v53\n\t" \
  "v_cvt_pk_bf16_f32 v185, v54, v55\n\t" \
  "v_cvt_pk_bf16_f32 v186, v56, v57\n\t" \
  "v_cvt_pk_bf16_f32 v187, v58, v59\n\t" \
  "v_cvt_pk_bf16_f32 v188, v60, v61\n\t" \
  "v_cvt_pk_bf16_f32 v189, v62, v63\n\t" \
  "v_cvt_pk_bf16_f32 v190, v64, v65\n\t" \
  "v_cvt_pk_bf16_f32 v191, v66, v67\n\t" \
  "v_cvt_pk_bf16_f32 v36, v68, v69\n\t" \
  "v_cvt_pk_bf16_f32 v37, v70, v71\n\t" \
  "v_cvt_pk_bf16_f32 v38, v72, v73\n\t" \
  "v_cvt_pk_bf16_f32 v39, v74, v75\n\t" \
  "v_cvt_pk_bf16_f32 v40, v76, v77\n\t" \
  "v_cvt_pk_bf16_f32 v41, v78, v79\n\t" \
  "v_cvt_pk_bf16_f32 v42, v80, v81\n\t" \
  "v_cvt_pk_bf16_f32 v43, v82, v83\n\t" \
  "s_waitcnt lgkmcnt(5)\n\t" \
  "v_mfma_f32_32x32x16_bf16 v[100:115], v[4:7], v[148:151], 0\n\t" \
  "s_waitcnt lgkmcnt(4)\n\t" \
  "v_mfma_f32_32x32x16_bf16 v[116:131], v[8:11], v[148:151], 0\n\t" \
  "ds_read_b128 v[28:31], v247 offset:12288\n\t" \
  "ds_read_b128 v[32:35], v247 offset:13312\n\t" \
  "s_waitcnt lgkmcnt(5)\n\t" \
  "v_mfma_f32_32x32x16_bf16 v[100:115], v[12:15], v[152:155], v[100:115]\n\t" \
  "s_waitcnt lgkmcnt(4)\n\t" \
  "v_mfma_f32_32x32x16_bf16 v[116:131], v[16:19], v[152:155], v[116:131]\n\t" \
  "ds_read_b128 v[4:7], v247 offset:14336\n\t" \
  "ds_read_b128 v[8:11], v247 offset:15360\n\t" \
  "s_waitcnt lgkmcnt(5)\n\t" \
  "v_mfma_f32_32x32x16_bf16 v[100:115], v[20:23], v[156:159], v[100:115]\n\t" \
  "s_waitcnt lgkmcnt(4)\n\t" \
  "v_mfma_f32_32x32x16_bf16 v[116:131], v[24:27], v[156:159], v[116:131]\n\t" \
  "ds_read_b128 v[12:15], v247 offset:16384\n\t" \
  "ds_read_b128 v[16:19], v247 offset:17408\n\t" \
  "s_waitcnt lgkmcnt(5)\n\t" \
  "v_mfma_f32_32x32x16_bf16 v[100:115], v[28:31], v[160:163], v[100:115]\n\t" \
  "s_waitcnt lgkmcnt(4)\n\t" \
  "v_mfma_f32_32x32x16_bf16 v[116:131], v[32:35], v[160:163], v[116:131]\n\t" \
  "ds_read_b128 v[20:23], v247 offset:18432\n\t" \
  "ds_read_b128 v[24:27], v247 offset:19456\n\t" \
  "s_waitcnt lgkmcnt(5)\n\t" \
  "v_mfma_f32_32x32x16_bf16 v[100:115], v[4:7], v[184:187], v[100:115]\n\t" \
  "ds_read_b128 v[28:31], v247 offset:20480\n\t" \
  "ds_read_b128 v[32:35], v247 offset:21504\n\t" \
  "s_waitcnt lgkmcnt(5)\n\t" \
  "v_mfma_f32_32x32x16_bf16 v[100:115], v[12:15], v[188:191], v[100:115]\n\t" \
  "s_waitcnt lgkmcnt(3)\n\t" \
  "v_mfma_f32_32x32x16_bf16 v[100:115], v[20:23], v[36:39], v[100:115]\n\t" \
  "s_waitcnt lgkmcnt(1)\n\t" \
  "v_mfma_f32_32x32x16_bf16 v[100:115], v[28:31], v[40:43], v[100:115]\n\t" \
  "s_waitcnt lgkmcnt(0)\n\t" \
  "s_branch 10f\n\t" \
  "8:\n\t" \
  "ds_write_b128 v243, v[188:191] offset:16\n\t" \
  "ds_read_b64_tr_b16 v[200:201], v244 offset:0\n\t" \
  "ds_read_b64_tr_b16 v[202:203], v244 offset:320\n\t" \
  "ds_read_b64_tr_b16 v[204:205], v244 offset:1280\n\t" \
  "ds_read_b64_tr_b16 v[206:207], v244 offset:1600\n\t" \
  "v_mfma_f32_16x16x32_bf16 v[132:135], v[144:147], v[188:191], v[132:135]\n\t" \
  "v_cndmask_b32 v224, 0, 24, vcc\n\t" \
  "v_xor_b32 v220, v234, v224\n\t" \
  "v_xor_b32 v221, v235, v224\n\t" \
  "v_xor_b32 v222, v236, v224\n\t" \
  "v_xor_b32 v223, v237, v224\n\t" \
  "v_xor_b32 v216, 16, v220\n\t" \
  "v_xor_b32 v217, 16, v221\n\t" \
  "v_xor_b32 v218, 16, v222\n\t" \
  "v_xor_b32 v219, 16, v223\n\t" \
  "ds_read_b64 v[148:149], v220\n\t" \
  "ds_read_b64 v[150:151], v216\n\t" \
  "ds_read_b64 v[152:153], v221\n\t" \
  "ds_read_b64 v[154:155], v217\n\t" \
  "ds_read_b64 v[156:157], v222\n\t" \
  "ds_read_b64 v[158:159], v218\n\t" \
  "ds_read_b64 v[160:161], v223\n\t" \
  "ds_read_b64 v[162:163], v219\n\t" \
  "ds_read_b128 v[4:7], v247 offset:6144\n\t" \
  "ds_read_b128 v[8:11], v247 offset:7168\n\t" \
  "s_waitcnt lgkmcnt(11)\n\t" \
  "ds_read_b128 v[12:15], v247 offset:8192\n\t" \
  "ds_read_b128 v[16:19], v247 offset:9216\n\t" \
  "ds_read_b128 v[20:23], v247 offset:10240\n\t" \
  "ds_read_b128 v[24:27], v247 offset:11264\n\t" \
  "v_subrev_u32 v225, %[b1], v241\n\t" \
  "v_lshl_add_u32 v225, v225, 5, v243\n\t" \
  "v_sub_u32 v225, v225, v224\n\t" \
  "v_mfma_f32_32x32x16_bf16 v[68:83], v[200:203], v[176:179], v[68:83]\n\t" \
  "s_waitcnt lgkmcnt(5)\n\t" \
  "v_mfma_f32_32x32x16_bf16 v[100:115], v[4:7], v[148:151], 0\n\t" \
  "v_mfma_f32_32x32x16_bf16 v[68:83], v[204:207], v[180:183], v[68:83]\n\t" \
  "s_waitcnt lgkmcnt(4)\n\t" \
  "v_mfma_f32_32x32x16_bf16 v[116:131], v[8:11], v[148:151], 0\n\t" \
  "ds_read_b128 v[28:31], v247 offset:12288\n\t" \
  "ds_read_b128 v[32:35], v247 offset:13312\n\t" \
  "s_waitcnt lgkmcnt(5)\n\t" \
  "v_mfma_f32_32x32x16_bf16 v[100:115], v[12:15], v[152:155], v[100:115]\n\t" \
  "v_mul_f32 v230, v230, v132\n\t" \
  "v_mul_f32 v231, v231, v132\n\t" \
  "v_cvt_pk_bf16_f32 v230, v230, v231\n\t" \
  "ds_write_b16 v245, v230 offset:7456\n\t" \
  "ds_write_b16_d16_hi v241, v230 offset:7584\n\t" \
  "ds_read_b128 v[200:203], v246 offset:7456\n\t" \
  "ds_read_b128 v[208:211], v246 offset:7488\n\t" \
  "v_cvt_pk_bf16_f32 v184, v52, v53\n\t" \
  "v_cvt_pk_bf16_f32 v185, v54, v55\n\t" \
  "v_cvt_pk_bf16_f32 v186, v56, v57\n\t" \
  "v_cvt_pk_bf16_f32 v187, v58, v59\n\t" \
  "s_waitcnt lgkmcnt(8)\n\t" \
  "v_mfma_f32_32x32x16_bf16 v[116:131], v[16:19], v[152:155], v[116:131]\n\t" \
  "ds_read_b128 v[4:7], v247 offset:14336\n\t" \
  "ds_read_b128 v[8:11], v247 offset:15360\n\t" \
  "s_waitcnt lgkmcnt(9)\n\t" \
  "v_mfma_f32_32x32x16_bf16 v[100:115], v[20:23], v[156:159], v[100:115]\n\t" \
  "v_cvt_pk_bf16_f32 v188, v60, v61\n\t" \
  "v_cvt_pk_bf16_f32 v189, v62, v63\n\t" \
  "v_cvt_pk_bf16_f32 v190, v64, v65\n\t" \
  "v_cvt_pk_bf16_f32 v191, v66, v67\n\t" \
  "s_waitcnt lgkmcnt(8)\n\t" \
  "v_mfma_f32_32x32x16_bf16 v[116:131], v[24:27], v[156:159], v[116:131]\n\t" \
  "ds_read_b128 v[12:15], v247 offset:16384\n\t" \
  "ds_read_b128 v[16:19], v247 offset:17408\n\t" \
  "s_waitcnt lgkmcnt(9)\n\t" \
  "v_mfma_f32_32x32x16_bf16 v[100:115], v[28:31], v[160:163], v[100:115]\n\t" \
  "v_cvt_pk_bf16_f32 v36, v68, v69\n\t" \
  "v_cvt_pk_bf16_f32 v37, v70, v71\n\t" \
  "v_cvt_pk_bf16_f32 v38, v72, v73\n\t" \
  "v_cvt_pk_bf16_f32 v39, v74, v75\n\t" \
  "s_waitcnt lgkmcnt(5)\n\t" \
  "v_mfma_f32_16x16x32_bf16 v[84:87], v[200:203], v[176:179], v[84:87]\n\t" \
  "v_cvt_pk_bf16_f32 v40, v76, v77\n\t" \
  "v_cvt_pk_bf16_f32 v41, v78, v79\n\t" \
  "v_cvt_pk_bf16_f32 v42, v80, v81\n\t" \
  "v_cvt_pk_bf16_f32 v43, v82, v83\n\t" \
  "v_mfma_f32_32x32x16_bf16 v[116:131], v[32:35], v[160:163], v[116:131]\n\t" \
  "ds_read_b128 v[20:23], v247 offset:18432\n\t" \
  "ds_read_b128 v[24:27], v247 offset:19456\n\t" \
  "s_waitcnt lgkmcnt(6)\n\t" \
  "v_mfma_f32_16x16x32_bf16 v[84:87], v[208:211], v[180:183], v[84:87]\n\t" \
  "s_waitcnt lgkmcnt(5)\n\t" \
  "v_mfma_f32_32x32x16_bf16 v[100:115], v[4:7], v[184:187], v[100:115]\n\t" \
  "ds_read_b128 v[28:31], v247 offset:20480\n\t" \
  "ds_read_b128 v[32:35], v247 offset:21504\n\t" \
  "s_waitcnt lgkmcnt(5)\n\t" \
  "v_mfma_f32_32x32x16_bf16 v[100:115], v[12:15], v[188:191], v[100:115]\n\t" \
  "s_waitcnt lgkmcnt(3)\n\t" \
  "v_mfma_f32_32x32x16_bf16 v[100:115], v[20:23], v[36:39], v[100:115]\n\t" \
  "s_waitcnt lgkmcnt(1)\n\t" \
  "v_mfma_f32_32x32x16_bf16 v[100:115], v[28:31], v[40:43], v[100:115]\n\t" \
  "s_waitcnt lgkmcnt(0)\n\t" \
  "10:\n\t" \
  "v_mov_b32 v212, 0xbfb8aa3b\n\t" \
  "v_mfma_f32_32x32x16_bf16 v[116:131], v[8:11], v[184:187], v[116:131]\n\t" \
  "v_mfma_f32_32x32x16_bf16 v[116:131], v[16:19], v[188:191], v[116:131]\n\t" \
  "s_nop 8\n\t" \
  "v_mul_f32 v192, 0xbfb8aa3b, v100\n\t" \
  "v_mul_f32 v193, 0xbfb8aa3b, v101\n\t" \
  "v_mul_f32 v194, 0xbfb8aa3b, v102\n\t" \
  "v_mul_f32 v195, 0xbfb8aa3b, v103\n\t" \
  "v_exp_f32 v192, v192\n\t" \
  "v_exp_f32 v193, v193\n\t" \
  "v_exp_f32 v194, v194\n\t" \
  "v_exp_f32 v195, v195\n\t" \
  "v_lshlrev_b32 v200, 16, v148\n\t" \
  "v_and_b32 v201, 0xffff0000, v148\n\t" \
  "v_lshlrev_b32 v202, 16, v149\n\t" \
  "v_and_b32 v203, 0xffff0000, v149\n\t" \
  "v_mfma_f32_32x32x16_bf16 v[116:131], v[24:27], v[36:39], v[116:131]\n\t" \
  "v_add_f32 v192, 1.0, v192\n\t" \
  "v_add_f32 v193, 1.0, v193\n\t" \
  "v_add_f32 v194, 1.0, v194\n\t" \
  "v_add_f32 v195, 1.0, v195\n\t" \
  "v_rcp_f32 v192, v192\n\t" \
  "v_rcp_f32 v193, v193\n\t" \
  "v_rcp_f32 v194, v194\n\t" \
  "v_rcp_f32 v195, v195\n\t" \
  "v_mfma_f32_32x32x16_bf16 v[116:131], v[32:35], v[40:43], v[116:131]\n\t" \
  "v_mul_f32 v196, 0xbfb8aa3b, v104\n\t" \
  "v_mul_f32 v197, 0xbfb8aa3b, v105\n\t" \
  "v_mul_f32 v198, 0xbfb8aa3b, v106\n\t" \
  "v_mul_f32 v199, 0xbfb8aa3b, v107\n\t" \
  "v_exp_f32 v196, v196\n\t" \
  "v_exp_f32 v197, v197\n\t" \
  "v_exp_f32 v198, v198\n\t" \
  "v_exp_f32 v199, v199\n\t" \
  "v_fmac_f32 v200, v100, v192\n\t" \
  "v_fmac_f32 v201, v101, v193\n\t" \
  "v_fmac_f32 v202, v102, v194\n\t" \
  "v_fmac_f32 v203, v103, v195\n\t" \
  "v_lshlrev_b32 v204, 16, v150\n\t" \
  "v_and_b32 v205, 0xffff0000, v150\n\t" \
  "v_lshlrev_b32 v206, 16, v151\n\t" \
  "v_and_b32 v207, 0xffff0000, v151\n\t" \
  "v_add_f32 v196, 1.0, v196\n\t" \
  "v_add_f32 v197, 1.0, v197\n\t" \
  "v_add_f32 v198, 1.0, v198\n\t" \
  "v_add_f32 v199, 1.0, v199\n\t" \
  "v_cvt_pk_bf16_f32 v176, v200, v201\n\t" \
  "v_cvt_pk_bf16_f32 v177, v202, v203\n\t" \
  "ds_write_b64 v225, v[176:177] offset:0\n\t" \
  "v_rcp_f32 v196, v196\n\t" \
  "v_rcp_f32 v197, v197\n\t" \
  "v_rcp_f32 v198, v198\n\t" \
  "v_rcp_f32 v199, v199\n\t" \
  "v_mul_f32 v192, 0xbfb8aa3b, v108\n\t" \
  "v_mul_f32 v193, 0xbfb8aa3b, v109\n\t" \
  "v_mul_f32 v194, 0xbfb8aa3b, v110\n\t" \
  "v_mul_f32 v195, 0xbfb8aa3b, v111\n\t" \
  "v_exp_f32 v192, v192\n\t" \
  "v_exp_f32 v193, v193\n\t" \
  "v_exp_f32 v194, v194\n\t" \
  "v_exp_f32 v195, v195\n\t" \
  "v_fmac_f32 v204, v104, v196\n\t" \
  "v_fmac_f32 v205, v105, v197\n\t" \
  "v_fmac_f32 v206, v106, v198\n\t" \
  "v_fmac_f32 v207, v107, v199\n\t" \
  "v_lshlrev_b32 v200, 16, v152\n\t" \
  "v_and_b32 v201, 0xffff0000, v152\n\t" \
  "v_lshlrev_b32 v202, 16, v153\n\t" \
  "v_and_b32 v203, 0xffff0000, v153\n\t" \
  "v_add_f32 v192, 1.0, v192\n\t" \
  "v_add_f32 v193, 1.0, v193\n\t" \
  "v_add_f32 v194, 1.0, v194\n\t" \
  "v_add_f32 v195, 1.0, v195\n\t" \
  "v_cvt_pk_bf16_f32 v178, v204, v205\n\t" \
  "v_cvt_pk_bf16_f32 v179, v206, v207\n\t" \
  "ds_write_b64 v225, v[178:179] offset:16\n\t" \
  "v_rcp_f32 v192, v192\n\t" \
  "v_rcp_f32 v193, v193\n\t" \
  "v_rcp_f32 v194, v194\n\t" \
  "v_rcp_f32 v195, v195\n\t" \
  "v_mul_f32 v196, 0xbfb8aa3b, v112\n\t" \
  "v_mul_f32 v197, 0xbfb8aa3b, v113\n\t" \
  "v_mul_f32 v198, 0xbfb8aa3b, v114\n\t" \
  "v_mul_f32 v199, 0xbfb8aa3b, v115\n\t" \
  "v_exp_f32 v196, v196\n\t" \
  "v_exp_f32 v197, v197\n\t" \
  "v_exp_f32 v198, v198\n\t" \
  "v_exp_f32 v199, v199\n\t" \
  "v_fmac_f32 v200, v108, v192\n\t" \
  "v_fmac_f32 v201, v109, v193\n\t" \
  "v_fmac_f32 v202, v110, v194\n\t" \
  "v_fmac_f32 v203, v111, v195\n\t" \
  "v_lshlrev_b32 v204, 16, v154\n\t" \
  "v_and_b32 v205, 0xffff0000, v154\n\t" \
  "v_lshlrev_b32 v206, 16, v155\n\t" \
  "v_and_b32 v207, 0xffff0000, v155\n\t" \
  "v_add_f32 v196, 1.0, v196\n\t" \
  "v_add_f32 v197, 1.0, v197\n\t" \
  "v_add_f32 v198, 1.0, v198\n\t" \
  "v_add_f32 v199, 1.0, v199\n\t" \
  "v_cvt_pk_bf16_f32 v180, v200, v201\n\t" \
  "v_cvt_pk_bf16_f32 v181, v202, v203\n\t" \
  "ds_write_b64 v225, v[180:181] offset:32\n\t" \
  "v_rcp_f32 v196, v196\n\t" \
  "v_rcp_f32 v197, v197\n\t" \
  "v_rcp_f32 v198, v198\n\t" \
  "v_rcp_f32 v199, v199\n\t" \
  "v_mul_f32 v192, 0xbfb8aa3b, v116\n\t" \
  "v_mul_f32 v193, 0xbfb8aa3b, v117\n\t" \
  "v_mul_f32 v194, 0xbfb8aa3b, v118\n\t" \
  "v_mul_f32 v195, 0xbfb8aa3b, v119\n\t" \
  "v_exp_f32 v192, v192\n\t" \
  "v_exp_f32 v193, v193\n\t" \
  "v_exp_f32 v194, v194\n\t" \
  "v_exp_f32 v195, v195\n\t" \
  "v_fmac_f32 v204, v112, v196\n\t" \
  "v_fmac_f32 v205, v113, v197\n\t" \
  "v_fmac_f32 v206, v114, v198\n\t" \
  "v_fmac_f32 v207, v115, v199\n\t" \
  "v_lshlrev_b32 v200, 16, v156\n\t" \
  "v_and_b32 v201, 0xffff0000, v156\n\t" \
  "v_lshlrev_b32 v202, 16, v157\n\t" \
  "v_and_b32 v203, 0xffff0000, v157\n\t" \
  "v_add_f32 v192, 1.0, v192\n\t" \
  "v_add_f32 v193, 1.0, v193\n\t" \
  "v_add_f32 v194, 1.0, v194\n\t" \
  "v_add_f32 v195, 1.0, v195\n\t" \
  "v_cvt_pk_bf16_f32 v182, v204, v205\n\t" \
  "v_cvt_pk_bf16_f32 v183, v206, v207\n\t" \
  "ds_write_b64 v225, v[182:183] offset:48\n\t" \
  "v_rcp_f32 v192, v192\n\t" \
  "v_rcp_f32 v193, v193\n\t" \
  "v_rcp_f32 v194, v194\n\t" \
  "v_rcp_f32 v195, v195\n\t" \
  "v_mul_f32 v196, 0xbfb8aa3b, v120\n\t" \
  "v_mul_f32 v197, 0xbfb8aa3b, v121\n\t" \
  "v_mul_f32 v198, 0xbfb8aa3b, v122\n\t" \
  "v_mul_f32 v199, 0xbfb8aa3b, v123\n\t" \
  "v_exp_f32 v196, v196\n\t" \
  "v_exp_f32 v197, v197\n\t" \
  "v_exp_f32 v198, v198\n\t" \
  "v_exp_f32 v199, v199\n\t" \
  "v_fmac_f32 v200, v116, v192\n\t" \
  "v_fmac_f32 v201, v117, v193\n\t" \
  "v_fmac_f32 v202, v118, v194\n\t" \
  "v_fmac_f32 v203, v119, v195\n\t" \
  "v_lshlrev_b32 v204, 16, v158\n\t" \
  "v_and_b32 v205, 0xffff0000, v158\n\t" \
  "v_lshlrev_b32 v206, 16, v159\n\t" \
  "v_and_b32 v207, 0xffff0000, v159\n\t" \
  "v_add_f32 v196, 1.0, v196\n\t" \
  "v_add_f32 v197, 1.0, v197\n\t" \
  "v_add_f32 v198, 1.0, v198\n\t" \
  "v_add_f32 v199, 1.0, v199\n\t" \
  "v_cvt_pk_bf16_f32 v176, v200, v201\n\t" \
  "v_cvt_pk_bf16_f32 v177, v202, v203\n\t" \
  "ds_write_b64 v225, v[176:177] offset:64\n\t" \
  "v_rcp_f32 v196, v196\n\t" \
  "v_rcp_f32 v197, v197\n\t" \
  "v_rcp_f32 v198, v198\n\t" \
  "v_rcp_f32 v199, v199\n\t" \
  "v_mul_f32 v192, 0xbfb8aa3b, v124\n\t" \
  "v_mul_f32 v193, 0xbfb8aa3b, v125\n\t" \
  "v_mul_f32 v194, 0xbfb8aa3b, v126\n\t" \
  "v_mul_f32 v195, 0xbfb8aa3b, v127\n\t" \
  "v_exp_f32 v192, v192\n\t" \
  "v_exp_f32 v193, v193\n\t" \
  "v_exp_f32 v194, v194\n\t" \
  "v_exp_f32 v195, v195\n\t" \
  "v_fmac_f32 v204, v120, v196\n\t" \
  "v_fmac_f32 v205, v121, v197\n\t" \
  "v_fmac_f32 v206, v122, v198\n\t" \
  "v_fmac_f32 v207, v123, v199\n\t" \
  "v_lshlrev_b32 v200, 16, v160\n\t" \
  "v_and_b32 v201, 0xffff0000, v160\n\t" \
  "v_lshlrev_b32 v202, 16, v161\n\t" \
  "v_and_b32 v203, 0xffff0000, v161\n\t" \
  "v_add_f32 v192, 1.0, v192\n\t" \
  "v_add_f32 v193, 1.0, v193\n\t" \
  "v_add_f32 v194, 1.0, v194\n\t" \
  "v_add_f32 v195, 1.0, v195\n\t" \
  "v_cvt_pk_bf16_f32 v178, v204, v205\n\t" \
  "v_cvt_pk_bf16_f32 v179, v206, v207\n\t" \
  "ds_write_b64 v225, v[178:179] offset:80\n\t" \
  "v_rcp_f32 v192, v192\n\t" \
  "v_rcp_f32 v193, v193\n\t" \
  "v_rcp_f32 v194, v194\n\t" \
  "v_rcp_f32 v195, v195\n\t" \
  "v_mul_f32 v196, 0xbfb8aa3b, v128\n\t" \
  "v_mul_f32 v197, 0xbfb8aa3b, v129\n\t" \
  "v_mul_f32 v198, 0xbfb8aa3b, v130\n\t" \
  "v_mul_f32 v199, 0xbfb8aa3b, v131\n\t" \
  "v_exp_f32 v196, v196\n\t" \
  "v_exp_f32 v197, v197\n\t" \
  "v_exp_f32 v198, v198\n\t" \
  "v_exp_f32 v199, v199\n\t" \
  "v_fmac_f32 v200, v124, v192\n\t" \
  "v_fmac_f32 v201, v125, v193\n\t" \
  "v_fmac_f32 v202, v126, v194\n\t" \
  "v_fmac_f32 v203, v127, v195\n\t" \
  "v_lshlrev_b32 v204, 16, v162\n\t" \
  "v_and_b32 v205, 0xffff0000, v162\n\t" \
  "v_lshlrev_b32 v206, 16, v163\n\t" \
  "v_and_b32 v207, 0xffff0000, v163\n\t" \
  "v_add_f32 v196, 1.0, v196\n\t" \
  "v_add_f32 v197, 1.0, v197\n\t" \
  "v_add_f32 v198, 1.0, v198\n\t" \
  "v_add_f32 v199, 1.0, v199\n\t" \
  "v_cvt_pk_bf16_f32 v180, v200, v201\n\t" \
  "v_cvt_pk_bf16_f32 v181, v202, v203\n\t" \
  "ds_write_b64 v225, v[180:181] offset:96\n\t" \
  "v_rcp_f32 v196, v196\n\t" \
  "v_rcp_f32 v197, v197\n\t" \
  "v_rcp_f32 v198, v198\n\t" \
  "v_rcp_f32 v199, v199\n\t" \
  "s_nop 0\n\t" \
  "v_fmac_f32 v204, v128, v196\n\t" \
  "v_fmac_f32 v205, v129, v197\n\t" \
  "v_fmac_f32 v206, v130, v198\n\t" \
  "v_fmac_f32 v207, v131, v199\n\t" \
  "v_cvt_pk_bf16_f32 v182, v204, v205\n\t" \
  "v_cvt_pk_bf16_f32 v183, v206, v207\n\t" \
  "ds_write_b64 v225, v[182:183] offset:112\n\t" \
  "9:\n\t" \
  "ds_write_b16 v233, v255 offset:5376\n\t" \
  "s_mov_b32 m0, %[keep]\n\t" \
  "s_setprio 0\n\t" \
  "s_nop 11\n\t" \
  ""
#define LOOP_ASM LOOP_ASM_FOLD

DI u32x4 tr_frag(unsigned p, int half) {
  const s16x4 lo = __builtin_amdgcn_ds_read_tr16_b64_v4i16((lds_s16x4_p)p);
  const s16x4 hi = __builtin_amdgcn_ds_read_tr16_b64_v4i16((lds_s16x4_p)(p + half));
  return __builtin_bit_cast(u32x4, __builtin_shufflevector(lo, hi, 0, 1, 2, 3, 4, 5, 6, 7));
}
DI u32x4 fold(u32x4 v) {
#pragma unroll
  for (int i = 0; i < 4; i++) v[i] = pk_bf16(bf2f(v[i] & 0xffffu) * -LOG2E, __uint_as_float(v[i] & 0xffff0000u) * -LOG2E);
  return v;
}

__global__ __launch_bounds__(512, 2) void egnn_kernel(const char* __restrict__ hmat, const char* __restrict__ pos,
                                                      const char* __restrict__ ea, const int* __restrict__ src,
                                                      const int* __restrict__ dst, const char* __restrict__ we,
                                                      const char* __restrict__ wx, const char* __restrict__ wh,
                                                      char* __restrict__ ws, uint16_t* __restrict__ out, int N, int E STAMP_ARG) {
  const int w = __builtin_amdgcn_readfirstlane(threadIdx.x >> 6);
  const int bkt = (blockIdx.x & 7) * 32 + (blockIdx.x >> 3);
  const int n0 = bkt * NB;
  STAMP(0);
  unsigned wl = L_WAVE + w * WSZ, mi = L_MI + w * MISZ;
  const auto rs_tab = __builtin_amdgcn_make_buffer_rsrc((void*)(ws + WS_TAB), 0, NBK * NBK * 8, 0x00020000);
  const auto rs_rec = __builtin_amdgcn_make_buffer_rsrc((void*)(ws + WS_REC), 0, NBK * CHS, 0x00020000);
  const auto rs_ea = __builtin_amdgcn_make_buffer_rsrc((void*)ea, 0, E * 32, 0x00020000);
  unsigned tag_lo, tag_hi;
  unsigned wdv0, wdv1;

  {
    const int lane = lane_id(), tid = w * 64 + lane, r = lane & 31, h = lane >> 5;
    const int e0 = bkt * CH;
    int ne = max(E - e0, 0);
    asm("" : "+s"(ne));
    ne = min(ne, CH);
    const auto rs_d = __builtin_amdgcn_make_buffer_rsrc((void*)(dst + e0), 0, ne * 4, 0x00020000);
    const auto rs_s = __builtin_amdgcn_make_buffer_rsrc((void*)(src + e0), 0, ne * 4, 0x00020000);
    unsigned d[7], sv[7];
    u32x2 xa, xb;
    unsigned long long ep;
    asm volatile("s_load_dwordx2 %0, %1, 0x0" : "=s"(ep) : "s"(ws + WS_EPOCH) : "memory");
#pragma unroll
    for (int i = 0; i < 7; i++)
      asm volatile("buffer_load_dword %0, %2, %3, %5 offen nt\n\tbuffer_load_dword %1, %2, %4, %5 offen nt"
                   : "=&v"(d[i]), "=&v"(sv[i]) : "v"(tid * 4), "s"(rs_d), "s"(rs_s), "s"(i * 2048) : "memory");
    LDS(unsigned, L_CNT + tid * 4) = 0;
    asm volatile("s_waitcnt lgkmcnt(0)\n\ts_barrier" : "+s"(ep) :: "memory");
    asm volatile("global_load_ushort %0, %4, %5\n\tglobal_load_ushort %1, %4, %5 offset:64\n\t"
                 "global_load_dwordx2 %2, %6, %7\n\tglobal_load_dwordx2 %3, %6, %7 offset:16"
                 : "=&v"(wdv0), "=&v"(wdv1), "=&v"(xa), "=&v"(xb)
                 : "v"(r * 2), "s"(we + 128 * 128), "v"(h * 8), "s"(wx + ((w + 2) & 3) * 32) : "memory");
    {
      const int j = lane >> 3, jl = (j & 3) * 128 + (lane & 7) * 16, jh = (j >> 2) * 128;
      const char* const h0 = wh + (w < 4 ? 16 * w : 64 + 32 * ((w - 4) >> 1) + 4 * (w & 1)) * 128;
      const char* const e0p = we + (w < 4 ? 64 + 16 * w : 16 * (w - 4)) * 128;
      const unsigned oh = jl + jh * (w < 4 ? 8 : 16), oe = jl + jh * (w < 4 ? 4 : 8);
      unsigned keep;
      asm volatile("s_mov_b32 %0, m0\n\ts_mov_b32 m0, %9\n\ts_nop 0\n\t"
                   "global_load_lds_dwordx4 %1, %4\n\tglobal_load_lds_dwordx4 %1, %5 offset:1024\n\t"
                   "global_load_lds_dwordx4 %2, %6 offset:2048\n\tglobal_load_lds_dwordx4 %2, %7 offset:3072\n\t"
                   "s_add_u32 m0, %9, 0x1000\n\ts_nop 0\n\tglobal_load_lds_dwordx4 %3, %8\n\ts_mov_b32 m0, %0"
                   : "=&s"(keep)
                   : "v"(oh), "v"(oe), "v"(w < 2 ? (lane >> 2) * 128 + (lane & 3) * 16 : 0),
                     "s"(h0), "s"(h0 + (w < 4 ? 4 : 8) * 128 - 1024), "s"(e0p - 2048), "s"(e0p + (w < 4 ? 8 : 4) * 128 - 3072),
                     "s"(we + 129 * 128 + (w & 1) * 64), "s"(wl)
                   : "memory", "scc");
    }
    unsigned ra[5], keep;
    {
      const unsigned cc0 = ((lane & 7) ^ (lane >> 4)) * 16, cc1 = ((lane & 7) ^ (4 + (lane >> 4))) * 16;
      const int nr = n0 + w + (lane & 56);
      ra[0] = min(nr, N - 1) * 128 + cc0; ra[1] = min(nr + 64, N - 1) * 128 + cc1; ra[2] = min(nr + 128, N - 1) * 128 + cc0;
      ra[3] = min(n0 + w + 192, N - 1) * 128 + cc1; ra[4] = mul24(min(n0 + w + min(lane >> 1, 24) * 8, N - 1), 6) + (lane & 1) * 2;
      asm volatile("" : "+v"(ra[0]), "+v"(ra[1]), "+v"(ra[2]), "+v"(ra[3]), "+v"(ra[4]));
    }
    asm volatile("s_waitcnt vmcnt(9)" : "+v"(d[0]), "+v"(d[1]), "+v"(d[2]), "+v"(d[3]), "+v"(d[4]), "+v"(d[5]), "+v"(d[6]),
                 "+v"(sv[0]), "+v"(sv[1]), "+v"(sv[2]), "+v"(sv[3]), "+v"(sv[4]), "+v"(sv[5]), "+v"(sv[6]) :: "memory");
    STAMP(10);
    {
      asm volatile("s_mov_b32 %0, m0\n\t"
                   "s_mov_b32 m0, %8\n\ts_nop 0\n\tglobal_load_lds_dwordx4 %1, %6\n\t"
                   "s_add_u32 m0, %8, 0x400\n\ts_nop 0\n\tglobal_load_lds_dwordx4 %2, %6\n\t"
                   "s_add_u32 m0, %8, 0x800\n\ts_nop 0\n\tglobal_load_lds_dwordx4 %3, %6\n\t"
                   "s_add_u32 m0, %8, 0xc00\n\ts_nop 0\n\tglobal_load_lds_dwordx4 %4, %6\n\t"
                   "s_add_u32 m0, %8, 0x1000\n\ts_nop 0\n\tglobal_load_lds_dword %5, %7\n\ts_mov_b32 m0, %0"
                   : "=&s"(keep)
                   : "v"(ra[0]), "v"(ra[1]), "v"(ra[2]), "v"(ra[3]), "v"(ra[4]), "s"(hmat), "s"(pos), "s"(mi)
                   : "memory", "scc");
    }
    unsigned ent, total;
    auto sort = [&](auto NS_, auto FULL_) __attribute__((always_inline)) {
      constexpr int NS = decltype(NS_)::value;
      constexpr bool FULL = decltype(FULL_)::value;
      unsigned bk[NS], rank[NS];
      u32x2 rec[NS];
#pragma unroll
      for (int i = 0; i < NS; i++) {
        const int el = i * 512 + tid;
        const unsigned b = mul24(d[i], 85599u) >> 24;
        const unsigned dl = d[i] - mul24(b, NB);
        bk[i] = (FULL || el < ne ? b : NBK + lane) * 4;
        rank[i] = __hip_atomic_fetch_add(&LDS(unsigned, L_CNT + bk[i]), 8u, __ATOMIC_RELAXED, __HIP_MEMORY_SCOPE_WORKGROUP);
        rec[i][0] = el;
        rec[i][1] = ((mul24(dl >> 3, 1272u) + dl) << 16) | sv[i] | 0x80000u;
      }
      LDS(unsigned, L_BASE + (NBK + lane) * 4) = 0x40000000u;
      barrier_lds();
      u32x4 cv = LDS(u32x4, L_CNT + lane * 16);
#pragma unroll
      for (int i = 0; i < 4; i++) cv[i] = (cv[i] + 8) & ~15u;
      const unsigned sum = cv[0] + cv[1] + cv[2] + cv[3], incl = wave_scan_incl(sum);
      const unsigned ex = incl - sum + L_REC;
      total = __builtin_amdgcn_readlane(incl, 63);
      const u32x4 bv = {ex, ex + cv[0], ex + cv[0] + cv[1], ex + cv[0] + cv[1] + cv[2]};
      LDS(u32x4, L_BASE + lane * 16) = bv;
      unsigned bs[NS];
#pragma unroll
      for (int i = 0; i < NS; i++) bs[i] = LDS(unsigned, L_BASE + bk[i]);
      ent = ((LDS(unsigned, L_BASE + (tid & 255) * 4) - L_REC) >> 4) | (LDS(unsigned, L_CNT + (tid & 255) * 4) << 9);
      const unsigned lim = L_REC + 8 * SEG + lane * 8;
#pragma unroll
      for (int i = 0; i < NS; i++) LDS(u32x2, FULL ? bs[i] + rank[i] : min(bs[i] + rank[i], lim)) = rec[i];
      barrier_lds();
    };
    if (ne == CH && w != 0) sort(std::integral_constant<int, 6>(), std::true_type());
    else sort(std::integral_constant<int, 7>(), std::false_type());
    LDS(unsigned, L_CNT + tid * 4) = 0;
    STAMP(11);
    tag_lo = (unsigned)(ep + 1) & 0xffu;
    tag_hi = (unsigned)((ep + 1) >> 8);
    {
      const u32x2 g = {ent | (tag_lo << 24), tag_hi};
      __builtin_amdgcn_raw_buffer_store_b64(g, rs_tab, tid < NBK ? bkt * (NBK * 8) + tid * 8 : 0x80000000u, 0, 16);
      const auto rs_out = __builtin_amdgcn_make_buffer_rsrc((void*)(ws + WS_REC + (size_t)bkt * CHS), 0, __builtin_amdgcn_readfirstlane(total), 0x00020000);
#pragma unroll
      for (int i = 0; i < 4; i++) {
        const u32x4 v = LDS(u32x4, L_REC + (i * 512 + tid) * 16);
        const u32x4 o = {v[1], v[3], v[0] | ((v[2] & 0xfffu) << 12) | (tag_lo << 24), tag_hi};
        __builtin_amdgcn_raw_buffer_store_b128(o, rs_out, (i * 512 + tid) * 16, 0, 16);
      }
    }
    STAMP(12);
    asm volatile("s_waitcnt vmcnt(10)" : "+v"(wdv0), "+v"(wdv1), "+v"(xa), "+v"(xb) :: "memory");
    {
      const unsigned tp = wl + (8 * h + ((lane >> 2) & 3)) * 128 + ((lane >> 4) & 1) * 32 + (lane & 3) * 8;
      u32x4 f[2], g[2];
#pragma unroll
      for (int t = 0; t < 2; t++) { f[t] = tr_frag(tp + 64 * t, 512); g[t] = tr_frag(tp + 2048 + 64 * t, 512); }
#pragma unroll
      for (int t = 0; t < 2; t++) {
        if (w < 4) { f[t] = fold(f[t]); g[t] = fold(g[t]); }
        LDS(u32x4, L_WH + (2 * w + t) * 1024 + lane * 16) = f[t];
        LDS(u32x4, wl + 2048 + t * 1024 + lane * 16) = g[t];
      }
      u32x4 x;
      if (w < 2) x = fold(tr_frag(wl + W_STE + (8 * h + ((lane >> 2) & 3)) * 64 + ((lane >> 4) & 1) * 32 + (lane & 3) * 8, 256));
      else {
        const unsigned m = (lane & 3) == 0 && ((lane >> 2) & 1) == ((lane >> 4) & 1) ? ~0u : 0u;
        x = (u32x4){xa[0] & m, xa[1] & m, xb[0] & m, xb[1] & m};
      }
      if (w < 6) LDS(u32x4, L_WJ + w * 1024 + lane * 16) = x;
    }
    if (tid >= 256 && tid < 256 + 26 * 4) {
      const int t = tid - 256, row = t >> 2, sx = (t >> 1) & 1, hh = t & 1;
      const int x = row < DUMMY ? row - 16 * sx - 4 * hh : -1;
      u32x4 v;
      v[0] = x == 0 ? 0x3f80u : x == 1 ? 0x3f800000u : 0u;  v[1] = x == 2 ? 0x3f80u : x == 3 ? 0x3f800000u : 0u;
      v[2] = x == 8 ? 0x3f80u : x == 9 ? 0x3f800000u : 0u;  v[3] = x == 10 ? 0x3f80u : x == 11 ? 0x3f800000u : 0u;
      LDS(u32x4, L_LUT + row * 80 + (t & 3) * 16) = v;
    }
    {
      const u32x4 z4 = {0u, 0u, 0u, 0u};
#pragma unroll
      for (int i = 0; i < 3; i++) { const int o = i * 1024 + lane * 16; if (o < 26 * 80 + 256) LDS(u32x4, wl + W_SI + o) = z4; }
    }
    barrier_lds();
    STAMP(14);
  }

  f32x16 Y0, Y1; f32x4 Yp = {0.f, 0.f, 0.f, 0.f};
#pragma unroll
  for (int i = 0; i < 16; i++) { Y0[i] = 0.f; Y1[i] = 0.f; }
  u32x32 W2; u32x16 PA; u32x8 W3; u32x4 WX0, WX1, WX2, WX3;
  int jbase = 0, part = -1, grp = w, gother = w;

  auto round = [&](bool first) __attribute__((always_inline)) -> bool {
    const int lane = lane_id(), tid = w * 64 + lane, r = lane & 31, h = lane >> 5;
    const unsigned zr = opaque(0);
    if (!first) { LDS(unsigned, L_CNT + tid * 4) = zr; barrier_lds(); }
    LDS(unsigned, L_BASE + lane * 4) = 0x40000000u;
    unsigned tb[4];
    {
      u32x2 t;
      for (int spin = 0;; spin++) {
        asm volatile("buffer_load_dwordx2 %0, %1, %2, %3 offen sc1\n\ts_waitcnt vmcnt(0)"
                     : "=&v"(t) : "v"((w * 32 + r) * (NBK * 8)), "s"(rs_tab), "s"(bkt * 8) : "memory");
        if (__builtin_amdgcn_ballot_w64(t[1] != tag_hi || (t[0] >> 24) != tag_lo) == 0) break;
        if (spin > (1 << 20)) { t[0] = 0; break; }
        __builtin_amdgcn_s_sleep(POLL_SLEEP);
      }
      LDS(unsigned, L_WAVE + w * WSZ + W_STE + r * 4) = t[0];
      const u32x4 t0 = LDS(u32x4, L_WAVE + w * WSZ + W_STE + (lane >> 3) * 16);
#pragma unroll
      for (int p = 0; p < 4; p++) tb[p] = t0[p];
    }
    STAMP(8);
    u32x4 gr[6];
    unsigned ad[6], left[6], eb[6];
    unsigned lmax = 0, nov = 0;
    bool big;
    {
      const unsigned jj = lane & (LG - 1), c0 = (unsigned)tid / LG * 4, oob = 0x80000000u + zr;
      const unsigned o0 = mul24(c0, CHS) + (jbase / 2 + jj) * 16, eb0 = mul24(c0, CH);
      unsigned off[4], len[4];
#pragma unroll
      for (int p = 0; p < 4; p++) {
        len[p] = (tb[p] >> 12) & 0xfffu;
        if (part >= 0 && part != 4 * p + (w & 3)) len[p] = 0;
        off[p] = o0 + (tb[p] & 0xfffu) * 16 + p * CHS;
        left[p] = len[p] - (jbase + 2 * jj);
        ad[p] = (int)left[p] > 0 ? off[p] : oob;
        eb[p] = eb0 + p * CH;
        asm volatile("buffer_load_dwordx4 %0, %1, %2, 0 offen sc1" : "=v"(gr[p]) : "v"(ad[p]), "s"(rs_rec) : "memory");
        lmax = max(lmax, tb[p] & 0xfff000u);
      }
      asm volatile("" : "+v"(len[0]), "+v"(len[1]), "+v"(len[2]), "+v"(len[3]));
      ad[4] = oob; ad[5] = oob; left[4] = zr; left[5] = zr; eb[4] = zr; eb[5] = zr;
      unsigned ov[4], s1 = 0;
#pragma unroll
      for (int p = 0; p < 4; p++) {
        ov[p] = (unsigned)min(max(((int)len[p] - (jbase + 2 * LG) + 1) >> 1, 0), LG);
        const bool sel = jj - nov < ov[p];
        ad[4] = sel ? off[p] + (LG - nov) * 16 : ad[4];
        left[4] = sel ? len[p] - (jbase + 2 * LG) - 2 * (jj - nov) : left[4];
        eb[4] = sel ? eb0 + p * CH : eb[4];
        nov += ov[p];
      }
      asm volatile("buffer_load_dwordx4 %0, %1, %2, 0 offen sc1" : "=v"(gr[4]) : "v"(ad[4]), "s"(rs_rec) : "memory");
      gr[5] = (u32x4){zr, zr, zr, zr};
      big = __builtin_amdgcn_ballot_w64(nov > LG) != 0;
      if (big) {
#pragma unroll
        for (int p = 0; p < 4; p++) {
          const bool sel = jj + LG - s1 < ov[p];
          ad[5] = sel ? off[p] + (2 * LG - s1) * 16 : ad[5];
          left[5] = sel ? len[p] - (jbase + 2 * LG) - 2 * (jj + LG - s1) : left[5];
          eb[5] = sel ? eb0 + p * CH : eb[5];
          s1 += ov[p];
        }
        asm volatile("buffer_load_dwordx4 %0, %1, %2, 0 offen sc1" : "+v"(gr[5]) : "v"(ad[5]), "s"(rs_rec) : "memory");
      }
    }
    unsigned sink = zr;
    STAMP(7);
    u32x4 w1f[8];
    if (first) {
#pragma unroll
      for (int i = 0; i < 8; i++) w1f[i] = LDS(u32x4, L_WAVE + (4 + (i >> 1)) * WSZ + 2048 + (i & 1) * 1024 + lane * 16);
#pragma unroll
      for (int i = 0; i < 8; i++) {
        const u32x4 v = LDS(u32x4, L_WAVE + (i >> 1) * WSZ + 2048 + (i & 1) * 1024 + lane * 16);
#pragma unroll
        for (int j = 0; j < 4; j++) W2[4 * i + j] = v[j];
      }
#pragma unroll
      for (int i = 0; i < 2; i++) {
        const u32x4 v = LDS(u32x4, L_WJ + i * 1024 + lane * 16);
#pragma unroll
        for (int j = 0; j < 4; j++) W3[4 * i + j] = v[j];
      }
      WX0 = LDS(u32x4, L_WJ + 2048 + lane * 16); WX1 = LDS(u32x4, L_WJ + 3072 + lane * 16);
      WX2 = LDS(u32x4, L_WJ + 4096 + lane * 16); WX3 = LDS(u32x4, L_WJ + 5120 + lane * 16);
    }
    STAMP(13);
    if ((lmax >> 12) > (unsigned)jbase + 4 * LG || nov > 2 * LG) __hip_atomic_fetch_or(&LDS(unsigned, L_FLAG), nov > 2 * LG ? 3u : 1u, __ATOMIC_RELAXED, __HIP_MEMORY_SCOPE_WORKGROUP);
    unsigned mk1 = (lane & 7) >= 1 ? ~zr : zr, mk2 = (lane & 7) >= 2 ? ~zr : zr, sg0 = L_REC + (lane >> 3) * SEG;
    unsigned lim = L_REC + 8 * SEG + lane * 8, own = n0 + min(8 * (r & 3) + (r >> 2), 24) * 8;
    unsigned ce = ((lane & 1) ^ ((lane >> 4) & 1)) * 16, cp = (lane & 1) * 2;
    unsigned cc0 = ((lane & 7) ^ (lane >> 4)) * 16, cc1 = ((lane & 7) ^ (4 + (lane >> 4))) * 16;
    unsigned q_s4 = L_REC + (lane >> 3) * 32 + 4;
    unsigned q_r2 = L_REC + (4 * ((lane >> 1) & 7) + (lane >> 4)) * 8;
    unsigned q_r1 = L_REC + (4 * (r & 7) + (r >> 3)) * 8 + 4;
    asm volatile("" : "+v"(mk1), "+v"(mk2), "+v"(sg0), "+v"(lim), "+v"(own), "+v"(ce), "+v"(cp), "+v"(cc0), "+v"(cc1), "+v"(q_s4),
                 "+v"(q_r2), "+v"(q_r1));
    asm volatile("s_waitcnt vmcnt(0)" : "+v"(gr[0]), "+v"(gr[1]), "+v"(gr[2]), "+v"(gr[3]), "+v"(gr[4]), "+v"(gr[5]) :: "memory");
    for (int spin = 0;; spin++) {
      bool bad = false;
#pragma unroll
      for (int p = 0; p < 6; p++) bad |= ((int)left[p] > 0) & ((gr[p][3] != tag_hi) | ((gr[p][2] >> 24) != tag_lo));
      if (__builtin_amdgcn_ballot_w64(bad) == 0 || spin > (1 << 20)) break;
      asm volatile("buffer_load_dwordx4 %0, %6, %12, 0 offen sc1\n\tbuffer_load_dwordx4 %1, %7, %12, 0 offen sc1\n\t"
                   "buffer_load_dwordx4 %2, %8, %12, 0 offen sc1\n\tbuffer_load_dwordx4 %3, %9, %12, 0 offen sc1\n\t"
                   "buffer_load_dwordx4 %4, %10, %12, 0 offen sc1\n\tbuffer_load_dwordx4 %5, %11, %12, 0 offen sc1\n\ts_waitcnt vmcnt(0)"
                   : "=&v"(gr[0]), "=&v"(gr[1]), "=&v"(gr[2]), "=&v"(gr[3]), "=&v"(gr[4]), "=&v"(gr[5])
                   : "v"(ad[0]), "v"(ad[1]), "v"(ad[2]), "v"(ad[3]), "v"(ad[4]), "v"(ad[5]), "s"(rs_rec) : "memory");
    }
    STAMP(1);
    u32x2 rc[12];
#pragma unroll
    for (int p = 0; p < 6; p++) {
      rc[2 * p] = (u32x2){(eb[p] + (gr[p][2] & 0xfffu)) << 5, (int)left[p] > 0 ? gr[p][0] : 0u};
      rc[2 * p + 1] = (u32x2){(eb[p] + ((gr[p][2] >> 12) & 0xfffu)) << 5, (int)left[p] > 1 ? gr[p][1] : 0u};
    }
    unsigned rk[12], ky[12];
#pragma unroll
    for (int p = 0; p < 12; p++) {
      if (p == 10 && !big) { ky[10] = ky[11] = 0; rk[10] = rk[11] = 0; break; }
      ky[p] = max((rc[p][1] >> 9) & 0x7fcu, (unsigned)lane * 4);
      rk[p] = __hip_atomic_fetch_add(&LDS(unsigned, L_CNT + ky[p]), 8u + zr, __ATOMIC_RELAXED, __HIP_MEMORY_SCOPE_WORKGROUP);
    }
    if (EPF) {
#pragma unroll
      for (int p = 0; p < 10; p++) {
        const unsigned a = (ky[p] & 0x47cu) - 0x400u < (unsigned)EPF * 4 ? rc[p][0] : 0x80000000u + zr;
        asm volatile("buffer_load_dword %0, %1, %2, 0 offen" : "+{v3}"(sink) : "v"(a), "s"(rs_ea) : "memory");
      }
    }
    barrier_lds();
    STAMP(2);
    unsigned g;
    const unsigned flagv = LDS(unsigned, L_FLAG);
    {
      const u32x4 cv = LDS(u32x4, L_CNT + 1024 + lane * 16);
      const unsigned sum = cv[0] + cv[1] + cv[2] + cv[3];
      g = sum + (__builtin_amdgcn_update_dpp(0, (int)sum, 0x111, 0xf, 0xf, true) & mk1);
      g += __builtin_amdgcn_update_dpp(0, (int)g, 0x112, 0xf, 0xf, true) & mk2;
      g += __builtin_amdgcn_update_dpp(0, (int)g, 0x114, 0xf, 0xa, false);
      const unsigned ex = g - sum + sg0;
      const u32x4 bv = {ex, ex + cv[0], ex + cv[0] + cv[1], ex + cv[0] + cv[1] + cv[2]};
      LDS(u32x4, L_BASE + 1024 + lane * 16) = bv;
    }
    unsigned bs[10];
#pragma unroll
    for (int p = 0; p < 10; p++) bs[p] = LDS(unsigned, L_BASE + ky[p]);
    const unsigned flag = __builtin_amdgcn_readfirstlane(flagv);
    const bool again = flag & 1;
    const bool over = (flag & 2) || __builtin_amdgcn_ballot_w64(g > (unsigned)(SEG - 256)) != 0;
    if (!over) {
#pragma unroll
      for (int p = 0; p < 10; p++) LDS(u32x2, min(bs[p] + rk[p], lim)) = rc[p];
      if (big) {
        LDS(u32x2, min(LDS(unsigned, L_BASE + ky[10]) + rk[10], lim)) = rc[10];
        LDS(u32x2, min(LDS(unsigned, L_BASE + ky[11]) + rk[11], lim)) = rc[11];
      }
    }
    if (first) {
      const unsigned key = (g << 3) | (lane >> 3);
      unsigned ks[8], rank = 0;
#pragma unroll
      for (int k = 0; k < 8; k++) ks[k] = __builtin_amdgcn_readlane(key, 8 * k + 7);
#pragma unroll
      for (int k = 0; k < 8; k++) rank += ks[k] > key;
      const unsigned long long sel = 0x8080808080808080ull;
      grp = __builtin_ctzll(__builtin_amdgcn_ballot_w64(rank == (unsigned)(w < 4 ? w : 11 - w)) & sel) >> 3;
      gother = __builtin_ctzll(__builtin_amdgcn_ballot_w64(rank == (unsigned)(w < 4 ? 7 - w : w - 4)) & sel) >> 3;
      wl = L_WAVE + grp * WSZ; mi = L_MI + grp * MISZ;
    }
    unsigned segn = over ? 0u : (unsigned)__builtin_amdgcn_readlane(g, 8 * grp + 7);
    const unsigned seg0 = grp * SEG;
    const int nt = (segn + 255) >> 8;
    {
      const u32x2 pad = {zr, DUMMY * (80u << 20)}, row = {zr, min(own + grp, (unsigned)N - 1) | (DUMMY * (80u << 20))};
      LDS(u32x2, L_REC + seg0 + segn + r * 8) = pad;
      LDS(u32x2, L_REC + seg0 + nt * 256 + r * 8) = row;
    }
    barrier_lds();
    STAMP(3);

    const unsigned o_s4 = q_s4 + seg0, o_r2 = q_r2 + seg0, o_r1 = q_r1 + seg0;
    {
      unsigned ns[4];
#pragma unroll
      for (int i = 0; i < 4; i++) ns[i] = LDS(uint16_t, o_s4 + i * 8);
      const u32x2 nr2 = LDS(u32x2, o_r2);
      dma_tile(hmat, ea, pos, ns[0] * 128 + cc0, ns[1] * 128 + cc1, ns[2] * 128 + cc0, ns[3] * 128 + cc1,
               nr2[0] | ce, (nr2[1] & 0xffffu) * 6 + cp, wl);
    }
    if (first) {
      u32x2 hb[8];
#pragma unroll
      for (int i = 0; i < 8; i++) hb[i] = LDS(u32x2, mi + r * 128 + ((i ^ ((r >> 1) & 7)) * 16) + h * 8);
      const u32x2 pg = LDS(u32x2, mi + 4096 + r * 8);
#pragma unroll
      for (int t = 0; t < 2; t++) {
        f32x16 P;
#pragma unroll
        for (int i = 0; i < 16; i++) P[i] = 0.f;
#pragma unroll
        for (int s = 0; s < 4; s++) {
          const u32x4 a = {hb[2 * s][0], hb[2 * s][1], hb[2 * s + 1][0], hb[2 * s + 1][1]};
          P = MFMA(as_bf(a), as_bf(w1f[2 * s + t]), P);
        }
        if (h) P[15] = bf2f(t ? wdv1 : wdv0);
#pragma unroll
        for (int i = 0; i < 16; i++) P[i] *= -LOG2E;
#pragma unroll
        for (int s = 0; s < 2; s++)
#pragma unroll
          for (int j = 0; j < 4; j++) PA[4 * (2 * s + t) + j] = pk_bf16(P[8 * s + 2 * j], P[8 * s + 2 * j + 1]);
      }
      const f32x4 pv = {bf2f(pg[0] & 0xffffu), __uint_as_float(pg[0] & 0xffff0000u), __uint_as_float(pg[1] & 0xffff0000u), 0.f};
      if (lane < DUMMY) LDS(f32x4, wl + W_SI + lane * 80 + 64) = pv;
      if (bkt == 0 && tid == 0) {
        const u32x2 e1 = {tag_lo | (tag_hi << 8), tag_hi >> 24};
        asm volatile("global_store_dwordx2 %0, %1, %2 sc1" :: "v"(zr), "v"(e1), "s"(ws + WS_EPOCH) : "memory");
      }
    }
    const unsigned nother = (__builtin_amdgcn_readlane(g, 8 * gother + 7) + 255) >> 8;
    const bool last_round = !over && !again && (part < 0 || part == 15);
    STAMP(4);

    {
      u32x16 A0; u32x8 A1;
      A0[0] = zr; A0[1] = zr;
#pragma unroll
      for (int s = 0; s < 4; s++) A0[2 + s] = wl + W_STH + r * 128 + (((2 * s + h) ^ ((r >> 1) & 7)) * 16);
      A0[6] = wl + r * 32 + ((h ^ ((r >> 3) & 1)) * 16);
      A0[7] = wl + r * 8;
      A0[8] = L_LUT + h * 16;
      A0[9] = wl + r * 2;
      A0[10] = wl + min(r, DUMMY) * 80 + h * 16;
      A0[11] = mi + r * MIL + h * 32;
      A0[12] = mi + (8 * h + ((lane >> 2) & 3)) * MIL + (4 * ((lane >> 4) & 1) + (lane & 3)) * 8;
      A0[13] = wl + h * 64 + r * 2;
      {
        const int row = (lane & 15) - 4 * ((lane >> 4) & 1);
        A0[14] = wl + ((unsigned)row < 3 ? row : 3) * 64 + h * 16;
      }
      A0[15] = L_WJ + lane * 16;
      A1[0] = cc0; A1[1] = cc1; A1[2] = ce; A1[3] = cp;
      A1[4] = o_s4 + 256; A1[5] = o_r2 + 256; A1[6] = o_r1;
      A1[7] = 0x3f800000u + zr;
      unsigned cnt = nt, keep;
#define PRIO_A 14
#define PRIO_B 7
      const int lastv = __builtin_amdgcn_readfirstlane(opaque(last_round ? 1 : 0));
      const int thr = __builtin_amdgcn_readfirstlane(w >= 4 ? max((int)(nother * PRIO_A - cnt * PRIO_B) >> 5, 0) : -1);
      asm volatile(LOOP_ASM
                   : "+{v[52:67]}"(Y0), "+{v[68:83]}"(Y1), "+{v[84:87]}"(Yp), "+{v[232:247]}"(A0), "+{v[248:255]}"(A1), "+{v3}"(sink),
                     [nt] "+s"(cnt), [keep] "=&s"(keep)
                   : "{v[4:35]}"(W2), "{v[36:51]}"(PA), "{v[88:95]}"(W3), "{v[96:99]}"(WX0), "{v[136:139]}"(WX1), "{v[140:143]}"(WX2), "{v[144:147]}"(WX3),
                     [h0] "s"(hmat), [h1] "s"(hmat - 1024), [h2] "s"(hmat - 2048), [h3] "s"(hmat - 3072), [ea] "s"(ea), [pos] "s"(pos - 1024),
                     [b1] "s"(wl), [b2] "s"(wl + W_STE), [posd] "s"(wl + W_SI + 64), [c128] "s"(128u), [thr] "s"(thr), [last] "s"(lastv),
                     [hsel] "s"(0xffffffff00000000ull)
                   : "memory", "scc", "vcc", CLOBBER_V);
    }
    if (over) part = 0;
    else if (part >= 0 && part < 15) part++;
    else { part = -1; jbase += 4 * LG; }
    return last_round;
  };
  if (!round(true)) {
    do {
      wait_vm0();
      barrier_lds();
      jbase = __builtin_amdgcn_readfirstlane(jbase); part = __builtin_amdgcn_readfirstlane(part);
    } while (!round(false));
  }
  STAMP(5);

  {
    const int lane = lane_id(), r = lane & 31, h = lane >> 5;
    Yp[0] *= -LN2; Yp[1] *= -LN2; Yp[2] *= -LN2;
    const auto rs_o = __builtin_amdgcn_make_buffer_rsrc((void*)out, 0, N * 128, 0x00020000);
#pragma unroll
    for (int i = 0; i < 4; i++) {
      const int q = 8 * i + (lane >> 3), dl = q * 8 + grp;
      const u32x4 v = LDS(u32x4, mi + q * MIP + (lane & 7) * 16);
      if (dl < NB) __builtin_amdgcn_raw_buffer_store_b128(v, rs_o, (n0 + dl) * 128 + (lane & 7) * 16, 0, OUT_AUX);
    }
    const int dl = r * 8 + grp;
    if (h == 0 && dl < NB && n0 + dl < N) {
      const f32x4 pd = LDS(f32x4, wl + W_SI + r * 80 + 64);
      uint16_t* po = out + (size_t)N * 64 + (size_t)(n0 + dl) * 3;
      const unsigned xy = pk_bf16(pd[0] + Yp[0], pd[1] + Yp[1]), zz = pk_bf16(pd[2] + Yp[2], 0.f);
      po[0] = (uint16_t)xy; po[1] = (uint16_t)(xy >> 16); po[2] = (uint16_t)zz;
    }
  }
  STAMP(6);
}

}

#define STAMP_PASS1
#define STAMP_PASS2

extern "C" void kernel_launch(void* const* d_in, const int* in_sizes, int n_in, void* d_out, int out_size,
                              void* d_ws, size_t ws_size, hipStream_t stream) {
  const int N = in_sizes[0] / 64, E = in_sizes[3];
  if (N > NBK * NB || E > NBK * CH || N < 1 || E < 1) return;
  (void)hipFuncSetAttribute((const void*)egnn_kernel, hipFuncAttributeMaxDynamicSharedMemorySize, L_TOTAL);
  egnn_kernel<<<NBK, 512, L_TOTAL, stream>>>((const char*)d_in[0], (const char*)d_in[1], (const char*)d_in[2], (const int*)d_in[3],
                                             (const int*)d_in[4], (const char*)d_in[5], (const char*)d_in[6], (const char*)d_in[7],
                                             (char*)d_ws, (uint16_t*)d_out, N, E STAMP_PASS2);
}
```

```cpp
#include <hip/hip_runtime.h>
#include <stdint.h>
#include <type_traits>

#define FOLD_LOG2E 1

namespace {

typedef __bf16 bf16x8 __attribute__((ext_vector_type(8)));
typedef __bf16 bf16x2 __attribute__((ext_vector_type(2)));
typedef float f32x16 __attribute__((ext_vector_type(16)));
typedef float f32x4 __attribute__((ext_vector_type(4)));
typedef unsigned u32x4 __attribute__((ext_vector_type(4)));
typedef unsigned u32x2 __attribute__((ext_vector_type(2)));
typedef unsigned u32x8 __attribute__((ext_vector_type(8)));
typedef unsigned u32x16 __attribute__((ext_vector_type(16)));
typedef unsigned u32x32 __attribute__((ext_vector_type(32)));
typedef short s16x4 __attribute__((ext_vector_type(4)));
typedef __attribute__((address_space(3))) s16x4* lds_s16x4_p;

#define DI __device__ __forceinline__
#define MFMA(a, b, c) __builtin_amdgcn_mfma_f32_32x32x16_bf16((a), (b), (c), 0, 0, 0)

#define POLL_SLEEP 2
#define EPF 2
#define OUT_AUX 16
constexpr int NBK = 256;
constexpr int LG = 8;
constexpr int NB = 196;
constexpr int CH = 3136;
constexpr float LOG2E = 1.4426950408889634f;
constexpr float LN2 = 0.6931471805599453f;

constexpr size_t WS_EPOCH = 0;
constexpr size_t WS_TAB = 256;
constexpr size_t WS_REC = WS_TAB + (size_t)NBK * NBK * 8;
constexpr int CHS = (CH + NBK) * 8;

constexpr int W_STH = 0, W_STE = 4096, W_STP = 5120, W_SI = 5376, W_TI = W_SI + 26 * 80, WSZ = W_TI + 256;
constexpr int DUMMY = 25;
constexpr int MIP = 144, MISZ = 32 * MIP;
constexpr int MIL = 80;
constexpr int SEG = 17 * 256;
constexpr int L_CNT = 0;
constexpr int L_BASE = L_CNT + 2048;
constexpr int L_FLAG = L_CNT + 512;
constexpr int L_REC = L_BASE + 2048;
constexpr int L_LUT = L_REC + 8 * SEG + 512;
constexpr int L_WAVE = L_LUT + 26 * 80;
constexpr int L_MI = L_WAVE + 8 * WSZ;
constexpr int L_WJ = L_MI + 8 * MISZ;
constexpr int L_WH = L_WJ + 6 * 1024;
constexpr int L_TOTAL = L_WH + 16 * 1024;
static_assert(L_TOTAL <= 163840, "LDS");
static_assert(WSZ % 16 == 0 && L_MI % 16 == 0 && L_LUT % 16 == 0, "align");

#pragma clang diagnostic ignored "-Wint-to-pointer-cast"
#define LDS(T, off) (*(__attribute__((address_space(3))) T*)(unsigned)(off))
DI unsigned mul24(unsigned a, unsigned b) { return (unsigned)__umul24(a, b); }
DI float bf2f(unsigned lo16) { return __uint_as_float(lo16 << 16); }
DI unsigned pk_bf16(float a, float b) { bf16x2 v = {(__bf16)a, (__bf16)b}; return __builtin_bit_cast(unsigned, v); }
DI bf16x8 as_bf(u32x4 v) { return __builtin_bit_cast(bf16x8, v); }
DI int opaque(int x) { asm volatile("" : "+v"(x)); return x; }
DI int lane_id() {
  int x; asm volatile("v_mbcnt_lo_u32_b32 %0, -1, 0\n\tv_mbcnt_hi_u32_b32 %0, -1, %0" : "=v"(x)); return x;
}
DI void wait_vm0() { asm volatile("s_waitcnt vmcnt(0)" ::: "memory"); }
DI void barrier_lds() { asm volatile("s_waitcnt lgkmcnt(0)\n\ts_barrier" ::: "memory"); }
DI void dma_tile(const void* hm, const void* ea, const void* ps, unsigned o0, unsigned o1, unsigned o2, unsigned o3,
                 unsigned oe, unsigned op, unsigned l) {
  unsigned keep;
  asm volatile("s_mov_b32 %0, m0\n\t"
               "s_add_u32 m0, %10, 0x1000\n\ts_nop 0\n\tglobal_load_lds_dwordx4 %5, %8\n\t"
               "s_mov_b32 m0, %10\n\ts_nop 0\n\tglobal_load_lds_dwordx4 %1, %7\n\t"
               "s_add_u32 m0, %10, 0x400\n\ts_nop 0\n\tglobal_load_lds_dwordx4 %2, %7\n\t"
               "s_add_u32 m0, %10, 0x800\n\ts_nop 0\n\tglobal_load_lds_dwordx4 %3, %7\n\t"
               "s_add_u32 m0, %10, 0xc00\n\ts_nop 0\n\tglobal_load_lds_dwordx4 %4, %7\n\t"
               "s_add_u32 m0, %10, 0x1400\n\ts_nop 0\n\tglobal_load_lds_dword %6, %9\n\t"
               "s_mov_b32 m0, %0"
               : "=&s"(keep) : "v"(o0), "v"(o1), "v"(o2), "v"(o3), "v"(oe), "v"(op), "s"(hm), "s"(ea), "s"(ps), "s"(l)
               : "memory", "scc");
}
template <int CTRL, int ROWMASK> DI unsigned dpp_add(unsigned x) {
  return x + (unsigned)__builtin_amdgcn_update_dpp(0, (int)x, CTRL, ROWMASK, 0xf, false);
}
DI unsigned wave_scan_incl(unsigned x) {
  x = dpp_add<0x111, 0xf>(x); x = dpp_add<0x112, 0xf>(x); x = dpp_add<0x114, 0xf>(x); x = dpp_add<0x118, 0xf>(x);
  x = dpp_add<0x142, 0xa>(x); x = dpp_add<0x143, 0xc>(x);
  return x;
}
#define STAMP(i)
#define STAMP_ARG

#define CLOBBER_V "v100", "v101", "v102", "v103", "v104", "v105", "v106", "v107", "v108", "v109", "v110", "v111", "v112", "v113", "v114", "v115", "v116", "v117", "v118", "v119", "v120", "v121", "v122", "v123", "v124", "v125", "v126", "v127", "v128", "v129", "v130", "v131", "v132", "v133", "v134", "v135", "v148", "v149", "v150", "v151", "v152", "v153", "v154", "v155", "v156", "v157", "v158", "v159", "v160", "v161", "v162", "v163", "v164", "v165", "v166", "v167", "v168", "v169", "v170", "v171", "v172", "v173", "v174", "v175", "v176", "v177", "v178", "v179", "v180", "v181", "v182", "v183", "v184", "v185", "v186", "v187", "v188", "v189", "v190", "v191", "v192", "v193", "v194", "v195", "v196", "v197", "v198", "v199", "v200", "v201", "v202", "v203", "v204", "v205", "v206", "v207", "v208", "v209", "v210", "v211", "v212", "v213", "v214", "v215", "v216", "v217", "v218", "v219", "v220", "v221", "v222", "v223", "v224", "v225", "v226", "v227", "v228", "v229", "v230", "v231"
#define LOOP_ASM_FOLD \
  "s_mov_b32 %[keep], m0\n\t" \
  "s_mov_b64 vcc, %[hsel]\n\t" \
  "ds_read2_b32 v[220:221], v252 offset0:0 offset1:2\n\t" \
  "ds_read2_b32 v[222:223], v252 offset0:4 offset1:6\n\t" \
  "ds_read_b64 v[226:227], v253\n\t" \
  "ds_read_b32 v232, v254\n\t" \
  "v_mov_b32 v176, 0\n\t" \
  "v_mov_b32 v177, 0\n\t" \
  "v_mov_b32 v178, 0\n\t" \
  "v_mov_b32 v179, 0\n\t" \
  "v_mov_b32 v180, 0\n\t" \
  "v_mov_b32 v181, 0\n\t" \
  "v_mov_b32 v182, 0\n\t" \
  "v_mov_b32 v183, 0\n\t" \
  "v_mov_b32 v184, 0\n\t" \
  "v_mov_b32 v185, 0\n\t" \
  "v_mov_b32 v186, 0\n\t" \
  "v_mov_b32 v187, 0\n\t" \
  "v_mov_b32 v188, 0\n\t" \
  "v_mov_b32 v189, 0\n\t" \
  "v_mov_b32 v190, 0\n\t" \
  "v_mov_b32 v191, 0\n\t" \
  "v_mov_b32 v230, 0\n\t" \
  "v_mov_b32 v231, 0\n\t" \
  "v_mov_b32 v208, 0\n\t" \
  "v_mov_b32 v209, 0\n\t" \
  "v_mov_b32 v210, 0\n\t" \
  "v_mov_b32 v211, 0\n\t" \
  "ds_write_b128 v243, v[184:187] offset:0\n\t" \
  "s_nop 0\n\t" \
  "v_mfma_f32_16x16x32_bf16 v[132:135], v[184:187], v[188:191], 0\n\t" \
  "v_add_u32 v252, 0x100, v252\n\t" \
  "v_add_u32 v253, 0x100, v253\n\t" \
  "v_add_u32 v254, 0x100, v254\n\t" \
  "s_waitcnt lgkmcnt(0)\n\t" \
  "v_lshrrev_b32 v232, 20, v232\n\t" \
  "v_add_u32 v233, %[posd], v232\n\t" \
  "ds_read_b128 v[216:219], v233\n\t" \
  "v_add_u32 v233, v240, v232\n\t" \
  "ds_read_b128 v[172:175], v233 offset:32\n\t" \
  "ds_read_b128 v[168:171], v233\n\t" \
  "v_add_u32 v233, v241, v232\n\t" \
  "ds_write_b16_d16_hi v233, v255 offset:5376\n\t" \
  "v_mad_u32_u16 v220, v220, %[c128], v248\n\t" \
  "v_mad_u32_u16 v221, v221, %[c128], v249\n\t" \
  "v_mad_u32_u16 v222, v222, %[c128], v248\n\t" \
  "v_mad_u32_u16 v223, v223, %[c128], v249\n\t" \
  "v_or_b32 v226, v226, v250\n\t" \
  "v_mad_u32_u16 v227, v227, 6, v251\n\t" \
  "s_waitcnt vmcnt(0)\n\t" \
  "ds_read_b128 v[148:151], v234\n\t" \
  "ds_read_b128 v[152:155], v235\n\t" \
  "ds_read_b128 v[156:159], v236\n\t" \
  "ds_read_b128 v[160:163], v237\n\t" \
  "ds_read_b128 v[164:167], v238 offset:4096\n\t" \
  "ds_read_b64 v[224:225], v239 offset:5120\n\t" \
  "s_waitcnt lgkmcnt(0)\n\t" \
  "s_cmp_lt_i32 %[nt], 1\n\t" \
  "s_cbranch_scc1 7f\n\t" \
  "s_mov_b32 m0, %[b2]\n\t" \
  "s_nop 0\n\t" \
  "global_load_lds_dwordx4 v226, %[ea]\n\t" \
  "global_load_lds_dword v227, %[pos] offset:1024\n\t" \
  "s_mov_b32 m0, %[b1]\n\t" \
  "s_nop 0\n\t" \
  "global_load_lds_dwordx4 v220, %[h0]\n\t" \
  "global_load_lds_dwordx4 v221, %[h1] offset:1024\n\t" \
  "global_load_lds_dwordx4 v222, %[h2] offset:2048\n\t" \
  "global_load_lds_dwordx4 v223, %[h3] offset:3072\n\t" \
  "7:\n\t" \
  "ds_read2_b32 v[220:221], v252 offset0:0 offset1:2\n\t" \
  "ds_read2_b32 v[222:223], v252 offset0:4 offset1:6\n\t" \
  "ds_read_b64 v[226:227], v253\n\t" \
  "ds_read_b32 v232, v254\n\t" \
  "v_add_u32 v252, 0x100, v252\n\t" \
  "v_add_u32 v253, 0x100, v253\n\t" \
  "v_add_u32 v254, 0x100, v254\n\t" \
  "v_and_b32 v225, 0xffff0000, v225\n\t" \
  "v_lshlrev_b32 v219, 16, v224\n\t" \
  "v_and_b32 v224, 0xffff0000, v224\n\t" \
  "v_sub_f32 v229, v218, v225\n\t" \
  "v_sub_f32 v216, v216, v219\n\t" \
  "v_sub_f32 v217, v217, v224\n\t" \
  "v_mul_f32 v218, v229, v229\n\t" \
  "v_cndmask_b32 v228, v216, v217, vcc\n\t" \
  "v_fmac_f32 v218, v216, v216\n\t" \
  "v_fmac_f32 v218, v217, v217\n\t" \
  "v_cvt_pk_bf16_f32 v218, v218, v218\n\t" \
  "v_cndmask_b32_sdwa v175, v175, v218, vcc dst_sel:WORD_1 dst_unused:UNUSED_PRESERVE src0_sel:WORD_1 src1_sel:WORD_1\n\t" \
  "v_mfma_f32_32x32x16_bf16 v[100:115], v[4:7], v[148:151], 0\n\t" \
  "v_mfma_f32_32x32x16_bf16 v[100:115], v[12:15], v[152:155], v[100:115]\n\t" \
  "v_mfma_f32_32x32x16_bf16 v[100:115], v[20:23], v[156:159], v[100:115]\n\t" \
  "v_mfma_f32_32x32x16_bf16 v[100:115], v[28:31], v[160:163], v[100:115]\n\t" \
  "v_mfma_f32_32x32x16_bf16 v[100:115], v[36:39], v[168:171], v[100:115]\n\t" \
  "v_mfma_f32_32x32x16_bf16 v[100:115], v[88:91], v[164:167], v[100:115]\n\t" \
  "v_mfma_f32_32x32x16_bf16 v[100:115], v[44:47], v[172:175], v[100:115]\n\t" \
  "s_cmp_eq_u32 %[nt], 0\n\t" \
  "s_nop 10\n\t" \
  "v_exp_f32 v192, v100\n\t" \
  "v_exp_f32 v193, v101\n\t" \
  "v_exp_f32 v194, v102\n\t" \
  "v_exp_f32 v195, v103\n\t" \
  "s_nop 0\n\t" \
  "v_add_f32 v192, 1.0, v192\n\t" \
  "v_add_f32 v193, 1.0, v193\n\t" \
  "v_add_f32 v194, 1.0, v194\n\t" \
  "v_add_f32 v195, 1.0, v195\n\t" \
  "s_cbranch_scc1 6f\n\t" \
  "s_cmp_lt_i32 %[thr], 0\n\t" \
  "s_cbranch_scc1 1f\n\t" \
  "s_setprio 1\n\t" \
  "1:\n\t" \
  "ds_write_b128 v243, v[188:191] offset:16\n\t" \
  "ds_read_b64_tr_b16 v[200:201], v244 offset:0\n\t" \
  "ds_read_b64_tr_b16 v[202:203], v244 offset:320\n\t" \
  "ds_read_b64_tr_b16 v[204:205], v244 offset:1280\n\t" \
  "ds_read_b64_tr_b16 v[206:207], v244 offset:1600\n\t" \
  "v_mfma_f32_16x16x32_bf16 v[132:135], v[144:147], v[188:191], v[132:135]\n\t" \
  "v_rcp_f32 v192, v192\n\t" \
  "v_rcp_f32 v193, v193\n\t" \
  "v_rcp_f32 v194, v194\n\t" \
  "v_rcp_f32 v195, v195\n\t" \
  "v_mfma_f32_32x32x16_bf16 v[116:131], v[8:11], v[148:151], 0\n\t" \
  "v_exp_f32 v196, v104\n\t" \
  "v_exp_f32 v197, v105\n\t" \
  "v_exp_f32 v198, v106\n\t" \
  "v_exp_f32 v199, v107\n\t" \
  "v_mfma_f32_32x32x16_bf16 v[116:131], v[16:19], v[152:155], v[116:131]\n\t" \
  "v_mul_f32 v192, v100, v192\n\t" \
  "v_mul_f32 v193, v101, v193\n\t" \
  "v_mul_f32 v194, v102, v194\n\t" \
  "v_mul_f32 v195, v103, v195\n\t" \
  "v_add_f32 v196, 1.0, v196\n\t" \
  "v_add_f32 v197, 1.0, v197\n\t" \
  "v_add_f32 v198, 1.0, v198\n\t" \
  "v_add_f32 v199, 1.0, v199\n\t" \
  "v_cvt_pk_bf16_f32 v184, v192, v193\n\t" \
  "v_cvt_pk_bf16_f32 v185, v194, v195\n\t" \
  "v_mfma_f32_32x32x16_bf16 v[116:131], v[24:27], v[156:159], v[116:131]\n\t" \
  "v_rcp_f32 v196, v196\n\t" \
  "v_rcp_f32 v197, v197\n\t" \
  "v_rcp_f32 v198, v198\n\t" \
  "v_rcp_f32 v199, v199\n\t" \
  "s_waitcnt lgkmcnt(2)\n\t" \
  "v_mfma_f32_32x32x16_bf16 v[68:83], v[200:203], v[176:179], v[68:83]\n\t" \
  "v_exp_f32 v192, v108\n\t" \
  "v_exp_f32 v193, v109\n\t" \
  "v_exp_f32 v194, v110\n\t" \
  "v_exp_f32 v195, v111\n\t" \
  "v_mfma_f32_32x32x16_bf16 v[116:131], v[32:35], v[160:163], v[116:131]\n\t" \
  "v_mul_f32 v196, v104, v196\n\t" \
  "v_mul_f32 v197, v105, v197\n\t" \
  "v_mul_f32 v198, v106, v198\n\t" \
  "v_mul_f32 v199, v107, v199\n\t" \
  "v_mul_f32 v230, v230, v132\n\t" \
  "v_mul_f32 v231, v231, v132\n\t" \
  "v_cvt_pk_bf16_f32 v230, v230, v231\n\t" \
  "ds_write_b16 v245, v230 offset:7456\n\t" \
  "ds_write_b16_d16_hi v241, v230 offset:7584\n\t" \
  "ds_read_b128 v[200:203], v246 offset:7456\n\t" \
  "ds_read_b128 v[208:211], v246 offset:7488\n\t" \
  "v_mov_b32 v230, v228\n\t" \
  "v_mov_b32 v231, v229\n\t" \
  "v_add_f32 v192, 1.0, v192\n\t" \
  "v_add_f32 v193, 1.0, v193\n\t" \
  "v_add_f32 v194, 1.0, v194\n\t" \
  "v_add_f32 v195, 1.0, v195\n\t" \
  "v_cvt_pk_bf16_f32 v186, v196, v197\n\t" \
  "v_cvt_pk_bf16_f32 v187, v198, v199\n\t" \
  "v_mfma_f32_32x32x16_bf16 v[116:131], v[40:43], v[168:171], v[116:131]\n\t" \
  "v_rcp_f32 v192, v192\n\t" \
  "v_rcp_f32 v193, v193\n\t" \
  "v_rcp_f32 v194, v194\n\t" \
  "v_rcp_f32 v195, v195\n\t" \
  "s_sub_u32 %[nt], %[nt], 1\n\t" \
  "s_cmp_lg_u32 %[nt], %[thr]\n\t" \
  "s_cbranch_scc1 3f\n\t" \
  "s_setprio 0\n\t" \
  "3:\n\t" \
  "v_mfma_f32_32x32x16_bf16 v[116:131], v[92:95], v[164:167], v[116:131]\n\t" \
  "v_exp_f32 v196, v112\n\t" \
  "v_exp_f32 v197, v113\n\t" \
  "v_exp_f32 v198, v114\n\t" \
  "v_exp_f32 v199, v115\n\t" \
  "v_mfma_f32_32x32x16_bf16 v[116:131], v[48:51], v[172:175], v[116:131]\n\t" \
  "v_mul_f32 v192, v108, v192\n\t" \
  "v_mul_f32 v193, v109, v193\n\t" \
  "v_mul_f32 v194, v110, v194\n\t" \
  "v_mul_f32 v195, v111, v195\n\t" \
  "s_waitcnt vmcnt(0)\n\t" \
  "ds_read_b128 v[148:151], v234\n\t" \
  "ds_read_b128 v[152:155], v235\n\t" \
  "ds_read_b128 v[156:159], v236\n\t" \
  "ds_read_b128 v[160:163], v237\n\t" \
  "ds_read_b128 v[164:167], v238 offset:4096\n\t" \
  "ds_read_b64 v[224:225], v239 offset:5120\n\t" \
  "ds_write_b128 v243, v[184:187] offset:0\n\t" \
  "v_add_f32 v196, 1.0, v196\n\t" \
  "v_add_f32 v197, 1.0, v197\n\t" \
  "v_add_f32 v198, 1.0, v198\n\t" \
  "v_add_f32 v199, 1.0, v199\n\t" \
  "v_cvt_pk_bf16_f32 v188, v192, v193\n\t" \
  "v_cvt_pk_bf16_f32 v189, v194, v195\n\t" \
  "v_mad_u32_u16 v220, v220, %[c128], v248\n\t" \
  "v_mad_u32_u16 v221, v221, %[c128], v249\n\t" \
  "v_mad_u32_u16 v222, v222, %[c128], v248\n\t" \
  "s_waitcnt lgkmcnt(11)\n\t" \
  "v_mfma_f32_32x32x16_bf16 v[68:83], v[204:207], v[180:183], v[68:83]\n\t" \
  "v_rcp_f32 v196, v196\n\t" \
  "v_rcp_f32 v197, v197\n\t" \
  "v_rcp_f32 v198, v198\n\t" \
  "v_rcp_f32 v199, v199\n\t" \
  "v_mad_u32_u16 v223, v223, %[c128], v249\n\t" \
  "v_or_b32 v226, v226, v250\n\t" \
  "v_mad_u32_u16 v227, v227, 6, v251\n\t" \
  "s_waitcnt lgkmcnt(8)\n\t" \
  "v_mfma_f32_16x16x32_bf16 v[84:87], v[200:203], v[176:179], v[84:87]\n\t" \
  "v_exp_f32 v192, v116\n\t" \
  "v_exp_f32 v193, v117\n\t" \
  "v_exp_f32 v194, v118\n\t" \
  "v_exp_f32 v195, v119\n\t" \
  "s_waitcnt lgkmcnt(7)\n\t" \
  "v_mfma_f32_16x16x32_bf16 v[84:87], v[208:211], v[180:183], v[84:87]\n\t" \
  "v_mul_f32 v196, v112, v196\n\t" \
  "v_mul_f32 v197, v113, v197\n\t" \
  "v_mul_f32 v198, v114, v198\n\t" \
  "v_mul_f32 v199, v115, v199\n\t" \
  "s_waitcnt lgkmcnt(1)\n\t" \
  "s_cmp_lt_i32 %[nt], 1\n\t" \
  "s_cbranch_scc1 4f\n\t" \
  "s_mov_b32 m0, %[b2]\n\t" \
  "s_nop 0\n\t" \
  "global_load_lds_dwordx4 v226, %[ea]\n\t" \
  "global_load_lds_dword v227, %[pos] offset:1024\n\t" \
  "s_mov_b32 m0, %[b1]\n\t" \
  "s_nop 0\n\t" \
  "global_load_lds_dwordx4 v220, %[h0]\n\t" \
  "global_load_lds_dwordx4 v221, %[h1] offset:1024\n\t" \
  "global_load_lds_dwordx4 v222, %[h2] offset:2048\n\t" \
  "global_load_lds_dwordx4 v223, %[h3] offset:3072\n\t" \
  "4:\n\t" \
  "v_add_f32 v192, 1.0, v192\n\t" \
  "v_add_f32 v193, 1.0, v193\n\t" \
  "v_add_f32 v194, 1.0, v194\n\t" \
  "v_add_f32 v195, 1.0, v195\n\t" \
  "v_cvt_pk_bf16_f32 v190, v196, v197\n\t" \
  "v_cvt_pk_bf16_f32 v191, v198, v199\n\t" \
  "v_lshrrev_b32 v232, 20, v232\n\t" \
  "v_add_u32 v220, %[posd], v232\n\t" \
  "v_add_u32 v221, v240, v232\n\t" \
  "ds_write_b128 v243, v[188:191] offset:16\n\t" \
  "ds_read_b128 v[176:179], v242 offset:5376\n\t" \
  "ds_read_b128 v[180:183], v242 offset:5408\n\t" \
  "ds_write_b16 v233, v255 offset:5376\n\t" \
  "ds_read_b128 v[216:219], v220\n\t" \
  "ds_read_b128 v[172:175], v221 offset:32\n\t" \
  "ds_read_b128 v[168:171], v221\n\t" \
  "ds_read_b64_tr_b16 v[200:201], v244 offset:0\n\t" \
  "ds_read_b64_tr_b16 v[202:203], v244 offset:320\n\t" \
  "ds_read_b64_tr_b16 v[204:205], v244 offset:1280\n\t" \
  "ds_read_b64_tr_b16 v[206:207], v244 offset:1600\n\t" \
  "v_add_u32 v233, v241, v232\n\t" \
  "ds_write_b16_d16_hi v233, v255 offset:5376\n\t" \
  "v_mfma_f32_16x16x32_bf16 v[132:135], v[96:99], v[184:187], 0\n\t" \
  "v_rcp_f32 v192, v192\n\t" \
  "v_rcp_f32 v193, v193\n\t" \
  "v_rcp_f32 v194, v194\n\t" \
  "v_rcp_f32 v195, v195\n\t" \
  "v_mfma_f32_32x32x16_bf16 v[100:115], v[4:7], v[148:151], 0\n\t" \
  "v_exp_f32 v196, v120\n\t" \
  "v_exp_f32 v197, v121\n\t" \
  "v_exp_f32 v198, v122\n\t" \
  "v_exp_f32 v199, v123\n\t" \
  "v_mfma_f32_32x32x16_bf16 v[100:115], v[12:15], v[152:155], v[100:115]\n\t" \
  "v_mul_f32 v192, v116, v192\n\t" \
  "v_mul_f32 v193, v117, v193\n\t" \
  "v_mul_f32 v194, v118, v194\n\t" \
  "v_mul_f32 v195, v119, v195\n\t" \
  "v_add_f32 v196, 1.0, v196\n\t" \
  "v_add_f32 v197, 1.0, v197\n\t" \
  "v_add_f32 v198, 1.0, v198\n\t" \
  "v_add_f32 v199, 1.0, v199\n\t" \
  "v_cvt_pk_bf16_f32 v184, v192, v193\n\t" \
  "v_cvt_pk_bf16_f32 v185, v194, v195\n\t" \
  "v_and_b32 v225, 0xffff0000, v225\n\t" \
  "s_waitcnt lgkmcnt(7)\n\t" \
  "v_lshlrev_b32 v219, 16, v224\n\t" \
  "v_and_b32 v224, 0xffff0000, v224\n\t" \
  "v_sub_f32 v229, v218, v225\n\t" \
  "v_sub_f32 v216, v216, v219\n\t" \
  "v_sub_f32 v217, v217, v224\n\t" \
  "v_mfma_f32_32x32x16_bf16 v[100:115], v[20:23], v[156:159], v[100:115]\n\t" \
  "v_rcp_f32 v196, v196\n\t" \
  "v_rcp_f32 v197, v197\n\t" \
  "v_rcp_f32 v198, v198\n\t" \
  "v_rcp_f32 v199, v199\n\t" \
  "v_mul_f32 v218, v229, v229\n\t" \
  "v_cndmask_b32 v228, v216, v217, vcc\n\t" \
  "v_fmac_f32 v218, v216, v216\n\t" \
  "v_fmac_f32 v218, v217, v217\n\t" \
  "v_cvt_pk_bf16_f32 v218, v218, v218\n\t" \
  "s_waitcnt lgkmcnt(6)\n\t" \
  "v_cndmask_b32_sdwa v175, v175, v218, vcc dst_sel:WORD_1 dst_unused:UNUSED_PRESERVE src0_sel:WORD_1 src1_sel:WORD_1\n\t" \
  "v_mfma_f32_16x16x32_bf16 v[132:135], v[136:139], v[188:191], v[132:135]\n\t" \
  "v_exp_f32 v192, v124\n\t" \
  "v_exp_f32 v193, v125\n\t" \
  "v_exp_f32 v194, v126\n\t" \
  "v_exp_f32 v195, v127\n\t" \
  "ds_read2_b32 v[220:221], v252 offset0:0 offset1:2\n\t" \
  "ds_read2_b32 v[222:223], v252 offset0:4 offset1:6\n\t" \
  "ds_read_b64 v[226:227], v253\n\t" \
  "ds_read_b32 v232, v254\n\t" \
  "v_mfma_f32_32x32x16_bf16 v[100:115], v[28:31], v[160:163], v[100:115]\n\t" \
  "v_mul_f32 v196, v120, v196\n\t" \
  "v_mul_f32 v197, v121, v197\n\t" \
  "v_mul_f32 v198, v122, v198\n\t" \
  "v_mul_f32 v199, v123, v199\n\t" \
  "v_add_u32 v252, 0x100, v252\n\t" \
  "v_add_u32 v253, 0x100, v253\n\t" \
  "v_add_u32 v254, 0x100, v254\n\t" \
  "v_add_f32 v192, 1.0, v192\n\t" \
  "v_add_f32 v193, 1.0, v193\n\t" \
  "v_add_f32 v194, 1.0, v194\n\t" \
  "v_add_f32 v195, 1.0, v195\n\t" \
  "v_cvt_pk_bf16_f32 v186, v196, v197\n\t" \
  "v_cvt_pk_bf16_f32 v187, v198, v199\n\t" \
  "s_waitcnt lgkmcnt(9)\n\t" \
  "v_mfma_f32_32x32x16_bf16 v[100:115], v[36:39], v[168:171], v[100:115]\n\t" \
  "v_rcp_f32 v192, v192\n\t" \
  "v_rcp_f32 v193, v193\n\t" \
  "v_rcp_f32 v194, v194\n\t" \
  "v_rcp_f32 v195, v195\n\t" \
  "v_mfma_f32_32x32x16_bf16 v[100:115], v[44:47], v[172:175], v[100:115]\n\t" \
  "v_exp_f32 v196, v128\n\t" \
  "v_exp_f32 v197, v129\n\t" \
  "v_exp_f32 v198, v130\n\t" \
  "v_exp_f32 v199, v131\n\t" \
  "v_mfma_f32_32x32x16_bf16 v[100:115], v[88:91], v[164:167], v[100:115]\n\t" \
  "v_mul_f32 v192, v124, v192\n\t" \
  "v_mul_f32 v193, v125, v193\n\t" \
  "v_mul_f32 v194, v126, v194\n\t" \
  "v_mul_f32 v195, v127, v195\n\t" \
  "ds_write_b128 v243, v[184:187] offset:0\n\t" \
  "v_add_f32 v196, 1.0, v196\n\t" \
  "v_add_f32 v197, 1.0, v197\n\t" \
  "v_add_f32 v198, 1.0, v198\n\t" \
  "v_add_f32 v199, 1.0, v199\n\t" \
  "v_cvt_pk_bf16_f32 v188, v192, v193\n\t" \
  "v_cvt_pk_bf16_f32 v189, v194, v195\n\t" \
  "s_waitcnt lgkmcnt(8)\n\t" \
  "v_mfma_f32_32x32x16_bf16 v[52:67], v[200:203], v[176:179], v[52:67]\n\t" \
  "v_rcp_f32 v196, v196\n\t" \
  "v_rcp_f32 v197, v197\n\t" \
  "v_rcp_f32 v198, v198\n\t" \
  "v_rcp_f32 v199, v199\n\t" \
  "s_waitcnt lgkmcnt(6)\n\t" \
  "v_mfma_f32_32x32x16_bf16 v[52:67], v[204:207], v[180:183], v[52:67]\n\t" \
  "v_exp_f32 v192, v100\n\t" \
  "v_exp_f32 v193, v101\n\t" \
  "v_exp_f32 v194, v102\n\t" \
  "v_exp_f32 v195, v103\n\t" \
  "s_cmp_eq_u32 %[nt], 0\n\t" \
  "v_mfma_f32_16x16x32_bf16 v[132:135], v[140:143], v[184:187], v[132:135]\n\t" \
  "v_mul_f32 v196, v128, v196\n\t" \
  "v_mul_f32 v197, v129, v197\n\t" \
  "v_mul_f32 v198, v130, v198\n\t" \
  "v_mul_f32 v199, v131, v199\n\t" \
  "v_add_f32 v192, 1.0, v192\n\t" \
  "v_add_f32 v193, 1.0, v193\n\t" \
  "v_add_f32 v194, 1.0, v194\n\t" \
  "v_add_f32 v195, 1.0, v195\n\t" \
  "v_cvt_pk_bf16_f32 v190, v196, v197\n\t" \
  "v_cvt_pk_bf16_f32 v191, v198, v199\n\t" \
  "s_cbranch_scc0 1b\n\t" \
  "s_waitcnt lgkmcnt(0)\n\t" \
  "s_cmp_lg_u32 %[last], 0\n\t" \
  "s_cbranch_scc1 8f\n\t" \
  "ds_write_b128 v243, v[188:191] offset:16\n\t" \
  "ds_read_b64_tr_b16 v[200:201], v244 offset:0\n\t" \
  "ds_read_b64_tr_b16 v[202:203], v244 offset:320\n\t" \
  "ds_read_b64_tr_b16 v[204:205], v244 offset:1280\n\t" \
  "ds_read_b64_tr_b16 v[206:207], v244 offset:1600\n\t" \
  "v_mfma_f32_16x16x32_bf16 v[132:135], v[144:147], v[188:191], v[132:135]\n\t" \
  "s_waitcnt lgkmcnt(0)\n\t" \
  "v_mfma_f32_32x32x16_bf16 v[68:83], v[200:203], v[176:179], v[68:83]\n\t" \
  "v_mfma_f32_32x32x16_bf16 v[68:83], v[204:207], v[180:183], v[68:83]\n\t" \
  "s_nop 8\n\t" \
  "v_mul_f32 v230, v230, v132\n\t" \
  "v_mul_f32 v231, v231, v132\n\t" \
  "v_cvt_pk_bf16_f32 v230, v230, v231\n\t" \
  "ds_write_b16 v245, v230 offset:7456\n\t" \
  "ds_write_b16_d16_hi v241, v230 offset:7584\n\t" \
  "ds_read_b128 v[200:203], v246 offset:7456\n\t" \
  "ds_read_b128 v[208:211], v246 offset:7488\n\t" \
  "s_waitcnt lgkmcnt(0)\n\t" \
  "v_mfma_f32_16x16x32_bf16 v[84:87], v[200:203], v[176:179], v[84:87]\n\t" \
  "v_mfma_f32_16x16x32_bf16 v[84:87], v[208:211], v[180:183], v[84:87]\n\t" \
  "s_branch 9f\n\t" \
  "6:\n\t" \
  "s_cmp_lg_u32 %[last], 0\n\t" \
  "s_cbranch_scc0 9f\n\t" \
  "s_waitcnt lgkmcnt(0)\n\t" \
  "v_cndmask_b32 v224, 0, 24, vcc\n\t" \
  "v_xor_b32 v220, v234, v224\n\t" \
  "v_xor_b32 v221, v235, v224\n\t" \
  "v_xor_b32 v222, v236, v224\n\t" \
  "v_xor_b32 v223, v237, v224\n\t" \
  "v_xor_b32 v216, 16, v220\n\t" \
  "v_xor_b32 v217, 16, v221\n\t" \
  "v_xor_b32 v218, 16, v222\n\t" \
  "v_xor_b32 v219, 16, v223\n\t" \
  "ds_read_b64 v[148:149], v220\n\t" \
  "ds_read_b64 v[150:151], v216\n\t" \
  "ds_read_b64 v[152:153], v221\n\t" \
  "ds_read_b64 v[154:155], v217\n\t" \
  "ds_read_b64 v[156:157], v222\n\t" \
  "ds_read_b64 v[158:159], v218\n\t" \
  "ds_read_b64 v[160:161], v223\n\t" \
  "ds_read_b64 v[162:163], v219\n\t" \
  "ds_read_b128 v[4:7], v247 offset:6144\n\t" \
  "ds_read_b128 v[8:11], v247 offset:7168\n\t" \
  "ds_read_b128 v[12:15], v247 offset:8192\n\t" \
  "ds_read_b128 v[16:19], v247 offset:9216\n\t" \
  "ds_read_b128 v[20:23], v247 offset:10240\n\t" \
  "ds_read_b128 v[24:27], v247 offset:11264\n\t" \
  "v_subrev_u32 v225, %[b1], v241\n\t" \
  "v_lshl_add_u32 v225, v225, 5, v243\n\t" \
  "v_sub_u32 v225, v225, v224\n\t" \
  "s_nop 4\n\t" \
  "v_cvt_pk_bf16_f32 v184, v52, v53\n\t" \
  "v_cvt_pk_bf16_f32 v185, v54, v55\n\t" \
  "v_cvt_pk_bf16_f32 v186, v56, v57\n\t" \
  "v_cvt_pk_bf16_f32 v187, v58, v59\n\t" \
  "v_cvt_pk_bf16_f32 v188, v60, v61\n\t" \
  "v_cvt_pk_bf16_f32 v189, v62, v63\n\t" \
  "v_cvt_pk_bf16_f32 v190, v64, v65\n\t" \
  "v_cvt_pk_bf16_f32 v191, v66, v67\n\t" \
  "v_cvt_pk_bf16_f32 v36, v68, v69\n\t" \
  "v_cvt_pk_bf16_f32 v37, v70, v71\n\t" \
  "v_cvt_pk_bf16_f32 v38, v72, v73\n\t" \
  "v_cvt_pk_bf16_f32 v39, v74, v75\n\t" \
  "v_cvt_pk_bf16_f32 v40, v76, v77\n\t" \
  "v_cvt_pk_bf16_f32 v41, v78, v79\n\t" \
  "v_cvt_pk_bf16_f32 v42, v80, v81\n\t" \
  "v_cvt_pk_bf16_f32 v43, v82, v83\n\t" \
  "s_waitcnt lgkmcnt(5)\n\t" \
  "v_mfma_f32_32x32x16_bf16 v[100:115], v[4:7], v[148:151], 0\n\t" \
  "s_waitcnt lgkmcnt(4)\n\t" \
  "v_mfma_f32_32x32x16_bf16 v[116:131], v[8:11], v[148:151], 0\n\t" \
  "ds_read_b128 v[28:31], v247 offset:12288\n\t" \
  "ds_read_b128 v[32:35], v247 offset:13312\n\t" \
  "s_waitcnt lgkmcnt(5)\n\t" \
  "v_mfma_f32_32x32x16_bf16 v[100:115], v[12:15], v[152:155], v[100:115]\n\t" \
  "s_waitcnt lgkmcnt(4)\n\t" \
  "v_mfma_f32_32x32x16_bf16 v[116:131], v[16:19], v[152:155], v[116:131]\n\t" \
  "ds_read_b128 v[4:7], v247 offset:14336\n\t" \
  "ds_read_b128 v[8:11], v247 offset:15360\n\t" \
  "s_waitcnt lgkmcnt(5)\n\t" \
  "v_mfma_f32_32x32x16_bf16 v[100:115], v[20:23], v[156:159], v[100:115]\n\t" \
  "s_waitcnt lgkmcnt(4)\n\t" \
  "v_mfma_f32_32x32x16_bf16 v[116:131], v[24:27], v[156:159], v[116:131]\n\t" \
  "ds_read_b128 v[12:15], v247 offset:16384\n\t" \
  "ds_read_b128 v[16:19], v247 offset:17408\n\t" \
  "s_waitcnt lgkmcnt(5)\n\t" \
  "v_mfma_f32_32x32x16_bf16 v[100:115], v[28:31], v[160:163], v[100:115]\n\t" \
  "s_waitcnt lgkmcnt(4)\n\t" \
  "v_mfma_f32_32x32x16_bf16 v[116:131], v[32:35], v[160:163], v[116:131]\n\t" \
  "ds_read_b128 v[20:23], v247 offset:18432\n\t" \
  "ds_read_b128 v[24:27], v247 offset:19456\n\t" \
  "s_waitcnt lgkmcnt(5)\n\t" \
  "v_mfma_f32_32x32x16_bf16 v[100:115], v[4:7], v[184:187], v[100:115]\n\t" \
  "ds_read_b128 v[28:31], v247 offset:20480\n\t" \
  "ds_read_b128 v[32:35], v247 offset:21504\n\t" \
  "s_waitcnt lgkmcnt(5)\n\t" \
  "v_mfma_f32_32x32x16_bf16 v[100:115], v[12:15], v[188:191], v[100:115]\n\t" \
  "s_waitcnt lgkmcnt(3)\n\t" \
  "v_mfma_f32_32x32x16_bf16 v[100:115], v[20:23], v[36:39], v[100:115]\n\t" \
  "s_waitcnt lgkmcnt(1)\n\t" \
  "v_mfma_f32_32x32x16_bf16 v[100:115], v[28:31], v[40:43], v[100:115]\n\t" \
  "s_waitcnt lgkmcnt(0)\n\t" \
  "s_branch 10f\n\t" \
  "8:\n\t" \
  "ds_write_b128 v243, v[188:191] offset:16\n\t" \
  "ds_read_b64_tr_b16 v[200:201], v244 offset:0\n\t" \
  "ds_read_b64_tr_b16 v[202:203], v244 offset:320\n\t" \
  "ds_read_b64_tr_b16 v[204:205], v244 offset:1280\n\t" \
  "ds_read_b64_tr_b16 v[206:207], v244 offset:1600\n\t" \
  "v_mfma_f32_16x16x32_bf16 v[132:135], v[144:147], v[188:191], v[132:135]\n\t" \
  "v_cndmask_b32 v224, 0, 24, vcc\n\t" \
  "v_xor_b32 v220, v234, v224\n\t" \
  "v_xor_b32 v221, v235, v224\n\t" \
  "v_xor_b32 v222, v236, v224\n\t" \
  "v_xor_b32 v223, v237, v224\n\t" \
  "v_xor_b32 v216, 16, v220\n\t" \
  "v_xor_b32 v217, 16, v221\n\t" \
  "v_xor_b32 v218, 16, v222\n\t" \
  "v_xor_b32 v219, 16, v223\n\t" \
  "ds_read_b64 v[148:149], v220\n\t" \
  "ds_read_b64 v[150:151], v216\n\t" \
  "ds_read_b64 v[152:153], v221\n\t" \
  "ds_read_b64 v[154:155], v217\n\t" \
  "ds_read_b64 v[156:157], v222\n\t" \
  "ds_read_b64 v[158:159], v218\n\t" \
  "ds_read_b64 v[160:161], v223\n\t" \
  "ds_read_b64 v[162:163], v219\n\t" \
  "ds_read_b128 v[4:7], v247 offset:6144\n\t" \
  "ds_read_b128 v[8:11], v247 offset:7168\n\t" \
  "s_waitcnt lgkmcnt(11)\n\t" \
  "ds_read_b128 v[12:15], v247 offset:8192\n\t" \
  "ds_read_b128 v[16:19], v247 offset:9216\n\t" \
  "ds_read_b128 v[20:23], v247 offset:10240\n\t" \
  "ds_read_b128 v[24:27], v247 offset:11264\n\t" \
  "v_subrev_u32 v225, %[b1], v241\n\t" \
  "v_lshl_add_u32 v225, v225, 5, v243\n\t" \
  "v_sub_u32 v225, v225, v224\n\t" \
  "v_mfma_f32_32x32x16_bf16 v[68:83], v[200:203], v[176:179], v[68:83]\n\t" \
  "s_waitcnt lgkmcnt(5)\n\t" \
  "v_mfma_f32_32x32x16_bf16 v[100:115], v[4:7], v[148:151], 0\n\t" \
  "v_mfma_f32_32x32x16_bf16 v[68:83], v[204:207], v[180:183], v[68:83]\n\t" \
  "s_waitcnt lgkmcnt(4)\n\t" \
  "v_mfma_f32_32x32x16_bf16 v[116:131], v[8:11], v[148:151], 0\n\t" \
  "ds_read_b128 v[28:31], v247 offset:12288\n\t" \
  "ds_read_b128 v[32:35], v247 offset:13312\n\t" \
  "s_waitcnt lgkmcnt(5)\n\t" \
  "v_mfma_f32_32x32x16_bf16 v[100:115], v[12:15], v[152:155], v[100:115]\n\t" \
  "v_mul_f32 v230, v230, v132\n\t" \
  "v_mul_f32 v231, v231, v132\n\t" \
  "v_cvt_pk_bf16_f32 v230, v230, v231\n\t" \
  "ds_write_b16 v245, v230 offset:7456\n\t" \
  "ds_write_b16_d16_hi v241, v230 offset:7584\n\t" \
  "ds_read_b128 v[200:203], v246 offset:7456\n\t" \
  "ds_read_b128 v[208:211], v246 offset:7488\n\t" \
  "v_cvt_pk_bf16_f32 v184, v52, v53\n\t" \
  "v_cvt_pk_bf16_f32 v185, v54, v55\n\t" \
  "v_cvt_pk_bf16_f32 v186, v56, v57\n\t" \
  "v_cvt_pk_bf16_f32 v187, v58, v59\n\t" \
  "s_waitcnt lgkmcnt(8)\n\t" \
  "v_mfma_f32_32x32x16_bf16 v[116:131], v[16:19], v[152:155], v[116:131]\n\t" \
  "ds_read_b128 v[4:7], v247 offset:14336\n\t" \
  "ds_read_b128 v[8:11], v247 offset:15360\n\t" \
  "s_waitcnt lgkmcnt(9)\n\t" \
  "v_mfma_f32_32x32x16_bf16 v[100:115], v[20:23], v[156:159], v[100:115]\n\t" \
  "v_cvt_pk_bf16_f32 v188, v60, v61\n\t" \
  "v_cvt_pk_bf16_f32 v189, v62, v63\n\t" \
  "v_cvt_pk_bf16_f32 v190, v64, v65\n\t" \
  "v_cvt_pk_bf16_f32 v191, v66, v67\n\t" \
  "s_waitcnt lgkmcnt(8)\n\t" \
  "v_mfma_f32_32x32x16_bf16 v[116:131], v[24:27], v[156:159], v[116:131]\n\t" \
  "ds_read_b128 v[12:15], v247 offset:16384\n\t" \
  "ds_read_b128 v[16:19], v247 offset:17408\n\t" \
  "s_waitcnt lgkmcnt(9)\n\t" \
  "v_mfma_f32_32x32x16_bf16 v[100:115], v[28:31], v[160:163], v[100:115]\n\t" \
  "v_cvt_pk_bf16_f32 v36, v68, v69\n\t" \
  "v_cvt_pk_bf16_f32 v37, v70, v71\n\t" \
  "v_cvt_pk_bf16_f32 v38, v72, v73\n\t" \
  "v_cvt_pk_bf16_f32 v39, v74, v75\n\t" \
  "s_waitcnt lgkmcnt(5)\n\t" \
  "v_mfma_f32_16x16x32_bf16 v[84:87], v[200:203], v[176:179], v[84:87]\n\t" \
  "v_cvt_pk_bf16_f32 v40, v76, v77\n\t" \
  "v_cvt_pk_bf16_f32 v41, v78, v79\n\t" \
  "v_cvt_pk_bf16_f32 v42, v80, v81\n\t" \
  "v_cvt_pk_bf16_f32 v43, v82, v83\n\t" \
  "v_mfma_f32_32x32x16_bf16 v[116:131], v[32:35], v[160:163], v[116:131]\n\t" \
  "ds_read_b128 v[20:23], v247 offset:18432\n\t" \
  "ds_read_b128 v[24:27], v247 offset:19456\n\t" \
  "s_waitcnt lgkmcnt(6)\n\t" \
  "v_mfma_f32_16x16x32_bf16 v[84:87], v[208:211], v[180:183], v[84:87]\n\t" \
  "s_waitcnt lgkmcnt(5)\n\t" \
  "v_mfma_f32_32x32x16_bf16 v[100:115], v[4:7], v[184:187], v[100:115]\n\t" \
  "ds_read_b128 v[28:31], v247 offset:20480\n\t" \
  "ds_read_b128 v[32:35], v247 offset:21504\n\t" \
  "s_waitcnt lgkmcnt(5)\n\t" \
  "v_mfma_f32_32x32x16_bf16 v[100:115], v[12:15], v[188:191], v[100:115]\n\t" \
  "s_waitcnt lgkmcnt(3)\n\t" \
  "v_mfma_f32_32x32x16_bf16 v[100:115], v[20:23], v[36:39], v[100:115]\n\t" \
  "s_waitcnt lgkmcnt(1)\n\t" \
  "v_mfma_f32_32x32x16_bf16 v[100:115], v[28:31], v[40:43], v[100:115]\n\t" \
  "s_waitcnt lgkmcnt(0)\n\t" \
  "10:\n\t" \
  "v_mov_b32 v212, 0xbfb8aa3b\n\t" \
  "v_mfma_f32_32x32x16_bf16 v[116:131], v[8:11], v[184:187], v[116:131]\n\t" \
  "v_mfma_f32_32x32x16_bf16 v[116:131], v[16:19], v[188:191], v[116:131]\n\t" \
  "s_nop 8\n\t" \
  "v_exp_f32 v192, v100\n\t" \
  "v_exp_f32 v193, v101\n\t" \
  "v_exp_f32 v194, v102\n\t" \
  "v_exp_f32 v195, v103\n\t" \
  "v_lshlrev_b32 v200, 16, v148\n\t" \
  "v_and_b32 v201, 0xffff0000, v148\n\t" \
  "v_lshlrev_b32 v202, 16, v149\n\t" \
  "v_and_b32 v203, 0xffff0000, v149\n\t" \
  "v_mfma_f32_32x32x16_bf16 v[116:131], v[24:27], v[36:39], v[116:131]\n\t" \
  "v_fmaak_f32 v192, v192, v212, 0xbfb8aa3b\n\t" \
  "v_fmaak_f32 v193, v193, v212, 0xbfb8aa3b\n\t" \
  "v_fmaak_f32 v194, v194, v212, 0xbfb8aa3b\n\t" \
  "v_fmaak_f32 v195, v195, v212, 0xbfb8aa3b\n\t" \
  "v_rcp_f32 v192, v192\n\t" \
  "v_rcp_f32 v193, v193\n\t" \
  "v_rcp_f32 v194, v194\n\t" \
  "v_rcp_f32 v195, v195\n\t" \
  "v_mfma_f32_32x32x16_bf16 v[116:131], v[32:35], v[40:43], v[116:131]\n\t" \
  "v_exp_f32 v196, v104\n\t" \
  "v_exp_f32 v197, v105\n\t" \
  "v_exp_f32 v198, v106\n\t" \
  "v_exp_f32 v199, v107\n\t" \
  "v_fmac_f32 v200, v100, v192\n\t" \
  "v_fmac_f32 v201, v101, v193\n\t" \
  "v_fmac_f32 v202, v102, v194\n\t" \
  "v_fmac_f32 v203, v103, v195\n\t" \
  "v_lshlrev_b32 v204, 16, v150\n\t" \
  "v_and_b32 v205, 0xffff0000, v150\n\t" \
  "v_lshlrev_b32 v206, 16, v151\n\t" \
  "v_and_b32 v207, 0xffff0000, v151\n\t" \
  "v_fmaak_f32 v196, v196, v212, 0xbfb8aa3b\n\t" \
  "v_fmaak_f32 v197, v197, v212, 0xbfb8aa3b\n\t" \
  "v_fmaak_f32 v198, v198, v212, 0xbfb8aa3b\n\t" \
  "v_fmaak_f32 v199, v199, v212, 0xbfb8aa3b\n\t" \
  "v_cvt_pk_bf16_f32 v176, v200, v201\n\t" \
  "v_cvt_pk_bf16_f32 v177, v202, v203\n\t" \
  "ds_write_b64 v225, v[176:177] offset:0\n\t" \
  "v_rcp_f32 v196, v196\n\t" \
  "v_rcp_f32 v197, v197\n\t" \
  "v_rcp_f32 v198, v198\n\t" \
  "v_rcp_f32 v199, v199\n\t" \
  "v_exp_f32 v192, v108\n\t" \
  "v_exp_f32 v193, v109\n\t" \
  "v_exp_f32 v194, v110\n\t" \
  "v_exp_f32 v195, v111\n\t" \
  "v_fmac_f32 v204, v104, v196\n\t" \
  "v_fmac_f32 v205, v105, v197\n\t" \
  "v_fmac_f32 v206, v106, v198\n\t" \
  "v_fmac_f32 v207, v107, v199\n\t" \
  "v_lshlrev_b32 v200, 16, v152\n\t" \
  "v_and_b32 v201, 0xffff0000, v152\n\t" \
  "v_lshlrev_b32 v202, 16, v153\n\t" \
  "v_and_b32 v203, 0xffff0000, v153\n\t" \
  "v_fmaak_f32 v192, v192, v212, 0xbfb8aa3b\n\t" \
  "v_fmaak_f32 v193, v193, v212, 0xbfb8aa3b\n\t" \
  "v_fmaak_f32 v194, v194, v212, 0xbfb8aa3b\n\t" \
  "v_fmaak_f32 v195, v195, v212, 0xbfb8aa3b\n\t" \
  "v_cvt_pk_bf16_f32 v178, v204, v205\n\t" \
  "v_cvt_pk_bf16_f32 v179, v206, v207\n\t" \
  "ds_write_b64 v225, v[178:179] offset:16\n\t" \
  "v_rcp_f32 v192, v192\n\t" \
  "v_rcp_f32 v193, v193\n\t" \
  "v_rcp_f32 v194, v194\n\t" \
  "v_rcp_f32 v195, v195\n\t" \
  "v_exp_f32 v196, v112\n\t" \
  "v_exp_f32 v197, v113\n\t" \
  "v_exp_f32 v198, v114\n\t" \
  "v_exp_f32 v199, v115\n\t" \
  "v_fmac_f32 v200, v108, v192\n\t" \
  "v_fmac_f32 v201, v109, v193\n\t" \
  "v_fmac_f32 v202, v110, v194\n\t" \
  "v_fmac_f32 v203, v111, v195\n\t" \
  "v_lshlrev_b32 v204, 16, v154\n\t" \
  "v_and_b32 v205, 0xffff0000, v154\n\t" \
  "v_lshlrev_b32 v206, 16, v155\n\t" \
  "v_and_b32 v207, 0xffff0000, v155\n\t" \
  "v_fmaak_f32 v196, v196, v212, 0xbfb8aa3b\n\t" \
  "v_fmaak_f32 v197, v197, v212, 0xbfb8aa3b\n\t" \
  "v_fmaak_f32 v198, v198, v212, 0xbfb8aa3b\n\t" \
  "v_fmaak_f32 v199, v199, v212, 0xbfb8aa3b\n\t" \
  "v_cvt_pk_bf16_f32 v180, v200, v201\n\t" \
  "v_cvt_pk_bf16_f32 v181, v202, v203\n\t" \
  "ds_write_b64 v225, v[180:181] offset:32\n\t" \
  "v_rcp_f32 v196, v196\n\t" \
  "v_rcp_f32 v197, v197\n\t" \
  "v_rcp_f32 v198, v198\n\t" \
  "v_rcp_f32 v199, v199\n\t" \
  "v_exp_f32 v192, v116\n\t" \
  "v_exp_f32 v193, v117\n\t" \
  "v_exp_f32 v194, v118\n\t" \
  "v_exp_f32 v195, v119\n\t" \
  "v_fmac_f32 v204, v112, v196\n\t" \
  "v_fmac_f32 v205, v113, v197\n\t" \
  "v_fmac_f32 v206, v114, v198\n\t" \
  "v_fmac_f32 v207, v115, v199\n\t" \
  "v_lshlrev_b32 v200, 16, v156\n\t" \
  "v_and_b32 v201, 0xffff0000, v156\n\t" \
  "v_lshlrev_b32 v202, 16, v157\n\t" \
  "v_and_b32 v203, 0xffff0000, v157\n\t" \
  "v_fmaak_f32 v192, v192, v212, 0xbfb8aa3b\n\t" \
  "v_fmaak_f32 v193, v193, v212, 0xbfb8aa3b\n\t" \
  "v_fmaak_f32 v194, v194, v212, 0xbfb8aa3b\n\t" \
  "v_fmaak_f32 v195, v195, v212, 0xbfb8aa3b\n\t" \
  "v_cvt_pk_bf16_f32 v182, v204, v205\n\t" \
  "v_cvt_pk_bf16_f32 v183, v206, v207\n\t" \
  "ds_write_b64 v225, v[182:183] offset:48\n\t" \
  "v_rcp_f32 v192, v192\n\t" \
  "v_rcp_f32 v193, v193\n\t" \
  "v_rcp_f32 v194, v194\n\t" \
  "v_rcp_f32 v195, v195\n\t" \
  "v_exp_f32 v196, v120\n\t" \
  "v_exp_f32 v197, v121\n\t" \
  "v_exp_f32 v198, v122\n\t" \
  "v_exp_f32 v199, v123\n\t" \
  "v_fmac_f32 v200, v116, v192\n\t" \
  "v_fmac_f32 v201, v117, v193\n\t" \
  "v_fmac_f32 v202, v118, v194\n\t" \
  "v_fmac_f32 v203, v119, v195\n\t" \
  "v_lshlrev_b32 v204, 16, v158\n\t" \
  "v_and_b32 v205, 0xffff0000, v158\n\t" \
  "v_lshlrev_b32 v206, 16, v159\n\t" \
  "v_and_b32 v207, 0xffff0000, v159\n\t" \
  "v_fmaak_f32 v196, v196, v212, 0xbfb8aa3b\n\t" \
  "v_fmaak_f32 v197, v197, v212, 0xbfb8aa3b\n\t" \
  "v_fmaak_f32 v198, v198, v212, 0xbfb8aa3b\n\t" \
  "v_fmaak_f32 v199, v199, v212, 0xbfb8aa3b\n\t" \
  "v_cvt_pk_bf16_f32 v176, v200, v201\n\t" \
  "v_cvt_pk_bf16_f32 v177, v202, v203\n\t" \
  "ds_write_b64 v225, v[176:177] offset:64\n\t" \
  "v_rcp_f32 v196, v196\n\t" \
  "v_rcp_f32 v197, v197\n\t" \
  "v_rcp_f32 v198, v198\n\t" \
  "v_rcp_f32 v199, v199\n\t" \
  "v_exp_f32 v192, v124\n\t" \
  "v_exp_f32 v193, v125\n\t" \
  "v_exp_f32 v194, v126\n\t" \
  "v_exp_f32 v195, v127\n\t" \
  "v_fmac_f32 v204, v120, v196\n\t" \
  "v_fmac_f32 v205, v121, v197\n\t" \
  "v_fmac_f32 v206, v122, v198\n\t" \
  "v_fmac_f32 v207, v123, v199\n\t" \
  "v_lshlrev_b32 v200, 16, v160\n\t" \
  "v_and_b32 v201, 0xffff0000, v160\n\t" \
  "v_lshlrev_b32 v202, 16, v161\n\t" \
  "v_and_b32 v203, 0xffff0000, v161\n\t" \
  "v_fmaak_f32 v192, v192, v212, 0xbfb8aa3b\n\t" \
  "v_fmaak_f32 v193, v193, v212, 0xbfb8aa3b\n\t" \
  "v_fmaak_f32 v194, v194, v212, 0xbfb8aa3b\n\t" \
  "v_fmaak_f32 v195, v195, v212, 0xbfb8aa3b\n\t" \
  "v_cvt_pk_bf16_f32 v178, v204, v205\n\t" \
  "v_cvt_pk_bf16_f32 v179, v206, v207\n\t" \
  "ds_write_b64 v225, v[178:179] offset:80\n\t" \
  "v_rcp_f32 v192, v192\n\t" \
  "v_rcp_f32 v193, v193\n\t" \
  "v_rcp_f32 v194, v194\n\t" \
  "v_rcp_f32 v195, v195\n\t" \
  "v_exp_f32 v196, v128\n\t" \
  "v_exp_f32 v197, v129\n\t" \
  "v_exp_f32 v198, v130\n\t" \
  "v_exp_f32 v199, v131\n\t" \
  "v_fmac_f32 v200, v124, v192\n\t" \
  "v_fmac_f32 v201, v125, v193\n\t" \
  "v_fmac_f32 v202, v126, v194\n\t" \
  "v_fmac_f32 v203, v127, v195\n\t" \
  "v_lshlrev_b32 v204, 16, v162\n\t" \
  "v_and_b32 v205, 0xffff0000, v162\n\t" \
  "v_lshlrev_b32 v206, 16, v163\n\t" \
  "v_and_b32 v207, 0xffff0000, v163\n\t" \
  "v_fmaak_f32 v196, v196, v212, 0xbfb8aa3b\n\t" \
  "v_fmaak_f32 v197, v197, v212, 0xbfb8aa3b\n\t" \
  "v_fmaak_f32 v198, v198, v212, 0xbfb8aa3b\n\t" \
  "v_fmaak_f32 v199, v199, v212, 0xbfb8aa3b\n\t" \
  "v_cvt_pk_bf16_f32 v180, v200, v201\n\t" \
  "v_cvt_pk_bf16_f32 v181, v202, v203\n\t" \
  "ds_write_b64 v225, v[180:181] offset:96\n\t" \
  "v_rcp_f32 v196, v196\n\t" \
  "v_rcp_f32 v197, v197\n\t" \
  "v_rcp_f32 v198, v198\n\t" \
  "v_rcp_f32 v199, v199\n\t" \
  "s_nop 0\n\t" \
  "v_fmac_f32 v204, v128, v196\n\t" \
  "v_fmac_f32 v205, v129, v197\n\t" \
  "v_fmac_f32 v206, v130, v198\n\t" \
  "v_fmac_f32 v207, v131, v199\n\t" \
  "v_cvt_pk_bf16_f32 v182, v204, v205\n\t" \
  "v_cvt_pk_bf16_f32 v183, v206, v207\n\t" \
  "ds_write_b64 v225, v[182:183] offset:112\n\t" \
  "9:\n\t" \
  "ds_write_b16 v233, v255 offset:5376\n\t" \
  "s_mov_b32 m0, %[keep]\n\t" \
  "s_setprio 0\n\t" \
  "s_nop 11\n\t" \
  ""
#define LOOP_ASM_PLAIN \
  "s_mov_b32 %[keep], m0\n\t" \
  "s_mov_b64 vcc, %[hsel]\n\t" \
  "ds_read2_b32 v[220:221], v252 offset0:0 offset1:2\n\t" \
  "ds_read2_b32 v[222:223], v252 offset0:4 offset1:6\n\t" \
  "ds_read_b64 v[226:227], v253\n\t" \
  "ds_read_b32 v232, v254\n\t" \
  "v_mov_b32 v176, 0\n\t" \
  "v_mov_b32 v177, 0\n\t" \
  "v_mov_b32 v178, 0\n\t" \
  "v_mov_b32 v179, 0\n\t" \
  "v_mov_b32 v180, 0\n\t" \
  "v_mov_b32 v181, 0\n\t" \
  "v_mov_b32 v182, 0\n\t" \
  "v_mov_b32 v183, 0\n\t" \
  "v_mov_b32 v184, 0\n\t" \
  "v_mov_b32 v185, 0\n\t" \
  "v_mov_b32 v186, 0\n\t" \
  "v_mov_b32 v187, 0\n\t" \
  "v_mov_b32 v188, 0\n\t" \
  "v_mov_b32 v189, 0\n\t" \
  "v_mov_b32 v190, 0\n\t" \
  "v_mov_b32 v191, 0\n\t" \
  "v_mov_b32 v230, 0\n\t" \
  "v_mov_b32 v231, 0\n\t" \
  "v_mov_b32 v208, 0\n\t" \
  "v_mov_b32 v209, 0\n\t" \
  "v_mov_b32 v210, 0\n\t" \
  "v_mov_b32 v211, 0\n\t" \
  "ds_write_b128 v243, v[184:187] offset:0\n\t" \
  "s_nop 0\n\t" \
  "v_mfma_f32_16x16x32_bf16 v[132:135], v[184:187], v[188:191], 0\n\t" \
  "v_add_u32 v252, 0x100, v252\n\t" \
  "v_add_u32 v253, 0x100, v253\n\t" \
  "v_add_u32 v254, 0x100, v254\n\t" \
  "s_waitcnt lgkmcnt(0)\n\t" \
  "v_lshrrev_b32 v232, 20, v232\n\t" \
  "v_add_u32 v233, %[posd], v232\n\t" \
  "ds_read_b128 v[216:219], v233\n\t" \
  "v_add_u32 v233, v240, v232\n\t" \
  "ds_read_b128 v[172:175], v233 offset:32\n\t" \
  "ds_read_b128 v[168:171], v233\n\t" \
  "v_add_u32 v233, v241, v232\n\t" \
  "ds_write_b16_d16_hi v233, v255 offset:5376\n\t" \
  "v_mad_u32_u16 v220, v220, %[c128], v248\n\t" \
  "v_mad_u32_u16 v221, v221, %[c128], v249\n\t" \
  "v_mad_u32_u16 v222, v222, %[c128], v248\n\t" \
  "v_mad_u32_u16 v223, v223, %[c128], v249\n\t" \
  "v_or_b32 v226, v226, v250\n\t" \
  "v_mad_u32_u16 v227, v227, 6, v251\n\t" \
  "s_waitcnt vmcnt(0)\n\t" \
  "ds_read_b128 v[148:151], v234\n\t" \
  "ds_read_b128 v[152:155], v235\n\t" \
  "ds_read_b128 v[156:159], v236\n\t" \
  "ds_read_b128 v[160:163], v237\n\t" \
  "ds_read_b128 v[164:167], v238 offset:4096\n\t" \
  "ds_read_b64 v[224:225], v239 offset:5120\n\t" \
  "s_waitcnt lgkmcnt(0)\n\t" \
  "s_cmp_lt_i32 %[nt], 1\n\t" \
  "s_cbranch_scc1 7f\n\t" \
  "s_mov_b32 m0, %[b2]\n\t" \
  "s_nop 0\n\t" \
  "global_load_lds_dwordx4 v226, %[ea]\n\t" \
  "global_load_lds_dword v227, %[pos] offset:1024\n\t" \
  "s_mov_b32 m0, %[b1]\n\t" \
  "s_nop 0\n\t" \
  "global_load_lds_dwordx4 v220, %[h0]\n\t" \
  "global_load_lds_dwordx4 v221, %[h1] offset:1024\n\t" \
  "global_load_lds_dwordx4 v222, %[h2] offset:2048\n\t" \
  "global_load_lds_dwordx4 v223, %[h3] offset:3072\n\t" \
  "7:\n\t" \
  "ds_read2_b32 v[220:221], v252 offset0:0 offset1:2\n\t" \
  "ds_read2_b32 v[222:223], v252 offset0:4 offset1:6\n\t" \
  "ds_read_b64 v[226:227], v253\n\t" \
  "ds_read_b32 v232, v254\n\t" \
  "v_add_u32 v252, 0x100, v252\n\t" \
  "v_add_u32 v253, 0x100, v253\n\t" \
  "v_add_u32 v254, 0x100, v254\n\t" \
  "v_and_b32 v225, 0xffff0000, v225\n\t" \
  "v_lshlrev_b32 v219, 16, v224\n\t" \
  "v_and_b32 v224, 0xffff0000, v224\n\t" \
  "v_sub_f32 v229, v218, v225\n\t" \
  "v_sub_f32 v216, v216, v219\n\t" \
  "v_sub_f32 v217, v217, v224\n\t" \
  "v_mul_f32 v218, v229, v229\n\t" \
  "v_cndmask_b32 v228, v216, v217, vcc\n\t" \
  "v_fmac_f32 v218, v216, v216\n\t" \
  "v_fmac_f32 v218, v217, v217\n\t" \
  "v_cvt_pk_bf16_f32 v218, v218, v218\n\t" \
  "v_cndmask_b32_sdwa v175, v175, v218, vcc dst_sel:WORD_1 dst_unused:UNUSED_PRESERVE src0_sel:WORD_1 src1_sel:WORD_1\n\t" \
  "v_mfma_f32_32x32x16_bf16 v[100:115], v[4:7], v[148:151], 0\n\t" \
  "v_mfma_f32_32x32x16_bf16 v[100:115], v[12:15], v[152:155], v[100:115]\n\t" \
  "v_mfma_f32_32x32x16_bf16 v[100:115], v[20:23], v[156:159], v[100:115]\n\t" \
  "v_mfma_f32_32x32x16_bf16 v[100:115], v[28:31], v[160:163], v[100:115]\n\t" \
  "v_mfma_f32_32x32x16_bf16 v[100:115], v[36:39], v[168:171], v[100:115]\n\t" \
  "v_mfma_f32_32x32x16_bf16 v[100:115], v[88:91], v[164:167], v[100:115]\n\t" \
  "v_mfma_f32_32x32x16_bf16 v[100:115], v[44:47], v[172:175], v[100:115]\n\t" \
  "s_cmp_eq_u32 %[nt], 0\n\t" \
  "s_nop 10\n\t" \
  "v_mul_f32 v192, 0xbfb8aa3b, v100\n\t" \
  "v_mul_f32 v193, 0xbfb8aa3b, v101\n\t" \
  "v_mul_f32 v194, 0xbfb8aa3b, v102\n\t" \
  "v_mul_f32 v195, 0xbfb8aa3b, v103\n\t" \
  "v_exp_f32 v192, v192\n\t" \
  "v_exp_f32 v193, v193\n\t" \
  "v_exp_f32 v194, v194\n\t" \
  "v_exp_f32 v195, v195\n\t" \
  "s_nop 0\n\t" \
  "v_add_f32 v192, 1.0, v192\n\t" \
  "v_add_f32 v193, 1.0, v193\n\t" \
  "v_add_f32 v194, 1.0, v194\n\t" \
  "v_add_f32 v195, 1.0, v195\n\t" \
  "s_cbranch_scc1 6f\n\t" \
  "s_cmp_lt_i32 %[thr], 0\n\t" \
  "s_cbranch_scc1 1f\n\t" \
  "s_setprio 1\n\t" \
  "1:\n\t" \
  "ds_write_b128 v243, v[188:191] offset:16\n\t" \
  "ds_read_b64_tr_b16 v[200:201], v244 offset:0\n\t" \
  "ds_read_b64_tr_b16 v[202:203], v244 offset:320\n\t" \
  "ds_read_b64_tr_b16 v[204:205], v244 offset:1280\n\t" \
  "ds_read_b64_tr_b16 v[206:207], v244 offset:1600\n\t" \
  "v_mfma_f32_16x16x32_bf16 v[132:135], v[144:147], v[188:191], v[132:135]\n\t" \
  "v_rcp_f32 v192, v192\n\t" \
  "v_rcp_f32 v193, v193\n\t" \
  "v_rcp_f32 v194, v194\n\t" \
  "v_rcp_f32 v195, v195\n\t" \
  "v_mfma_f32_32x32x16_bf16 v[116:131], v[8:11], v[148:151], 0\n\t" \
  "v_mul_f32 v196, 0xbfb8aa3b, v104\n\t" \
  "v_mul_f32 v197, 0xbfb8aa3b, v105\n\t" \
  "v_mul_f32 v198, 0xbfb8aa3b, v106\n\t" \
  "v_mul_f32 v199, 0xbfb8aa3b, v107\n\t" \
  "v_exp_f32 v196, v196\n\t" \
  "v_exp_f32 v197, v197\n\t" \
  "v_exp_f32 v198, v198\n\t" \
  "v_exp_f32 v199, v199\n\t" \
  "v_mfma_f32_32x32x16_bf16 v[116:131], v[16:19], v[152:155], v[116:131]\n\t" \
  "v_mul_f32 v192, v100, v192\n\t" \
  "v_mul_f32 v193, v101, v193\n\t" \
  "v_mul_f32 v194, v102, v194\n\t" \
  "v_mul_f32 v195, v103, v195\n\t" \
  "v_add_f32 v196, 1.0, v196\n\t" \
  "v_add_f32 v197, 1.0, v197\n\t" \
  "v_add_f32 v198, 1.0, v198\n\t" \
  "v_add_f32 v199, 1.0, v199\n\t" \
  "v_cvt_pk_bf16_f32 v184, v192, v193\n\t" \
  "v_cvt_pk_bf16_f32 v185, v194, v195\n\t" \
  "v_mfma_f32_32x32x16_bf16 v[116:131], v[24:27], v[156:159], v[116:131]\n\t" \
  "v_rcp_f32 v196, v196\n\t" \
  "v_rcp_f32 v197, v197\n\t" \
  "v_rcp_f32 v198, v198\n\t" \
  "v_rcp_f32 v199, v199\n\t" \
  "s_waitcnt lgkmcnt(2)\n\t" \
  "v_mfma_f32_32x32x16_bf16 v[68:83], v[200:203], v[176:179], v[68:83]\n\t" \
  "v_mul_f32 v192, 0xbfb8aa3b, v108\n\t" \
  "v_mul_f32 v193, 0xbfb8aa3b, v109\n\t" \
  "v_mul_f32 v194, 0xbfb8aa3b, v110\n\t" \
  "v_mul_f32 v195, 0xbfb8aa3b, v111\n\t" \
  "v_exp_f32 v192, v192\n\t" \
  "v_exp_f32 v193, v193\n\t" \
  "v_exp_f32 v194, v194\n\t" \
  "v_exp_f32 v195, v195\n\t" \
  "v_mfma_f32_32x32x16_bf16 v[116:131], v[32:35], v[160:163], v[116:131]\n\t" \
  "v_mul_f32 v196, v104, v196\n\t" \
  "v_mul_f32 v197, v105, v197\n\t" \
  "v_mul_f32 v198, v106, v198\n\t" \
  "v_mul_f32 v199, v107, v199\n\t" \
  "v_mul_f32 v230, v230, v132\n\t" \
  "v_mul_f32 v231, v231, v132\n\t" \
  "v_cvt_pk_bf16_f32 v230, v230, v231\n\t" \
  "ds_write_b16 v245, v230 offset:7456\n\t" \
  "ds_write_b16_d16_hi v241, v230 offset:7584\n\t" \
  "ds_read_b128 v[200:203], v246 offset:7456\n\t" \
  "ds_read_b128 v[208:211], v246 offset:7488\n\t" \
  "v_mov_b32 v230, v228\n\t" \
  "v_mov_b32 v231, v229\n\t" \
  "v_add_f32 v192, 1.0, v192\n\t" \
  "v_add_f32 v193, 1.0, v193\n\t" \
  "v_add_f32 v194, 1.0, v194\n\t" \
  "v_add_f32 v195, 1.0, v195\n\t" \
  "v_cvt_pk_bf16_f32 v186, v196, v197\n\t" \
  "v_cvt_pk_bf16_f32 v187, v198, v199\n\t" \
  "v_mfma_f32_32x32x16_bf16 v[116:131], v[40:43], v[168:171], v[116:131]\n\t" \
  "v_rcp_f32 v192, v192\n\t" \
  "v_rcp_f32 v193, v193\n\t" \
  "v_rcp_f32 v194, v194\n\t" \
  "v_rcp_f32 v195, v195\n\t" \
  "s_sub_u32 %[nt], %[nt], 1\n\t" \
  "s_cmp_lg_u32 %[nt], %[thr]\n\t" \
  "s_cbranch_scc1 3f\n\t" \
  "s_setprio 0\n\t" \
  "3:\n\t" \
  "v_mfma_f32_32x32x16_bf16 v[116:131], v[92:95], v[164:167], v[116:131]\n\t" \
  "v_mul_f32 v196, 0xbfb8aa3b, v112\n\t" \
  "v_mul_f32 v197, 0xbfb8aa3b, v113\n\t" \
  "v_mul_f32 v198, 0xbfb8aa3b, v114\n\t" \
  "v_mul_f32 v199, 0xbfb8aa3b, v115\n\t" \
  "v_exp_f32 v196, v196\n\t" \
  "v_exp_f32 v197, v197\n\t" \
  "v_exp_f32 v198, v198\n\t" \
  "v_exp_f32 v199, v199\n\t" \
  "v_mfma_f32_32x32x16_bf16 v[116:131], v[48:51], v[172:175], v[116:131]\n\t" \
  "v_mul_f32 v192, v108, v192\n\t" \
  "v_mul_f32 v193, v109, v193\n\t" \
  "v_mul_f32 v194, v110, v194\n\t" \
  "v_mul_f32 v195, v111, v195\n\t" \
  "s_waitcnt vmcnt(0)\n\t" \
  "ds_read_b128 v[148:151], v234\n\t" \
  "ds_read_b128 v[152:155], v235\n\t" \
  "ds_read_b128 v[156:159], v236\n\t" \
  "ds_read_b128 v[160:163], v237\n\t" \
  "ds_read_b128 v[164:167], v238 offset:4096\n\t" \
  "ds_read_b64 v[224:225], v239 offset:5120\n\t" \
  "ds_write_b128 v243, v[184:187] offset:0\n\t" \
  "v_add_f32 v196, 1.0, v196\n\t" \
  "v_add_f32 v197, 1.0, v197\n\t" \
  "v_add_f32 v198, 1.0, v198\n\t" \
  "v_add_f32 v199, 1.0, v199\n\t" \
  "v_cvt_pk_bf16_f32 v188, v192, v193\n\t" \
  "v_cvt_pk_bf16_f32 v189, v194, v195\n\t" \
  "v_mad_u32_u16 v220, v220, %[c128], v248\n\t" \
  "v_mad_u32_u16 v221, v221, %[c128], v249\n\t" \
  "v_mad_u32_u16 v222, v222, %[c128], v248\n\t" \
  "s_waitcnt lgkmcnt(11)\n\t" \
  "v_mfma_f32_32x32x16_bf16 v[68:83], v[204:207], v[180:183], v[68:83]\n\t" \
  "v_rcp_f32 v196, v196\n\t" \
  "v_rcp_f32 v197, v197\n\t" \
  "v_rcp_f32 v198, v198\n\t" \
  "v_rcp_f32 v199, v199\n\t" \
  "v_mad_u32_u16 v223, v223, %[c128], v249\n\t" \
  "v_or_b32 v226, v226, v250\n\t" \
  "v_mad_u32_u16 v227, v227, 6, v251\n\t" \
  "s_waitcnt lgkmcnt(8)\n\t" \
  "v_mfma_f32_16x16x32_bf16 v[84:87], v[200:203], v[176:179], v[84:87]\n\t" \
  "v_mul_f32 v192, 0xbfb8aa3b, v116\n\t" \
  "v_mul_f32 v193, 0xbfb8aa3b, v117\n\t" \
  "v_mul_f32 v194, 0xbfb8aa3b, v118\n\t" \
  "v_mul_f32 v195, 0xbfb8aa3b, v119\n\t" \
  "v_exp_f32 v192, v192\n\t" \
  "v_exp_f32 v193, v193\n\t" \
  "v_exp_f32 v194, v194\n\t" \
  "v_exp_f32 v195, v195\n\t" \
  "s_waitcnt lgkmcnt(7)\n\t" \
  "v_mfma_f32_16x16x32_bf16 v[84:87], v[208:211], v[180:183], v[84:87]\n\t" \
  "v_mul_f32 v196, v112, v196\n\t" \
  "v_mul_f32 v197, v113, v197\n\t" \
  "v_mul_f32 v198, v114, v198\n\t" \
  "v_mul_f32 v199, v115, v199\n\t" \
  "s_waitcnt lgkmcnt(1)\n\t" \
  "s_cmp_lt_i32 %[nt], 1\n\t" \
  "s_cbranch_scc1 4f\n\t" \
  "s_mov_b32 m0, %[b2]\n\t" \
  "s_nop 0\n\t" \
  "global_load_lds_dwordx4 v226, %[ea]\n\t" \
  "global_load_lds_dword v227, %[pos] offset:1024\n\t" \
  "s_mov_b32 m0, %[b1]\n\t" \
  "s_nop 0\n\t" \
  "global_load_lds_dwordx4 v220, %[h0]\n\t" \
  "global_load_lds_dwordx4 v221, %[h1] offset:1024\n\t" \
  "global_load_lds_dwordx4 v222, %[h2] offset:2048\n\t" \
  "global_load_lds_dwordx4 v223, %[h3] offset:3072\n\t" \
  "4:\n\t" \
  "v_add_f32 v192, 1.0, v192\n\t" \
  "v_add_f32 v193, 1.0, v193\n\t" \
  "v_add_f32 v194, 1.0, v194\n\t" \
  "v_add_f32 v195, 1.0, v195\n\t" \
  "v_cvt_pk_bf16_f32 v190, v196, v197\n\t" \
  "v_cvt_pk_bf16_f32 v191, v198, v199\n\t" \
  "v_lshrrev_b32 v232, 20, v232\n\t" \
  "v_add_u32 v220, %[posd], v232\n\t" \
  "v_add_u32 v221, v240, v232\n\t" \
  "ds_write_b128 v243, v[188:191] offset:16\n\t" \
  "ds_read_b128 v[176:179], v242 offset:5376\n\t" \
  "ds_read_b128 v[180:183], v242 offset:5408\n\t" \
  "ds_write_b16 v233, v255 offset:5376\n\t" \
  "ds_read_b128 v[216:219], v220\n\t" \
  "ds_read_b128 v[172:175], v221 offset:32\n\t" \
  "ds_read_b128 v[168:171], v221\n\t" \
  "ds_read_b64_tr_b16 v[200:201], v244 offset:0\n\t" \
  "ds_read_b64_tr_b16 v[202:203], v244 offset:320\n\t" \
  "ds_read_b64_tr_b16 v[204:205], v244 offset:1280\n\t" \
  "ds_read_b64_tr_b16 v[206:207], v244 offset:1600\n\t" \
  "v_add_u32 v233, v241, v232\n\t" \
  "ds_write_b16_d16_hi v233, v255 offset:5376\n\t" \
  "v_mfma_f32_16x16x32_bf16 v[132:135], v[96:99], v[184:187], 0\n\t" \
  "v_rcp_f32 v192, v192\n\t" \
  "v_rcp_f32 v193, v193\n\t" \
  "v_rcp_f32 v194, v194\n\t" \
  "v_rcp_f32 v195, v195\n\t" \
  "v_mfma_f32_32x32x16_bf16 v[100:115], v[4:7], v[148:151], 0\n\t" \
  "v_mul_f32 v196, 0xbfb8aa3b, v120\n\t" \
  "v_mul_f32 v197, 0xbfb8aa3b, v121\n\t" \
  "v_mul_f32 v198, 0xbfb8aa3b, v122\n\t" \
  "v_mul_f32 v199, 0xbfb8aa3b, v123\n\t" \
  "v_exp_f32 v196, v196\n\t" \
  "v_exp_f32 v197, v197\n\t" \
  "v_exp_f32 v198, v198\n\t" \
  "v_exp_f32 v199, v199\n\t" \
  "v_mfma_f32_32x32x16_bf16 v[100:115], v[12:15], v[152:155], v[100:115]\n\t" \
  "v_mul_f32 v192, v116, v192\n\t" \
  "v_mul_f32 v193, v117, v193\n\t" \
  "v_mul_f32 v194, v118, v194\n\t" \
  "v_mul_f32 v195, v119, v195\n\t" \
  "v_add_f32 v196, 1.0, v196\n\t" \
  "v_add_f32 v197, 1.0, v197\n\t" \
  "v_add_f32 v198, 1.0, v198\n\t" \
  "v_add_f32 v199, 1.0, v199\n\t" \
  "v_cvt_pk_bf16_f32 v184, v192, v193\n\t" \
  "v_cvt_pk_bf16_f32 v185, v194, v195\n\t" \
  "v_and_b32 v225, 0xffff0000, v225\n\t" \
  "s_waitcnt lgkmcnt(7)\n\t" \
  "v_lshlrev_b32 v219, 16, v224\n\t" \
  "v_and_b32 v224, 0xffff0000, v224\n\t" \
  "v_sub_f32 v229, v218, v225\n\t" \
  "v_sub_f32 v216, v216, v219\n\t" \
  "v_sub_f32 v217, v217, v224\n\t" \
  "v_mfma_f32_32x32x16_bf16 v[100:115], v[20:23], v[156:159], v[100:115]\n\t" \
  "v_rcp_f32 v196, v196\n\t" \
  "v_rcp_f32 v197, v197\n\t" \
  "v_rcp_f32 v198, v198\n\t" \
  "v_rcp_f32 v199, v199\n\t" \
  "v_mul_f32 v218, v229, v229\n\t" \
  "v_cndmask_b32 v228, v216, v217, vcc\n\t" \
  "v_fmac_f32 v218, v216, v216\n\t" \
  "v_fmac_f32 v218, v217, v217\n\t" \
  "v_cvt_pk_bf16_f32 v218, v218, v218\n\t" \
  "s_waitcnt lgkmcnt(6)\n\t" \
  "v_cndmask_b32_sdwa v175, v175, v218, vcc dst_sel:WORD_1 dst_unused:UNUSED_PRESERVE src0_sel:WORD_1 src1_sel:WORD_1\n\t" \
  "v_mfma_f32_16x16x32_bf16 v[132:135], v[136:139], v[188:191], v[132:135]\n\t" \
  "v_mul_f32 v192, 0xbfb8aa3b, v124\n\t" \
  "v_mul_f32 v193, 0xbfb8aa3b, v125\n\t" \
  "v_mul_f32 v194, 0xbfb8aa3b, v126\n\t" \
  "v_mul_f32 v195, 0xbfb8aa3b, v127\n\t" \
  "v_exp_f32 v192, v192\n\t" \
  "v_exp_f32 v193, v193\n\t" \
  "v_exp_f32 v194, v194\n\t" \
  "v_exp_f32 v195, v195\n\t" \
  "ds_read2_b32 v[220:221], v252 offset0:0 offset1:2\n\t" \
  "ds_read2_b32 v[222:223], v252 offset0:4 offset1:6\n\t" \
  "ds_read_b64 v[226:227], v253\n\t" \
  "ds_read_b32 v232, v254\n\t" \
  "v_mfma_f32_32x32x16_bf16 v[100:115], v[28:31], v[160:163], v[100:115]\n\t" \
  "v_mul_f32 v196, v120, v196\n\t" \
  "v_mul_f32 v197, v121, v197\n\t" \
  "v_mul_f32 v198, v122, v198\n\t" \
  "v_mul_f32 v199, v123, v199\n\t" \
  "v_add_u32 v252, 0x100, v252\n\t" \
  "v_add_u32 v253, 0x100, v253\n\t" \
  "v_add_u32 v254, 0x100, v254\n\t" \
  "v_add_f32 v192, 1.0, v192\n\t" \
  "v_add_f32 v193, 1.0, v193\n\t" \
  "v_add_f32 v194, 1.0, v194\n\t" \
  "v_add_f32 v195, 1.0, v195\n\t" \
  "v_cvt_pk_bf16_f32 v186, v196, v197\n\t" \
  "v_cvt_pk_bf16_f32 v187, v198, v199\n\t" \
  "s_waitcnt lgkmcnt(9)\n\t" \
  "v_mfma_f32_32x32x16_bf16 v[100:115], v[36:39], v[168:171], v[100:115]\n\t" \
  "v_rcp_f32 v192, v192\n\t" \
  "v_rcp_f32 v193, v193\n\t" \
  "v_rcp_f32 v194, v194\n\t" \
  "v_rcp_f32 v195, v195\n\t" \
  "v_mfma_f32_32x32x16_bf16 v[100:115], v[44:47], v[172:175], v[100:115]\n\t" \
  "v_mul_f32 v196, 0xbfb8aa3b, v128\n\t" \
  "v_mul_f32 v197, 0xbfb8aa3b, v129\n\t" \
  "v_mul_f32 v198, 0xbfb8aa3b, v130\n\t" \
  "v_mul_f32 v199, 0xbfb8aa3b, v131\n\t" \
  "v_exp_f32 v196, v196\n\t" \
  "v_exp_f32 v197, v197\n\t" \
  "v_exp_f32 v198, v198\n\t" \
  "v_exp_f32 v199, v199\n\t" \
  "v_mfma_f32_32x32x16_bf16 v[100:115], v[88:91], v[164:167], v[100:115]\n\t" \
  "v_mul_f32 v192, v124, v192\n\t" \
  "v_mul_f32 v193, v125, v193\n\t" \
  "v_mul_f32 v194, v126, v194\n\t" \
  "v_mul_f32 v195, v127, v195\n\t" \
  "ds_write_b128 v243, v[184:187] offset:0\n\t" \
  "v_add_f32 v196, 1.0, v196\n\t" \
  "v_add_f32 v197, 1.0, v197\n\t" \
  "v_add_f32 v198, 1.0, v198\n\t" \
  "v_add_f32 v199, 1.0, v199\n\t" \
  "v_cvt_pk_bf16_f32 v188, v192, v193\n\t" \
  "v_cvt_pk_bf16_f32 v189, v194, v195\n\t" \
  "s_waitcnt lgkmcnt(8)\n\t" \
  "v_mfma_f32_32x32x16_bf16 v[52:67], v[200:203], v[176:179], v[52:67]\n\t" \
  "v_rcp_f32 v196, v196\n\t" \
  "v_rcp_f32 v197, v197\n\t" \
  "v_rcp_f32 v198, v198\n\t" \
  "v_rcp_f32 v199, v199\n\t" \
  "s_waitcnt lgkmcnt(6)\n\t" \
  "v_mfma_f32_32x32x16_bf16 v[52:67], v[204:207], v[180:183], v[52:67]\n\t" \
  "v_mul_f32 v192, 0xbfb8aa3b, v100\n\t" \
  "v_mul_f32 v193, 0xbfb8aa3b, v101\n\t" \
  "v_mul_f32 v194, 0xbfb8aa3b, v102\n\t" \
  "v_mul_f32 v195, 0xbfb8aa3b, v103\n\t" \
  "v_exp_f32 v192, v192\n\t" \
  "v_exp_f32 v193, v193\n\t" \
  "v_exp_f32 v194, v194\n\t" \
  "v_exp_f32 v195, v195\n\t" \
  "s_cmp_eq_u32 %[nt], 0\n\t" \
  "v_mfma_f32_16x16x32_bf16 v[132:135], v[140:143], v[184:187], v[132:135]\n\t" \
  "v_mul_f32 v196, v128, v196\n\t" \
  "v_mul_f32 v197, v129, v197\n\t" \
  "v_mul_f32 v198, v130, v198\n\t" \
  "v_mul_f32 v199, v131, v199\n\t" \
  "v_add_f32 v192, 1.0, v192\n\t" \
  "v_add_f32 v193, 1.0, v193\n\t" \
  "v_add_f32 v194, 1.0, v194\n\t" \
  "v_add_f32 v195, 1.0, v195\n\t" \
  "v_cvt_pk_bf16_f32 v190, v196, v197\n\t" \
  "v_cvt_pk_bf16_f32 v191, v198, v199\n\t" \
  "s_cbranch_scc0 1b\n\t" \
  "s_waitcnt lgkmcnt(0)\n\t" \
  "s_cmp_lg_u32 %[last], 0\n\t" \
  "s_cbranch_scc1 8f\n\t" \
  "ds_write_b128 v243, v[188:191] offset:16\n\t" \
  "ds_read_b64_tr_b16 v[200:201], v244 offset:0\n\t" \
  "ds_read_b64_tr_b16 v[202:203], v244 offset:320\n\t" \
  "ds_read_b64_tr_b16 v[204:205], v244 offset:1280\n\t" \
  "ds_read_b64_tr_b16 v[206:207], v244 offset:1600\n\t" \
  "v_mfma_f32_16x16x32_bf16 v[132:135], v[144:147], v[188:191], v[132:135]\n\t" \
  "s_waitcnt lgkmcnt(0)\n\t" \
  "v_mfma_f32_32x32x16_bf16 v[68:83], v[200:203], v[176:179], v[68:83]\n\t" \
  "v_mfma_f32_32x32x16_bf16 v[68:83], v[204:207], v[180:183], v[68:83]\n\t" \
  "s_nop 8\n\t" \
  "v_mul_f32 v230, v230, v132\n\t" \
  "v_mul_f32 v231, v231, v132\n\t" \
  "v_cvt_pk_bf16_f32 v230, v230, v231\n\t" \
  "ds_write_b16 v245, v230 offset:7456\n\t" \
  "ds_write_b16_d16_hi v241, v230 offset:7584\n\t" \
  "ds_read_b128 v[200:203], v246 offset:7456\n\t" \
  "ds_read_b128 v[208:211], v246 offset:7488\n\t" \
  "s_waitcnt lgkmcnt(0)\n\t" \
  "v_mfma_f32_16x16x32_bf16 v[84:87], v[200:203], v[176:179], v[84:87]\n\t" \
  "v_mfma_f32_16x16x32_bf16 v[84:87], v[208:211], v[180:183], v[84:87]\n\t" \
  "s_branch 9f\n\t" \
  "6:\n\t" \
  "s_cmp_lg_u32 %[last], 0\n\t" \
  "s_cbranch_scc0 9f\n\t" \
  "s_waitcnt lgkmcnt(0)\n\t" \
  "v_cndmask_b32 v224, 0, 24, vcc\n\t" \
  "v_xor_b32 v220, v234, v224\n\t" \
  "v_xor_b32 v221, v235, v224\n\t" \
  "v_xor_b32 v222, v236, v224\n\t" \
  "v_xor_b32 v223, v237, v224\n\t" \
  "v_xor_b32 v216, 16, v220\n\t" \
  "v_xor_b32 v217, 16, v221\n\t" \
  "v_xor_b32 v218, 16, v222\n\t" \
  "v_xor_b32 v219, 16, v223\n\t" \
  "ds_read_b64 v[148:149], v220\n\t" \
  "ds_read_b64 v[150:151], v216\n\t" \
  "ds_read_b64 v[152:153], v221\n\t" \
  "ds_read_b64 v[154:155], v217\n\t" \
  "ds_read_b64 v[156:157], v222\n\t" \
  "ds_read_b64 v[158:159], v218\n\t" \
  "ds_read_b64 v[160:161], v223\n\t" \
  "ds_read_b64 v[162:163], v219\n\t" \
  "ds_read_b128 v[4:7], v247 offset:6144\n\t" \
  "ds_read_b128 v[8:11], v247 offset:7168\n\t" \
  "ds_read_b128 v[12:15], v247 offset:8192\n\t" \
  "ds_read_b128 v[16:19], v247 offset:9216\n\t" \
  "ds_read_b128 v[20:23], v247 offset:10240\n\t" \
  "ds_read_b128 v[24:27], v247 offset:11264\n\t" \
  "v_subrev_u32 v225, %[b1], v241\n\t" \
  "v_lshl_add_u32 v225, v225, 5, v243\n\t" \
  "v_sub_u32 v225, v225, v224\n\t" \
  "s_nop 4\n\t" \
  "v_cvt_pk_bf16_f32 v184, v52, v53\n\t" \
  "v_cvt_pk_bf16_f32 v185, v54, v55\n\t" \
  "v_cvt_pk_bf16_f32 v186, v56, v57\n\t" \
  "v_cvt_pk_bf16_f32 v187, v58, v59\n\t" \
  "v_cvt_pk_bf16_f32 v188, v60, v61\n\t" \
  "v_cvt_pk_bf16_f32 v189, v62, v63\n\t" \
  "v_cvt_pk_bf16_f32 v190, v64, v65\n\t" \
  "v_cvt_pk_bf16_f32 v191, v66, v67\n\t" \
  "v_cvt_pk_bf16_f32 v36, v68, v69\n\t" \
  "v_cvt_pk_bf16_f32 v37, v70, v71\n\t" \
  "v_cvt_pk_bf16_f32 v38, v72, v73\n\t" \
  "v_cvt_pk_bf16_f32 v39, v74, v75\n\t" \
  "v_cvt_pk_bf16_f32 v40, v76, v77\n\t" \
  "v_cvt_pk_bf16_f32 v41, v78, v79\n\t" \
  "v_cvt_pk_bf16_f32 v42, v80, v81\n\t" \
  "v_cvt_pk_bf16_f32 v43, v82, v83\n\t" \
  "s_waitcnt lgkmcnt(5)\n\t" \
  "v_mfma_f32_32x32x16_bf16 v[100:115], v[4:7], v[148:151], 0\n\t" \
  "s_waitcnt lgkmcnt(4)\n\t" \
  "v_mfma_f32_32x32x16_bf16 v[116:131], v[8:11], v[148:151], 0\n\t" \
  "ds_read_b128 v[28:31], v247 offset:12288\n\t" \
  "ds_read_b128 v[32:35], v247 offset:13312\n\t" \
  "s_waitcnt lgkmcnt(5)\n\t" \
  "v_mfma_f32_32x32x16_bf16 v[100:115], v[12:15], v[152:155], v[100:115]\n\t" \
  "s_waitcnt lgkmcnt(4)\n\t" \
  "v_mfma_f32_32x32x16_bf16 v[116:131], v[16:19], v[152:155], v[116:131]\n\t" \
  "ds_read_b128 v[4:7], v247 offset:14336\n\t" \
  "ds_read_b128 v[8:11], v247 offset:15360\n\t" \
  "s_waitcnt lgkmcnt(5)\n\t" \
  "v_mfma_f32_32x32x16_bf16 v[100:115], v[20:23], v[156:159], v[100:115]\n\t" \
  "s_waitcnt lgkmcnt(4)\n\t" \
  "v_mfma_f32_32x32x16_bf16 v[116:131], v[24:27], v[156:159], v[116:131]\n\t" \
  "ds_read_b128 v[12:15], v247 offset:16384\n\t" \
  "ds_read_b128 v[16:19], v247 offset:17408\n\t" \
  "s_waitcnt lgkmcnt(5)\n\t" \
  "v_mfma_f32_32x32x16_bf16 v[100:115], v[28:31], v[160:163], v[100:115]\n\t" \
  "s_waitcnt lgkmcnt(4)\n\t" \
  "v_mfma_f32_32x32x16_bf16 v[116:131], v[32:35], v[160:163], v[116:131]\n\t" \
  "ds_read_b128 v[20:23], v247 offset:18432\n\t" \
  "ds_read_b128 v[24:27], v247 offset:19456\n\t" \
  "s_waitcnt lgkmcnt(5)\n\t" \
  "v_mfma_f32_32x32x16_bf16 v[100:115], v[4:7], v[184:187], v[100:115]\n\t" \
  "ds_read_b128 v[28:31], v247 offset:20480\n\t" \
  "ds_read_b128 v[32:35], v247 offset:21504\n\t" \
  "s_waitcnt lgkmcnt(5)\n\t" \
  "v_mfma_f32_32x32x16_bf16 v[100:115], v[12:15], v[188:191], v[100:115]\n\t" \
  "s_waitcnt lgkmcnt(3)\n\t" \
  "v_mfma_f32_32x32x16_bf16 v[100:115], v[20:23], v[36:39], v[100:115]\n\t" \
  "s_waitcnt lgkmcnt(1)\n\t" \
  "v_mfma_f32_32x32x16_bf16 v[100:115], v[28:31], v[40:43], v[100:115]\n\t" \
  "s_waitcnt lgkmcnt(0)\n\t" \
  "s_branch 10f\n\t" \
  "8:\n\t" \
  "ds_write_b128 v243, v[188:191] offset:16\n\t" \
  "ds_read_b64_tr_b16 v[200:201], v244 offset:0\n\t" \
  "ds_read_b64_tr_b16 v[202:203], v244 offset:320\n\t" \
  "ds_read_b64_tr_b16 v[204:205], v244 offset:1280\n\t" \
  "ds_read_b64_tr_b16 v[206:207], v244 offset:1600\n\t" \
  "v_mfma_f32_16x16x32_bf16 v[132:135], v[144:147], v[188:191], v[132:135]\n\t" \
  "v_cndmask_b32 v224, 0, 24, vcc\n\t" \
  "v_xor_b32 v220, v234, v224\n\t" \
  "v_xor_b32 v221, v235, v224\n\t" \
  "v_xor_b32 v222, v236, v224\n\t" \
  "v_xor_b32 v223, v237, v224\n\t" \
  "v_xor_b32 v216, 16, v220\n\t" \
  "v_xor_b32 v217, 16, v221\n\t" \
  "v_xor_b32 v218, 16, v222\n\t" \
  "v_xor_b32 v219, 16, v223\n\t" \
  "ds_read_b64 v[148:149], v220\n\t" \
  "ds_read_b64 v[150:151], v216\n\t" \
  "ds_read_b64 v[152:153], v221\n\t" \
  "ds_read_b64 v[154:155], v217\n\t" \
  "ds_read_b64 v[156:157], v222\n\t" \
  "ds_read_b64 v[158:159], v218\n\t" \
  "ds_read_b64 v[160:161], v223\n\t" \
  "ds_read_b64 v[162:163], v219\n\t" \
  "ds_read_b128 v[4:7], v247 offset:6144\n\t" \
  "ds_read_b128 v[8:11], v247 offset:7168\n\t" \
  "s_waitcnt lgkmcnt(11)\n\t" \
  "ds_read_b128 v[12:15], v247 offset:8192\n\t" \
  "ds_read_b128 v[16:19], v247 offset:9216\n\t" \
  "ds_read_b128 v[20:23], v247 offset:10240\n\t" \
  "ds_read_b128 v[24:27], v247 offset:11264\n\t" \
  "v_subrev_u32 v225, %[b1], v241\n\t" \
  "v_lshl_add_u32 v225, v225, 5, v243\n\t" \
  "v_sub_u32 v225, v225, v224\n\t" \
  "v_mfma_f32_32x32x16_bf16 v[68:83], v[200:203], v[176:179], v[68:83]\n\t" \
  "s_waitcnt lgkmcnt(5)\n\t" \
  "v_mfma_f32_32x32x16_bf16 v[100:115], v[4:7], v[148:151], 0\n\t" \
  "v_mfma_f32_32x32x16_bf16 v[68:83], v[204:207], v[180:183], v[68:83]\n\t" \
  "s_waitcnt lgkmcnt(4)\n\t" \
  "v_mfma_f32_32x32x16_bf16 v[116:131], v[8:11], v[148:151], 0\n\t" \
  "ds_read_b128 v[28:31], v247 offset:12288\n\t" \
  "ds_read_b128 v[32:35], v247 offset:13312\n\t" \
  "s_waitcnt lgkmcnt(5)\n\t" \
  "v_mfma_f32_32x32x16_bf16 v[100:115], v[12:15], v[152:155], v[100:115]\n\t" \
  "v_mul_f32 v230, v230, v132\n\t" \
  "v_mul_f32 v231, v231, v132\n\t" \
  "v_cvt_pk_bf16_f32 v230, v230, v231\n\t" \
  "ds_write_b16 v245, v230 offset:7456\n\t" \
  "ds_write_b16_d16_hi v241, v230 offset:7584\n\t" \
  "ds_read_b128 v[200:203], v246 offset:7456\n\t" \
  "ds_read_b128 v[208:211], v246 offset:7488\n\t" \
  "v_cvt_pk_bf16_f32 v184, v52, v53\n\t" \
  "v_cvt_pk_bf16_f32 v185, v54, v55\n\t" \
  "v_cvt_pk_bf16_f32 v186, v56, v57\n\t" \
  "v_cvt_pk_bf16_f32 v187, v58, v59\n\t" \
  "s_waitcnt lgkmcnt(8)\n\t" \
  "v_mfma_f32_32x32x16_bf16 v[116:131], v[16:19], v[152:155], v[116:131]\n\t" \
  "ds_read_b128 v[4:7], v247 offset:14336\n\t" \
  "ds_read_b128 v[8:11], v247 offset:15360\n\t" \
  "s_waitcnt lgkmcnt(9)\n\t" \
  "v_mfma_f32_32x32x16_bf16 v[100:115], v[20:23], v[156:159], v[100:115]\n\t" \
  "v_cvt_pk_bf16_f32 v188, v60, v61\n\t" \
  "v_cvt_pk_bf16_f32 v189, v62, v63\n\t" \
  "v_cvt_pk_bf16_f32 v190, v64, v65\n\t" \
  "v_cvt_pk_bf16_f32 v191, v66, v67\n\t" \
  "s_waitcnt lgkmcnt(8)\n\t" \
  "v_mfma_f32_32x32x16_bf16 v[116:131], v[24:27], v[156:159], v[116:131]\n\t" \
  "ds_read_b128 v[12:15], v247 offset:16384\n\t" \
  "ds_read_b128 v[16:19], v247 offset:17408\n\t" \
  "s_waitcnt lgkmcnt(9)\n\t" \
  "v_mfma_f32_32x32x16_bf16 v[100:115], v[28:31], v[160:163], v[100:115]\n\t" \
  "v_cvt_pk_bf16_f32 v36, v68, v69\n\t" \
  "v_cvt_pk_bf16_f32 v37, v70, v71\n\t" \
  "v_cvt_pk_bf16_f32 v38, v72, v73\n\t" \
  "v_cvt_pk_bf16_f32 v39, v74, v75\n\t" \
  "s_waitcnt lgkmcnt(5)\n\t" \
  "v_mfma_f32_16x16x32_bf16 v[84:87], v[200:203], v[176:179], v[84:87]\n\t" \
  "v_cvt_pk_bf16_f32 v40, v76, v77\n\t" \
  "v_cvt_pk_bf16_f32 v41, v78, v79\n\t" \
  "v_cvt_pk_bf16_f32 v42, v80, v81\n\t" \
  "v_cvt_pk_bf16_f32 v43, v82, v83\n\t" \
  "v_mfma_f32_32x32x16_bf16 v[116:131], v[32:35], v[160:163], v[116:131]\n\t" \
  "ds_read_b128 v[20:23], v247 offset:18432\n\t" \
  "ds_read_b128 v[24:27], v247 offset:19456\n\t" \
  "s_waitcnt lgkmcnt(6)\n\t" \
  "v_mfma_f32_16x16x32_bf16 v[84:87], v[208:211], v[180:183], v[84:87]\n\t" \
  "s_waitcnt lgkmcnt(5)\n\t" \
  "v_mfma_f32_32x32x16_bf16 v[100:115], v[4:7], v[184:187], v[100:115]\n\t" \
  "ds_read_b128 v[28:31], v247 offset:20480\n\t" \
  "ds_read_b128 v[32:35], v247 offset:21504\n\t" \
  "s_waitcnt lgkmcnt(5)\n\t" \
  "v_mfma_f32_32x32x16_bf16 v[100:115], v[12:15], v[188:191], v[100:115]\n\t" \
  "s_waitcnt lgkmcnt(3)\n\t" \
  "v_mfma_f32_32x32x16_bf16 v[100:115], v[20:23], v[36:39], v[100:115]\n\t" \
  "s_waitcnt lgkmcnt(1)\n\t" \
  "v_mfma_f32_32x32x16_bf16 v[100:115], v[28:31], v[40:43], v[100:115]\n\t" \
  "s_waitcnt lgkmcnt(0)\n\t" \
  "10:\n\t" \
  "v_mov_b32 v212, 0xbfb8aa3b\n\t" \
  "v_mfma_f32_32x32x16_bf16 v[116:131], v[8:11], v[184:187], v[116:131]\n\t" \
  "v_mfma_f32_32x32x16_bf16 v[116:131], v[16:19], v[188:191], v[116:131]\n\t" \
  "s_nop 8\n\t" \
  "v_mul_f32 v192, 0xbfb8aa3b, v100\n\t" \
  "v_mul_f32 v193, 0xbfb8aa3b, v101\n\t" \
  "v_mul_f32 v194, 0xbfb8aa3b, v102\n\t" \
  "v_mul_f32 v195, 0xbfb8aa3b, v103\n\t" \
  "v_exp_f32 v192, v192\n\t" \
  "v_exp_f32 v193, v193\n\t" \
  "v_exp_f32 v194, v194\n\t" \
  "v_exp_f32 v195, v195\n\t" \
  "v_lshlrev_b32 v200, 16, v148\n\t" \
  "v_and_b32 v201, 0xffff0000, v148\n\t" \
  "v_lshlrev_b32 v202, 16, v149\n\t" \
  "v_and_b32 v203, 0xffff0000, v149\n\t" \
  "v_mfma_f32_32x32x16_bf16 v[116:131], v[24:27], v[36:39], v[116:131]\n\t" \
  "v_add_f32 v192, 1.0, v192\n\t" \
  "v_add_f32 v193, 1.0, v193\n\t" \
  "v_add_f32 v194, 1.0, v194\n\t" \
  "v_add_f32 v195, 1.0, v195\n\t" \
  "v_rcp_f32 v192, v192\n\t" \
  "v_rcp_f32 v193, v193\n\t" \
  "v_rcp_f32 v194, v194\n\t" \
  "v_rcp_f32 v195, v195\n\t" \
  "v_mfma_f32_32x32x16_bf16 v[116:131], v[32:35], v[40:43], v[116:131]\n\t" \
  "v_mul_f32 v196, 0xbfb8aa3b, v104\n\t" \
  "v_mul_f32 v197, 0xbfb8aa3b, v105\n\t" \
  "v_mul_f32 v198, 0xbfb8aa3b, v106\n\t" \
  "v_mul_f32 v199, 0xbfb8aa3b, v107\n\t" \
  "v_exp_f32 v196, v196\n\t" \
  "v_exp_f32 v197, v197\n\t" \
  "v_exp_f32 v198, v198\n\t" \
  "v_exp_f32 v199, v199\n\t" \
  "v_fmac_f32 v200, v100, v192\n\t" \
  "v_fmac_f32 v201, v101, v193\n\t" \
  "v_fmac_f32 v202, v102, v194\n\t" \
  "v_fmac_f32 v203, v103, v195\n\t" \
  "v_lshlrev_b32 v204, 16, v150\n\t" \
  "v_and_b32 v205, 0xffff0000, v150\n\t" \
  "v_lshlrev_b32 v206, 16, v151\n\t" \
  "v_and_b32 v207, 0xffff0000, v151\n\t" \
  "v_add_f32 v196, 1.0, v196\n\t" \
  "v_add_f32 v197, 1.0, v197\n\t" \
  "v_add_f32 v198, 1.0, v198\n\t" \
  "v_add_f32 v199, 1.0, v199\n\t" \
  "v_cvt_pk_bf16_f32 v176, v200, v201\n\t" \
  "v_cvt_pk_bf16_f32 v177, v202, v203\n\t" \
  "ds_write_b64 v225, v[176:177] offset:0\n\t" \
  "v_rcp_f32 v196, v196\n\t" \
  "v_rcp_f32 v197, v197\n\t" \
  "v_rcp_f32 v198, v198\n\t" \
  "v_rcp_f32 v199, v199\n\t" \
  "v_mul_f32 v192, 0xbfb8aa3b, v108\n\t" \
  "v_mul_f32 v193, 0xbfb8aa3b, v109\n\t" \
  "v_mul_f32 v194, 0xbfb8aa3b, v110\n\t" \
  "v_mul_f32 v195, 0xbfb8aa3b, v111\n\t" \
  "v_exp_f32 v192, v192\n\t" \
  "v_exp_f32 v193, v193\n\t" \
  "v_exp_f32 v194, v194\n\t" \
  "v_exp_f32 v195, v195\n\t" \
  "v_fmac_f32 v204, v104, v196\n\t" \
  "v_fmac_f32 v205, v105, v197\n\t" \
  "v_fmac_f32 v206, v106, v198\n\t" \
  "v_fmac_f32 v207, v107, v199\n\t" \
  "v_lshlrev_b32 v200, 16, v152\n\t" \
  "v_and_b32 v201, 0xffff0000, v152\n\t" \
  "v_lshlrev_b32 v202, 16, v153\n\t" \
  "v_and_b32 v203, 0xffff0000, v153\n\t" \
  "v_add_f32 v192, 1.0, v192\n\t" \
  "v_add_f32 v193, 1.0, v193\n\t" \
  "v_add_f32 v194, 1.0, v194\n\t" \
  "v_add_f32 v195, 1.0, v195\n\t" \
  "v_cvt_pk_bf16_f32 v178, v204, v205\n\t" \
  "v_cvt_pk_bf16_f32 v179, v206, v207\n\t" \
  "ds_write_b64 v225, v[178:179] offset:16\n\t" \
  "v_rcp_f32 v192, v192\n\t" \
  "v_rcp_f32 v193, v193\n\t" \
  "v_rcp_f32 v194, v194\n\t" \
  "v_rcp_f32 v195, v195\n\t" \
  "v_mul_f32 v196, 0xbfb8aa3b, v112\n\t" \
  "v_mul_f32 v197, 0xbfb8aa3b, v113\n\t" \
  "v_mul_f32 v198, 0xbfb8aa3b, v114\n\t" \
  "v_mul_f32 v199, 0xbfb8aa3b, v115\n\t" \
  "v_exp_f32 v196, v196\n\t" \
  "v_exp_f32 v197, v197\n\t" \
  "v_exp_f32 v198, v198\n\t" \
  "v_exp_f32 v199, v199\n\t" \
  "v_fmac_f32 v200, v108, v192\n\t" \
  "v_fmac_f32 v201, v109, v193\n\t" \
  "v_fmac_f32 v202, v110, v194\n\t" \
  "v_fmac_f32 v203, v111, v195\n\t" \
  "v_lshlrev_b32 v204, 16, v154\n\t" \
  "v_and_b32 v205, 0xffff0000, v154\n\t" \
  "v_lshlrev_b32 v206, 16, v155\n\t" \
  "v_and_b32 v207, 0xffff0000, v155\n\t" \
  "v_add_f32 v196, 1.0, v196\n\t" \
  "v_add_f32 v197, 1.0, v197\n\t" \
  "v_add_f32 v198, 1.0, v198\n\t" \
  "v_add_f32 v199, 1.0, v199\n\t" \
  "v_cvt_pk_bf16_f32 v180, v200, v201\n\t" \
  "v_cvt_pk_bf16_f32 v181, v202, v203\n\t" \
  "ds_write_b64 v225, v[180:181] offset:32\n\t" \
  "v_rcp_f32 v196, v196\n\t" \
  "v_rcp_f32 v197, v197\n\t" \
  "v_rcp_f32 v198, v198\n\t" \
  "v_rcp_f32 v199, v199\n\t" \
  "v_mul_f32 v192, 0xbfb8aa3b, v116\n\t" \
  "v_mul_f32 v193, 0xbfb8aa3b, v117\n\t" \
  "v_mul_f32 v194, 0xbfb8aa3b, v118\n\t" \
  "v_mul_f32 v195, 0xbfb8aa3b, v119\n\t" \
  "v_exp_f32 v192, v192\n\t" \
  "v_exp_f32 v193, v193\n\t" \
  "v_exp_f32 v194, v194\n\t" \
  "v_exp_f32 v195, v195\n\t" \
  "v_fmac_f32 v204, v112, v196\n\t" \
  "v_fmac_f32 v205, v113, v197\n\t" \
  "v_fmac_f32 v206, v114, v198\n\t" \
  "v_fmac_f32 v207, v115, v199\n\t" \
  "v_lshlrev_b32 v200, 16, v156\n\t" \
  "v_and_b32 v201, 0xffff0000, v156\n\t" \
  "v_lshlrev_b32 v202, 16, v157\n\t" \
  "v_and_b32 v203, 0xffff0000, v157\n\t" \
  "v_add_f32 v192, 1.0, v192\n\t" \
  "v_add_f32 v193, 1.0, v193\n\t" \
  "v_add_f32 v194, 1.0, v194\n\t" \
  "v_add_f32 v195, 1.0, v195\n\t" \
  "v_cvt_pk_bf16_f32 v182, v204, v205\n\t" \
  "v_cvt_pk_bf16_f32 v183, v206, v207\n\t" \
  "ds_write_b64 v225, v[182:183] offset:48\n\t" \
  "v_rcp_f32 v192, v192\n\t" \
  "v_rcp_f32 v193, v193\n\t" \
  "v_rcp_f32 v194, v194\n\t" \
  "v_rcp_f32 v195, v195\n\t" \
  "v_mul_f32 v196, 0xbfb8aa3b, v120\n\t" \
  "v_mul_f32 v197, 0xbfb8aa3b, v121\n\t" \
  "v_mul_f32 v198, 0xbfb8aa3b, v122\n\t" \
  "v_mul_f32 v199, 0xbfb8aa3b, v123\n\t" \
  "v_exp_f32 v196, v196\n\t" \
  "v_exp_f32 v197, v197\n\t" \
  "v_exp_f32 v198, v198\n\t" \
  "v_exp_f32 v199, v199\n\t" \
  "v_fmac_f32 v200, v116, v192\n\t" \
  "v_fmac_f32 v201, v117, v193\n\t" \
  "v_fmac_f32 v202, v118, v194\n\t" \
  "v_fmac_f32 v203, v119, v195\n\t" \
  "v_lshlrev_b32 v204, 16, v158\n\t" \
  "v_and_b32 v205, 0xffff0000, v158\n\t" \
  "v_lshlrev_b32 v206, 16, v159\n\t" \
  "v_and_b32 v207, 0xffff0000, v159\n\t" \
  "v_add_f32 v196, 1.0, v196\n\t" \
  "v_add_f32 v197, 1.0, v197\n\t" \
  "v_add_f32 v198, 1.0, v198\n\t" \
  "v_add_f32 v199, 1.0, v199\n\t" \
  "v_cvt_pk_bf16_f32 v176, v200, v201\n\t" \
  "v_cvt_pk_bf16_f32 v177, v202, v203\n\t" \
  "ds_write_b64 v225, v[176:177] offset:64\n\t" \
  "v_rcp_f32 v196, v196\n\t" \
  "v_rcp_f32 v197, v197\n\t" \
  "v_rcp_f32 v198, v198\n\t" \
  "v_rcp_f32 v199, v199\n\t" \
  "v_mul_f32 v192, 0xbfb8aa3b, v124\n\t" \
  "v_mul_f32 v193, 0xbfb8aa3b, v125\n\t" \
  "v_mul_f32 v194, 0xbfb8aa3b, v126\n\t" \
  "v_mul_f32 v195, 0xbfb8aa3b, v127\n\t" \
  "v_exp_f32 v192, v192\n\t" \
  "v_exp_f32 v193, v193\n\t" \
  "v_exp_f32 v194, v194\n\t" \
  "v_exp_f32 v195, v195\n\t" \
  "v_fmac_f32 v204, v120, v196\n\t" \
  "v_fmac_f32 v205, v121, v197\n\t" \
  "v_fmac_f32 v206, v122, v198\n\t" \
  "v_fmac_f32 v207, v123, v199\n\t" \
  "v_lshlrev_b32 v200, 16, v160\n\t" \
  "v_and_b32 v201, 0xffff0000, v160\n\t" \
  "v_lshlrev_b32 v202, 16, v161\n\t" \
  "v_and_b32 v203, 0xffff0000, v161\n\t" \
  "v_add_f32 v192, 1.0, v192\n\t" \
  "v_add_f32 v193, 1.0, v193\n\t" \
  "v_add_f32 v194, 1.0, v194\n\t" \
  "v_add_f32 v195, 1.0, v195\n\t" \
  "v_cvt_pk_bf16_f32 v178, v204, v205\n\t" \
  "v_cvt_pk_bf16_f32 v179, v206, v207\n\t" \
  "ds_write_b64 v225, v[178:179] offset:80\n\t" \
  "v_rcp_f32 v192, v192\n\t" \
  "v_rcp_f32 v193, v193\n\t" \
  "v_rcp_f32 v194, v194\n\t" \
  "v_rcp_f32 v195, v195\n\t" \
  "v_mul_f32 v196, 0xbfb8aa3b, v128\n\t" \
  "v_mul_f32 v197, 0xbfb8aa3b, v129\n\t" \
  "v_mul_f32 v198, 0xbfb8aa3b, v130\n\t" \
  "v_mul_f32 v199, 0xbfb8aa3b, v131\n\t" \
  "v_exp_f32 v196, v196\n\t" \
  "v_exp_f32 v197, v197\n\t" \
  "v_exp_f32 v198, v198\n\t" \
  "v_exp_f32 v199, v199\n\t" \
  "v_fmac_f32 v200, v124, v192\n\t" \
  "v_fmac_f32 v201, v125, v193\n\t" \
  "v_fmac_f32 v202, v126, v194\n\t" \
  "v_fmac_f32 v203, v127, v195\n\t" \
  "v_lshlrev_b32 v204, 16, v162\n\t" \
  "v_and_b32 v205, 0xffff0000, v162\n\t" \
  "v_lshlrev_b32 v206, 16, v163\n\t" \
  "v_and_b32 v207, 0xffff0000, v163\n\t" \
  "v_add_f32 v196, 1.0, v196\n\t" \
  "v_add_f32 v197, 1.0, v197\n\t" \
  "v_add_f32 v198, 1.0, v198\n\t" \
  "v_add_f32 v199, 1.0, v199\n\t" \
  "v_cvt_pk_bf16_f32 v180, v200, v201\n\t" \
  "v_cvt_pk_bf16_f32 v181, v202, v203\n\t" \
  "ds_write_b64 v225, v[180:181] offset:96\n\t" \
  "v_rcp_f32 v196, v196\n\t" \
  "v_rcp_f32 v197, v197\n\t" \
  "v_rcp_f32 v198, v198\n\t" \
  "v_rcp_f32 v199, v199\n\t" \
  "s_nop 0\n\t" \
  "v_fmac_f32 v204, v128, v196\n\t" \
  "v_fmac_f32 v205, v129, v197\n\t" \
  "v_fmac_f32 v206, v130, v198\n\t" \
  "v_fmac_f32 v207, v131, v199\n\t" \
  "v_cvt_pk_bf16_f32 v182, v204, v205\n\t" \
  "v_cvt_pk_bf16_f32 v183, v206, v207\n\t" \
  "ds_write_b64 v225, v[182:183] offset:112\n\t" \
  "9:\n\t" \
  "ds_write_b16 v233, v255 offset:5376\n\t" \
  "s_mov_b32 m0, %[keep]\n\t" \
  "s_setprio 0\n\t" \
  "s_nop 11\n\t" \
  ""
#define LOOP_ASM LOOP_ASM_FOLD

DI u32x4 tr_frag(unsigned p, int half) {
  const s16x4 lo = __builtin_amdgcn_ds_read_tr16_b64_v4i16((lds_s16x4_p)p);
  const s16x4 hi = __builtin_amdgcn_ds_read_tr16_b64_v4i16((lds_s16x4_p)(p + half));
  return __builtin_bit_cast(u32x4, __builtin_shufflevector(lo, hi, 0, 1, 2, 3, 4, 5, 6, 7));
}
DI u32x4 fold(u32x4 v) {
#pragma unroll
  for (int i = 0; i < 4; i++) v[i] = pk_bf16(bf2f(v[i] & 0xffffu) * -LOG2E, __uint_as_float(v[i] & 0xffff0000u) * -LOG2E);
  return v;
}

__global__ __launch_bounds__(512, 2) void egnn_kernel(const char* __restrict__ hmat, const char* __restrict__ pos,
                                                      const char* __restrict__ ea, const int* __restrict__ src,
                                                      const int* __restrict__ dst, const char* __restrict__ we,
                                                      const char* __restrict__ wx, const char* __restrict__ wh,
                                                      char* __restrict__ ws, uint16_t* __restrict__ out, int N, int E STAMP_ARG) {
  const int w = __builtin_amdgcn_readfirstlane(threadIdx.x >> 6);
  const int bkt = (blockIdx.x & 7) * 32 + (blockIdx.x >> 3);
  const int n0 = bkt * NB;
  STAMP(0);
  unsigned wl = L_WAVE + w * WSZ, mi = L_MI + w * MISZ;
  const auto rs_tab = __builtin_amdgcn_make_buffer_rsrc((void*)(ws + WS_TAB), 0, NBK * NBK * 8, 0x00020000);
  const auto rs_rec = __builtin_amdgcn_make_buffer_rsrc((void*)(ws + WS_REC), 0, NBK * CHS, 0x00020000);
  const auto rs_ea = __builtin_amdgcn_make_buffer_rsrc((void*)ea, 0, E * 32, 0x00020000);
  unsigned tag_lo, tag_hi;
  unsigned wdv0, wdv1;

  {
    const int lane = lane_id(), tid = w * 64 + lane, r = lane & 31, h = lane >> 5;
    const int e0 = bkt * CH;
    int ne = max(E - e0, 0);
    asm("" : "+s"(ne));
    ne = min(ne, CH);
    const auto rs_d = __builtin_amdgcn_make_buffer_rsrc((void*)(dst + e0), 0, ne * 4, 0x00020000);
    const auto rs_s = __builtin_amdgcn_make_buffer_rsrc((void*)(src + e0), 0, ne * 4, 0x00020000);
    unsigned d[7], sv[7];
    u32x2 xa, xb;
    unsigned long long ep;
    asm volatile("s_load_dwordx2 %0, %1, 0x0" : "=s"(ep) : "s"(ws + WS_EPOCH) : "memory");
#pragma unroll
    for (int i = 0; i < 7; i++)
      asm volatile("buffer_load_dword %0, %2, %3, %5 offen nt\n\tbuffer_load_dword %1, %2, %4, %5 offen nt"
                   : "=&v"(d[i]), "=&v"(sv[i]) : "v"(tid * 4), "s"(rs_d), "s"(rs_s), "s"(i * 2048) : "memory");
    LDS(unsigned, L_CNT + tid * 4) = 0;
    asm volatile("s_waitcnt lgkmcnt(0)\n\ts_barrier" : "+s"(ep) :: "memory");
    asm volatile("global_load_ushort %0, %4, %5\n\tglobal_load_ushort %1, %4, %5 offset:64\n\t"
                 "global_load_dwordx2 %2, %6, %7\n\tglobal_load_dwordx2 %3, %6, %7 offset:16"
                 : "=&v"(wdv0), "=&v"(wdv1), "=&v"(xa), "=&v"(xb)
                 : "v"(r * 2), "s"(we + 128 * 128), "v"(h * 8), "s"(wx + ((w + 2) & 3) * 32) : "memory");
    {
      const int j = lane >> 3, jl = (j & 3) * 128 + (lane & 7) * 16, jh = (j >> 2) * 128;
      const char* const h0 = wh + (w < 4 ? 16 * w : 64 + 32 * ((w - 4) >> 1) + 4 * (w & 1)) * 128;
      const char* const e0p = we + (w < 4 ? 64 + 16 * w : 16 * (w - 4)) * 128;
      const unsigned oh = jl + jh * (w < 4 ? 8 : 16), oe = jl + jh * (w < 4 ? 4 : 8);
      unsigned keep;
      asm volatile("s_mov_b32 %0, m0\n\ts_mov_b32 m0, %9\n\ts_nop 0\n\t"
                   "global_load_lds_dwordx4 %1, %4\n\tglobal_load_lds_dwordx4 %1, %5 offset:1024\n\t"
                   "global_load_lds_dwordx4 %2, %6 offset:2048\n\tglobal_load_lds_dwordx4 %2, %7 offset:3072\n\t"
                   "s_add_u32 m0, %9, 0x1000\n\ts_nop 0\n\tglobal_load_lds_dwordx4 %3, %8\n\ts_mov_b32 m0, %0"
                   : "=&s"(keep)
                   : "v"(oh), "v"(oe), "v"(w < 2 ? (lane >> 2) * 128 + (lane & 3) * 16 : 0),
                     "s"(h0), "s"(h0 + (w < 4 ? 4 : 8) * 128 - 1024), "s"(e0p - 2048), "s"(e0p + (w < 4 ? 8 : 4) * 128 - 3072),
                     "s"(we + 129 * 128 + (w & 1) * 64), "s"(wl)
                   : "memory", "scc");
    }
    unsigned ra[5], keep;
    {
      const unsigned cc0 = ((lane & 7) ^ (lane >> 4)) * 16, cc1 = ((lane & 7) ^ (4 + (lane >> 4))) * 16;
      const int nr = n0 + w + (lane & 56);
      ra[0] = min(nr, N - 1) * 128 + cc0; ra[1] = min(nr + 64, N - 1) * 128 + cc1; ra[2] = min(nr + 128, N - 1) * 128 + cc0;
      ra[3] = min(n0 + w + 192, N - 1) * 128 + cc1; ra[4] = mul24(min(n0 + w + min(lane >> 1, 24) * 8, N - 1), 6) + (lane & 1) * 2;
      asm volatile("" : "+v"(ra[0]), "+v"(ra[1]), "+v"(ra[2]), "+v"(ra[3]), "+v"(ra[4]));
    }
    asm volatile("s_waitcnt vmcnt(9)" : "+v"(d[0]), "+v"(d[1]), "+v"(d[2]), "+v"(d[3]), "+v"(d[4]), "+v"(d[5]), "+v"(d[6]),
                 "+v"(sv[0]), "+v"(sv[1]), "+v"(sv[2]), "+v"(sv[3]), "+v"(sv[4]), "+v"(sv[5]), "+v"(sv[6]) :: "memory");
    STAMP(10);
    {
      asm volatile("s_mov_b32 %0, m0\n\t"
                   "s_mov_b32 m0, %8\n\ts_nop 0\n\tglobal_load_lds_dwordx4 %1, %6\n\t"
                   "s_add_u32 m0, %8, 0x400\n\ts_nop 0\n\tglobal_load_lds_dwordx4 %2, %6\n\t"
                   "s_add_u32 m0, %8, 0x800\n\ts_nop 0\n\tglobal_load_lds_dwordx4 %3, %6\n\t"
                   "s_add_u32 m0, %8, 0xc00\n\ts_nop 0\n\tglobal_load_lds_dwordx4 %4, %6\n\t"
                   "s_add_u32 m0, %8, 0x1000\n\ts_nop 0\n\tglobal_load_lds_dword %5, %7\n\ts_mov_b32 m0, %0"
                   : "=&s"(keep)
                   : "v"(ra[0]), "v"(ra[1]), "v"(ra[2]), "v"(ra[3]), "v"(ra[4]), "s"(hmat), "s"(pos), "s"(mi)
                   : "memory", "scc");
    }
    unsigned ent, total;
    auto sort = [&](auto NS_, auto FULL_) __attribute__((always_inline)) {
      constexpr int NS = decltype(NS_)::value;
      constexpr bool FULL = decltype(FULL_)::value;
      unsigned bk[NS], rank[NS];
      u32x2 rec[NS];
#pragma unroll
      for (int i = 0; i < NS; i++) {
        const int el = i * 512 + tid;
        const unsigned b = mul24(d[i], 85599u) >> 24;
        const unsigned dl = d[i] - mul24(b, NB);
        bk[i] = (FULL || el < ne ? b : NBK + lane) * 4;
        rank[i] = __hip_atomic_fetch_add(&LDS(unsigned, L_CNT + bk[i]), 8u, __ATOMIC_RELAXED, __HIP_MEMORY_SCOPE_WORKGROUP);
        rec[i][0] = el;
        rec[i][1] = ((mul24(dl >> 3, 1272u) + dl) << 16) | sv[i] | 0x80000u;
      }
      LDS(unsigned, L_BASE + (NBK + lane) * 4) = 0x40000000u;
      barrier_lds();
      u32x4 cv = LDS(u32x4, L_CNT + lane * 16);
#pragma unroll
      for (int i = 0; i < 4; i++) cv[i] = (cv[i] + 8) & ~15u;
      const unsigned sum = cv[0] + cv[1] + cv[2] + cv[3], incl = wave_scan_incl(sum);
      const unsigned ex = incl - sum + L_REC;
      total = __builtin_amdgcn_readlane(incl, 63);
      const u32x4 bv = {ex, ex + cv[0], ex + cv[0] + cv[1], ex + cv[0] + cv[1] + cv[2]};
      LDS(u32x4, L_BASE + lane * 16) = bv;
      unsigned bs[NS];
#pragma unroll
      for (int i = 0; i < NS; i++) bs[i] = LDS(unsigned, L_BASE + bk[i]);
      ent = ((LDS(unsigned, L_BASE + (tid & 255) * 4) - L_REC) >> 4) | (LDS(unsigned, L_CNT + (tid & 255) * 4) << 9);
      const unsigned lim = L_REC + 8 * SEG + lane * 8;
#pragma unroll
      for (int i = 0; i < NS; i++) LDS(u32x2, FULL ? bs[i] + rank[i] : min(bs[i] + rank[i], lim)) = rec[i];
      barrier_lds();
    };
    if (ne == CH && w != 0) sort(std::integral_constant<int, 6>(), std::true_type());
    else sort(std::integral_constant<int, 7>(), std::false_type());
    LDS(unsigned, L_CNT + tid * 4) = 0;
    STAMP(11);
    tag_lo = (unsigned)(ep + 1) & 0xffu;
    tag_hi = (unsigned)((ep + 1) >> 8);
    {
      const u32x2 g = {ent | (tag_lo << 24), tag_hi};
      __builtin_amdgcn_raw_buffer_store_b64(g, rs_tab, tid < NBK ? bkt * (NBK * 8) + tid * 8 : 0x80000000u, 0, 16);
      const auto rs_out = __builtin_amdgcn_make_buffer_rsrc((void*)(ws + WS_REC + (size_t)bkt * CHS), 0, __builtin_amdgcn_readfirstlane(total), 0x00020000);
#pragma unroll
      for (int i = 0; i < 4; i++) {
        const u32x4 v = LDS(u32x4, L_REC + (i * 512 + tid) * 16);
        const u32x4 o = {v[1], v[3], v[0] | ((v[2] & 0xfffu) << 12) | (tag_lo << 24), tag_hi};
        __builtin_amdgcn_raw_buffer_store_b128(o, rs_out, (i * 512 + tid) * 16, 0, 16);
      }
    }
    STAMP(12);
    asm volatile("s_waitcnt vmcnt(10)" : "+v"(wdv0), "+v"(wdv1), "+v"(xa), "+v"(xb) :: "memory");
    {
      const unsigned tp = wl + (8 * h + ((lane >> 2) & 3)) * 128 + ((lane >> 4) & 1) * 32 + (lane & 3) * 8;
      u32x4 f[2], g[2];
#pragma unroll
      for (int t = 0; t < 2; t++) { f[t] = tr_frag(tp + 64 * t, 512); g[t] = tr_frag(tp + 2048 + 64 * t, 512); }
#pragma unroll
      for (int t = 0; t < 2; t++) {
        if (w < 4) { f[t] = fold(f[t]); g[t] = fold(g[t]); }
        LDS(u32x4, L_WH + (2 * w + t) * 1024 + lane * 16) = f[t];
        LDS(u32x4, wl + 2048 + t * 1024 + lane * 16) = g[t];
      }
      u32x4 x;
      if (w < 2) x = fold(tr_frag(wl + W_STE + (8 * h + ((lane >> 2) & 3)) * 64 + ((lane >> 4) & 1) * 32 + (lane & 3) * 8, 256));
      else {
        const unsigned m = (lane & 3) == 0 && ((lane >> 2) & 1) == ((lane >> 4) & 1) ? ~0u : 0u;
        x = (u32x4){xa[0] & m, xa[1] & m, xb[0] & m, xb[1] & m};
      }
      if (w < 6) LDS(u32x4, L_WJ + w * 1024 + lane * 16) = x;
    }
    if (tid >= 256 && tid < 256 + 26 * 4) {
      const int t = tid - 256, row = t >> 2, sx = (t >> 1) & 1, hh = t & 1;
      const int x = row < DUMMY ? row - 16 * sx - 4 * hh : -1;
      u32x4 v;
      v[0] = x == 0 ? 0x3f80u : x == 1 ? 0x3f800000u : 0u;  v[1] = x == 2 ? 0x3f80u : x == 3 ? 0x3f800000u : 0u;
      v[2] = x == 8 ? 0x3f80u : x == 9 ? 0x3f800000u : 0u;  v[3] = x == 10 ? 0x3f80u : x == 11 ? 0x3f800000u : 0u;
      LDS(u32x4, L_LUT + row * 80 + (t & 3) * 16) = v;
    }
    {
      const u32x4 z4 = {0u, 0u, 0u, 0u};
#pragma unroll
      for (int i = 0; i < 3; i++) { const int o = i * 1024 + lane * 16; if (o < 26 * 80 + 256) LDS(u32x4, wl + W_SI + o) = z4; }
    }
    barrier_lds();
    STAMP(14);
  }

  f32x16 Y0, Y1; f32x4 Yp = {0.f, 0.f, 0.f, 0.f};
#pragma unroll
  for (int i = 0; i < 16; i++) { Y0[i] = 0.f; Y1[i] = 0.f; }
  u32x32 W2; u32x16 PA; u32x8 W3; u32x4 WX0, WX1, WX2, WX3;
  int jbase = 0, part = -1, grp = w, gother = w;

  auto round = [&](bool first) __attribute__((always_inline)) -> bool {
    const int lane = lane_id(), tid = w * 64 + lane, r = lane & 31, h = lane >> 5;
    const unsigned zr = opaque(0);
    if (!first) { LDS(unsigned, L_CNT + tid * 4) = zr; barrier_lds(); }
    LDS(unsigned, L_BASE + lane * 4) = 0x40000000u;
    unsigned tb[4];
    {
      u32x2 t;
      for (int spin = 0;; spin++) {
        asm volatile("buffer_load_dwordx2 %0, %1, %2, %3 offen sc1\n\ts_waitcnt vmcnt(0)"
                     : "=&v"(t) : "v"((w * 32 + r) * (NBK * 8)), "s"(rs_tab), "s"(bkt * 8) : "memory");
        if (__builtin_amdgcn_ballot_w64(t[1] != tag_hi || (t[0] >> 24) != tag_lo) == 0) break;
        if (spin > (1 << 20)) { t[0] = 0; break; }
        __builtin_amdgcn_s_sleep(POLL_SLEEP);
      }
      LDS(unsigned, L_WAVE + w * WSZ + W_STE + r * 4) = t[0];
      const u32x4 t0 = LDS(u32x4, L_WAVE + w * WSZ + W_STE + (lane >> 3) * 16);
#pragma unroll
      for (int p = 0; p < 4; p++) tb[p] = t0[p];
    }
    STAMP(8);
    u32x4 gr[6];
    unsigned ad[6], left[6], eb[6];
    unsigned lmax = 0, nov = 0;
    bool big;
    {
      const unsigned jj = lane & (LG - 1), c0 = (unsigned)tid / LG * 4, oob = 0x80000000u + zr;
      const unsigned o0 = mul24(c0, CHS) + (jbase / 2 + jj) * 16, eb0 = mul24(c0, CH);
      unsigned off[4], len[4];
#pragma unroll
      for (int p = 0; p < 4; p++) {
        len[p] = (tb[p] >> 12) & 0xfffu;
        if (part >= 0 && part != 4 * p + (w & 3)) len[p] = 0;
        off[p] = o0 + (tb[p] & 0xfffu) * 16 + p * CHS;
        left[p] = len[p] - (jbase + 2 * jj);
        ad[p] = (int)left[p] > 0 ? off[p] : oob;
        eb[p] = eb0 + p * CH;
        asm volatile("buffer_load_dwordx4 %0, %1, %2, 0 offen sc1" : "=v"(gr[p]) : "v"(ad[p]), "s"(rs_rec) : "memory");
        lmax = max(lmax, tb[p] & 0xfff000u);
      }
      asm volatile("" : "+v"(len[0]), "+v"(len[1]), "+v"(len[2]), "+v"(len[3]));
      ad[4] = oob; ad[5] = oob; left[4] = zr; left[5] = zr; eb[4] = zr; eb[5] = zr;
      unsigned ov[4], s1 = 0;
#pragma unroll
      for (int p = 0; p < 4; p++) {
        ov[p] = (unsigned)min(max(((int)len[p] - (jbase + 2 * LG) + 1) >> 1, 0), LG);
        const bool sel = jj - nov < ov[p];
        ad[4] = sel ? off[p] + (LG - nov) * 16 : ad[4];
        left[4] = sel ? len[p] - (jbase + 2 * LG) - 2 * (jj - nov) : left[4];
        eb[4] = sel ? eb0 + p * CH : eb[4];
        nov += ov[p];
      }
      asm volatile("buffer_load_dwordx4 %0, %1, %2, 0 offen sc1" : "=v"(gr[4]) : "v"(ad[4]), "s"(rs_rec) : "memory");
      gr[5] = (u32x4){zr, zr, zr, zr};
      big = __builtin_amdgcn_ballot_w64(nov > LG) != 0;
      if (big) {
#pragma unroll
        for (int p = 0; p < 4; p++) {
          const bool sel = jj + LG - s1 < ov[p];
          ad[5] = sel ? off[p] + (2 * LG - s1) * 16 : ad[5];
          left[5] = sel ? len[p] - (jbase + 2 * LG) - 2 * (jj + LG - s1) : left[5];
          eb[5] = sel ? eb0 + p * CH : eb[5];
          s1 += ov[p];
        }
        asm volatile("buffer_load_dwordx4 %0, %1, %2, 0 offen sc1" : "+v"(gr[5]) : "v"(ad[5]), "s"(rs_rec) : "memory");
      }
    }
    unsigned sink = zr;
    STAMP(7);
    u32x4 w1f[8];
    if (first) {
#pragma unroll
      for (int i = 0; i < 8; i++) w1f[i] = LDS(u32x4, L_WAVE + (4 + (i >> 1)) * WSZ + 2048 + (i & 1) * 1024 + lane * 16);
#pragma unroll
      for (int i = 0; i < 8; i++) {
        const u32x4 v = LDS(u32x4, L_WAVE + (i >> 1) * WSZ + 2048 + (i & 1) * 1024 + lane * 16);
#pragma unroll
        for (int j = 0; j < 4; j++) W2[4 * i + j] = v[j];
      }
#pragma unroll
      for (int i = 0; i < 2; i++) {
        const u32x4 v = LDS(u32x4, L_WJ + i * 1024 + lane * 16);
#pragma unroll
        for (int j = 0; j < 4; j++) W3[4 * i + j] = v[j];
      }
      WX0 = LDS(u32x4, L_WJ + 2048 + lane * 16); WX1 = LDS(u32x4, L_WJ + 3072 + lane * 16);
      WX2 = LDS(u32x4, L_WJ + 4096 + lane * 16); WX3 = LDS(u32x4, L_WJ + 5120 + lane * 16);
    }
    STAMP(13);
    if ((lmax >> 12) > (unsigned)jbase + 4 * LG || nov > 2 * LG) __hip_atomic_fetch_or(&LDS(unsigned, L_FLAG), nov > 2 * LG ? 3u : 1u, __ATOMIC_RELAXED, __HIP_MEMORY_SCOPE_WORKGROUP);
    unsigned mk1 = (lane & 7) >= 1 ? ~zr : zr, mk2 = (lane & 7) >= 2 ? ~zr : zr, sg0 = L_REC + (lane >> 3) * SEG;
    unsigned lim = L_REC + 8 * SEG + lane * 8, own = n0 + min(8 * (r & 3) + (r >> 2), 24) * 8;
    unsigned ce = ((lane & 1) ^ ((lane >> 4) & 1)) * 16, cp = (lane & 1) * 2;
    unsigned cc0 = ((lane & 7) ^ (lane >> 4)) * 16, cc1 = ((lane & 7) ^ (4 + (lane >> 4))) * 16;
    unsigned q_s4 = L_REC + (lane >> 3) * 32 + 4;
    unsigned q_r2 = L_REC + (4 * ((lane >> 1) & 7) + (lane >> 4)) * 8;
    unsigned q_r1 = L_REC + (4 * (r & 7) + (r >> 3)) * 8 + 4;
    asm volatile("" : "+v"(mk1), "+v"(mk2), "+v"(sg0), "+v"(lim), "+v"(own), "+v"(ce), "+v"(cp), "+v"(cc0), "+v"(cc1), "+v"(q_s4),
                 "+v"(q_r2), "+v"(q_r1));
    asm volatile("s_waitcnt vmcnt(0)" : "+v"(gr[0]), "+v"(gr[1]), "+v"(gr[2]), "+v"(gr[3]), "+v"(gr[4]), "+v"(gr[5]) :: "memory");
    for (int spin = 0;; spin++) {
      bool bad = false;
#pragma unroll
      for (int p = 0; p < 6; p++) bad |= ((int)left[p] > 0) & ((gr[p][3] != tag_hi) | ((gr[p][2] >> 24) != tag_lo));
      if (__builtin_amdgcn_ballot_w64(bad) == 0 || spin > (1 << 20)) break;
      asm volatile("buffer_load_dwordx4 %0, %6, %12, 0 offen sc1\n\tbuffer_load_dwordx4 %1, %7, %12, 0 offen sc1\n\t"
                   "buffer_load_dwordx4 %2, %8, %12, 0 offen sc1\n\tbuffer_load_dwordx4 %3, %9, %12, 0 offen sc1\n\t"
                   "buffer_load_dwordx4 %4, %10, %12, 0 offen sc1\n\tbuffer_load_dwordx4 %5, %11, %12, 0 offen sc1\n\ts_waitcnt vmcnt(0)"
                   : "=&v"(gr[0]), "=&v"(gr[1]), "=&v"(gr[2]), "=&v"(gr[3]), "=&v"(gr[4]), "=&v"(gr[5])
                   : "v"(ad[0]), "v"(ad[1]), "v"(ad[2]), "v"(ad[3]), "v"(ad[4]), "v"(ad[5]), "s"(rs_rec) : "memory");
    }
    STAMP(1);
    u32x2 rc[12];
#pragma unroll
    for (int p = 0; p < 6; p++) {
      rc[2 * p] = (u32x2){(eb[p] + (gr[p][2] & 0xfffu)) << 5, (int)left[p] > 0 ? gr[p][0] : 0u};
      rc[2 * p + 1] = (u32x2){(eb[p] + ((gr[p][2] >> 12) & 0xfffu)) << 5, (int)left[p] > 1 ? gr[p][1] : 0u};
    }
    unsigned rk[12], ky[12];
#pragma unroll
    for (int p = 0; p < 12; p++) {
      if (p == 10 && !big) { ky[10] = ky[11] = 0; rk[10] = rk[11] = 0; break; }
      ky[p] = max((rc[p][1] >> 9) & 0x7fcu, (unsigned)lane * 4);
      rk[p] = __hip_atomic_fetch_add(&LDS(unsigned, L_CNT + ky[p]), 8u + zr, __ATOMIC_RELAXED, __HIP_MEMORY_SCOPE_WORKGROUP);
    }
    if (EPF) {
#pragma unroll
      for (int p = 0; p < 10; p++) {
        const unsigned a = (ky[p] & 0x47cu) - 0x400u < (unsigned)EPF * 4 ? rc[p][0] : 0x80000000u + zr;
        asm volatile("buffer_load_dword %0, %1, %2, 0 offen" : "+{v3}"(sink) : "v"(a), "s"(rs_ea) : "memory");
      }
    }
    barrier_lds();
    STAMP(2);
    unsigned g;
    const unsigned flagv = LDS(unsigned, L_FLAG);
    {
      const u32x4 cv = LDS(u32x4, L_CNT + 1024 + lane * 16);
      const unsigned sum = cv[0] + cv[1] + cv[2] + cv[3];
      g = sum + (__builtin_amdgcn_update_dpp(0, (int)sum, 0x111, 0xf, 0xf, true) & mk1);
      g += __builtin_amdgcn_update_dpp(0, (int)g, 0x112, 0xf, 0xf, true) & mk2;
      g += __builtin_amdgcn_update_dpp(0, (int)g, 0x114, 0xf, 0xa, false);
      const unsigned ex = g - sum + sg0;
      const u32x4 bv = {ex, ex + cv[0], ex + cv[0] + cv[1], ex + cv[0] + cv[1] + cv[2]};
      LDS(u32x4, L_BASE + 1024 + lane * 16) = bv;
    }
    unsigned bs[10];
#pragma unroll
    for (int p = 0; p < 10; p++) bs[p] = LDS(unsigned, L_BASE + ky[p]);
    const unsigned flag = __builtin_amdgcn_readfirstlane(flagv);
    const bool again = flag & 1;
    const bool over = (flag & 2) || __builtin_amdgcn_ballot_w64(g > (unsigned)(SEG - 256)) != 0;
    if (!over) {
#pragma unroll
      for (int p = 0; p < 10; p++) LDS(u32x2, min(bs[p] + rk[p], lim)) = rc[p];
      if (big) {
        LDS(u32x2, min(LDS(unsigned, L_BASE + ky[10]) + rk[10], lim)) = rc[10];
        LDS(u32x2, min(LDS(unsigned, L_BASE + ky[11]) + rk[11], lim)) = rc[11];
      }
    }
    if (first) {
      const unsigned key = (g << 3) | (lane >> 3);
      unsigned ks[8], rank = 0;
#pragma unroll
      for (int k = 0; k < 8; k++) ks[k] = __builtin_amdgcn_readlane(key, 8 * k + 7);
#pragma unroll
      for (int k = 0; k < 8; k++) rank += ks[k] > key;
      const unsigned long long sel = 0x8080808080808080ull;
      grp = __builtin_ctzll(__builtin_amdgcn_ballot_w64(rank == (unsigned)(w < 4 ? w : 11 - w)) & sel) >> 3;
      gother = __builtin_ctzll(__builtin_amdgcn_ballot_w64(rank == (unsigned)(w < 4 ? 7 - w : w - 4)) & sel) >> 3;
      wl = L_WAVE + grp * WSZ; mi = L_MI + grp * MISZ;
    }
    unsigned segn = over ? 0u : (unsigned)__builtin_amdgcn_readlane(g, 8 * grp + 7);
    const unsigned seg0 = grp * SEG;
    const int nt = (segn + 255) >> 8;
    {
      const u32x2 pad = {zr, DUMMY * (80u << 20)}, row = {zr, min(own + grp, (unsigned)N - 1) | (DUMMY * (80u << 20))};
      LDS(u32x2, L_REC + seg0 + segn + r * 8) = pad;
      LDS(u32x2, L_REC + seg0 + nt * 256 + r * 8) = row;
    }
    barrier_lds();
    STAMP(3);

    const unsigned o_s4 = q_s4 + seg0, o_r2 = q_r2 + seg0, o_r1 = q_r1 + seg0;
    {
      unsigned ns[4];
#pragma unroll
      for (int i = 0; i < 4; i++) ns[i] = LDS(uint16_t, o_s4 + i * 8);
      const u32x2 nr2 = LDS(u32x2, o_r2);
      dma_tile(hmat, ea, pos, ns[0] * 128 + cc0, ns[1] * 128 + cc1, ns[2] * 128 + cc0, ns[3] * 128 + cc1,
               nr2[0] | ce, (nr2[1] & 0xffffu) * 6 + cp, wl);
    }
    if (first) {
      u32x2 hb[8];
#pragma unroll
      for (int i = 0; i < 8; i++) hb[i] = LDS(u32x2, mi + r * 128 + ((i ^ ((r >> 1) & 7)) * 16) + h * 8);
      const u32x2 pg = LDS(u32x2, mi + 4096 + r * 8);
#pragma unroll
      for (int t = 0; t < 2; t++) {
        f32x16 P;
#pragma unroll
        for (int i = 0; i < 16; i++) P[i] = 0.f;
#pragma unroll
        for (int s = 0; s < 4; s++) {
          const u32x4 a = {hb[2 * s][0], hb[2 * s][1], hb[2 * s + 1][0], hb[2 * s + 1][1]};
          P = MFMA(as_bf(a), as_bf(w1f[2 * s + t]), P);
        }
        if (h) P[15] = bf2f(t ? wdv1 : wdv0);
#pragma unroll
        for (int i = 0; i < 16; i++) P[i] *= -LOG2E;
#pragma unroll
        for (int s = 0; s < 2; s++)
#pragma unroll
          for (int j = 0; j < 4; j++) PA[4 * (2 * s + t) + j] = pk_bf16(P[8 * s + 2 * j], P[8 * s + 2 * j + 1]);
      }
      const f32x4 pv = {bf2f(pg[0] & 0xffffu), __uint_as_float(pg[0] & 0xffff0000u), __uint_as_float(pg[1] & 0xffff0000u), 0.f};
      if (lane < DUMMY) LDS(f32x4, wl + W_SI + lane * 80 + 64) = pv;
      if (bkt == 0 && tid == 0) {
        const u32x2 e1 = {tag_lo | (tag_hi << 8), tag_hi >> 24};
        asm volatile("global_store_dwordx2 %0, %1, %2 sc1" :: "v"(zr), "v"(e1), "s"(ws + WS_EPOCH) : "memory");
      }
    }
    const unsigned nother = (__builtin_amdgcn_readlane(g, 8 * gother + 7) + 255) >> 8;
    const bool last_round = !over && !again && (part < 0 || part == 15);
    STAMP(4);

    {
      u32x16 A0; u32x8 A1;
      A0[0] = zr; A0[1] = zr;
#pragma unroll
      for (int s = 0; s < 4; s++) A0[2 + s] = wl + W_STH + r * 128 + (((2 * s + h) ^ ((r >> 1) & 7)) * 16);
      A0[6] = wl + r * 32 + ((h ^ ((r >> 3) & 1)) * 16);
      A0[7] = wl + r * 8;
      A0[8] = L_LUT + h * 16;
      A0[9] = wl + r * 2;
      A0[10] = wl + min(r, DUMMY) * 80 + h * 16;
      A0[11] = mi + r * MIL + h * 32;
      A0[12] = mi + (8 * h + ((lane >> 2) & 3)) * MIL + (4 * ((lane >> 4) & 1) + (lane & 3)) * 8;
      A0[13] = wl + h * 64 + r * 2;
      {
        const int row = (lane & 15) - 4 * ((lane >> 4) & 1);
        A0[14] = wl + ((unsigned)row < 3 ? row : 3) * 64 + h * 16;
      }
      A0[15] = L_WJ + lane * 16;
      A1[0] = cc0; A1[1] = cc1; A1[2] = ce; A1[3] = cp;
      A1[4] = o_s4 + 256; A1[5] = o_r2 + 256; A1[6] = o_r1;
      A1[7] = 0x3f800000u + zr;
      unsigned cnt = nt, keep;
#define PRIO_A 14
#define PRIO_B 7
      const int lastv = __builtin_amdgcn_readfirstlane(opaque(last_round ? 1 : 0));
      const int thr = __builtin_amdgcn_readfirstlane(w >= 4 ? max((int)(nother * PRIO_A - cnt * PRIO_B) >> 5, 0) : -1);
      asm volatile(LOOP_ASM
                   : "+{v[52:67]}"(Y0), "+{v[68:83]}"(Y1), "+{v[84:87]}"(Yp), "+{v[232:247]}"(A0), "+{v[248:255]}"(A1), "+{v3}"(sink),
                     [nt] "+s"(cnt), [keep] "=&s"(keep)
                   : "{v[4:35]}"(W2), "{v[36:51]}"(PA), "{v[88:95]}"(W3), "{v[96:99]}"(WX0), "{v[136:139]}"(WX1), "{v[140:143]}"(WX2), "{v[144:147]}"(WX3),
                     [h0] "s"(hmat), [h1] "s"(hmat - 1024), [h2] "s"(hmat - 2048), [h3] "s"(hmat - 3072), [ea] "s"(ea), [pos] "s"(pos - 1024),
                     [b1] "s"(wl), [b2] "s"(wl + W_STE), [posd] "s"(wl + W_SI + 64), [c128] "s"(128u), [thr] "s"(thr), [last] "s"(lastv),
                     [hsel] "s"(0xffffffff00000000ull)
                   : "memory", "scc", "vcc", CLOBBER_V);
    }
    if (over) part = 0;
    else if (part >= 0 && part < 15) part++;
    else { part = -1; jbase += 4 * LG; }
    return last_round;
  };
  if (!round(true)) {
    do {
      wait_vm0();
      barrier_lds();
      jbase = __builtin_amdgcn_readfirstlane(jbase); part = __builtin_amdgcn_readfirstlane(part);
    } while (!round(false));
  }
  STAMP(5);

  {
    const int lane = lane_id(), r = lane & 31, h = lane >> 5;
    Yp[0] *= -LN2; Yp[1] *= -LN2; Yp[2] *= -LN2;
    const auto rs_o = __builtin_amdgcn_make_buffer_rsrc((void*)out, 0, N * 128, 0x00020000);
#pragma unroll
    for (int i = 0; i < 4; i++) {
      const int q = 8 * i + (lane >> 3), dl = q * 8 + grp;
      const u32x4 v = LDS(u32x4, mi + q * MIP + (lane & 7) * 16);
      if (dl < NB) __builtin_amdgcn_raw_buffer_store_b128(v, rs_o, (n0 + dl) * 128 + (lane & 7) * 16, 0, OUT_AUX);
    }
    const int dl = r * 8 + grp;
    if (h == 0 && dl < NB && n0 + dl < N) {
      const f32x4 pd = LDS(f32x4, wl + W_SI + r * 80 + 64);
      uint16_t* po = out + (size_t)N * 64 + (size_t)(n0 + dl) * 3;
      const unsigned xy = pk_bf16(pd[0] + Yp[0], pd[1] + Yp[1]), zz = pk_bf16(pd[2] + Yp[2], 0.f);
      po[0] = (uint16_t)xy; po[1] = (uint16_t)(xy >> 16); po[2] = (uint16_t)zz;
    }
  }
  STAMP(6);
}

}

#define STAMP_PASS1
#define STAMP_PASS2

extern "C" void kernel_launch(void* const* d_in, const int* in_sizes, int n_in, void* d_out, int out_size,
                              void* d_ws, size_t ws_size, hipStream_t stream) {
  const int N = in_sizes[0] / 64, E = in_sizes[3];
  if (N > NBK * NB || E > NBK * CH || N < 1 || E < 1) return;
  (void)hipFuncSetAttribute((const void*)egnn_kernel, hipFuncAttributeMaxDynamicSharedMemorySize, L_TOTAL);
  egnn_kernel<<<NBK, 512, L_TOTAL, stream>>>((const char*)d_in[0], (const char*)d_in[1], (const char*)d_in[2], (const int*)d_in[3],
                                             (const int*)d_in[4], (const char*)d_in[5], (const char*)d_in[6], (const char*)d_in[7],
                                             (char*)d_ws, (uint16_t*)d_out, N, E STAMP_PASS2);
}
```

```cpp
#include <hip/hip_runtime.h>
#include <stdint.h>
#include <type_traits>

#define FOLD_LOG2E 1

namespace {

typedef __bf16 bf16x8 __attribute__((ext_vector_type(8)));
typedef __bf16 bf16x2 __attribute__((ext_vector_type(2)));
typedef float f32x16 __attribute__((ext_vector_type(16)));
typedef float f32x4 __attribute__((ext_vector_type(4)));
typedef unsigned u32x4 __attribute__((ext_vector_type(4)));
typedef unsigned u32x2 __attribute__((ext_vector_type(2)));
typedef unsigned u32x8 __attribute__((ext_vector_type(8)));
typedef unsigned u32x16 __attribute__((ext_vector_type(16)));
typedef unsigned u32x32 __attribute__((ext_vector_type(32)));
typedef short s16x4 __attribute__((ext_vector_type(4)));
typedef __attribute__((address_space(3))) s16x4* lds_s16x4_p;

#define DI __device__ __forceinline__
#define MFMA(a, b, c) __builtin_amdgcn_mfma_f32_32x32x16_bf16((a), (b), (c), 0, 0, 0)

#define POLL_SLEEP 2
#define DYNP
#define DYNP_BIAS (w >> 2)
#define EPF 2
#define OUT_AUX 16
constexpr int NBK = 256;
constexpr int LG = 8;
constexpr int NB = 196;
constexpr int CH = 3136;
constexpr float LOG2E = 1.4426950408889634f;
constexpr float LN2 = 0.6931471805599453f;

constexpr size_t WS_EPOCH = 0;
constexpr size_t WS_TAB = 256;
constexpr size_t WS_REC = WS_TAB + (size_t)NBK * NBK * 8;
constexpr int CHS = (CH + NBK) * 8;

constexpr int W_STH = 0, W_STE = 4096, W_STP = 5120, W_SI = 5376, W_TI = W_SI + 26 * 80, WSZ = W_TI + 256;
constexpr int DUMMY = 25;
constexpr int MIP = 144, MISZ = 32 * MIP;
constexpr int MIL = 80;
constexpr int SEG = 17 * 256;
constexpr int L_CNT = 0;
constexpr int L_BASE = L_CNT + 2048;
constexpr int L_FLAG = L_CNT + 512;
constexpr int L_REC = L_BASE + 2048;
constexpr int L_LUT = L_REC + 8 * SEG + 512;
constexpr int L_WAVE = L_LUT + 26 * 80;
constexpr int L_MI = L_WAVE + 8 * WSZ;
constexpr int L_WJ = L_MI + 8 * MISZ;
constexpr int L_WH = L_WJ + 6 * 1024;
constexpr int L_TOTAL = L_WH + 16 * 1024;
static_assert(L_TOTAL <= 163840, "LDS");
static_assert(WSZ % 16 == 0 && L_MI % 16 == 0 && L_LUT % 16 == 0, "align");

#pragma clang diagnostic ignored "-Wint-to-pointer-cast"
#define LDS(T, off) (*(__attribute__((address_space(3))) T*)(unsigned)(off))
DI unsigned mul24(unsigned a, unsigned b) { return (unsigned)__umul24(a, b); }
DI float bf2f(unsigned lo16) { return __uint_as_float(lo16 << 16); }
DI unsigned pk_bf16(float a, float b) { bf16x2 v = {(__bf16)a, (__bf16)b}; return __builtin_bit_cast(unsigned, v); }
DI bf16x8 as_bf(u32x4 v) { return __builtin_bit_cast(bf16x8, v); }
DI int opaque(int x) { asm volatile("" : "+v"(x)); return x; }
DI int lane_id() {
  int x; asm volatile("v_mbcnt_lo_u32_b32 %0, -1, 0\n\tv_mbcnt_hi_u32_b32 %0, -1, %0" : "=v"(x)); return x;
}
DI void wait_vm0() { asm volatile("s_waitcnt vmcnt(0)" ::: "memory"); }
DI void barrier_lds() { asm volatile("s_waitcnt lgkmcnt(0)\n\ts_barrier" ::: "memory"); }
DI void dma_tile(const void* hm, const void* ea, const void* ps, unsigned o0, unsigned o1, unsigned o2, unsigned o3,
                 unsigned oe, unsigned op, unsigned l) {
  unsigned keep;
  asm volatile("s_mov_b32 %0, m0\n\t"
               "s_add_u32 m0, %10, 0x1000\n\ts_nop 0\n\tglobal_load_lds_dwordx4 %5, %8\n\t"
               "s_mov_b32 m0, %10\n\ts_nop 0\n\tglobal_load_lds_dwordx4 %1, %7\n\t"
               "s_add_u32 m0, %10, 0x400\n\ts_nop 0\n\tglobal_load_lds_dwordx4 %2, %7\n\t"
               "s_add_u32 m0, %10, 0x800\n\ts_nop 0\n\tglobal_load_lds_dwordx4 %3, %7\n\t"
               "s_add_u32 m0, %10, 0xc00\n\ts_nop 0\n\tglobal_load_lds_dwordx4 %4, %7\n\t"
               "s_add_u32 m0, %10, 0x1400\n\ts_nop 0\n\tglobal_load_lds_dword %6, %9\n\t"
               "s_mov_b32 m0, %0"
               : "=&s"(keep) : "v"(o0), "v"(o1), "v"(o2), "v"(o3), "v"(oe), "v"(op), "s"(hm), "s"(ea), "s"(ps), "s"(l)
               : "memory", "scc");
}
template <int CTRL, int ROWMASK> DI unsigned dpp_add(unsigned x) {
  return x + (unsigned)__builtin_amdgcn_update_dpp(0, (int)x, CTRL, ROWMASK, 0xf, false);
}
DI unsigned wave_scan_incl(unsigned x) {
  x = dpp_add<0x111, 0xf>(x); x = dpp_add<0x112, 0xf>(x); x = dpp_add<0x114, 0xf>(x); x = dpp_add<0x118, 0xf>(x);
  x = dpp_add<0x142, 0xa>(x); x = dpp_add<0x143, 0xc>(x);
  return x;
}
#define STAMP(i)
#define STAMP_ARG

#define CLOBBER_V "v100", "v101", "v102", "v103", "v104", "v105", "v106", "v107", "v108", "v109", "v110", "v111", "v112", "v113", "v114", "v115", "v116", "v117", "v118", "v119", "v120", "v121", "v122", "v123", "v124", "v125", "v126", "v127", "v128", "v129", "v130", "v131", "v132", "v133", "v134", "v135", "v148", "v149", "v150", "v151", "v152", "v153", "v154", "v155", "v156", "v157", "v158", "v159", "v160", "v161", "v162", "v163", "v164", "v165", "v166", "v167", "v168", "v169", "v170", "v171", "v172", "v173", "v174", "v175", "v176", "v177", "v178", "v179", "v180", "v181", "v182", "v183", "v184", "v185", "v186", "v187", "v188", "v189", "v190", "v191", "v192", "v193", "v194", "v195", "v196", "v197", "v198", "v199", "v200", "v201", "v202", "v203", "v204", "v205", "v206", "v207", "v208", "v209", "v210", "v211", "v214", "v215", "v216", "v217", "v218", "v219", "v220", "v221", "v222", "v223", "v224", "v225", "v226", "v227", "v228", "v229", "v230", "v231"
#define LOOP_ASM_FOLD \
  "s_mov_b32 %[keep], m0\n\t" \
  "s_mov_b64 vcc, %[hsel]\n\t" \
  "ds_read2_b32 v[220:221], v252 offset0:0 offset1:2\n\t" \
  "ds_read2_b32 v[222:223], v252 offset0:4 offset1:6\n\t" \
  "ds_read_b64 v[226:227], v253\n\t" \
  "ds_read_b32 v232, v254\n\t" \
  "v_mov_b32 v176, 0\n\t" \
  "v_mov_b32 v177, 0\n\t" \
  "v_mov_b32 v178, 0\n\t" \
  "v_mov_b32 v179, 0\n\t" \
  "v_mov_b32 v180, 0\n\t" \
  "v_mov_b32 v181, 0\n\t" \
  "v_mov_b32 v182, 0\n\t" \
  "v_mov_b32 v183, 0\n\t" \
  "v_mov_b32 v184, 0\n\t" \
  "v_mov_b32 v185, 0\n\t" \
  "v_mov_b32 v186, 0\n\t" \
  "v_mov_b32 v187, 0\n\t" \
  "v_mov_b32 v188, 0\n\t" \
  "v_mov_b32 v189, 0\n\t" \
  "v_mov_b32 v190, 0\n\t" \
  "v_mov_b32 v191, 0\n\t" \
  "v_mov_b32 v230, 0\n\t" \
  "v_mov_b32 v231, 0\n\t" \
  "v_mov_b32 v208, 0\n\t" \
  "v_mov_b32 v209, 0\n\t" \
  "v_mov_b32 v210, 0\n\t" \
  "v_mov_b32 v211, 0\n\t" \
  "ds_write_b128 v243, v[184:187] offset:0\n\t" \
  "s_nop 0\n\t" \
  "v_mfma_f32_16x16x32_bf16 v[132:135], v[184:187], v[188:191], 0\n\t" \
  "v_add_u32 v252, 0x100, v252\n\t" \
  "v_add_u32 v253, 0x100, v253\n\t" \
  "v_add_u32 v254, 0x100, v254\n\t" \
  "s_waitcnt lgkmcnt(0)\n\t" \
  "v_lshrrev_b32 v232, 20, v232\n\t" \
  "v_add_u32 v233, %[posd], v232\n\t" \
  "ds_read_b128 v[216:219], v233\n\t" \
  "v_add_u32 v233, v240, v232\n\t" \
  "ds_read_b128 v[172:175], v233 offset:32\n\t" \
  "ds_read_b128 v[168:171], v233\n\t" \
  "v_add_u32 v233, v241, v232\n\t" \
  "ds_write_b16_d16_hi v233, v255 offset:5376\n\t" \
  "v_mad_u32_u16 v220, v220, %[c128], v248\n\t" \
  "v_mad_u32_u16 v221, v221, %[c128], v249\n\t" \
  "v_mad_u32_u16 v222, v222, %[c128], v248\n\t" \
  "v_mad_u32_u16 v223, v223, %[c128], v249\n\t" \
  "v_or_b32 v226, v226, v250\n\t" \
  "v_mad_u32_u16 v227, v227, 6, v251\n\t" \
  "s_waitcnt vmcnt(0)\n\t" \
  "ds_read_b128 v[148:151], v234\n\t" \
  "ds_read_b128 v[152:155], v235\n\t" \
  "ds_read_b128 v[156:159], v236\n\t" \
  "ds_read_b128 v[160:163], v237\n\t" \
  "ds_read_b128 v[164:167], v238 offset:4096\n\t" \
  "ds_read_b64 v[224:225], v239 offset:5120\n\t" \
  "s_waitcnt lgkmcnt(0)\n\t" \
  "s_cmp_lt_i32 %[nt], 1\n\t" \
  "s_cbranch_scc1 7f\n\t" \
  "s_mov_b32 m0, %[b2]\n\t" \
  "s_nop 0\n\t" \
  "global_load_lds_dwordx4 v226, %[ea]\n\t" \
  "global_load_lds_dword v227, %[pos] offset:1024\n\t" \
  "s_mov_b32 m0, %[b1]\n\t" \
  "s_nop 0\n\t" \
  "global_load_lds_dwordx4 v220, %[h0]\n\t" \
  "global_load_lds_dwordx4 v221, %[h1] offset:1024\n\t" \
  "global_load_lds_dwordx4 v222, %[h2] offset:2048\n\t" \
  "global_load_lds_dwordx4 v223, %[h3] offset:3072\n\t" \
  "7:\n\t" \
  "ds_read2_b32 v[220:221], v252 offset0:0 offset1:2\n\t" \
  "ds_read2_b32 v[222:223], v252 offset0:4 offset1:6\n\t" \
  "ds_read_b64 v[226:227], v253\n\t" \
  "ds_read_b32 v232, v254\n\t" \
  "v_add_u32 v252, 0x100, v252\n\t" \
  "v_add_u32 v253, 0x100, v253\n\t" \
  "v_add_u32 v254, 0x100, v254\n\t" \
  "v_and_b32 v225, 0xffff0000, v225\n\t" \
  "v_lshlrev_b32 v219, 16, v224\n\t" \
  "v_and_b32 v224, 0xffff0000, v224\n\t" \
  "v_sub_f32 v229, v218, v225\n\t" \
  "v_sub_f32 v216, v216, v219\n\t" \
  "v_sub_f32 v217, v217, v224\n\t" \
  "v_mul_f32 v218, v229, v229\n\t" \
  "v_cndmask_b32 v228, v216, v217, vcc\n\t" \
  "v_fmac_f32 v218, v216, v216\n\t" \
  "v_fmac_f32 v218, v217, v217\n\t" \
  "v_cvt_pk_bf16_f32 v218, v218, v218\n\t" \
  "v_cndmask_b32_sdwa v175, v175, v218, vcc dst_sel:WORD_1 dst_unused:UNUSED_PRESERVE src0_sel:WORD_1 src1_sel:WORD_1\n\t" \
  "v_mfma_f32_32x32x16_bf16 v[100:115], v[4:7], v[148:151], 0\n\t" \
  "v_mfma_f32_32x32x16_bf16 v[100:115], v[12:15], v[152:155], v[100:115]\n\t" \
  "v_mfma_f32_32x32x16_bf16 v[100:115], v[20:23], v[156:159], v[100:115]\n\t" \
  "v_mfma_f32_32x32x16_bf16 v[100:115], v[28:31], v[160:163], v[100:115]\n\t" \
  "v_mfma_f32_32x32x16_bf16 v[100:115], v[36:39], v[168:171], v[100:115]\n\t" \
  "v_mfma_f32_32x32x16_bf16 v[100:115], v[88:91], v[164:167], v[100:115]\n\t" \
  "v_mfma_f32_32x32x16_bf16 v[100:115], v[44:47], v[172:175], v[100:115]\n\t" \
  "s_cmp_eq_u32 %[nt], 0\n\t" \
  "s_nop 10\n\t" \
  "v_exp_f32 v192, v100\n\t" \
  "v_exp_f32 v193, v101\n\t" \
  "v_exp_f32 v194, v102\n\t" \
  "v_exp_f32 v195, v103\n\t" \
  "s_nop 0\n\t" \
  "v_add_f32 v192, 1.0, v192\n\t" \
  "v_add_f32 v193, 1.0, v193\n\t" \
  "v_add_f32 v194, 1.0, v194\n\t" \
  "v_add_f32 v195, 1.0, v195\n\t" \
  "s_cbranch_scc1 6f\n\t" \
  "s_cmp_lt_i32 %[thr], 0\n\t" \
  "s_cbranch_scc1 1f\n\t" \
  "s_setprio 1\n\t" \
  "1:\n\t" \
  "ds_write_b128 v243, v[188:191] offset:16\n\t" \
  "ds_read_b64_tr_b16 v[200:201], v244 offset:0\n\t" \
  "ds_read_b64_tr_b16 v[202:203], v244 offset:320\n\t" \
  "ds_read_b64_tr_b16 v[204:205], v244 offset:1280\n\t" \
  "ds_read_b64_tr_b16 v[206:207], v244 offset:1600\n\t" \
  "v_mfma_f32_16x16x32_bf16 v[132:135], v[144:147], v[188:191], v[132:135]\n\t" \
  "v_rcp_f32 v192, v192\n\t" \
  "v_rcp_f32 v193, v193\n\t" \
  "v_rcp_f32 v194, v194\n\t" \
  "v_rcp_f32 v195, v195\n\t" \
  "v_mfma_f32_32x32x16_bf16 v[116:131], v[8:11], v[148:151], 0\n\t" \
  "v_exp_f32 v196, v104\n\t" \
  "v_exp_f32 v197, v105\n\t" \
  "v_exp_f32 v198, v106\n\t" \
  "v_exp_f32 v199, v107\n\t" \
  "v_mfma_f32_32x32x16_bf16 v[116:131], v[16:19], v[152:155], v[116:131]\n\t" \
  "v_mul_f32 v192, v100, v192\n\t" \
  "v_mul_f32 v193, v101, v193\n\t" \
  "v_mul_f32 v194, v102, v194\n\t" \
  "v_mul_f32 v195, v103, v195\n\t" \
  "v_add_f32 v196, 1.0, v196\n\t" \
  "v_add_f32 v197, 1.0, v197\n\t" \
  "v_add_f32 v198, 1.0, v198\n\t" \
  "v_add_f32 v199, 1.0, v199\n\t" \
  "v_cvt_pk_bf16_f32 v184, v192, v193\n\t" \
  "v_cvt_pk_bf16_f32 v185, v194, v195\n\t" \
  "v_mfma_f32_32x32x16_bf16 v[116:131], v[24:27], v[156:159], v[116:131]\n\t" \
  "v_rcp_f32 v196, v196\n\t" \
  "v_rcp_f32 v197, v197\n\t" \
  "v_rcp_f32 v198, v198\n\t" \
  "v_rcp_f32 v199, v199\n\t" \
  "s_waitcnt lgkmcnt(2)\n\t" \
  "v_mfma_f32_32x32x16_bf16 v[68:83], v[200:203], v[176:179], v[68:83]\n\t" \
  "v_exp_f32 v192, v108\n\t" \
  "v_exp_f32 v193, v109\n\t" \
  "v_exp_f32 v194, v110\n\t" \
  "v_exp_f32 v195, v111\n\t" \
  "v_mfma_f32_32x32x16_bf16 v[116:131], v[32:35], v[160:163], v[116:131]\n\t" \
  "v_mul_f32 v196, v104, v196\n\t" \
  "v_mul_f32 v197, v105, v197\n\t" \
  "v_mul_f32 v198, v106, v198\n\t" \
  "v_mul_f32 v199, v107, v199\n\t" \
  "v_mul_f32 v230, v230, v132\n\t" \
  "v_mul_f32 v231, v231, v132\n\t" \
  "v_cvt_pk_bf16_f32 v230, v230, v231\n\t" \
  "ds_write_b16 v245, v230 offset:7456\n\t" \
  "ds_write_b16_d16_hi v241, v230 offset:7584\n\t" \
  "ds_read_b128 v[200:203], v246 offset:7456\n\t" \
  "ds_read_b128 v[208:211], v246 offset:7488\n\t" \
  "v_mov_b32 v230, v228\n\t" \
  "v_mov_b32 v231, v229\n\t" \
  "v_add_f32 v192, 1.0, v192\n\t" \
  "v_add_f32 v193, 1.0, v193\n\t" \
  "v_add_f32 v194, 1.0, v194\n\t" \
  "v_add_f32 v195, 1.0, v195\n\t" \
  "v_cvt_pk_bf16_f32 v186, v196, v197\n\t" \
  "v_cvt_pk_bf16_f32 v187, v198, v199\n\t" \
  "v_mfma_f32_32x32x16_bf16 v[116:131], v[40:43], v[168:171], v[116:131]\n\t" \
  "v_rcp_f32 v192, v192\n\t" \
  "v_rcp_f32 v193, v193\n\t" \
  "v_rcp_f32 v194, v194\n\t" \
  "v_rcp_f32 v195, v195\n\t" \
  "s_sub_u32 %[nt], %[nt], 1\n\t" \
  "s_cmp_lg_u32 %[nt], %[thr]\n\t" \
  "s_cbranch_scc1 3f\n\t" \
  "s_setprio 0\n\t" \
  "3:\n\t" \
  "v_mfma_f32_32x32x16_bf16 v[116:131], v[92:95], v[164:167], v[116:131]\n\t" \
  "v_exp_f32 v196, v112\n\t" \
  "v_exp_f32 v197, v113\n\t" \
  "v_exp_f32 v198, v114\n\t" \
  "v_exp_f32 v199, v115\n\t" \
  "v_mfma_f32_32x32x16_bf16 v[116:131], v[48:51], v[172:175], v[116:131]\n\t" \
  "v_mul_f32 v192, v108, v192\n\t" \
  "v_mul_f32 v193, v109, v193\n\t" \
  "v_mul_f32 v194, v110, v194\n\t" \
  "v_mul_f32 v195, v111, v195\n\t" \
  "s_waitcnt vmcnt(0)\n\t" \
  "ds_read_b128 v[148:151], v234\n\t" \
  "ds_read_b128 v[152:155], v235\n\t" \
  "ds_read_b128 v[156:159], v236\n\t" \
  "ds_read_b128 v[160:163], v237\n\t" \
  "ds_read_b128 v[164:167], v238 offset:4096\n\t" \
  "ds_read_b64 v[224:225], v239 offset:5120\n\t" \
  "ds_write_b128 v243, v[184:187] offset:0\n\t" \
  "v_add_f32 v196, 1.0, v196\n\t" \
  "v_add_f32 v197, 1.0, v197\n\t" \
  "v_add_f32 v198, 1.0, v198\n\t" \
  "v_add_f32 v199, 1.0, v199\n\t" \
  "v_cvt_pk_bf16_f32 v188, v192, v193\n\t" \
  "v_cvt_pk_bf16_f32 v189, v194, v195\n\t" \
  "v_mad_u32_u16 v220, v220, %[c128], v248\n\t" \
  "v_mad_u32_u16 v221, v221, %[c128], v249\n\t" \
  "v_mad_u32_u16 v222, v222, %[c128], v248\n\t" \
  "s_waitcnt lgkmcnt(11)\n\t" \
  "v_mfma_f32_32x32x16_bf16 v[68:83], v[204:207], v[180:183], v[68:83]\n\t" \
  "v_rcp_f32 v196, v196\n\t" \
  "v_rcp_f32 v197, v197\n\t" \
  "v_rcp_f32 v198, v198\n\t" \
  "v_rcp_f32 v199, v199\n\t" \
  "v_mad_u32_u16 v223, v223, %[c128], v249\n\t" \
  "v_or_b32 v226, v226, v250\n\t" \
  "v_mad_u32_u16 v227, v227, 6, v251\n\t" \
  "s_waitcnt lgkmcnt(8)\n\t" \
  "v_mfma_f32_16x16x32_bf16 v[84:87], v[200:203], v[176:179], v[84:87]\n\t" \
  "v_exp_f32 v192, v116\n\t" \
  "v_exp_f32 v193, v117\n\t" \
  "v_exp_f32 v194, v118\n\t" \
  "v_exp_f32 v195, v119\n\t" \
  "s_waitcnt lgkmcnt(7)\n\t" \
  "v_mfma_f32_16x16x32_bf16 v[84:87], v[208:211], v[180:183], v[84:87]\n\t" \
  "v_mul_f32 v196, v112, v196\n\t" \
  "v_mul_f32 v197, v113, v197\n\t" \
  "v_mul_f32 v198, v114, v198\n\t" \
  "v_mul_f32 v199, v115, v199\n\t" \
  "s_waitcnt lgkmcnt(1)\n\t" \
  "s_cmp_lt_i32 %[nt], 1\n\t" \
  "s_cbranch_scc1 4f\n\t" \
  "s_mov_b32 m0, %[b2]\n\t" \
  "s_nop 0\n\t" \
  "global_load_lds_dwordx4 v226, %[ea]\n\t" \
  "global_load_lds_dword v227, %[pos] offset:1024\n\t" \
  "s_mov_b32 m0, %[b1]\n\t" \
  "s_nop 0\n\t" \
  "global_load_lds_dwordx4 v220, %[h0]\n\t" \
  "global_load_lds_dwordx4 v221, %[h1] offset:1024\n\t" \
  "global_load_lds_dwordx4 v222, %[h2] offset:2048\n\t" \
  "global_load_lds_dwordx4 v223, %[h3] offset:3072\n\t" \
  "4:\n\t" \
  "v_add_f32 v192, 1.0, v192\n\t" \
  "v_add_f32 v193, 1.0, v193\n\t" \
  "v_add_f32 v194, 1.0, v194\n\t" \
  "v_add_f32 v195, 1.0, v195\n\t" \
  "v_cvt_pk_bf16_f32 v190, v196, v197\n\t" \
  "v_cvt_pk_bf16_f32 v191, v198, v199\n\t" \
  "v_lshrrev_b32 v232, 20, v232\n\t" \
  "v_add_u32 v220, %[posd], v232\n\t" \
  "v_add_u32 v221, v240, v232\n\t" \
  "ds_write_b128 v243, v[188:191] offset:16\n\t" \
  "ds_read_b128 v[176:179], v242 offset:5376\n\t" \
  "ds_read_b128 v[180:183], v242 offset:5408\n\t" \
  "ds_write_b16 v233, v255 offset:5376\n\t" \
  "ds_read_b128 v[216:219], v220\n\t" \
  "ds_read_b128 v[172:175], v221 offset:32\n\t" \
  "ds_read_b128 v[168:171], v221\n\t" \
  "ds_read_b64_tr_b16 v[200:201], v244 offset:0\n\t" \
  "ds_read_b64_tr_b16 v[202:203], v244 offset:320\n\t" \
  "ds_read_b64_tr_b16 v[204:205], v244 offset:1280\n\t" \
  "ds_read_b64_tr_b16 v[206:207], v244 offset:1600\n\t" \
  "v_add_u32 v233, v241, v232\n\t" \
  "ds_write_b16_d16_hi v233, v255 offset:5376\n\t" \
  "v_mfma_f32_16x16x32_bf16 v[132:135], v[96:99], v[184:187], 0\n\t" \
  "v_rcp_f32 v192, v192\n\t" \
  "v_rcp_f32 v193, v193\n\t" \
  "v_rcp_f32 v194, v194\n\t" \
  "v_rcp_f32 v195, v195\n\t" \
  "v_mfma_f32_32x32x16_bf16 v[100:115], v[4:7], v[148:151], 0\n\t" \
  "v_exp_f32 v196, v120\n\t" \
  "v_exp_f32 v197, v121\n\t" \
  "v_exp_f32 v198, v122\n\t" \
  "v_exp_f32 v199, v123\n\t" \
  "v_mfma_f32_32x32x16_bf16 v[100:115], v[12:15], v[152:155], v[100:115]\n\t" \
  "v_mul_f32 v192, v116, v192\n\t" \
  "v_mul_f32 v193, v117, v193\n\t" \
  "v_mul_f32 v194, v118, v194\n\t" \
  "v_mul_f32 v195, v119, v195\n\t" \
  "v_add_f32 v196, 1.0, v196\n\t" \
  "v_add_f32 v197, 1.0, v197\n\t" \
  "v_add_f32 v198, 1.0, v198\n\t" \
  "v_add_f32 v199, 1.0, v199\n\t" \
  "v_cvt_pk_bf16_f32 v184, v192, v193\n\t" \
  "v_cvt_pk_bf16_f32 v185, v194, v195\n\t" \
  "v_and_b32 v225, 0xffff0000, v225\n\t" \
  "s_waitcnt lgkmcnt(7)\n\t" \
  "v_lshlrev_b32 v219, 16, v224\n\t" \
  "v_and_b32 v224, 0xffff0000, v224\n\t" \
  "v_sub_f32 v229, v218, v225\n\t" \
  "v_sub_f32 v216, v216, v219\n\t" \
  "v_sub_f32 v217, v217, v224\n\t" \
  "v_mfma_f32_32x32x16_bf16 v[100:115], v[20:23], v[156:159], v[100:115]\n\t" \
  "v_rcp_f32 v196, v196\n\t" \
  "v_rcp_f32 v197, v197\n\t" \
  "v_rcp_f32 v198, v198\n\t" \
  "v_rcp_f32 v199, v199\n\t" \
  "v_mul_f32 v218, v229, v229\n\t" \
  "v_cndmask_b32 v228, v216, v217, vcc\n\t" \
  "v_fmac_f32 v218, v216, v216\n\t" \
  "v_fmac_f32 v218, v217, v217\n\t" \
  "v_cvt_pk_bf16_f32 v218, v218, v218\n\t" \
  "s_waitcnt lgkmcnt(6)\n\t" \
  "v_cndmask_b32_sdwa v175, v175, v218, vcc dst_sel:WORD_1 dst_unused:UNUSED_PRESERVE src0_sel:WORD_1 src1_sel:WORD_1\n\t" \
  "v_mfma_f32_16x16x32_bf16 v[132:135], v[136:139], v[188:191], v[132:135]\n\t" \
  "v_exp_f32 v192, v124\n\t" \
  "v_exp_f32 v193, v125\n\t" \
  "v_exp_f32 v194, v126\n\t" \
  "v_exp_f32 v195, v127\n\t" \
  "ds_read2_b32 v[220:221], v252 offset0:0 offset1:2\n\t" \
  "ds_read2_b32 v[222:223], v252 offset0:4 offset1:6\n\t" \
  "ds_read_b64 v[226:227], v253\n\t" \
  "ds_read_b32 v232, v254\n\t" \
  "v_mfma_f32_32x32x16_bf16 v[100:115], v[28:31], v[160:163], v[100:115]\n\t" \
  "v_mul_f32 v196, v120, v196\n\t" \
  "v_mul_f32 v197, v121, v197\n\t" \
  "v_mul_f32 v198, v122, v198\n\t" \
  "v_mul_f32 v199, v123, v199\n\t" \
  "v_add_u32 v252, 0x100, v252\n\t" \
  "v_add_u32 v253, 0x100, v253\n\t" \
  "v_add_u32 v254, 0x100, v254\n\t" \
  "v_add_f32 v192, 1.0, v192\n\t" \
  "v_add_f32 v193, 1.0, v193\n\t" \
  "v_add_f32 v194, 1.0, v194\n\t" \
  "v_add_f32 v195, 1.0, v195\n\t" \
  "v_cvt_pk_bf16_f32 v186, v196, v197\n\t" \
  "v_cvt_pk_bf16_f32 v187, v198, v199\n\t" \
  "s_waitcnt lgkmcnt(9)\n\t" \
  "v_mfma_f32_32x32x16_bf16 v[100:115], v[36:39], v[168:171], v[100:115]\n\t" \
  "v_rcp_f32 v192, v192\n\t" \
  "v_rcp_f32 v193, v193\n\t" \
  "v_rcp_f32 v194, v194\n\t" \
  "v_rcp_f32 v195, v195\n\t" \
  "v_mfma_f32_32x32x16_bf16 v[100:115], v[44:47], v[172:175], v[100:115]\n\t" \
  "v_exp_f32 v196, v128\n\t" \
  "v_exp_f32 v197, v129\n\t" \
  "v_exp_f32 v198, v130\n\t" \
  "v_exp_f32 v199, v131\n\t" \
  "v_mfma_f32_32x32x16_bf16 v[100:115], v[88:91], v[164:167], v[100:115]\n\t" \
  "v_mul_f32 v192, v124, v192\n\t" \
  "v_mul_f32 v193, v125, v193\n\t" \
  "v_mul_f32 v194, v126, v194\n\t" \
  "v_mul_f32 v195, v127, v195\n\t" \
  "ds_write_b128 v243, v[184:187] offset:0\n\t" \
  "v_add_f32 v196, 1.0, v196\n\t" \
  "v_add_f32 v197, 1.0, v197\n\t" \
  "v_add_f32 v198, 1.0, v198\n\t" \
  "v_add_f32 v199, 1.0, v199\n\t" \
  "v_cvt_pk_bf16_f32 v188, v192, v193\n\t" \
  "v_cvt_pk_bf16_f32 v189, v194, v195\n\t" \
  "s_waitcnt lgkmcnt(8)\n\t" \
  "v_mfma_f32_32x32x16_bf16 v[52:67], v[200:203], v[176:179], v[52:67]\n\t" \
  "v_rcp_f32 v196, v196\n\t" \
  "v_rcp_f32 v197, v197\n\t" \
  "v_rcp_f32 v198, v198\n\t" \
  "v_rcp_f32 v199, v199\n\t" \
  "s_waitcnt lgkmcnt(6)\n\t" \
  "v_mfma_f32_32x32x16_bf16 v[52:67], v[204:207], v[180:183], v[52:67]\n\t" \
  "v_exp_f32 v192, v100\n\t" \
  "v_exp_f32 v193, v101\n\t" \
  "v_exp_f32 v194, v102\n\t" \
  "v_exp_f32 v195, v103\n\t" \
  "s_cmp_eq_u32 %[nt], 0\n\t" \
  "v_mfma_f32_16x16x32_bf16 v[132:135], v[140:143], v[184:187], v[132:135]\n\t" \
  "v_mul_f32 v196, v128, v196\n\t" \
  "v_mul_f32 v197, v129, v197\n\t" \
  "v_mul_f32 v198, v130, v198\n\t" \
  "v_mul_f32 v199, v131, v199\n\t" \
  "v_add_f32 v192, 1.0, v192\n\t" \
  "v_add_f32 v193, 1.0, v193\n\t" \
  "v_add_f32 v194, 1.0, v194\n\t" \
  "v_add_f32 v195, 1.0, v195\n\t" \
  "v_cvt_pk_bf16_f32 v190, v196, v197\n\t" \
  "v_cvt_pk_bf16_f32 v191, v198, v199\n\t" \
  "s_cbranch_scc0 1b\n\t" \
  "s_waitcnt lgkmcnt(0)\n\t" \
  "s_cmp_lg_u32 %[last], 0\n\t" \
  "s_cbranch_scc1 8f\n\t" \
  "ds_write_b128 v243, v[188:191] offset:16\n\t" \
  "ds_read_b64_tr_b16 v[200:201], v244 offset:0\n\t" \
  "ds_read_b64_tr_b16 v[202:203], v244 offset:320\n\t" \
  "ds_read_b64_tr_b16 v[204:205], v244 offset:1280\n\t" \
  "ds_read_b64_tr_b16 v[206:207], v244 offset:1600\n\t" \
  "v_mfma_f32_16x16x32_bf16 v[132:135], v[144:147], v[188:191], v[132:135]\n\t" \
  "s_waitcnt lgkmcnt(0)\n\t" \
  "v_mfma_f32_32x32x16_bf16 v[68:83], v[200:203], v[176:179], v[68:83]\n\t" \
  "v_mfma_f32_32x32x16_bf16 v[68:83], v[204:207], v[180:183], v[68:83]\n\t" \
  "s_nop 8\n\t" \
  "v_mul_f32 v230, v230, v132\n\t" \
  "v_mul_f32 v231, v231, v132\n\t" \
  "v_cvt_pk_bf16_f32 v230, v230, v231\n\t" \
  "ds_write_b16 v245, v230 offset:7456\n\t" \
  "ds_write_b16_d16_hi v241, v230 offset:7584\n\t" \
  "ds_read_b128 v[200:203], v246 offset:7456\n\t" \
  "ds_read_b128 v[208:211], v246 offset:7488\n\t" \
  "s_waitcnt lgkmcnt(0)\n\t" \
  "v_mfma_f32_16x16x32_bf16 v[84:87], v[200:203], v[176:179], v[84:87]\n\t" \
  "v_mfma_f32_16x16x32_bf16 v[84:87], v[208:211], v[180:183], v[84:87]\n\t" \
  "s_branch 9f\n\t" \
  "6:\n\t" \
  "s_cmp_lg_u32 %[last], 0\n\t" \
  "s_cbranch_scc0 9f\n\t" \
  "s_waitcnt lgkmcnt(0)\n\t" \
  "v_cndmask_b32 v224, 0, 24, vcc\n\t" \
  "v_xor_b32 v220, v234, v224\n\t" \
  "v_xor_b32 v221, v235, v224\n\t" \
  "v_xor_b32 v222, v236, v224\n\t" \
  "v_xor_b32 v223, v237, v224\n\t" \
  "v_xor_b32 v216, 16, v220\n\t" \
  "v_xor_b32 v217, 16, v221\n\t" \
  "v_xor_b32 v218, 16, v222\n\t" \
  "v_xor_b32 v219, 16, v223\n\t" \
  "ds_read_b64 v[148:149], v220\n\t" \
  "ds_read_b64 v[150:151], v216\n\t" \
  "ds_read_b64 v[152:153], v221\n\t" \
  "ds_read_b64 v[154:155], v217\n\t" \
  "ds_read_b64 v[156:157], v222\n\t" \
  "ds_read_b64 v[158:159], v218\n\t" \
  "ds_read_b64 v[160:161], v223\n\t" \
  "ds_read_b64 v[162:163], v219\n\t" \
  "ds_read_b128 v[4:7], v247 offset:6144\n\t" \
  "ds_read_b128 v[8:11], v247 offset:7168\n\t" \
  "ds_read_b128 v[12:15], v247 offset:8192\n\t" \
  "ds_read_b128 v[16:19], v247 offset:9216\n\t" \
  "ds_read_b128 v[20:23], v247 offset:10240\n\t" \
  "ds_read_b128 v[24:27], v247 offset:11264\n\t" \
  "v_subrev_u32 v225, %[b1], v241\n\t" \
  "v_lshl_add_u32 v225, v225, 5, v243\n\t" \
  "v_sub_u32 v225, v225, v224\n\t" \
  "s_nop 4\n\t" \
  "v_cvt_pk_bf16_f32 v184, v52, v53\n\t" \
  "v_cvt_pk_bf16_f32 v185, v54, v55\n\t" \
  "v_cvt_pk_bf16_f32 v186, v56, v57\n\t" \
  "v_cvt_pk_bf16_f32 v187, v58, v59\n\t" \
  "v_cvt_pk_bf16_f32 v188, v60, v61\n\t" \
  "v_cvt_pk_bf16_f32 v189, v62, v63\n\t" \
  "v_cvt_pk_bf16_f32 v190, v64, v65\n\t" \
  "v_cvt_pk_bf16_f32 v191, v66, v67\n\t" \
  "v_cvt_pk_bf16_f32 v36, v68, v69\n\t" \
  "v_cvt_pk_bf16_f32 v37, v70, v71\n\t" \
  "v_cvt_pk_bf16_f32 v38, v72, v73\n\t" \
  "v_cvt_pk_bf16_f32 v39, v74, v75\n\t" \
  "v_cvt_pk_bf16_f32 v40, v76, v77\n\t" \
  "v_cvt_pk_bf16_f32 v41, v78, v79\n\t" \
  "v_cvt_pk_bf16_f32 v42, v80, v81\n\t" \
  "v_cvt_pk_bf16_f32 v43, v82, v83\n\t" \
  "s_waitcnt lgkmcnt(5)\n\t" \
  "v_mfma_f32_32x32x16_bf16 v[100:115], v[4:7], v[148:151], 0\n\t" \
  "s_waitcnt lgkmcnt(4)\n\t" \
  "v_mfma_f32_32x32x16_bf16 v[116:131], v[8:11], v[148:151], 0\n\t" \
  "ds_read_b128 v[28:31], v247 offset:12288\n\t" \
  "ds_read_b128 v[32:35], v247 offset:13312\n\t" \
  "s_waitcnt lgkmcnt(5)\n\t" \
  "v_mfma_f32_32x32x16_bf16 v[100:115], v[12:15], v[152:155], v[100:115]\n\t" \
  "s_waitcnt lgkmcnt(4)\n\t" \
  "v_mfma_f32_32x32x16_bf16 v[116:131], v[16:19], v[152:155], v[116:131]\n\t" \
  "ds_read_b128 v[4:7], v247 offset:14336\n\t" \
  "ds_read_b128 v[8:11], v247 offset:15360\n\t" \
  "s_waitcnt lgkmcnt(5)\n\t" \
  "v_mfma_f32_32x32x16_bf16 v[100:115], v[20:23], v[156:159], v[100:115]\n\t" \
  "s_waitcnt lgkmcnt(4)\n\t" \
  "v_mfma_f32_32x32x16_bf16 v[116:131], v[24:27], v[156:159], v[116:131]\n\t" \
  "ds_read_b128 v[12:15], v247 offset:16384\n\t" \
  "ds_read_b128 v[16:19], v247 offset:17408\n\t" \
  "s_waitcnt lgkmcnt(5)\n\t" \
  "v_mfma_f32_32x32x16_bf16 v[100:115], v[28:31], v[160:163], v[100:115]\n\t" \
  "s_waitcnt lgkmcnt(4)\n\t" \
  "v_mfma_f32_32x32x16_bf16 v[116:131], v[32:35], v[160:163], v[116:131]\n\t" \
  "ds_read_b128 v[20:23], v247 offset:18432\n\t" \
  "ds_read_b128 v[24:27], v247 offset:19456\n\t" \
  "s_waitcnt lgkmcnt(5)\n\t" \
  "v_mfma_f32_32x32x16_bf16 v[100:115], v[4:7], v[184:187], v[100:115]\n\t" \
  "ds_read_b128 v[28:31], v247 offset:20480\n\t" \
  "ds_read_b128 v[32:35], v247 offset:21504\n\t" \
  "s_waitcnt lgkmcnt(5)\n\t" \
  "v_mfma_f32_32x32x16_bf16 v[100:115], v[12:15], v[188:191], v[100:115]\n\t" \
  "s_waitcnt lgkmcnt(3)\n\t" \
  "v_mfma_f32_32x32x16_bf16 v[100:115], v[20:23], v[36:39], v[100:115]\n\t" \
  "s_waitcnt lgkmcnt(1)\n\t" \
  "v_mfma_f32_32x32x16_bf16 v[100:115], v[28:31], v[40:43], v[100:115]\n\t" \
  "s_waitcnt lgkmcnt(0)\n\t" \
  "s_branch 10f\n\t" \
  "8:\n\t" \
  "ds_write_b128 v243, v[188:191] offset:16\n\t" \
  "ds_read_b64_tr_b16 v[200:201], v244 offset:0\n\t" \
  "ds_read_b64_tr_b16 v[202:203], v244 offset:320\n\t" \
  "ds_read_b64_tr_b16 v[204:205], v244 offset:1280\n\t" \
  "ds_read_b64_tr_b16 v[206:207], v244 offset:1600\n\t" \
  "v_mfma_f32_16x16x32_bf16 v[132:135], v[144:147], v[188:191], v[132:135]\n\t" \
  "v_cndmask_b32 v224, 0, 24, vcc\n\t" \
  "v_xor_b32 v220, v234, v224\n\t" \
  "v_xor_b32 v221, v235, v224\n\t" \
  "v_xor_b32 v222, v236, v224\n\t" \
  "v_xor_b32 v223, v237, v224\n\t" \
  "v_xor_b32 v216, 16, v220\n\t" \
  "v_xor_b32 v217, 16, v221\n\t" \
  "v_xor_b32 v218, 16, v222\n\t" \
  "v_xor_b32 v219, 16, v223\n\t" \
  "ds_read_b64 v[148:149], v220\n\t" \
  "ds_read_b64 v[150:151], v216\n\t" \
  "ds_read_b64 v[152:153], v221\n\t" \
  "ds_read_b64 v[154:155], v217\n\t" \
  "ds_read_b64 v[156:157], v222\n\t" \
  "ds_read_b64 v[158:159], v218\n\t" \
  "ds_read_b64 v[160:161], v223\n\t" \
  "ds_read_b64 v[162:163], v219\n\t" \
  "ds_read_b128 v[4:7], v247 offset:6144\n\t" \
  "ds_read_b128 v[8:11], v247 offset:7168\n\t" \
  "s_waitcnt lgkmcnt(11)\n\t" \
  "ds_read_b128 v[12:15], v247 offset:8192\n\t" \
  "ds_read_b128 v[16:19], v247 offset:9216\n\t" \
  "ds_read_b128 v[20:23], v247 offset:10240\n\t" \
  "ds_read_b128 v[24:27], v247 offset:11264\n\t" \
  "v_subrev_u32 v225, %[b1], v241\n\t" \
  "v_lshl_add_u32 v225, v225, 5, v243\n\t" \
  "v_sub_u32 v225, v225, v224\n\t" \
  "v_mfma_f32_32x32x16_bf16 v[68:83], v[200:203], v[176:179], v[68:83]\n\t" \
  "s_waitcnt lgkmcnt(5)\n\t" \
  "v_mfma_f32_32x32x16_bf16 v[100:115], v[4:7], v[148:151], 0\n\t" \
  "v_mfma_f32_32x32x16_bf16 v[68:83], v[204:207], v[180:183], v[68:83]\n\t" \
  "s_waitcnt lgkmcnt(4)\n\t" \
  "v_mfma_f32_32x32x16_bf16 v[116:131], v[8:11], v[148:151], 0\n\t" \
  "ds_read_b128 v[28:31], v247 offset:12288\n\t" \
  "ds_read_b128 v[32:35], v247 offset:13312\n\t" \
  "s_waitcnt lgkmcnt(5)\n\t" \
  "v_mfma_f32_32x32x16_bf16 v[100:115], v[12:15], v[152:155], v[100:115]\n\t" \
  "v_mul_f32 v230, v230, v132\n\t" \
  "v_mul_f32 v231, v231, v132\n\t" \
  "v_cvt_pk_bf16_f32 v230, v230, v231\n\t" \
  "ds_write_b16 v245, v230 offset:7456\n\t" \
  "ds_write_b16_d16_hi v241, v230 offset:7584\n\t" \
  "ds_read_b128 v[200:203], v246 offset:7456\n\t" \
  "ds_read_b128 v[208:211], v246 offset:7488\n\t" \
  "v_cvt_pk_bf16_f32 v184, v52, v53\n\t" \
  "v_cvt_pk_bf16_f32 v185, v54, v55\n\t" \
  "v_cvt_pk_bf16_f32 v186, v56, v57\n\t" \
  "v_cvt_pk_bf16_f32 v187, v58, v59\n\t" \
  "s_waitcnt lgkmcnt(8)\n\t" \
  "v_mfma_f32_32x32x16_bf16 v[116:131], v[16:19], v[152:155], v[116:131]\n\t" \
  "ds_read_b128 v[4:7], v247 offset:14336\n\t" \
  "ds_read_b128 v[8:11], v247 offset:15360\n\t" \
  "s_waitcnt lgkmcnt(9)\n\t" \
  "v_mfma_f32_32x32x16_bf16 v[100:115], v[20:23], v[156:159], v[100:115]\n\t" \
  "v_cvt_pk_bf16_f32 v188, v60, v61\n\t" \
  "v_cvt_pk_bf16_f32 v189, v62, v63\n\t" \
  "v_cvt_pk_bf16_f32 v190, v64, v65\n\t" \
  "v_cvt_pk_bf16_f32 v191, v66, v67\n\t" \
  "s_waitcnt lgkmcnt(8)\n\t" \
  "v_mfma_f32_32x32x16_bf16 v[116:131], v[24:27], v[156:159], v[116:131]\n\t" \
  "ds_read_b128 v[12:15], v247 offset:16384\n\t" \
  "ds_read_b128 v[16:19], v247 offset:17408\n\t" \
  "s_waitcnt lgkmcnt(9)\n\t" \
  "v_mfma_f32_32x32x16_bf16 v[100:115], v[28:31], v[160:163], v[100:115]\n\t" \
  "v_cvt_pk_bf16_f32 v36, v68, v69\n\t" \
  "v_cvt_pk_bf16_f32 v37, v70, v71\n\t" \
  "v_cvt_pk_bf16_f32 v38, v72, v73\n\t" \
  "v_cvt_pk_bf16_f32 v39, v74, v75\n\t" \
  "s_waitcnt lgkmcnt(5)\n\t" \
  "v_mfma_f32_16x16x32_bf16 v[84:87], v[200:203], v[176:179], v[84:87]\n\t" \
  "v_cvt_pk_bf16_f32 v40, v76, v77\n\t" \
  "v_cvt_pk_bf16_f32 v41, v78, v79\n\t" \
  "v_cvt_pk_bf16_f32 v42, v80, v81\n\t" \
  "v_cvt_pk_bf16_f32 v43, v82, v83\n\t" \
  "v_mfma_f32_32x32x16_bf16 v[116:131], v[32:35], v[160:163], v[116:131]\n\t" \
  "ds_read_b128 v[20:23], v247 offset:18432\n\t" \
  "ds_read_b128 v[24:27], v247 offset:19456\n\t" \
  "s_waitcnt lgkmcnt(6)\n\t" \
  "v_mfma_f32_16x16x32_bf16 v[84:87], v[208:211], v[180:183], v[84:87]\n\t" \
  "s_waitcnt lgkmcnt(5)\n\t" \
  "v_mfma_f32_32x32x16_bf16 v[100:115], v[4:7], v[184:187], v[100:115]\n\t" \
  "ds_read_b128 v[28:31], v247 offset:20480\n\t" \
  "ds_read_b128 v[32:35], v247 offset:21504\n\t" \
  "s_waitcnt lgkmcnt(5)\n\t" \
  "v_mfma_f32_32x32x16_bf16 v[100:115], v[12:15], v[188:191], v[100:115]\n\t" \
  "s_waitcnt lgkmcnt(3)\n\t" \
  "v_mfma_f32_32x32x16_bf16 v[100:115], v[20:23], v[36:39], v[100:115]\n\t" \
  "s_waitcnt lgkmcnt(1)\n\t" \
  "v_mfma_f32_32x32x16_bf16 v[100:115], v[28:31], v[40:43], v[100:115]\n\t" \
  "s_waitcnt lgkmcnt(0)\n\t" \
  "10:\n\t" \
  "v_mov_b32 v212, 0xbfb8aa3b\n\t" \
  "v_mfma_f32_32x32x16_bf16 v[116:131], v[8:11], v[184:187], v[116:131]\n\t" \
  "v_mfma_f32_32x32x16_bf16 v[116:131], v[16:19], v[188:191], v[116:131]\n\t" \
  "s_nop 8\n\t" \
  "v_exp_f32 v192, v100\n\t" \
  "v_exp_f32 v193, v101\n\t" \
  "v_exp_f32 v194, v102\n\t" \
  "v_exp_f32 v195, v103\n\t" \
  "v_lshlrev_b32 v200, 16, v148\n\t" \
  "v_and_b32 v201, 0xffff0000, v148\n\t" \
  "v_lshlrev_b32 v202, 16, v149\n\t" \
  "v_and_b32 v203, 0xffff0000, v149\n\t" \
  "v_mfma_f32_32x32x16_bf16 v[116:131], v[24:27], v[36:39], v[116:131]\n\t" \
  "v_fmaak_f32 v192, v192, v212, 0xbfb8aa3b\n\t" \
  "v_fmaak_f32 v193, v193, v212, 0xbfb8aa3b\n\t" \
  "v_fmaak_f32 v194, v194, v212, 0xbfb8aa3b\n\t" \
  "v_fmaak_f32 v195, v195, v212, 0xbfb8aa3b\n\t" \
  "v_rcp_f32 v192, v192\n\t" \
  "v_rcp_f32 v193, v193\n\t" \
  "v_rcp_f32 v194, v194\n\t" \
  "v_rcp_f32 v195, v195\n\t" \
  "v_mfma_f32_32x32x16_bf16 v[116:131], v[32:35], v[40:43], v[116:131]\n\t" \
  "v_exp_f32 v196, v104\n\t" \
  "v_exp_f32 v197, v105\n\t" \
  "v_exp_f32 v198, v106\n\t" \
  "v_exp_f32 v199, v107\n\t" \
  "v_fmac_f32 v200, v100, v192\n\t" \
  "v_fmac_f32 v201, v101, v193\n\t" \
  "v_fmac_f32 v202, v102, v194\n\t" \
  "v_fmac_f32 v203, v103, v195\n\t" \
  "v_lshlrev_b32 v204, 16, v150\n\t" \
  "v_and_b32 v205, 0xffff0000, v150\n\t" \
  "v_lshlrev_b32 v206, 16, v151\n\t" \
  "v_and_b32 v207, 0xffff0000, v151\n\t" \
  "v_fmaak_f32 v196, v196, v212, 0xbfb8aa3b\n\t" \
  "v_fmaak_f32 v197, v197, v212, 0xbfb8aa3b\n\t" \
  "v_fmaak_f32 v198, v198, v212, 0xbfb8aa3b\n\t" \
  "v_fmaak_f32 v199, v199, v212, 0xbfb8aa3b\n\t" \
  "v_cvt_pk_bf16_f32 v176, v200, v201\n\t" \
  "v_cvt_pk_bf16_f32 v177, v202, v203\n\t" \
  "ds_write_b64 v225, v[176:177] offset:0\n\t" \
  "v_rcp_f32 v196, v196\n\t" \
  "v_rcp_f32 v197, v197\n\t" \
  "v_rcp_f32 v198, v198\n\t" \
  "v_rcp_f32 v199, v199\n\t" \
  "v_exp_f32 v192, v108\n\t" \
  "v_exp_f32 v193, v109\n\t" \
  "v_exp_f32 v194, v110\n\t" \
  "v_exp_f32 v195, v111\n\t" \
  "v_fmac_f32 v204, v104, v196\n\t" \
  "v_fmac_f32 v205, v105, v197\n\t" \
  "v_fmac_f32 v206, v106, v198\n\t" \
  "v_fmac_f32 v207, v107, v199\n\t" \
  "v_lshlrev_b32 v200, 16, v152\n\t" \
  "v_and_b32 v201, 0xffff0000, v152\n\t" \
  "v_lshlrev_b32 v202, 16, v153\n\t" \
  "v_and_b32 v203, 0xffff0000, v153\n\t" \
  "v_fmaak_f32 v192, v192, v212, 0xbfb8aa3b\n\t" \
  "v_fmaak_f32 v193, v193, v212, 0xbfb8aa3b\n\t" \
  "v_fmaak_f32 v194, v194, v212, 0xbfb8aa3b\n\t" \
  "v_fmaak_f32 v195, v195, v212, 0xbfb8aa3b\n\t" \
  "v_cvt_pk_bf16_f32 v178, v204, v205\n\t" \
  "v_cvt_pk_bf16_f32 v179, v206, v207\n\t" \
  "ds_write_b64 v225, v[178:179] offset:16\n\t" \
  "v_rcp_f32 v192, v192\n\t" \
  "v_rcp_f32 v193, v193\n\t" \
  "v_rcp_f32 v194, v194\n\t" \
  "v_rcp_f32 v195, v195\n\t" \
  "v_exp_f32 v196, v112\n\t" \
  "v_exp_f32 v197, v113\n\t" \
  "v_exp_f32 v198, v114\n\t" \
  "v_exp_f32 v199, v115\n\t" \
  "v_fmac_f32 v200, v108, v192\n\t" \
  "v_fmac_f32 v201, v109, v193\n\t" \
  "v_fmac_f32 v202, v110, v194\n\t" \
  "v_fmac_f32 v203, v111, v195\n\t" \
  "v_lshlrev_b32 v204, 16, v154\n\t" \
  "v_and_b32 v205, 0xffff0000, v154\n\t" \
  "v_lshlrev_b32 v206, 16, v155\n\t" \
  "v_and_b32 v207, 0xffff0000, v155\n\t" \
  "v_fmaak_f32 v196, v196, v212, 0xbfb8aa3b\n\t" \
  "v_fmaak_f32 v197, v197, v212, 0xbfb8aa3b\n\t" \
  "v_fmaak_f32 v198, v198, v212, 0xbfb8aa3b\n\t" \
  "v_fmaak_f32 v199, v199, v212, 0xbfb8aa3b\n\t" \
  "v_cvt_pk_bf16_f32 v180, v200, v201\n\t" \
  "v_cvt_pk_bf16_f32 v181, v202, v203\n\t" \
  "ds_write_b64 v225, v[180:181] offset:32\n\t" \
  "v_rcp_f32 v196, v196\n\t" \
  "v_rcp_f32 v197, v197\n\t" \
  "v_rcp_f32 v198, v198\n\t" \
  "v_rcp_f32 v199, v199\n\t" \
  "v_exp_f32 v192, v116\n\t" \
  "v_exp_f32 v193, v117\n\t" \
  "v_exp_f32 v194, v118\n\t" \
  "v_exp_f32 v195, v119\n\t" \
  "v_fmac_f32 v204, v112, v196\n\t" \
  "v_fmac_f32 v205, v113, v197\n\t" \
  "v_fmac_f32 v206, v114, v198\n\t" \
  "v_fmac_f32 v207, v115, v199\n\t" \
  "v_lshlrev_b32 v200, 16, v156\n\t" \
  "v_and_b32 v201, 0xffff0000, v156\n\t" \
  "v_lshlrev_b32 v202, 16, v157\n\t" \
  "v_and_b32 v203, 0xffff0000, v157\n\t" \
  "v_fmaak_f32 v192, v192, v212, 0xbfb8aa3b\n\t" \
  "v_fmaak_f32 v193, v193, v212, 0xbfb8aa3b\n\t" \
  "v_fmaak_f32 v194, v194, v212, 0xbfb8aa3b\n\t" \
  "v_fmaak_f32 v195, v195, v212, 0xbfb8aa3b\n\t" \
  "v_cvt_pk_bf16_f32 v182, v204, v205\n\t" \
  "v_cvt_pk_bf16_f32 v183, v206, v207\n\t" \
  "ds_write_b64 v225, v[182:183] offset:48\n\t" \
  "v_rcp_f32 v192, v192\n\t" \
  "v_rcp_f32 v193, v193\n\t" \
  "v_rcp_f32 v194, v194\n\t" \
  "v_rcp_f32 v195, v195\n\t" \
  "v_exp_f32 v196, v120\n\t" \
  "v_exp_f32 v197, v121\n\t" \
  "v_exp_f32 v198, v122\n\t" \
  "v_exp_f32 v199, v123\n\t" \
  "v_fmac_f32 v200, v116, v192\n\t" \
  "v_fmac_f32 v201, v117, v193\n\t" \
  "v_fmac_f32 v202, v118, v194\n\t" \
  "v_fmac_f32 v203, v119, v195\n\t" \
  "v_lshlrev_b32 v204, 16, v158\n\t" \
  "v_and_b32 v205, 0xffff0000, v158\n\t" \
  "v_lshlrev_b32 v206, 16, v159\n\t" \
  "v_and_b32 v207, 0xffff0000, v159\n\t" \
  "v_fmaak_f32 v196, v196, v212, 0xbfb8aa3b\n\t" \
  "v_fmaak_f32 v197, v197, v212, 0xbfb8aa3b\n\t" \
  "v_fmaak_f32 v198, v198, v212, 0xbfb8aa3b\n\t" \
  "v_fmaak_f32 v199, v199, v212, 0xbfb8aa3b\n\t" \
  "v_cvt_pk_bf16_f32 v176, v200, v201\n\t" \
  "v_cvt_pk_bf16_f32 v177, v202, v203\n\t" \
  "ds_write_b64 v225, v[176:177] offset:64\n\t" \
  "v_rcp_f32 v196, v196\n\t" \
  "v_rcp_f32 v197, v197\n\t" \
  "v_rcp_f32 v198, v198\n\t" \
  "v_rcp_f32 v199, v199\n\t" \
  "v_exp_f32 v192, v124\n\t" \
  "v_exp_f32 v193, v125\n\t" \
  "v_exp_f32 v194, v126\n\t" \
  "v_exp_f32 v195, v127\n\t" \
  "v_fmac_f32 v204, v120, v196\n\t" \
  "v_fmac_f32 v205, v121, v197\n\t" \
  "v_fmac_f32 v206, v122, v198\n\t" \
  "v_fmac_f32 v207, v123, v199\n\t" \
  "v_lshlrev_b32 v200, 16, v160\n\t" \
  "v_and_b32 v201, 0xffff0000, v160\n\t" \
  "v_lshlrev_b32 v202, 16, v161\n\t" \
  "v_and_b32 v203, 0xffff0000, v161\n\t" \
  "v_fmaak_f32 v192, v192, v212, 0xbfb8aa3b\n\t" \
  "v_fmaak_f32 v193, v193, v212, 0xbfb8aa3b\n\t" \
  "v_fmaak_f32 v194, v194, v212, 0xbfb8aa3b\n\t" \
  "v_fmaak_f32 v195, v195, v212, 0xbfb8aa3b\n\t" \
  "v_cvt_pk_bf16_f32 v178, v204, v205\n\t" \
  "v_cvt_pk_bf16_f32 v179, v206, v207\n\t" \
  "ds_write_b64 v225, v[178:179] offset:80\n\t" \
  "v_rcp_f32 v192, v192\n\t" \
  "v_rcp_f32 v193, v193\n\t" \
  "v_rcp_f32 v194, v194\n\t" \
  "v_rcp_f32 v195, v195\n\t" \
  "v_exp_f32 v196, v128\n\t" \
  "v_exp_f32 v197, v129\n\t" \
  "v_exp_f32 v198, v130\n\t" \
  "v_exp_f32 v199, v131\n\t" \
  "v_fmac_f32 v200, v124, v192\n\t" \
  "v_fmac_f32 v201, v125, v193\n\t" \
  "v_fmac_f32 v202, v126, v194\n\t" \
  "v_fmac_f32 v203, v127, v195\n\t" \
  "v_lshlrev_b32 v204, 16, v162\n\t" \
  "v_and_b32 v205, 0xffff0000, v162\n\t" \
  "v_lshlrev_b32 v206, 16, v163\n\t" \
  "v_and_b32 v207, 0xffff0000, v163\n\t" \
  "v_fmaak_f32 v196, v196, v212, 0xbfb8aa3b\n\t" \
  "v_fmaak_f32 v197, v197, v212, 0xbfb8aa3b\n\t" \
  "v_fmaak_f32 v198, v198, v212, 0xbfb8aa3b\n\t" \
  "v_fmaak_f32 v199, v199, v212, 0xbfb8aa3b\n\t" \
  "v_cvt_pk_bf16_f32 v180, v200, v201\n\t" \
  "v_cvt_pk_bf16_f32 v181, v202, v203\n\t" \
  "ds_write_b64 v225, v[180:181] offset:96\n\t" \
  "v_rcp_f32 v196, v196\n\t" \
  "v_rcp_f32 v197, v197\n\t" \
  "v_rcp_f32 v198, v198\n\t" \
  "v_rcp_f32 v199, v199\n\t" \
  "s_nop 0\n\t" \
  "v_fmac_f32 v204, v128, v196\n\t" \
  "v_fmac_f32 v205, v129, v197\n\t" \
  "v_fmac_f32 v206, v130, v198\n\t" \
  "v_fmac_f32 v207, v131, v199\n\t" \
  "v_cvt_pk_bf16_f32 v182, v204, v205\n\t" \
  "v_cvt_pk_bf16_f32 v183, v206, v207\n\t" \
  "ds_write_b64 v225, v[182:183] offset:112\n\t" \
  "9:\n\t" \
  "ds_write_b16 v233, v255 offset:5376\n\t" \
  "s_mov_b32 m0, %[keep]\n\t" \
  "s_setprio 0\n\t" \
  "s_nop 11\n\t" \
  ""
#define LOOP_ASM_PLAIN \
  "s_mov_b32 %[keep], m0\n\t" \
  "s_mov_b64 vcc, %[hsel]\n\t" \
  "ds_read2_b32 v[220:221], v252 offset0:0 offset1:2\n\t" \
  "ds_read2_b32 v[222:223], v252 offset0:4 offset1:6\n\t" \
  "ds_read_b64 v[226:227], v253\n\t" \
  "ds_read_b32 v232, v254\n\t" \
  "v_mov_b32 v176, 0\n\t" \
  "v_mov_b32 v177, 0\n\t" \
  "v_mov_b32 v178, 0\n\t" \
  "v_mov_b32 v179, 0\n\t" \
  "v_mov_b32 v180, 0\n\t" \
  "v_mov_b32 v181, 0\n\t" \
  "v_mov_b32 v182, 0\n\t" \
  "v_mov_b32 v183, 0\n\t" \
  "v_mov_b32 v184, 0\n\t" \
  "v_mov_b32 v185, 0\n\t" \
  "v_mov_b32 v186, 0\n\t" \
  "v_mov_b32 v187, 0\n\t" \
  "v_mov_b32 v188, 0\n\t" \
  "v_mov_b32 v189, 0\n\t" \
  "v_mov_b32 v190, 0\n\t" \
  "v_mov_b32 v191, 0\n\t" \
  "v_mov_b32 v230, 0\n\t" \
  "v_mov_b32 v231, 0\n\t" \
  "v_mov_b32 v208, 0\n\t" \
  "v_mov_b32 v209, 0\n\t" \
  "v_mov_b32 v210, 0\n\t" \
  "v_mov_b32 v211, 0\n\t" \
  "ds_write_b128 v243, v[184:187] offset:0\n\t" \
  "s_nop 0\n\t" \
  "v_mfma_f32_16x16x32_bf16 v[132:135], v[184:187], v[188:191], 0\n\t" \
  "v_add_u32 v252, 0x100, v252\n\t" \
  "v_add_u32 v253, 0x100, v253\n\t" \
  "v_add_u32 v254, 0x100, v254\n\t" \
  "s_waitcnt lgkmcnt(0)\n\t" \
  "v_lshrrev_b32 v232, 20, v232\n\t" \
  "v_add_u32 v233, %[posd], v232\n\t" \
  "ds_read_b128 v[216:219], v233\n\t" \
  "v_add_u32 v233, v240, v232\n\t" \
  "ds_read_b128 v[172:175], v233 offset:32\n\t" \
  "ds_read_b128 v[168:171], v233\n\t" \
  "v_add_u32 v233, v241, v232\n\t" \
  "ds_write_b16_d16_hi v233, v255 offset:5376\n\t" \
  "v_mad_u32_u16 v220, v220, %[c128], v248\n\t" \
  "v_mad_u32_u16 v221, v221, %[c128], v249\n\t" \
  "v_mad_u32_u16 v222, v222, %[c128], v248\n\t" \
  "v_mad_u32_u16 v223, v223, %[c128], v249\n\t" \
  "v_or_b32 v226, v226, v250\n\t" \
  "v_mad_u32_u16 v227, v227, 6, v251\n\t" \
  "s_waitcnt vmcnt(0)\n\t" \
  "ds_read_b128 v[148:151], v234\n\t" \
  "ds_read_b128 v[152:155], v235\n\t" \
  "ds_read_b128 v[156:159], v236\n\t" \
  "ds_read_b128 v[160:163], v237\n\t" \
  "ds_read_b128 v[164:167], v238 offset:4096\n\t" \
  "ds_read_b64 v[224:225], v239 offset:5120\n\t" \
  "s_waitcnt lgkmcnt(0)\n\t" \
  "s_cmp_lt_i32 %[nt], 1\n\t" \
  "s_cbranch_scc1 7f\n\t" \
  "s_mov_b32 m0, %[b2]\n\t" \
  "s_nop 0\n\t" \
  "global_load_lds_dwordx4 v226, %[ea]\n\t" \
  "global_load_lds_dword v227, %[pos] offset:1024\n\t" \
  "s_mov_b32 m0, %[b1]\n\t" \
  "s_nop 0\n\t" \
  "global_load_lds_dwordx4 v220, %[h0]\n\t" \
  "global_load_lds_dwordx4 v221, %[h1] offset:1024\n\t" \
  "global_load_lds_dwordx4 v222, %[h2] offset:2048\n\t" \
  "global_load_lds_dwordx4 v223, %[h3] offset:3072\n\t" \
  "7:\n\t" \
  "ds_read2_b32 v[220:221], v252 offset0:0 offset1:2\n\t" \
  "ds_read2_b32 v[222:223], v252 offset0:4 offset1:6\n\t" \
  "ds_read_b64 v[226:227], v253\n\t" \
  "ds_read_b32 v232, v254\n\t" \
  "v_add_u32 v252, 0x100, v252\n\t" \
  "v_add_u32 v253, 0x100, v253\n\t" \
  "v_add_u32 v254, 0x100, v254\n\t" \
  "v_and_b32 v225, 0xffff0000, v225\n\t" \
  "v_lshlrev_b32 v219, 16, v224\n\t" \
  "v_and_b32 v224, 0xffff0000, v224\n\t" \
  "v_sub_f32 v229, v218, v225\n\t" \
  "v_sub_f32 v216, v216, v219\n\t" \
  "v_sub_f32 v217, v217, v224\n\t" \
  "v_mul_f32 v218, v229, v229\n\t" \
  "v_cndmask_b32 v228, v216, v217, vcc\n\t" \
  "v_fmac_f32 v218, v216, v216\n\t" \
  "v_fmac_f32 v218, v217, v217\n\t" \
  "v_cvt_pk_bf16_f32 v218, v218, v218\n\t" \
  "v_cndmask_b32_sdwa v175, v175, v218, vcc dst_sel:WORD_1 dst_unused:UNUSED_PRESERVE src0_sel:WORD_1 src1_sel:WORD_1\n\t" \
  "v_mfma_f32_32x32x16_bf16 v[100:115], v[4:7], v[148:151], 0\n\t" \
  "v_mfma_f32_32x32x16_bf16 v[100:115], v[12:15], v[152:155], v[100:115]\n\t" \
  "v_mfma_f32_32x32x16_bf16 v[100:115], v[20:23], v[156:159], v[100:115]\n\t" \
  "v_mfma_f32_32x32x16_bf16 v[100:115], v[28:31], v[160:163], v[100:115]\n\t" \
  "v_mfma_f32_32x32x16_bf16 v[100:115], v[36:39], v[168:171], v[100:115]\n\t" \
  "v_mfma_f32_32x32x16_bf16 v[100:115], v[88:91], v[164:167], v[100:115]\n\t" \
  "v_mfma_f32_32x32x16_bf16 v[100:115], v[44:47], v[172:175], v[100:115]\n\t" \
  "s_cmp_eq_u32 %[nt], 0\n\t" \
  "s_nop 10\n\t" \
  "v_mul_f32 v192, 0xbfb8aa3b, v100\n\t" \
  "v_mul_f32 v193, 0xbfb8aa3b, v101\n\t" \
  "v_mul_f32 v194, 0xbfb8aa3b, v102\n\t" \
  "v_mul_f32 v195, 0xbfb8aa3b, v103\n\t" \
  "v_exp_f32 v192, v192\n\t" \
  "v_exp_f32 v193, v193\n\t" \
  "v_exp_f32 v194, v194\n\t" \
  "v_exp_f32 v195, v195\n\t" \
  "s_nop 0\n\t" \
  "v_add_f32 v192, 1.0, v192\n\t" \
  "v_add_f32 v193, 1.0, v193\n\t" \
  "v_add_f32 v194, 1.0, v194\n\t" \
  "v_add_f32 v195, 1.0, v195\n\t" \
  "s_cbranch_scc1 6f\n\t" \
  "s_cmp_lt_i32 %[thr], 0\n\t" \
  "s_cbranch_scc1 1f\n\t" \
  "s_setprio 1\n\t" \
  "1:\n\t" \
  "ds_write_b128 v243, v[188:191] offset:16\n\t" \
  "ds_read_b64_tr_b16 v[200:201], v244 offset:0\n\t" \
  "ds_read_b64_tr_b16 v[202:203], v244 offset:320\n\t" \
  "ds_read_b64_tr_b16 v[204:205], v244 offset:1280\n\t" \
  "ds_read_b64_tr_b16 v[206:207], v244 offset:1600\n\t" \
  "v_mfma_f32_16x16x32_bf16 v[132:135], v[144:147], v[188:191], v[132:135]\n\t" \
  "v_rcp_f32 v192, v192\n\t" \
  "v_rcp_f32 v193, v193\n\t" \
  "v_rcp_f32 v194, v194\n\t" \
  "v_rcp_f32 v195, v195\n\t" \
  "v_mfma_f32_32x32x16_bf16 v[116:131], v[8:11], v[148:151], 0\n\t" \
  "v_mul_f32 v196, 0xbfb8aa3b, v104\n\t" \
  "v_mul_f32 v197, 0xbfb8aa3b, v105\n\t" \
  "v_mul_f32 v198, 0xbfb8aa3b, v106\n\t" \
  "v_mul_f32 v199, 0xbfb8aa3b, v107\n\t" \
  "v_exp_f32 v196, v196\n\t" \
  "v_exp_f32 v197, v197\n\t" \
  "v_exp_f32 v198, v198\n\t" \
  "v_exp_f32 v199, v199\n\t" \
  "v_mfma_f32_32x32x16_bf16 v[116:131], v[16:19], v[152:155], v[116:131]\n\t" \
  "v_mul_f32 v192, v100, v192\n\t" \
  "v_mul_f32 v193, v101, v193\n\t" \
  "v_mul_f32 v194, v102, v194\n\t" \
  "v_mul_f32 v195, v103, v195\n\t" \
  "v_add_f32 v196, 1.0, v196\n\t" \
  "v_add_f32 v197, 1.0, v197\n\t" \
  "v_add_f32 v198, 1.0, v198\n\t" \
  "v_add_f32 v199, 1.0, v199\n\t" \
  "v_cvt_pk_bf16_f32 v184, v192, v193\n\t" \
  "v_cvt_pk_bf16_f32 v185, v194, v195\n\t" \
  "v_mfma_f32_32x32x16_bf16 v[116:131], v[24:27], v[156:159], v[116:131]\n\t" \
  "v_rcp_f32 v196, v196\n\t" \
  "v_rcp_f32 v197, v197\n\t" \
  "v_rcp_f32 v198, v198\n\t" \
  "v_rcp_f32 v199, v199\n\t" \
  "s_waitcnt lgkmcnt(2)\n\t" \
  "v_mfma_f32_32x32x16_bf16 v[68:83], v[200:203], v[176:179], v[68:83]\n\t" \
  "v_mul_f32 v192, 0xbfb8aa3b, v108\n\t" \
  "v_mul_f32 v193, 0xbfb8aa3b, v109\n\t" \
  "v_mul_f32 v194, 0xbfb8aa3b, v110\n\t" \
  "v_mul_f32 v195, 0xbfb8aa3b, v111\n\t" \
  "v_exp_f32 v192, v192\n\t" \
  "v_exp_f32 v193, v193\n\t" \
  "v_exp_f32 v194, v194\n\t" \
  "v_exp_f32 v195, v195\n\t" \
  "v_mfma_f32_32x32x16_bf16 v[116:131], v[32:35], v[160:163], v[116:131]\n\t" \
  "v_mul_f32 v196, v104, v196\n\t" \
  "v_mul_f32 v197, v105, v197\n\t" \
  "v_mul_f32 v198, v106, v198\n\t" \
  "v_mul_f32 v199, v107, v199\n\t" \
  "v_mul_f32 v230, v230, v132\n\t" \
  "v_mul_f32 v231, v231, v132\n\t" \
  "v_cvt_pk_bf16_f32 v230, v230, v231\n\t" \
  "ds_write_b16 v245, v230 offset:7456\n\t" \
  "ds_write_b16_d16_hi v241, v230 offset:7584\n\t" \
  "ds_read_b128 v[200:203], v246 offset:7456\n\t" \
  "ds_read_b128 v[208:211], v246 offset:7488\n\t" \
  "v_mov_b32 v230, v228\n\t" \
  "v_mov_b32 v231, v229\n\t" \
  "v_add_f32 v192, 1.0, v192\n\t" \
  "v_add_f32 v193, 1.0, v193\n\t" \
  "v_add_f32 v194, 1.0, v194\n\t" \
  "v_add_f32 v195, 1.0, v195\n\t" \
  "v_cvt_pk_bf16_f32 v186, v196, v197\n\t" \
  "v_cvt_pk_bf16_f32 v187, v198, v199\n\t" \
  "v_mfma_f32_32x32x16_bf16 v[116:131], v[40:43], v[168:171], v[116:131]\n\t" \
  "v_rcp_f32 v192, v192\n\t" \
  "v_rcp_f32 v193, v193\n\t" \
  "v_rcp_f32 v194, v194\n\t" \
  "v_rcp_f32 v195, v195\n\t" \
  "s_sub_u32 %[nt], %[nt], 1\n\t" \
  "s_cmp_lg_u32 %[nt], %[thr]\n\t" \
  "s_cbranch_scc1 3f\n\t" \
  "s_setprio 0\n\t" \
  "3:\n\t" \
  "v_mfma_f32_32x32x16_bf16 v[116:131], v[92:95], v[164:167], v[116:131]\n\t" \
  "v_mul_f32 v196, 0xbfb8aa3b, v112\n\t" \
  "v_mul_f32 v197, 0xbfb8aa3b, v113\n\t" \
  "v_mul_f32 v198, 0xbfb8aa3b, v114\n\t" \
  "v_mul_f32 v199, 0xbfb8aa3b, v115\n\t" \
  "v_exp_f32 v196, v196\n\t" \
  "v_exp_f32 v197, v197\n\t" \
  "v_exp_f32 v198, v198\n\t" \
  "v_exp_f32 v199, v199\n\t" \
  "v_mfma_f32_32x32x16_bf16 v[116:131], v[48:51], v[172:175], v[116:131]\n\t" \
  "v_mul_f32 v192, v108, v192\n\t" \
  "v_mul_f32 v193, v109, v193\n\t" \
  "v_mul_f32 v194, v110, v194\n\t" \
  "v_mul_f32 v195, v111, v195\n\t" \
  "s_waitcnt vmcnt(0)\n\t" \
  "ds_read_b128 v[148:151], v234\n\t" \
  "ds_read_b128 v[152:155], v235\n\t" \
  "ds_read_b128 v[156:159], v236\n\t" \
  "ds_read_b128 v[160:163], v237\n\t" \
  "ds_read_b128 v[164:167], v238 offset:4096\n\t" \
  "ds_read_b64 v[224:225], v239 offset:5120\n\t" \
  "ds_write_b128 v243, v[184:187] offset:0\n\t" \
  "v_add_f32 v196, 1.0, v196\n\t" \
  "v_add_f32 v197, 1.0, v197\n\t" \
  "v_add_f32 v198, 1.0, v198\n\t" \
  "v_add_f32 v199, 1.0, v199\n\t" \
  "v_cvt_pk_bf16_f32 v188, v192, v193\n\t" \
  "v_cvt_pk_bf16_f32 v189, v194, v195\n\t" \
  "v_mad_u32_u16 v220, v220, %[c128], v248\n\t" \
  "v_mad_u32_u16 v221, v221, %[c128], v249\n\t" \
  "v_mad_u32_u16 v222, v222, %[c128], v248\n\t" \
  "s_waitcnt lgkmcnt(11)\n\t" \
  "v_mfma_f32_32x32x16_bf16 v[68:83], v[204:207], v[180:183], v[68:83]\n\t" \
  "v_rcp_f32 v196, v196\n\t" \
  "v_rcp_f32 v197, v197\n\t" \
  "v_rcp_f32 v198, v198\n\t" \
  "v_rcp_f32 v199, v199\n\t" \
  "v_mad_u32_u16 v223, v223, %[c128], v249\n\t" \
  "v_or_b32 v226, v226, v250\n\t" \
  "v_mad_u32_u16 v227, v227, 6, v251\n\t" \
  "s_waitcnt lgkmcnt(8)\n\t" \
  "v_mfma_f32_16x16x32_bf16 v[84:87], v[200:203], v[176:179], v[84:87]\n\t" \
  "v_mul_f32 v192, 0xbfb8aa3b, v116\n\t" \
  "v_mul_f32 v193, 0xbfb8aa3b, v117\n\t" \
  "v_mul_f32 v194, 0xbfb8aa3b, v118\n\t" \
  "v_mul_f32 v195, 0xbfb8aa3b, v119\n\t" \
  "v_exp_f32 v192, v192\n\t" \
  "v_exp_f32 v193, v193\n\t" \
  "v_exp_f32 v194, v194\n\t" \
  "v_exp_f32 v195, v195\n\t" \
  "s_waitcnt lgkmcnt(7)\n\t" \
  "v_mfma_f32_16x16x32_bf16 v[84:87], v[208:211], v[180:183], v[84:87]\n\t" \
  "v_mul_f32 v196, v112, v196\n\t" \
  "v_mul_f32 v197, v113, v197\n\t" \
  "v_mul_f32 v198, v114, v198\n\t" \
  "v_mul_f32 v199, v115, v199\n\t" \
  "s_waitcnt lgkmcnt(1)\n\t" \
  "s_cmp_lt_i32 %[nt], 1\n\t" \
  "s_cbranch_scc1 4f\n\t" \
  "s_mov_b32 m0, %[b2]\n\t" \
  "s_nop 0\n\t" \
  "global_load_lds_dwordx4 v226, %[ea]\n\t" \
  "global_load_lds_dword v227, %[pos] offset:1024\n\t" \
  "s_mov_b32 m0, %[b1]\n\t" \
  "s_nop 0\n\t" \
  "global_load_lds_dwordx4 v220, %[h0]\n\t" \
  "global_load_lds_dwordx4 v221, %[h1] offset:1024\n\t" \
  "global_load_lds_dwordx4 v222, %[h2] offset:2048\n\t" \
  "global_load_lds_dwordx4 v223, %[h3] offset:3072\n\t" \
  "4:\n\t" \
  "v_add_f32 v192, 1.0, v192\n\t" \
  "v_add_f32 v193, 1.0, v193\n\t" \
  "v_add_f32 v194, 1.0, v194\n\t" \
  "v_add_f32 v195, 1.0, v195\n\t" \
  "v_cvt_pk_bf16_f32 v190, v196, v197\n\t" \
  "v_cvt_pk_bf16_f32 v191, v198, v199\n\t" \
  "v_lshrrev_b32 v232, 20, v232\n\t" \
  "v_add_u32 v220, %[posd], v232\n\t" \
  "v_add_u32 v221, v240, v232\n\t" \
  "ds_write_b128 v243, v[188:191] offset:16\n\t" \
  "ds_read_b128 v[176:179], v242 offset:5376\n\t" \
  "ds_read_b128 v[180:183], v242 offset:5408\n\t" \
  "ds_write_b16 v233, v255 offset:5376\n\t" \
  "ds_read_b128 v[216:219], v220\n\t" \
  "ds_read_b128 v[172:175], v221 offset:32\n\t" \
  "ds_read_b128 v[168:171], v221\n\t" \
  "ds_read_b64_tr_b16 v[200:201], v244 offset:0\n\t" \
  "ds_read_b64_tr_b16 v[202:203], v244 offset:320\n\t" \
  "ds_read_b64_tr_b16 v[204:205], v244 offset:1280\n\t" \
  "ds_read_b64_tr_b16 v[206:207], v244 offset:1600\n\t" \
  "v_add_u32 v233, v241, v232\n\t" \
  "ds_write_b16_d16_hi v233, v255 offset:5376\n\t" \
  "v_mfma_f32_16x16x32_bf16 v[132:135], v[96:99], v[184:187], 0\n\t" \
  "v_rcp_f32 v192, v192\n\t" \
  "v_rcp_f32 v193, v193\n\t" \
  "v_rcp_f32 v194, v194\n\t" \
  "v_rcp_f32 v195, v195\n\t" \
  "v_mfma_f32_32x32x16_bf16 v[100:115], v[4:7], v[148:151], 0\n\t" \
  "v_mul_f32 v196, 0xbfb8aa3b, v120\n\t" \
  "v_mul_f32 v197, 0xbfb8aa3b, v121\n\t" \
  "v_mul_f32 v198, 0xbfb8aa3b, v122\n\t" \
  "v_mul_f32 v199, 0xbfb8aa3b, v123\n\t" \
  "v_exp_f32 v196, v196\n\t" \
  "v_exp_f32 v197, v197\n\t" \
  "v_exp_f32 v198, v198\n\t" \
  "v_exp_f32 v199, v199\n\t" \
  "v_mfma_f32_32x32x16_bf16 v[100:115], v[12:15], v[152:155], v[100:115]\n\t" \
  "v_mul_f32 v192, v116, v192\n\t" \
  "v_mul_f32 v193, v117, v193\n\t" \
  "v_mul_f32 v194, v118, v194\n\t" \
  "v_mul_f32 v195, v119, v195\n\t" \
  "v_add_f32 v196, 1.0, v196\n\t" \
  "v_add_f32 v197, 1.0, v197\n\t" \
  "v_add_f32 v198, 1.0, v198\n\t" \
  "v_add_f32 v199, 1.0, v199\n\t" \
  "v_cvt_pk_bf16_f32 v184, v192, v193\n\t" \
  "v_cvt_pk_bf16_f32 v185, v194, v195\n\t" \
  "v_and_b32 v225, 0xffff0000, v225\n\t" \
  "s_waitcnt lgkmcnt(7)\n\t" \
  "v_lshlrev_b32 v219, 16, v224\n\t" \
  "v_and_b32 v224, 0xffff0000, v224\n\t" \
  "v_sub_f32 v229, v218, v225\n\t" \
  "v_sub_f32 v216, v216, v219\n\t" \
  "v_sub_f32 v217, v217, v224\n\t" \
  "v_mfma_f32_32x32x16_bf16 v[100:115], v[20:23], v[156:159], v[100:115]\n\t" \
  "v_rcp_f32 v196, v196\n\t" \
  "v_rcp_f32 v197, v197\n\t" \
  "v_rcp_f32 v198, v198\n\t" \
  "v_rcp_f32 v199, v199\n\t" \
  "v_mul_f32 v218, v229, v229\n\t" \
  "v_cndmask_b32 v228, v216, v217, vcc\n\t" \
  "v_fmac_f32 v218, v216, v216\n\t" \
  "v_fmac_f32 v218, v217, v217\n\t" \
  "v_cvt_pk_bf16_f32 v218, v218, v218\n\t" \
  "s_waitcnt lgkmcnt(6)\n\t" \
  "v_cndmask_b32_sdwa v175, v175, v218, vcc dst_sel:WORD_1 dst_unused:UNUSED_PRESERVE src0_sel:WORD_1 src1_sel:WORD_1\n\t" \
  "v_mfma_f32_16x16x32_bf16 v[132:135], v[136:139], v[188:191], v[132:135]\n\t" \
  "v_mul_f32 v192, 0xbfb8aa3b, v124\n\t" \
  "v_mul_f32 v193, 0xbfb8aa3b, v125\n\t" \
  "v_mul_f32 v194, 0xbfb8aa3b, v126\n\t" \
  "v_mul_f32 v195, 0xbfb8aa3b, v127\n\t" \
  "v_exp_f32 v192, v192\n\t" \
  "v_exp_f32 v193, v193\n\t" \
  "v_exp_f32 v194, v194\n\t" \
  "v_exp_f32 v195, v195\n\t" \
  "ds_read2_b32 v[220:221], v252 offset0:0 offset1:2\n\t" \
  "ds_read2_b32 v[222:223], v252 offset0:4 offset1:6\n\t" \
  "ds_read_b64 v[226:227], v253\n\t" \
  "ds_read_b32 v232, v254\n\t" \
  "v_mfma_f32_32x32x16_bf16 v[100:115], v[28:31], v[160:163], v[100:115]\n\t" \
  "v_mul_f32 v196, v120, v196\n\t" \
  "v_mul_f32 v197, v121, v197\n\t" \
  "v_mul_f32 v198, v122, v198\n\t" \
  "v_mul_f32 v199, v123, v199\n\t" \
  "v_add_u32 v252, 0x100, v252\n\t" \
  "v_add_u32 v253, 0x100, v253\n\t" \
  "v_add_u32 v254, 0x100, v254\n\t" \
  "v_add_f32 v192, 1.0, v192\n\t" \
  "v_add_f32 v193, 1.0, v193\n\t" \
  "v_add_f32 v194, 1.0, v194\n\t" \
  "v_add_f32 v195, 1.0, v195\n\t" \
  "v_cvt_pk_bf16_f32 v186, v196, v197\n\t" \
  "v_cvt_pk_bf16_f32 v187, v198, v199\n\t" \
  "s_waitcnt lgkmcnt(9)\n\t" \
  "v_mfma_f32_32x32x16_bf16 v[100:115], v[36:39], v[168:171], v[100:115]\n\t" \
  "v_rcp_f32 v192, v192\n\t" \
  "v_rcp_f32 v193, v193\n\t" \
  "v_rcp_f32 v194, v194\n\t" \
  "v_rcp_f32 v195, v195\n\t" \
  "v_mfma_f32_32x32x16_bf16 v[100:115], v[44:47], v[172:175], v[100:115]\n\t" \
  "v_mul_f32 v196, 0xbfb8aa3b, v128\n\t" \
  "v_mul_f32 v197, 0xbfb8aa3b, v129\n\t" \
  "v_mul_f32 v198, 0xbfb8aa3b, v130\n\t" \
  "v_mul_f32 v199, 0xbfb8aa3b, v131\n\t" \
  "v_exp_f32 v196, v196\n\t" \
  "v_exp_f32 v197, v197\n\t" \
  "v_exp_f32 v198, v198\n\t" \
  "v_exp_f32 v199, v199\n\t" \
  "v_mfma_f32_32x32x16_bf16 v[100:115], v[88:91], v[164:167], v[100:115]\n\t" \
  "v_mul_f32 v192, v124, v192\n\t" \
  "v_mul_f32 v193, v125, v193\n\t" \
  "v_mul_f32 v194, v126, v194\n\t" \
  "v_mul_f32 v195, v127, v195\n\t" \
  "ds_write_b128 v243, v[184:187] offset:0\n\t" \
  "v_add_f32 v196, 1.0, v196\n\t" \
  "v_add_f32 v197, 1.0, v197\n\t" \
  "v_add_f32 v198, 1.0, v198\n\t" \
  "v_add_f32 v199, 1.0, v199\n\t" \
  "v_cvt_pk_bf16_f32 v188, v192, v193\n\t" \
  "v_cvt_pk_bf16_f32 v189, v194, v195\n\t" \
  "s_waitcnt lgkmcnt(8)\n\t" \
  "v_mfma_f32_32x32x16_bf16 v[52:67], v[200:203], v[176:179], v[52:67]\n\t" \
  "v_rcp_f32 v196, v196\n\t" \
  "v_rcp_f32 v197, v197\n\t" \
  "v_rcp_f32 v198, v198\n\t" \
  "v_rcp_f32 v199, v199\n\t" \
  "s_waitcnt lgkmcnt(6)\n\t" \
  "v_mfma_f32_32x32x16_bf16 v[52:67], v[204:207], v[180:183], v[52:67]\n\t" \
  "v_mul_f32 v192, 0xbfb8aa3b, v100\n\t" \
  "v_mul_f32 v193, 0xbfb8aa3b, v101\n\t" \
  "v_mul_f32 v194, 0xbfb8aa3b, v102\n\t" \
  "v_mul_f32 v195, 0xbfb8aa3b, v103\n\t" \
  "v_exp_f32 v192, v192\n\t" \
  "v_exp_f32 v193, v193\n\t" \
  "v_exp_f32 v194, v194\n\t" \
  "v_exp_f32 v195, v195\n\t" \
  "s_cmp_eq_u32 %[nt], 0\n\t" \
  "v_mfma_f32_16x16x32_bf16 v[132:135], v[140:143], v[184:187], v[132:135]\n\t" \
  "v_mul_f32 v196, v128, v196\n\t" \
  "v_mul_f32 v197, v129, v197\n\t" \
  "v_mul_f32 v198, v130, v198\n\t" \
  "v_mul_f32 v199, v131, v199\n\t" \
  "v_add_f32 v192, 1.0, v192\n\t" \
  "v_add_f32 v193, 1.0, v193\n\t" \
  "v_add_f32 v194, 1.0, v194\n\t" \
  "v_add_f32 v195, 1.0, v195\n\t" \
  "v_cvt_pk_bf16_f32 v190, v196, v197\n\t" \
  "v_cvt_pk_bf16_f32 v191, v198, v199\n\t" \
  "s_cbranch_scc0 1b\n\t" \
  "s_waitcnt lgkmcnt(0)\n\t" \
  "s_cmp_lg_u32 %[last], 0\n\t" \
  "s_cbranch_scc1 8f\n\t" \
  "ds_write_b128 v243, v[188:191] offset:16\n\t" \
  "ds_read_b64_tr_b16 v[200:201], v244 offset:0\n\t" \
  "ds_read_b64_tr_b16 v[202:203], v244 offset:320\n\t" \
  "ds_read_b64_tr_b16 v[204:205], v244 offset:1280\n\t" \
  "ds_read_b64_tr_b16 v[206:207], v244 offset:1600\n\t" \
  "v_mfma_f32_16x16x32_bf16 v[132:135], v[144:147], v[188:191], v[132:135]\n\t" \
  "s_waitcnt lgkmcnt(0)\n\t" \
  "v_mfma_f32_32x32x16_bf16 v[68:83], v[200:203], v[176:179], v[68:83]\n\t" \
  "v_mfma_f32_32x32x16_bf16 v[68:83], v[204:207], v[180:183], v[68:83]\n\t" \
  "s_nop 8\n\t" \
  "v_mul_f32 v230, v230, v132\n\t" \
  "v_mul_f32 v231, v231, v132\n\t" \
  "v_cvt_pk_bf16_f32 v230, v230, v231\n\t" \
  "ds_write_b16 v245, v230 offset:7456\n\t" \
  "ds_write_b16_d16_hi v241, v230 offset:7584\n\t" \
  "ds_read_b128 v[200:203], v246 offset:7456\n\t" \
  "ds_read_b128 v[208:211], v246 offset:7488\n\t" \
  "s_waitcnt lgkmcnt(0)\n\t" \
  "v_mfma_f32_16x16x32_bf16 v[84:87], v[200:203], v[176:179], v[84:87]\n\t" \
  "v_mfma_f32_16x16x32_bf16 v[84:87], v[208:211], v[180:183], v[84:87]\n\t" \
  "s_branch 9f\n\t" \
  "6:\n\t" \
  "s_cmp_lg_u32 %[last], 0\n\t" \
  "s_cbranch_scc0 9f\n\t" \
  "s_waitcnt lgkmcnt(0)\n\t" \
  "v_cndmask_b32 v224, 0, 24, vcc\n\t" \
  "v_xor_b32 v220, v234, v224\n\t" \
  "v_xor_b32 v221, v235, v224\n\t" \
  "v_xor_b32 v222, v236, v224\n\t" \
  "v_xor_b32 v223, v237, v224\n\t" \
  "v_xor_b32 v216, 16, v220\n\t" \
  "v_xor_b32 v217, 16, v221\n\t" \
  "v_xor_b32 v218, 16, v222\n\t" \
  "v_xor_b32 v219, 16, v223\n\t" \
  "ds_read_b64 v[148:149], v220\n\t" \
  "ds_read_b64 v[150:151], v216\n\t" \
  "ds_read_b64 v[152:153], v221\n\t" \
  "ds_read_b64 v[154:155], v217\n\t" \
  "ds_read_b64 v[156:157], v222\n\t" \
  "ds_read_b64 v[158:159], v218\n\t" \
  "ds_read_b64 v[160:161], v223\n\t" \
  "ds_read_b64 v[162:163], v219\n\t" \
  "ds_read_b128 v[4:7], v247 offset:6144\n\t" \
  "ds_read_b128 v[8:11], v247 offset:7168\n\t" \
  "ds_read_b128 v[12:15], v247 offset:8192\n\t" \
  "ds_read_b128 v[16:19], v247 offset:9216\n\t" \
  "ds_read_b128 v[20:23], v247 offset:10240\n\t" \
  "ds_read_b128 v[24:27], v247 offset:11264\n\t" \
  "v_subrev_u32 v225, %[b1], v241\n\t" \
  "v_lshl_add_u32 v225, v225, 5, v243\n\t" \
  "v_sub_u32 v225, v225, v224\n\t" \
  "s_nop 4\n\t" \
  "v_cvt_pk_bf16_f32 v184, v52, v53\n\t" \
  "v_cvt_pk_bf16_f32 v185, v54, v55\n\t" \
  "v_cvt_pk_bf16_f32 v186, v56, v57\n\t" \
  "v_cvt_pk_bf16_f32 v187, v58, v59\n\t" \
  "v_cvt_pk_bf16_f32 v188, v60, v61\n\t" \
  "v_cvt_pk_bf16_f32 v189, v62, v63\n\t" \
  "v_cvt_pk_bf16_f32 v190, v64, v65\n\t" \
  "v_cvt_pk_bf16_f32 v191, v66, v67\n\t" \
  "v_cvt_pk_bf16_f32 v36, v68, v69\n\t" \
  "v_cvt_pk_bf16_f32 v37, v70, v71\n\t" \
  "v_cvt_pk_bf16_f32 v38, v72, v73\n\t" \
  "v_cvt_pk_bf16_f32 v39, v74, v75\n\t" \
  "v_cvt_pk_bf16_f32 v40, v76, v77\n\t" \
  "v_cvt_pk_bf16_f32 v41, v78, v79\n\t" \
  "v_cvt_pk_bf16_f32 v42, v80, v81\n\t" \
  "v_cvt_pk_bf16_f32 v43, v82, v83\n\t" \
  "s_waitcnt lgkmcnt(5)\n\t" \
  "v_mfma_f32_32x32x16_bf16 v[100:115], v[4:7], v[148:151], 0\n\t" \
  "s_waitcnt lgkmcnt(4)\n\t" \
  "v_mfma_f32_32x32x16_bf16 v[116:131], v[8:11], v[148:151], 0\n\t" \
  "ds_read_b128 v[28:31], v247 offset:12288\n\t" \
  "ds_read_b128 v[32:35], v247 offset:13312\n\t" \
  "s_waitcnt lgkmcnt(5)\n\t" \
  "v_mfma_f32_32x32x16_bf16 v[100:115], v[12:15], v[152:155], v[100:115]\n\t" \
  "s_waitcnt lgkmcnt(4)\n\t" \
  "v_mfma_f32_32x32x16_bf16 v[116:131], v[16:19], v[152:155], v[116:131]\n\t" \
  "ds_read_b128 v[4:7], v247 offset:14336\n\t" \
  "ds_read_b128 v[8:11], v247 offset:15360\n\t" \
  "s_waitcnt lgkmcnt(5)\n\t" \
  "v_mfma_f32_32x32x16_bf16 v[100:115], v[20:23], v[156:159], v[100:115]\n\t" \
  "s_waitcnt lgkmcnt(4)\n\t" \
  "v_mfma_f32_32x32x16_bf16 v[116:131], v[24:27], v[156:159], v[116:131]\n\t" \
  "ds_read_b128 v[12:15], v247 offset:16384\n\t" \
  "ds_read_b128 v[16:19], v247 offset:17408\n\t" \
  "s_waitcnt lgkmcnt(5)\n\t" \
  "v_mfma_f32_32x32x16_bf16 v[100:115], v[28:31], v[160:163], v[100:115]\n\t" \
  "s_waitcnt lgkmcnt(4)\n\t" \
  "v_mfma_f32_32x32x16_bf16 v[116:131], v[32:35], v[160:163], v[116:131]\n\t" \
  "ds_read_b128 v[20:23], v247 offset:18432\n\t" \
  "ds_read_b128 v[24:27], v247 offset:19456\n\t" \
  "s_waitcnt lgkmcnt(5)\n\t" \
  "v_mfma_f32_32x32x16_bf16 v[100:115], v[4:7], v[184:187], v[100:115]\n\t" \
  "ds_read_b128 v[28:31], v247 offset:20480\n\t" \
  "ds_read_b128 v[32:35], v247 offset:21504\n\t" \
  "s_waitcnt lgkmcnt(5)\n\t" \
  "v_mfma_f32_32x32x16_bf16 v[100:115], v[12:15], v[188:191], v[100:115]\n\t" \
  "s_waitcnt lgkmcnt(3)\n\t" \
  "v_mfma_f32_32x32x16_bf16 v[100:115], v[20:23], v[36:39], v[100:115]\n\t" \
  "s_waitcnt lgkmcnt(1)\n\t" \
  "v_mfma_f32_32x32x16_bf16 v[100:115], v[28:31], v[40:43], v[100:115]\n\t" \
  "s_waitcnt lgkmcnt(0)\n\t" \
  "s_branch 10f\n\t" \
  "8:\n\t" \
  "ds_write_b128 v243, v[188:191] offset:16\n\t" \
  "ds_read_b64_tr_b16 v[200:201], v244 offset:0\n\t" \
  "ds_read_b64_tr_b16 v[202:203], v244 offset:320\n\t" \
  "ds_read_b64_tr_b16 v[204:205], v244 offset:1280\n\t" \
  "ds_read_b64_tr_b16 v[206:207], v244 offset:1600\n\t" \
  "v_mfma_f32_16x16x32_bf16 v[132:135], v[144:147], v[188:191], v[132:135]\n\t" \
  "v_cndmask_b32 v224, 0, 24, vcc\n\t" \
  "v_xor_b32 v220, v234, v224\n\t" \
  "v_xor_b32 v221, v235, v224\n\t" \
  "v_xor_b32 v222, v236, v224\n\t" \
  "v_xor_b32 v223, v237, v224\n\t" \
  "v_xor_b32 v216, 16, v220\n\t" \
  "v_xor_b32 v217, 16, v221\n\t" \
  "v_xor_b32 v218, 16, v222\n\t" \
  "v_xor_b32 v219, 16, v223\n\t" \
  "ds_read_b64 v[148:149], v220\n\t" \
  "ds_read_b64 v[150:151], v216\n\t" \
  "ds_read_b64 v[152:153], v221\n\t" \
  "ds_read_b64 v[154:155], v217\n\t" \
  "ds_read_b64 v[156:157], v222\n\t" \
  "ds_read_b64 v[158:159], v218\n\t" \
  "ds_read_b64 v[160:161], v223\n\t" \
  "ds_read_b64 v[162:163], v219\n\t" \
  "ds_read_b128 v[4:7], v247 offset:6144\n\t" \
  "ds_read_b128 v[8:11], v247 offset:7168\n\t" \
  "s_waitcnt lgkmcnt(11)\n\t" \
  "ds_read_b128 v[12:15], v247 offset:8192\n\t" \
  "ds_read_b128 v[16:19], v247 offset:9216\n\t" \
  "ds_read_b128 v[20:23], v247 offset:10240\n\t" \
  "ds_read_b128 v[24:27], v247 offset:11264\n\t" \
  "v_subrev_u32 v225, %[b1], v241\n\t" \
  "v_lshl_add_u32 v225, v225, 5, v243\n\t" \
  "v_sub_u32 v225, v225, v224\n\t" \
  "v_mfma_f32_32x32x16_bf16 v[68:83], v[200:203], v[176:179], v[68:83]\n\t" \
  "s_waitcnt lgkmcnt(5)\n\t" \
  "v_mfma_f32_32x32x16_bf16 v[100:115], v[4:7], v[148:151], 0\n\t" \
  "v_mfma_f32_32x32x16_bf16 v[68:83], v[204:207], v[180:183], v[68:83]\n\t" \
  "s_waitcnt lgkmcnt(4)\n\t" \
  "v_mfma_f32_32x32x16_bf16 v[116:131], v[8:11], v[148:151], 0\n\t" \
  "ds_read_b128 v[28:31], v247 offset:12288\n\t" \
  "ds_read_b128 v[32:35], v247 offset:13312\n\t" \
  "s_waitcnt lgkmcnt(5)\n\t" \
  "v_mfma_f32_32x32x16_bf16 v[100:115], v[12:15], v[152:155], v[100:115]\n\t" \
  "v_mul_f32 v230, v230, v132\n\t" \
  "v_mul_f32 v231, v231, v132\n\t" \
  "v_cvt_pk_bf16_f32 v230, v230, v231\n\t" \
  "ds_write_b16 v245, v230 offset:7456\n\t" \
  "ds_write_b16_d16_hi v241, v230 offset:7584\n\t" \
  "ds_read_b128 v[200:203], v246 offset:7456\n\t" \
  "ds_read_b128 v[208:211], v246 offset:7488\n\t" \
  "v_cvt_pk_bf16_f32 v184, v52, v53\n\t" \
  "v_cvt_pk_bf16_f32 v185, v54, v55\n\t" \
  "v_cvt_pk_bf16_f32 v186, v56, v57\n\t" \
  "v_cvt_pk_bf16_f32 v187, v58, v59\n\t" \
  "s_waitcnt lgkmcnt(8)\n\t" \
  "v_mfma_f32_32x32x16_bf16 v[116:131], v[16:19], v[152:155], v[116:131]\n\t" \
  "ds_read_b128 v[4:7], v247 offset:14336\n\t" \
  "ds_read_b128 v[8:11], v247 offset:15360\n\t" \
  "s_waitcnt lgkmcnt(9)\n\t" \
  "v_mfma_f32_32x32x16_bf16 v[100:115], v[20:23], v[156:159], v[100:115]\n\t" \
  "v_cvt_pk_bf16_f32 v188, v60, v61\n\t" \
  "v_cvt_pk_bf16_f32 v189, v62, v63\n\t" \
  "v_cvt_pk_bf16_f32 v190, v64, v65\n\t" \
  "v_cvt_pk_bf16_f32 v191, v66, v67\n\t" \
  "s_waitcnt lgkmcnt(8)\n\t" \
  "v_mfma_f32_32x32x16_bf16 v[116:131], v[24:27], v[156:159], v[116:131]\n\t" \
  "ds_read_b128 v[12:15], v247 offset:16384\n\t" \
  "ds_read_b128 v[16:19], v247 offset:17408\n\t" \
  "s_waitcnt lgkmcnt(9)\n\t" \
  "v_mfma_f32_32x32x16_bf16 v[100:115], v[28:31], v[160:163], v[100:115]\n\t" \
  "v_cvt_pk_bf16_f32 v36, v68, v69\n\t" \
  "v_cvt_pk_bf16_f32 v37, v70, v71\n\t" \
  "v_cvt_pk_bf16_f32 v38, v72, v73\n\t" \
  "v_cvt_pk_bf16_f32 v39, v74, v75\n\t" \
  "s_waitcnt lgkmcnt(5)\n\t" \
  "v_mfma_f32_16x16x32_bf16 v[84:87], v[200:203], v[176:179], v[84:87]\n\t" \
  "v_cvt_pk_bf16_f32 v40, v76, v77\n\t" \
  "v_cvt_pk_bf16_f32 v41, v78, v79\n\t" \
  "v_cvt_pk_bf16_f32 v42, v80, v81\n\t" \
  "v_cvt_pk_bf16_f32 v43, v82, v83\n\t" \
  "v_mfma_f32_32x32x16_bf16 v[116:131], v[32:35], v[160:163], v[116:131]\n\t" \
  "ds_read_b128 v[20:23], v247 offset:18432\n\t" \
  "ds_read_b128 v[24:27], v247 offset:19456\n\t" \
  "s_waitcnt lgkmcnt(6)\n\t" \
  "v_mfma_f32_16x16x32_bf16 v[84:87], v[208:211], v[180:183], v[84:87]\n\t" \
  "s_waitcnt lgkmcnt(5)\n\t" \
  "v_mfma_f32_32x32x16_bf16 v[100:115], v[4:7], v[184:187], v[100:115]\n\t" \
  "ds_read_b128 v[28:31], v247 offset:20480\n\t" \
  "ds_read_b128 v[32:35], v247 offset:21504\n\t" \
  "s_waitcnt lgkmcnt(5)\n\t" \
  "v_mfma_f32_32x32x16_bf16 v[100:115], v[12:15], v[188:191], v[100:115]\n\t" \
  "s_waitcnt lgkmcnt(3)\n\t" \
  "v_mfma_f32_32x32x16_bf16 v[100:115], v[20:23], v[36:39], v[100:115]\n\t" \
  "s_waitcnt lgkmcnt(1)\n\t" \
  "v_mfma_f32_32x32x16_bf16 v[100:115], v[28:31], v[40:43], v[100:115]\n\t" \
  "s_waitcnt lgkmcnt(0)\n\t" \
  "10:\n\t" \
  "v_mov_b32 v212, 0xbfb8aa3b\n\t" \
  "v_mfma_f32_32x32x16_bf16 v[116:131], v[8:11], v[184:187], v[116:131]\n\t" \
  "v_mfma_f32_32x32x16_bf16 v[116:131], v[16:19], v[188:191], v[116:131]\n\t" \
  "s_nop 8\n\t" \
  "v_mul_f32 v192, 0xbfb8aa3b, v100\n\t" \
  "v_mul_f32 v193, 0xbfb8aa3b, v101\n\t" \
  "v_mul_f32 v194, 0xbfb8aa3b, v102\n\t" \
  "v_mul_f32 v195, 0xbfb8aa3b, v103\n\t" \
  "v_exp_f32 v192, v192\n\t" \
  "v_exp_f32 v193, v193\n\t" \
  "v_exp_f32 v194, v194\n\t" \
  "v_exp_f32 v195, v195\n\t" \
  "v_lshlrev_b32 v200, 16, v148\n\t" \
  "v_and_b32 v201, 0xffff0000, v148\n\t" \
  "v_lshlrev_b32 v202, 16, v149\n\t" \
  "v_and_b32 v203, 0xffff0000, v149\n\t" \
  "v_mfma_f32_32x32x16_bf16 v[116:131], v[24:27], v[36:39], v[116:131]\n\t" \
  "v_add_f32 v192, 1.0, v192\n\t" \
  "v_add_f32 v193, 1.0, v193\n\t" \
  "v_add_f32 v194, 1.0, v194\n\t" \
  "v_add_f32 v195, 1.0, v195\n\t" \
  "v_rcp_f32 v192, v192\n\t" \
  "v_rcp_f32 v193, v193\n\t" \
  "v_rcp_f32 v194, v194\n\t" \
  "v_rcp_f32 v195, v195\n\t" \
  "v_mfma_f32_32x32x16_bf16 v[116:131], v[32:35], v[40:43], v[116:131]\n\t" \
  "v_mul_f32 v196, 0xbfb8aa3b, v104\n\t" \
  "v_mul_f32 v197, 0xbfb8aa3b, v105\n\t" \
  "v_mul_f32 v198, 0xbfb8aa3b, v106\n\t" \
  "v_mul_f32 v199, 0xbfb8aa3b, v107\n\t" \
  "v_exp_f32 v196, v196\n\t" \
  "v_exp_f32 v197, v197\n\t" \
  "v_exp_f32 v198, v198\n\t" \
  "v_exp_f32 v199, v199\n\t" \
  "v_fmac_f32 v200, v100, v192\n\t" \
  "v_fmac_f32 v201, v101, v193\n\t" \
  "v_fmac_f32 v202, v102, v194\n\t" \
  "v_fmac_f32 v203, v103, v195\n\t" \
  "v_lshlrev_b32 v204, 16, v150\n\t" \
  "v_and_b32 v205, 0xffff0000, v150\n\t" \
  "v_lshlrev_b32 v206, 16, v151\n\t" \
  "v_and_b32 v207, 0xffff0000, v151\n\t" \
  "v_add_f32 v196, 1.0, v196\n\t" \
  "v_add_f32 v197, 1.0, v197\n\t" \
  "v_add_f32 v198, 1.0, v198\n\t" \
  "v_add_f32 v199, 1.0, v199\n\t" \
  "v_cvt_pk_bf16_f32 v176, v200, v201\n\t" \
  "v_cvt_pk_bf16_f32 v177, v202, v203\n\t" \
  "ds_write_b64 v225, v[176:177] offset:0\n\t" \
  "v_rcp_f32 v196, v196\n\t" \
  "v_rcp_f32 v197, v197\n\t" \
  "v_rcp_f32 v198, v198\n\t" \
  "v_rcp_f32 v199, v199\n\t" \
  "v_mul_f32 v192, 0xbfb8aa3b, v108\n\t" \
  "v_mul_f32 v193, 0xbfb8aa3b, v109\n\t" \
  "v_mul_f32 v194, 0xbfb8aa3b, v110\n\t" \
  "v_mul_f32 v195, 0xbfb8aa3b, v111\n\t" \
  "v_exp_f32 v192, v192\n\t" \
  "v_exp_f32 v193, v193\n\t" \
  "v_exp_f32 v194, v194\n\t" \
  "v_exp_f32 v195, v195\n\t" \
  "v_fmac_f32 v204, v104, v196\n\t" \
  "v_fmac_f32 v205, v105, v197\n\t" \
  "v_fmac_f32 v206, v106, v198\n\t" \
  "v_fmac_f32 v207, v107, v199\n\t" \
  "v_lshlrev_b32 v200, 16, v152\n\t" \
  "v_and_b32 v201, 0xffff0000, v152\n\t" \
  "v_lshlrev_b32 v202, 16, v153\n\t" \
  "v_and_b32 v203, 0xffff0000, v153\n\t" \
  "v_add_f32 v192, 1.0, v192\n\t" \
  "v_add_f32 v193, 1.0, v193\n\t" \
  "v_add_f32 v194, 1.0, v194\n\t" \
  "v_add_f32 v195, 1.0, v195\n\t" \
  "v_cvt_pk_bf16_f32 v178, v204, v205\n\t" \
  "v_cvt_pk_bf16_f32 v179, v206, v207\n\t" \
  "ds_write_b64 v225, v[178:179] offset:16\n\t" \
  "v_rcp_f32 v192, v192\n\t" \
  "v_rcp_f32 v193, v193\n\t" \
  "v_rcp_f32 v194, v194\n\t" \
  "v_rcp_f32 v195, v195\n\t" \
  "v_mul_f32 v196, 0xbfb8aa3b, v112\n\t" \
  "v_mul_f32 v197, 0xbfb8aa3b, v113\n\t" \
  "v_mul_f32 v198, 0xbfb8aa3b, v114\n\t" \
  "v_mul_f32 v199, 0xbfb8aa3b, v115\n\t" \
  "v_exp_f32 v196, v196\n\t" \
  "v_exp_f32 v197, v197\n\t" \
  "v_exp_f32 v198, v198\n\t" \
  "v_exp_f32 v199, v199\n\t" \
  "v_fmac_f32 v200, v108, v192\n\t" \
  "v_fmac_f32 v201, v109, v193\n\t" \
  "v_fmac_f32 v202, v110, v194\n\t" \
  "v_fmac_f32 v203, v111, v195\n\t" \
  "v_lshlrev_b32 v204, 16, v154\n\t" \
  "v_and_b32 v205, 0xffff0000, v154\n\t" \
  "v_lshlrev_b32 v206, 16, v155\n\t" \
  "v_and_b32 v207, 0xffff0000, v155\n\t" \
  "v_add_f32 v196, 1.0, v196\n\t" \
  "v_add_f32 v197, 1.0, v197\n\t" \
  "v_add_f32 v198, 1.0, v198\n\t" \
  "v_add_f32 v199, 1.0, v199\n\t" \
  "v_cvt_pk_bf16_f32 v180, v200, v201\n\t" \
  "v_cvt_pk_bf16_f32 v181, v202, v203\n\t" \
  "ds_write_b64 v225, v[180:181] offset:32\n\t" \
  "v_rcp_f32 v196, v196\n\t" \
  "v_rcp_f32 v197, v197\n\t" \
  "v_rcp_f32 v198, v198\n\t" \
  "v_rcp_f32 v199, v199\n\t" \
  "v_mul_f32 v192, 0xbfb8aa3b, v116\n\t" \
  "v_mul_f32 v193, 0xbfb8aa3b, v117\n\t" \
  "v_mul_f32 v194, 0xbfb8aa3b, v118\n\t" \
  "v_mul_f32 v195, 0xbfb8aa3b, v119\n\t" \
  "v_exp_f32 v192, v192\n\t" \
  "v_exp_f32 v193, v193\n\t" \
  "v_exp_f32 v194, v194\n\t" \
  "v_exp_f32 v195, v195\n\t" \
  "v_fmac_f32 v204, v112, v196\n\t" \
  "v_fmac_f32 v205, v113, v197\n\t" \
  "v_fmac_f32 v206, v114, v198\n\t" \
  "v_fmac_f32 v207, v115, v199\n\t" \
  "v_lshlrev_b32 v200, 16, v156\n\t" \
  "v_and_b32 v201, 0xffff0000, v156\n\t" \
  "v_lshlrev_b32 v202, 16, v157\n\t" \
  "v_and_b32 v203, 0xffff0000, v157\n\t" \
  "v_add_f32 v192, 1.0, v192\n\t" \
  "v_add_f32 v193, 1.0, v193\n\t" \
  "v_add_f32 v194, 1.0, v194\n\t" \
  "v_add_f32 v195, 1.0, v195\n\t" \
  "v_cvt_pk_bf16_f32 v182, v204, v205\n\t" \
  "v_cvt_pk_bf16_f32 v183, v206, v207\n\t" \
  "ds_write_b64 v225, v[182:183] offset:48\n\t" \
  "v_rcp_f32 v192, v192\n\t" \
  "v_rcp_f32 v193, v193\n\t" \
  "v_rcp_f32 v194, v194\n\t" \
  "v_rcp_f32 v195, v195\n\t" \
  "v_mul_f32 v196, 0xbfb8aa3b, v120\n\t" \
  "v_mul_f32 v197, 0xbfb8aa3b, v121\n\t" \
  "v_mul_f32 v198, 0xbfb8aa3b, v122\n\t" \
  "v_mul_f32 v199, 0xbfb8aa3b, v123\n\t" \
  "v_exp_f32 v196, v196\n\t" \
  "v_exp_f32 v197, v197\n\t" \
  "v_exp_f32 v198, v198\n\t" \
  "v_exp_f32 v199, v199\n\t" \
  "v_fmac_f32 v200, v116, v192\n\t" \
  "v_fmac_f32 v201, v117, v193\n\t" \
  "v_fmac_f32 v202, v118, v194\n\t" \
  "v_fmac_f32 v203, v119, v195\n\t" \
  "v_lshlrev_b32 v204, 16, v158\n\t" \
  "v_and_b32 v205, 0xffff0000, v158\n\t" \
  "v_lshlrev_b32 v206, 16, v159\n\t" \
  "v_and_b32 v207, 0xffff0000, v159\n\t" \
  "v_add_f32 v196, 1.0, v196\n\t" \
  "v_add_f32 v197, 1.0, v197\n\t" \
  "v_add_f32 v198, 1.0, v198\n\t" \
  "v_add_f32 v199, 1.0, v199\n\t" \
  "v_cvt_pk_bf16_f32 v176, v200, v201\n\t" \
  "v_cvt_pk_bf16_f32 v177, v202, v203\n\t" \
  "ds_write_b64 v225, v[176:177] offset:64\n\t" \
  "v_rcp_f32 v196, v196\n\t" \
  "v_rcp_f32 v197, v197\n\t" \
  "v_rcp_f32 v198, v198\n\t" \
  "v_rcp_f32 v199, v199\n\t" \
  "v_mul_f32 v192, 0xbfb8aa3b, v124\n\t" \
  "v_mul_f32 v193, 0xbfb8aa3b, v125\n\t" \
  "v_mul_f32 v194, 0xbfb8aa3b, v126\n\t" \
  "v_mul_f32 v195, 0xbfb8aa3b, v127\n\t" \
  "v_exp_f32 v192, v192\n\t" \
  "v_exp_f32 v193, v193\n\t" \
  "v_exp_f32 v194, v194\n\t" \
  "v_exp_f32 v195, v195\n\t" \
  "v_fmac_f32 v204, v120, v196\n\t" \
  "v_fmac_f32 v205, v121, v197\n\t" \
  "v_fmac_f32 v206, v122, v198\n\t" \
  "v_fmac_f32 v207, v123, v199\n\t" \
  "v_lshlrev_b32 v200, 16, v160\n\t" \
  "v_and_b32 v201, 0xffff0000, v160\n\t" \
  "v_lshlrev_b32 v202, 16, v161\n\t" \
  "v_and_b32 v203, 0xffff0000, v161\n\t" \
  "v_add_f32 v192, 1.0, v192\n\t" \
  "v_add_f32 v193, 1.0, v193\n\t" \
  "v_add_f32 v194, 1.0, v194\n\t" \
  "v_add_f32 v195, 1.0, v195\n\t" \
  "v_cvt_pk_bf16_f32 v178, v204, v205\n\t" \
  "v_cvt_pk_bf16_f32 v179, v206, v207\n\t" \
  "ds_write_b64 v225, v[178:179] offset:80\n\t" \
  "v_rcp_f32 v192, v192\n\t" \
  "v_rcp_f32 v193, v193\n\t" \
  "v_rcp_f32 v194, v194\n\t" \
  "v_rcp_f32 v195, v195\n\t" \
  "v_mul_f32 v196, 0xbfb8aa3b, v128\n\t" \
  "v_mul_f32 v197, 0xbfb8aa3b, v129\n\t" \
  "v_mul_f32 v198, 0xbfb8aa3b, v130\n\t" \
  "v_mul_f32 v199, 0xbfb8aa3b, v131\n\t" \
  "v_exp_f32 v196, v196\n\t" \
  "v_exp_f32 v197, v197\n\t" \
  "v_exp_f32 v198, v198\n\t" \
  "v_exp_f32 v199, v199\n\t" \
  "v_fmac_f32 v200, v124, v192\n\t" \
  "v_fmac_f32 v201, v125, v193\n\t" \
  "v_fmac_f32 v202, v126, v194\n\t" \
  "v_fmac_f32 v203, v127, v195\n\t" \
  "v_lshlrev_b32 v204, 16, v162\n\t" \
  "v_and_b32 v205, 0xffff0000, v162\n\t" \
  "v_lshlrev_b32 v206, 16, v163\n\t" \
  "v_and_b32 v207, 0xffff0000, v163\n\t" \
  "v_add_f32 v196, 1.0, v196\n\t" \
  "v_add_f32 v197, 1.0, v197\n\t" \
  "v_add_f32 v198, 1.0, v198\n\t" \
  "v_add_f32 v199, 1.0, v199\n\t" \
  "v_cvt_pk_bf16_f32 v180, v200, v201\n\t" \
  "v_cvt_pk_bf16_f32 v181, v202, v203\n\t" \
  "ds_write_b64 v225, v[180:181] offset:96\n\t" \
  "v_rcp_f32 v196, v196\n\t" \
  "v_rcp_f32 v197, v197\n\t" \
  "v_rcp_f32 v198, v198\n\t" \
  "v_rcp_f32 v199, v199\n\t" \
  "s_nop 0\n\t" \
  "v_fmac_f32 v204, v128, v196\n\t" \
  "v_fmac_f32 v205, v129, v197\n\t" \
  "v_fmac_f32 v206, v130, v198\n\t" \
  "v_fmac_f32 v207, v131, v199\n\t" \
  "v_cvt_pk_bf16_f32 v182, v204, v205\n\t" \
  "v_cvt_pk_bf16_f32 v183, v206, v207\n\t" \
  "ds_write_b64 v225, v[182:183] offset:112\n\t" \
  "9:\n\t" \
  "ds_write_b16 v233, v255 offset:5376\n\t" \
  "s_mov_b32 m0, %[keep]\n\t" \
  "s_setprio 0\n\t" \
  "s_nop 11\n\t" \
  ""
#define LOOP_ASM LOOP_ASM_FOLD

DI u32x4 tr_frag(unsigned p, int half) {
  const s16x4 lo = __builtin_amdgcn_ds_read_tr16_b64_v4i16((lds_s16x4_p)p);
  const s16x4 hi = __builtin_amdgcn_ds_read_tr16_b64_v4i16((lds_s16x4_p)(p + half));
  return __builtin_bit_cast(u32x4, __builtin_shufflevector(lo, hi, 0, 1, 2, 3, 4, 5, 6, 7));
}
DI u32x4 fold(u32x4 v) {
#pragma unroll
  for (int i = 0; i < 4; i++) v[i] = pk_bf16(bf2f(v[i] & 0xffffu) * -LOG2E, __uint_as_float(v[i] & 0xffff0000u) * -LOG2E);
  return v;
}

__global__ __launch_bounds__(512, 2) void egnn_kernel(const char* __restrict__ hmat, const char* __restrict__ pos,
                                                      const char* __restrict__ ea, const int* __restrict__ src,
                                                      const int* __restrict__ dst, const char* __restrict__ we,
                                                      const char* __restrict__ wx, const char* __restrict__ wh,
                                                      char* __restrict__ ws, uint16_t* __restrict__ out, int N, int E STAMP_ARG) {
  const int w = __builtin_amdgcn_readfirstlane(threadIdx.x >> 6);
  const int bkt = (blockIdx.x & 7) * 32 + (blockIdx.x >> 3);
  const int n0 = bkt * NB;
  STAMP(0);
  unsigned wl = L_WAVE + w * WSZ, mi = L_MI + w * MISZ;
  const auto rs_tab = __builtin_amdgcn_make_buffer_rsrc((void*)(ws + WS_TAB), 0, NBK * NBK * 8, 0x00020000);
  const auto rs_rec = __builtin_amdgcn_make_buffer_rsrc((void*)(ws + WS_REC), 0, NBK * CHS, 0x00020000);
  const auto rs_ea = __builtin_amdgcn_make_buffer_rsrc((void*)ea, 0, E * 32, 0x00020000);
  unsigned tag_lo, tag_hi;
  unsigned wdv0, wdv1;

  {
    const int lane = lane_id(), tid = w * 64 + lane, r = lane & 31, h = lane >> 5;
    const int e0 = bkt * CH;
    int ne = max(E - e0, 0);
    asm("" : "+s"(ne));
    ne = min(ne, CH);
    const auto rs_d = __builtin_amdgcn_make_buffer_rsrc((void*)(dst + e0), 0, ne * 4, 0x00020000);
    const auto rs_s = __builtin_amdgcn_make_buffer_rsrc((void*)(src + e0), 0, ne * 4, 0x00020000);
    unsigned d[7], sv[7];
    u32x2 xa, xb;
    unsigned long long ep;
    asm volatile("s_load_dwordx2 %0, %1, 0x0" : "=s"(ep) : "s"(ws + WS_EPOCH) : "memory");
#pragma unroll
    for (int i = 0; i < 7; i++)
      asm volatile("buffer_load_dword %0, %2, %3, %5 offen nt\n\tbuffer_load_dword %1, %2, %4, %5 offen nt"
                   : "=&v"(d[i]), "=&v"(sv[i]) : "v"(tid * 4), "s"(rs_d), "s"(rs_s), "s"(i * 2048) : "memory");
    LDS(unsigned, L_CNT + tid * 4) = 0;
    asm volatile("s_waitcnt lgkmcnt(0)\n\ts_barrier" : "+s"(ep) :: "memory");
    asm volatile("global_load_ushort %0, %4, %5\n\tglobal_load_ushort %1, %4, %5 offset:64\n\t"
                 "global_load_dwordx2 %2, %6, %7\n\tglobal_load_dwordx2 %3, %6, %7 offset:16"
                 : "=&v"(wdv0), "=&v"(wdv1), "=&v"(xa), "=&v"(xb)
                 : "v"(r * 2), "s"(we + 128 * 128), "v"(h * 8), "s"(wx + ((w + 2) & 3) * 32) : "memory");
    {
      const int j = lane >> 3, jl = (j & 3) * 128 + (lane & 7) * 16, jh = (j >> 2) * 128;
      const char* const h0 = wh + (w < 4 ? 16 * w : 64 + 32 * ((w - 4) >> 1) + 4 * (w & 1)) * 128;
      const char* const e0p = we + (w < 4 ? 64 + 16 * w : 16 * (w - 4)) * 128;
      const unsigned oh = jl + jh * (w < 4 ? 8 : 16), oe = jl + jh * (w < 4 ? 4 : 8);
      unsigned keep;
      asm volatile("s_mov_b32 %0, m0\n\ts_mov_b32 m0, %9\n\ts_nop 0\n\t"
                   "global_load_lds_dwordx4 %1, %4\n\tglobal_load_lds_dwordx4 %1, %5 offset:1024\n\t"
                   "global_load_lds_dwordx4 %2, %6 offset:2048\n\tglobal_load_lds_dwordx4 %2, %7 offset:3072\n\t"
                   "s_add_u32 m0, %9, 0x1000\n\ts_nop 0\n\tglobal_load_lds_dwordx4 %3, %8\n\ts_mov_b32 m0, %0"
                   : "=&s"(keep)
                   : "v"(oh), "v"(oe), "v"(w < 2 ? (lane >> 2) * 128 + (lane & 3) * 16 : 0),
                     "s"(h0), "s"(h0 + (w < 4 ? 4 : 8) * 128 - 1024), "s"(e0p - 2048), "s"(e0p + (w < 4 ? 8 : 4) * 128 - 3072),
                     "s"(we + 129 * 128 + (w & 1) * 64), "s"(wl)
                   : "memory", "scc");
    }
    unsigned ra[5], keep;
    {
      const unsigned cc0 = ((lane & 7) ^ (lane >> 4)) * 16, cc1 = ((lane & 7) ^ (4 + (lane >> 4))) * 16;
      const int nr = n0 + w + (lane & 56);
      ra[0] = min(nr, N - 1) * 128 + cc0; ra[1] = min(nr + 64, N - 1) * 128 + cc1; ra[2] = min(nr + 128, N - 1) * 128 + cc0;
      ra[3] = min(n0 + w + 192, N - 1) * 128 + cc1; ra[4] = mul24(min(n0 + w + min(lane >> 1, 24) * 8, N - 1), 6) + (lane & 1) * 2;
      asm volatile("" : "+v"(ra[0]), "+v"(ra[1]), "+v"(ra[2]), "+v"(ra[3]), "+v"(ra[4]));
    }
    asm volatile("s_waitcnt vmcnt(9)" : "+v"(d[0]), "+v"(d[1]), "+v"(d[2]), "+v"(d[3]), "+v"(d[4]), "+v"(d[5]), "+v"(d[6]),
                 "+v"(sv[0]), "+v"(sv[1]), "+v"(sv[2]), "+v"(sv[3]), "+v"(sv[4]), "+v"(sv[5]), "+v"(sv[6]) :: "memory");
    STAMP(10);
    {
      asm volatile("s_mov_b32 %0, m0\n\t"
                   "s_mov_b32 m0, %8\n\ts_nop 0\n\tglobal_load_lds_dwordx4 %1, %6\n\t"
                   "s_add_u32 m0, %8, 0x400\n\ts_nop 0\n\tglobal_load_lds_dwordx4 %2, %6\n\t"
                   "s_add_u32 m0, %8, 0x800\n\ts_nop 0\n\tglobal_load_lds_dwordx4 %3, %6\n\t"
                   "s_add_u32 m0, %8, 0xc00\n\ts_nop 0\n\tglobal_load_lds_dwordx4 %4, %6\n\t"
                   "s_add_u32 m0, %8, 0x1000\n\ts_nop 0\n\tglobal_load_lds_dword %5, %7\n\ts_mov_b32 m0, %0"
                   : "=&s"(keep)
                   : "v"(ra[0]), "v"(ra[1]), "v"(ra[2]), "v"(ra[3]), "v"(ra[4]), "s"(hmat), "s"(pos), "s"(mi)
                   : "memory", "scc");
    }
    unsigned ent, total;
    auto sort = [&](auto NS_, auto FULL_) __attribute__((always_inline)) {
      constexpr int NS = decltype(NS_)::value;
      constexpr bool FULL = decltype(FULL_)::value;
      unsigned bk[NS], rank[NS];
      u32x2 rec[NS];
#pragma unroll
      for (int i = 0; i < NS; i++) {
        const int el = i * 512 + tid;
        const unsigned b = mul24(d[i], 85599u) >> 24;
        const unsigned dl = d[i] - mul24(b, NB);
        bk[i] = (FULL || el < ne ? b : NBK + lane) * 4;
        rank[i] = __hip_atomic_fetch_add(&LDS(unsigned, L_CNT + bk[i]), 8u, __ATOMIC_RELAXED, __HIP_MEMORY_SCOPE_WORKGROUP);
        rec[i][0] = el;
        rec[i][1] = ((mul24(dl >> 3, 1272u) + dl) << 16) | sv[i] | 0x80000u;
      }
      LDS(unsigned, L_BASE + (NBK + lane) * 4) = 0x40000000u;
      barrier_lds();
      u32x4 cv = LDS(u32x4, L_CNT + lane * 16);
#pragma unroll
      for (int i = 0; i < 4; i++) cv[i] = (cv[i] + 8) & ~15u;
      const unsigned sum = cv[0] + cv[1] + cv[2] + cv[3], incl = wave_scan_incl(sum);
      const unsigned ex = incl - sum + L_REC;
      total = __builtin_amdgcn_readlane(incl, 63);
      const u32x4 bv = {ex, ex + cv[0], ex + cv[0] + cv[1], ex + cv[0] + cv[1] + cv[2]};
      LDS(u32x4, L_BASE + lane * 16) = bv;
      unsigned bs[NS];
#pragma unroll
      for (int i = 0; i < NS; i++) bs[i] = LDS(unsigned, L_BASE + bk[i]);
      ent = ((LDS(unsigned, L_BASE + (tid & 255) * 4) - L_REC) >> 4) | (LDS(unsigned, L_CNT + (tid & 255) * 4) << 9);
      const unsigned lim = L_REC + 8 * SEG + lane * 8;
#pragma unroll
      for (int i = 0; i < NS; i++) LDS(u32x2, FULL ? bs[i] + rank[i] : min(bs[i] + rank[i], lim)) = rec[i];
      barrier_lds();
    };
    if (ne == CH && w != 0) sort(std::integral_constant<int, 6>(), std::true_type());
    else sort(std::integral_constant<int, 7>(), std::false_type());
    LDS(unsigned, L_CNT + tid * 4) = 0;
    STAMP(11);
    tag_lo = (unsigned)(ep + 1) & 0xffu;
    tag_hi = (unsigned)((ep + 1) >> 8);
    {
      const u32x2 g = {ent | (tag_lo << 24), tag_hi};
      __builtin_amdgcn_raw_buffer_store_b64(g, rs_tab, tid < NBK ? bkt * (NBK * 8) + tid * 8 : 0x80000000u, 0, 16);
      const auto rs_out = __builtin_amdgcn_make_buffer_rsrc((void*)(ws + WS_REC + (size_t)bkt * CHS), 0, __builtin_amdgcn_readfirstlane(total), 0x00020000);
#pragma unroll
      for (int i = 0; i < 4; i++) {
        const u32x4 v = LDS(u32x4, L_REC + (i * 512 + tid) * 16);
        const u32x4 o = {v[1], v[3], v[0] | ((v[2] & 0xfffu) << 12) | (tag_lo << 24), tag_hi};
        __builtin_amdgcn_raw_buffer_store_b128(o, rs_out, (i * 512 + tid) * 16, 0, 16);
      }
    }
    STAMP(12);
    asm volatile("s_waitcnt vmcnt(10)" : "+v"(wdv0), "+v"(wdv1), "+v"(xa), "+v"(xb) :: "memory");
    {
      const unsigned tp = wl + (8 * h + ((lane >> 2) & 3)) * 128 + ((lane >> 4) & 1) * 32 + (lane & 3) * 8;
      u32x4 f[2], g[2];
#pragma unroll
      for (int t = 0; t < 2; t++) { f[t] = tr_frag(tp + 64 * t, 512); g[t] = tr_frag(tp + 2048 + 64 * t, 512); }
#pragma unroll
      for (int t = 0; t < 2; t++) {
        if (w < 4) { f[t] = fold(f[t]); g[t] = fold(g[t]); }
        LDS(u32x4, L_WH + (2 * w + t) * 1024 + lane * 16) = f[t];
        LDS(u32x4, wl + 2048 + t * 1024 + lane * 16) = g[t];
      }
      u32x4 x;
      if (w < 2) x = fold(tr_frag(wl + W_STE + (8 * h + ((lane >> 2) & 3)) * 64 + ((lane >> 4) & 1) * 32 + (lane & 3) * 8, 256));
      else {
        const unsigned m = (lane & 3) == 0 && ((lane >> 2) & 1) == ((lane >> 4) & 1) ? ~0u : 0u;
        x = (u32x4){xa[0] & m, xa[1] & m, xb[0] & m, xb[1] & m};
      }
      if (w < 6) LDS(u32x4, L_WJ + w * 1024 + lane * 16) = x;
    }
    if (tid >= 256 && tid < 256 + 26 * 4) {
      const int t = tid - 256, row = t >> 2, sx = (t >> 1) & 1, hh = t & 1;
      const int x = row < DUMMY ? row - 16 * sx - 4 * hh : -1;
      u32x4 v;
      v[0] = x == 0 ? 0x3f80u : x == 1 ? 0x3f800000u : 0u;  v[1] = x == 2 ? 0x3f80u : x == 3 ? 0x3f800000u : 0u;
      v[2] = x == 8 ? 0x3f80u : x == 9 ? 0x3f800000u : 0u;  v[3] = x == 10 ? 0x3f80u : x == 11 ? 0x3f800000u : 0u;
      LDS(u32x4, L_LUT + row * 80 + (t & 3) * 16) = v;
    }
    {
      const u32x4 z4 = {0u, 0u, 0u, 0u};
#pragma unroll
      for (int i = 0; i < 3; i++) { const int o = i * 1024 + lane * 16; if (o < 26 * 80 + 256) LDS(u32x4, wl + W_SI + o) = z4; }
    }
    barrier_lds();
    STAMP(14);
  }

  f32x16 Y0, Y1; f32x4 Yp = {0.f, 0.f, 0.f, 0.f};
#pragma unroll
  for (int i = 0; i < 16; i++) { Y0[i] = 0.f; Y1[i] = 0.f; }
  u32x32 W2; u32x16 PA; u32x8 W3; u32x4 WX0, WX1, WX2, WX3;
  int jbase = 0, part = -1, grp = w, gother = w;

  auto round = [&](bool first) __attribute__((always_inline)) -> bool {
    const int lane = lane_id(), tid = w * 64 + lane, r = lane & 31, h = lane >> 5;
    const unsigned zr = opaque(0);
    if (!first) { LDS(unsigned, L_CNT + tid * 4) = zr; barrier_lds(); }
    LDS(unsigned, L_BASE + lane * 4) = 0x40000000u;
    unsigned tb[4];
    {
      u32x2 t;
      for (int spin = 0;; spin++) {
        asm volatile("buffer_load_dwordx2 %0, %1, %2, %3 offen sc1\n\ts_waitcnt vmcnt(0)"
                     : "=&v"(t) : "v"((w * 32 + r) * (NBK * 8)), "s"(rs_tab), "s"(bkt * 8) : "memory");
        if (__builtin_amdgcn_ballot_w64(t[1] != tag_hi || (t[0] >> 24) != tag_lo) == 0) break;
        if (spin > (1 << 20)) { t[0] = 0; break; }
        __builtin_amdgcn_s_sleep(POLL_SLEEP);
      }
      LDS(unsigned, L_WAVE + w * WSZ + W_STE + r * 4) = t[0];
      const u32x4 t0 = LDS(u32x4, L_WAVE + w * WSZ + W_STE + (lane >> 3) * 16);
#pragma unroll
      for (int p = 0; p < 4; p++) tb[p] = t0[p];
    }
    STAMP(8);
    u32x4 gr[6];
    unsigned ad[6], left[6], eb[6];
    unsigned lmax = 0, nov = 0;
    bool big;
    {
      const unsigned jj = lane & (LG - 1), c0 = (unsigned)tid / LG * 4, oob = 0x80000000u + zr;
      const unsigned o0 = mul24(c0, CHS) + (jbase / 2 + jj) * 16, eb0 = mul24(c0, CH);
      unsigned off[4], len[4];
#pragma unroll
      for (int p = 0; p < 4; p++) {
        len[p] = (tb[p] >> 12) & 0xfffu;
        if (part >= 0 && part != 4 * p + (w & 3)) len[p] = 0;
        off[p] = o0 + (tb[p] & 0xfffu) * 16 + p * CHS;
        left[p] = len[p] - (jbase + 2 * jj);
        ad[p] = (int)left[p] > 0 ? off[p] : oob;
        eb[p] = eb0 + p * CH;
        asm volatile("buffer_load_dwordx4 %0, %1, %2, 0 offen sc1" : "=v"(gr[p]) : "v"(ad[p]), "s"(rs_rec) : "memory");
        lmax = max(lmax, tb[p] & 0xfff000u);
      }
      asm volatile("" : "+v"(len[0]), "+v"(len[1]), "+v"(len[2]), "+v"(len[3]));
      ad[4] = oob; ad[5] = oob; left[4] = zr; left[5] = zr; eb[4] = zr; eb[5] = zr;
      unsigned ov[4], s1 = 0;
#pragma unroll
      for (int p = 0; p < 4; p++) {
        ov[p] = (unsigned)min(max(((int)len[p] - (jbase + 2 * LG) + 1) >> 1, 0), LG);
        const bool sel = jj - nov < ov[p];
        ad[4] = sel ? off[p] + (LG - nov) * 16 : ad[4];
        left[4] = sel ? len[p] - (jbase + 2 * LG) - 2 * (jj - nov) : left[4];
        eb[4] = sel ? eb0 + p * CH : eb[4];
        nov += ov[p];
      }
      asm volatile("buffer_load_dwordx4 %0, %1, %2, 0 offen sc1" : "=v"(gr[4]) : "v"(ad[4]), "s"(rs_rec) : "memory");
      gr[5] = (u32x4){zr, zr, zr, zr};
      big = __builtin_amdgcn_ballot_w64(nov > LG) != 0;
      if (big) {
#pragma unroll
        for (int p = 0; p < 4; p++) {
          const bool sel = jj + LG - s1 < ov[p];
          ad[5] = sel ? off[p] + (2 * LG - s1) * 16 : ad[5];
          left[5] = sel ? len[p] - (jbase + 2 * LG) - 2 * (jj + LG - s1) : left[5];
          eb[5] = sel ? eb0 + p * CH : eb[5];
          s1 += ov[p];
        }
        asm volatile("buffer_load_dwordx4 %0, %1, %2, 0 offen sc1" : "+v"(gr[5]) : "v"(ad[5]), "s"(rs_rec) : "memory");
      }
    }
    unsigned sink = zr;
    STAMP(7);
    u32x4 w1f[8];
    if (first) {
#pragma unroll
      for (int i = 0; i < 8; i++) w1f[i] = LDS(u32x4, L_WAVE + (4 + (i >> 1)) * WSZ + 2048 + (i & 1) * 1024 + lane * 16);
#pragma unroll
      for (int i = 0; i < 8; i++) {
        const u32x4 v = LDS(u32x4, L_WAVE + (i >> 1) * WSZ + 2048 + (i & 1) * 1024 + lane * 16);
#pragma unroll
        for (int j = 0; j < 4; j++) W2[4 * i + j] = v[j];
      }
#pragma unroll
      for (int i = 0; i < 2; i++) {
        const u32x4 v = LDS(u32x4, L_WJ + i * 1024 + lane * 16);
#pragma unroll
        for (int j = 0; j < 4; j++) W3[4 * i + j] = v[j];
      }
      WX0 = LDS(u32x4, L_WJ + 2048 + lane * 16); WX1 = LDS(u32x4, L_WJ + 3072 + lane * 16);
      WX2 = LDS(u32x4, L_WJ + 4096 + lane * 16); WX3 = LDS(u32x4, L_WJ + 5120 + lane * 16);
    }
    STAMP(13);
    if ((lmax >> 12) > (unsigned)jbase + 4 * LG || nov > 2 * LG) __hip_atomic_fetch_or(&LDS(unsigned, L_FLAG), nov > 2 * LG ? 3u : 1u, __ATOMIC_RELAXED, __HIP_MEMORY_SCOPE_WORKGROUP);
    unsigned mk1 = (lane & 7) >= 1 ? ~zr : zr, mk2 = (lane & 7) >= 2 ? ~zr : zr, sg0 = L_REC + (lane >> 3) * SEG;
    unsigned lim = L_REC + 8 * SEG + lane * 8, own = n0 + min(8 * (r & 3) + (r >> 2), 24) * 8;
    unsigned ce = ((lane & 1) ^ ((lane >> 4) & 1)) * 16, cp = (lane & 1) * 2;
    unsigned cc0 = ((lane & 7) ^ (lane >> 4)) * 16, cc1 = ((lane & 7) ^ (4 + (lane >> 4))) * 16;
    unsigned q_s4 = L_REC + (lane >> 3) * 32 + 4;
    unsigned q_r2 = L_REC + (4 * ((lane >> 1) & 7) + (lane >> 4)) * 8;
    unsigned q_r1 = L_REC + (4 * (r & 7) + (r >> 3)) * 8 + 4;
    asm volatile("" : "+v"(mk1), "+v"(mk2), "+v"(sg0), "+v"(lim), "+v"(own), "+v"(ce), "+v"(cp), "+v"(cc0), "+v"(cc1), "+v"(q_s4),
                 "+v"(q_r2), "+v"(q_r1));
    asm volatile("s_waitcnt vmcnt(0)" : "+v"(gr[0]), "+v"(gr[1]), "+v"(gr[2]), "+v"(gr[3]), "+v"(gr[4]), "+v"(gr[5]) :: "memory");
    for (int spin = 0;; spin++) {
      bool bad = false;
#pragma unroll
      for (int p = 0; p < 6; p++) bad |= ((int)left[p] > 0) & ((gr[p][3] != tag_hi) | ((gr[p][2] >> 24) != tag_lo));
      if (__builtin_amdgcn_ballot_w64(bad) == 0 || spin > (1 << 20)) break;
      asm volatile("buffer_load_dwordx4 %0, %6, %12, 0 offen sc1\n\tbuffer_load_dwordx4 %1, %7, %12, 0 offen sc1\n\t"
                   "buffer_load_dwordx4 %2, %8, %12, 0 offen sc1\n\tbuffer_load_dwordx4 %3, %9, %12, 0 offen sc1\n\t"
                   "buffer_load_dwordx4 %4, %10, %12, 0 offen sc1\n\tbuffer_load_dwordx4 %5, %11, %12, 0 offen sc1\n\ts_waitcnt vmcnt(0)"
                   : "=&v"(gr[0]), "=&v"(gr[1]), "=&v"(gr[2]), "=&v"(gr[3]), "=&v"(gr[4]), "=&v"(gr[5])
                   : "v"(ad[0]), "v"(ad[1]), "v"(ad[2]), "v"(ad[3]), "v"(ad[4]), "v"(ad[5]), "s"(rs_rec) : "memory");
    }
    STAMP(1);
    u32x2 rc[12];
#pragma unroll
    for (int p = 0; p < 6; p++) {
      rc[2 * p] = (u32x2){(eb[p] + (gr[p][2] & 0xfffu)) << 5, (int)left[p] > 0 ? gr[p][0] : 0u};
      rc[2 * p + 1] = (u32x2){(eb[p] + ((gr[p][2] >> 12) & 0xfffu)) << 5, (int)left[p] > 1 ? gr[p][1] : 0u};
    }
    unsigned rk[12], ky[12];
#pragma unroll
    for (int p = 0; p < 12; p++) {
      if (p == 10 && !big) { ky[10] = ky[11] = 0; rk[10] = rk[11] = 0; break; }
      ky[p] = max((rc[p][1] >> 9) & 0x7fcu, (unsigned)lane * 4);
      rk[p] = __hip_atomic_fetch_add(&LDS(unsigned, L_CNT + ky[p]), 8u + zr, __ATOMIC_RELAXED, __HIP_MEMORY_SCOPE_WORKGROUP);
    }
    if (EPF) {
#pragma unroll
      for (int p = 0; p < 10; p++) {
        const unsigned a = (ky[p] & 0x47cu) - 0x400u < (unsigned)EPF * 4 ? rc[p][0] : 0x80000000u + zr;
        asm volatile("buffer_load_dword %0, %1, %2, 0 offen" : "+{v3}"(sink) : "v"(a), "s"(rs_ea) : "memory");
      }
    }
    barrier_lds();
    STAMP(2);
    unsigned g;
    const unsigned flagv = LDS(unsigned, L_FLAG);
    {
      const u32x4 cv = LDS(u32x4, L_CNT + 1024 + lane * 16);
      const unsigned sum = cv[0] + cv[1] + cv[2] + cv[3];
      g = sum + (__builtin_amdgcn_update_dpp(0, (int)sum, 0x111, 0xf, 0xf, true) & mk1);
      g += __builtin_amdgcn_update_dpp(0, (int)g, 0x112, 0xf, 0xf, true) & mk2;
      g += __builtin_amdgcn_update_dpp(0, (int)g, 0x114, 0xf, 0xa, false);
      const unsigned ex = g - sum + sg0;
      const u32x4 bv = {ex, ex + cv[0], ex + cv[0] + cv[1], ex + cv[0] + cv[1] + cv[2]};
      LDS(u32x4, L_BASE + 1024 + lane * 16) = bv;
    }
    unsigned bs[10];
#pragma unroll
    for (int p = 0; p < 10; p++) bs[p] = LDS(unsigned, L_BASE + ky[p]);
    const unsigned flag = __builtin_amdgcn_readfirstlane(flagv);
    const bool again = flag & 1;
    const bool over = (flag & 2) || __builtin_amdgcn_ballot_w64(g > (unsigned)(SEG - 256)) != 0;
    if (!over) {
#pragma unroll
      for (int p = 0; p < 10; p++) LDS(u32x2, min(bs[p] + rk[p], lim)) = rc[p];
      if (big) {
        LDS(u32x2, min(LDS(unsigned, L_BASE + ky[10]) + rk[10], lim)) = rc[10];
        LDS(u32x2, min(LDS(unsigned, L_BASE + ky[11]) + rk[11], lim)) = rc[11];
      }
    }
    if (first) {
      const unsigned key = (g << 3) | (lane >> 3);
      unsigned ks[8], rank = 0;
#pragma unroll
      for (int k = 0; k < 8; k++) ks[k] = __builtin_amdgcn_readlane(key, 8 * k + 7);
#pragma unroll
      for (int k = 0; k < 8; k++) rank += ks[k] > key;
      const unsigned long long sel = 0x8080808080808080ull;
      grp = __builtin_ctzll(__builtin_amdgcn_ballot_w64(rank == (unsigned)(w < 4 ? w : 11 - w)) & sel) >> 3;
      gother = __builtin_ctzll(__builtin_amdgcn_ballot_w64(rank == (unsigned)(w < 4 ? 7 - w : w - 4)) & sel) >> 3;
      wl = L_WAVE + grp * WSZ; mi = L_MI + grp * MISZ;
    }
    unsigned segn = over ? 0u : (unsigned)__builtin_amdgcn_readlane(g, 8 * grp + 7);
    const unsigned seg0 = grp * SEG;
    const int nt = (segn + 255) >> 8;
    {
      const u32x2 pad = {zr, DUMMY * (80u << 20)}, row = {zr, min(own + grp, (unsigned)N - 1) | (DUMMY * (80u << 20))};
      LDS(u32x2, L_REC + seg0 + segn + r * 8) = pad;
      LDS(u32x2, L_REC + seg0 + nt * 256 + r * 8) = row;
    }
    if (lane == 0) LDS(unsigned, wl + W_SI + DUMMY * 80 + 76) = nt;
    barrier_lds();
    STAMP(3);

    const unsigned o_s4 = q_s4 + seg0, o_r2 = q_r2 + seg0, o_r1 = q_r1 + seg0;
    {
      unsigned ns[4];
#pragma unroll
      for (int i = 0; i < 4; i++) ns[i] = LDS(uint16_t, o_s4 + i * 8);
      const u32x2 nr2 = LDS(u32x2, o_r2);
      dma_tile(hmat, ea, pos, ns[0] * 128 + cc0, ns[1] * 128 + cc1, ns[2] * 128 + cc0, ns[3] * 128 + cc1,
               nr2[0] | ce, (nr2[1] & 0xffffu) * 6 + cp, wl);
    }
    if (first) {
      u32x2 hb[8];
#pragma unroll
      for (int i = 0; i < 8; i++) hb[i] = LDS(u32x2, mi + r * 128 + ((i ^ ((r >> 1) & 7)) * 16) + h * 8);
      const u32x2 pg = LDS(u32x2, mi + 4096 + r * 8);
#pragma unroll
      for (int t = 0; t < 2; t++) {
        f32x16 P;
#pragma unroll
        for (int i = 0; i < 16; i++) P[i] = 0.f;
#pragma unroll
        for (int s = 0; s < 4; s++) {
          const u32x4 a = {hb[2 * s][0], hb[2 * s][1], hb[2 * s + 1][0], hb[2 * s + 1][1]};
          P = MFMA(as_bf(a), as_bf(w1f[2 * s + t]), P);
        }
        if (h) P[15] = bf2f(t ? wdv1 : wdv0);
#pragma unroll
        for (int i = 0; i < 16; i++) P[i] *= -LOG2E;
#pragma unroll
        for (int s = 0; s < 2; s++)
#pragma unroll
          for (int j = 0; j < 4; j++) PA[4 * (2 * s + t) + j] = pk_bf16(P[8 * s + 2 * j], P[8 * s + 2 * j + 1]);
      }
      const f32x4 pv = {bf2f(pg[0] & 0xffffu), __uint_as_float(pg[0] & 0xffff0000u), __uint_as_float(pg[1] & 0xffff0000u), 0.f};
      if (lane < DUMMY) LDS(f32x4, wl + W_SI + lane * 80 + 64) = pv;
      if (bkt == 0 && tid == 0) {
        const u32x2 e1 = {tag_lo | (tag_hi << 8), tag_hi >> 24};
        asm volatile("global_store_dwordx2 %0, %1, %2 sc1" :: "v"(zr), "v"(e1), "s"(ws + WS_EPOCH) : "memory");
      }
    }
    const unsigned nother = (__builtin_amdgcn_readlane(g, 8 * gother + 7) + 255) >> 8;
    const bool last_round = !over && !again && (part < 0 || part == 15);
    STAMP(4);

    {
      u32x16 A0; u32x8 A1;
      A0[0] = zr; A0[1] = zr;
#pragma unroll
      for (int s = 0; s < 4; s++) A0[2 + s] = wl + W_STH + r * 128 + (((2 * s + h) ^ ((r >> 1) & 7)) * 16);
      A0[6] = wl + r * 32 + ((h ^ ((r >> 3) & 1)) * 16);
      A0[7] = wl + r * 8;
      A0[8] = L_LUT + h * 16;
      A0[9] = wl + r * 2;
      A0[10] = wl + min(r, DUMMY) * 80 + h * 16;
      A0[11] = mi + r * MIL + h * 32;
      A0[12] = mi + (8 * h + ((lane >> 2) & 3)) * MIL + (4 * ((lane >> 4) & 1) + (lane & 3)) * 8;
      A0[13] = wl + h * 64 + r * 2;
      {
        const int row = (lane & 15) - 4 * ((lane >> 4) & 1);
        A0[14] = wl + ((unsigned)row < 3 ? row : 3) * 64 + h * 16;
      }
      A0[15] = L_WJ + lane * 16;
      A1[0] = cc0; A1[1] = cc1; A1[2] = ce; A1[3] = cp;
      A1[4] = o_s4 + 256; A1[5] = o_r2 + 256; A1[6] = o_r1;
      A1[7] = 0x3f800000u + zr;
      unsigned cnt = nt, keep;
#define PRIO_A 14
#define PRIO_B 7
      const int lastv = __builtin_amdgcn_readfirstlane(opaque(last_round ? 1 : 0));
      const int thr = DYNP_BIAS;
      u32x2 A2 = {wl + W_SI + DUMMY * 80 + 76, (unsigned)(L_WAVE + gother * WSZ + W_SI + DUMMY * 80 + 76)};
      unsigned tmp;
      asm volatile(LOOP_ASM
                   : "+{v[52:67]}"(Y0), "+{v[68:83]}"(Y1), "+{v[84:87]}"(Yp), "+{v[232:247]}"(A0), "+{v[248:255]}"(A1), "+{v3}"(sink),
                     "+{v[212:213]}"(A2), [tmp] "=&s"(tmp),
                     [nt] "+s"(cnt), [keep] "=&s"(keep)
                   : "{v[4:35]}"(W2), "{v[36:51]}"(PA), "{v[88:95]}"(W3), "{v[96:99]}"(WX0), "{v[136:139]}"(WX1), "{v[140:143]}"(WX2), "{v[144:147]}"(WX3),
                     [h0] "s"(hmat), [h1] "s"(hmat - 1024), [h2] "s"(hmat - 2048), [h3] "s"(hmat - 3072), [ea] "s"(ea), [pos] "s"(pos - 1024),
                     [b1] "s"(wl), [b2] "s"(wl + W_STE), [posd] "s"(wl + W_SI + 64), [c128] "s"(128u), [thr] "s"(thr), [last] "s"(lastv),
                     [hsel] "s"(0xffffffff00000000ull)
                   : "memory", "scc", "vcc", CLOBBER_V);
    }
    if (over) part = 0;
    else if (part >= 0 && part < 15) part++;
    else { part = -1; jbase += 4 * LG; }
    return last_round;
  };
  if (!round(true)) {
    do {
      wait_vm0();
      barrier_lds();
      jbase = __builtin_amdgcn_readfirstlane(jbase); part = __builtin_amdgcn_readfirstlane(part);
    } while (!round(false));
  }
  STAMP(5);

  {
    const int lane = lane_id(), r = lane & 31, h = lane >> 5;
    Yp[0] *= -LN2; Yp[1] *= -LN2; Yp[2] *= -LN2;
    const auto rs_o = __builtin_amdgcn_make_buffer_rsrc((void*)out, 0, N * 128, 0x00020000);
#pragma unroll
    for (int i = 0; i < 4; i++) {
      const int q = 8 * i + (lane >> 3), dl = q * 8 + grp;
      const u32x4 v = LDS(u32x4, mi + q * MIP + (lane & 7) * 16);
      if (dl < NB) __builtin_amdgcn_raw_buffer_store_b128(v, rs_o, (n0 + dl) * 128 + (lane & 7) * 16, 0, OUT_AUX);
    }
    const int dl = r * 8 + grp;
    if (h == 0 && dl < NB && n0 + dl < N) {
      const f32x4 pd = LDS(f32x4, wl + W_SI + r * 80 + 64);
      uint16_t* po = out + (size_t)N * 64 + (size_t)(n0 + dl) * 3;
      const unsigned xy = pk_bf16(pd[0] + Yp[0], pd[1] + Yp[1]), zz = pk_bf16(pd[2] + Yp[2], 0.f);
      po[0] = (uint16_t)xy; po[1] = (uint16_t)(xy >> 16); po[2] = (uint16_t)zz;
    }
  }
  STAMP(6);
}

}

#define STAMP_PASS1
#define STAMP_PASS2

extern "C" void kernel_launch(void* const* d_in, const int* in_sizes, int n_in, void* d_out, int out_size,
                              void* d_ws, size_t ws_size, hipStream_t stream) {
  const int N = in_sizes[0] / 64, E = in_sizes[3];
  if (N > NBK * NB || E > NBK * CH || N < 1 || E < 1) return;
  (void)hipFuncSetAttribute((const void*)egnn_kernel, hipFuncAttributeMaxDynamicSharedMemorySize, L_TOTAL);
  egnn_kernel<<<NBK, 512, L_TOTAL, stream>>>((const char*)d_in[0], (const char*)d_in[1], (const char*)d_in[2], (const int*)d_in[3],
                                             (const int*)d_in[4], (const char*)d_in[5], (const char*)d_in[6], (const char*)d_in[7],
                                             (char*)d_ws, (uint16_t*)d_out, N, E STAMP_PASS2);
}
```

```cpp
#include <hip/hip_runtime.h>
#include <stdint.h>
#include <type_traits>

#define FOLD_LOG2E 1

namespace {

typedef __bf16 bf16x8 __attribute__((ext_vector_type(8)));
typedef __bf16 bf16x2 __attribute__((ext_vector_type(2)));
typedef float f32x16 __attribute__((ext_vector_type(16)));
typedef float f32x4 __attribute__((ext_vector_type(4)));
typedef unsigned u32x4 __attribute__((ext_vector_type(4)));
typedef unsigned u32x2 __attribute__((ext_vector_type(2)));
typedef unsigned u32x8 __attribute__((ext_vector_type(8)));
typedef unsigned u32x16 __attribute__((ext_vector_type(16)));
typedef unsigned u32x32 __attribute__((ext_vector_type(32)));
typedef short s16x4 __attribute__((ext_vector_type(4)));
typedef __attribute__((address_space(3))) s16x4* lds_s16x4_p;

#define DI __device__ __forceinline__
#define MFMA(a, b, c) __builtin_amdgcn_mfma_f32_32x32x16_bf16((a), (b), (c), 0, 0, 0)

#define POLL_SLEEP 2
#define EPF 2
#define OUT_AUX 16
constexpr int NBK = 256;
constexpr int LG = 8;
constexpr int NB = 196;
constexpr int CH = 3136;
constexpr float LOG2E = 1.4426950408889634f;
constexpr float LN2 = 0.6931471805599453f;

constexpr size_t WS_EPOCH = 0;
constexpr size_t WS_TAB = 256;
constexpr size_t WS_REC = WS_TAB + (size_t)NBK * NBK * 8;
constexpr int CHS = (CH + NBK) * 8;

constexpr int W_STH = 0, W_STE = 4096, W_STP = 5120, W_SI = 5376, W_TI = W_SI + 26 * 80, WSZ = W_TI + 256;
constexpr int DUMMY = 25;
constexpr int MIP = 144, MISZ = 32 * MIP;
constexpr int MIL = 80;
constexpr int SEG = 17 * 256;
constexpr int L_CNT = 0;
constexpr int L_BASE = L_CNT + 2048;
constexpr int L_FLAG = L_CNT + 512;
constexpr int L_REC = L_BASE + 2048;
constexpr int L_LUT = L_REC + 8 * SEG + 512;
constexpr int L_WAVE = L_LUT + 26 * 80;
constexpr int L_MI = L_WAVE + 8 * WSZ;
constexpr int L_WJ = L_MI + 8 * MISZ;
constexpr int L_WH = L_WJ + 6 * 1024;
constexpr int L_TOTAL = L_WH + 16 * 1024;
static_assert(L_TOTAL <= 163840, "LDS");
static_assert(WSZ % 16 == 0 && L_MI % 16 == 0 && L_LUT % 16 == 0, "align");

#pragma clang diagnostic ignored "-Wint-to-pointer-cast"
#define LDS(T, off) (*(__attribute__((address_space(3))) T*)(unsigned)(off))
DI unsigned mul24(unsigned a, unsigned b) { return (unsigned)__umul24(a, b); }
DI float bf2f(unsigned lo16) { return __uint_as_float(lo16 << 16); }
DI unsigned pk_bf16(float a, float b) { bf16x2 v = {(__bf16)a, (__bf16)b}; return __builtin_bit_cast(unsigned, v); }
DI bf16x8 as_bf(u32x4 v) { return __builtin_bit_cast(bf16x8, v); }
DI int opaque(int x) { asm volatile("" : "+v"(x)); return x; }
DI int lane_id() {
  int x; asm volatile("v_mbcnt_lo_u32_b32 %0, -1, 0\n\tv_mbcnt_hi_u32_b32 %0, -1, %0" : "=v"(x)); return x;
}
DI void wait_vm0() { asm volatile("s_waitcnt vmcnt(0)" ::: "memory"); }
DI void barrier_lds() { asm volatile("s_waitcnt lgkmcnt(0)\n\ts_barrier" ::: "memory"); }
DI void dma_tile(const void* hm, const void* ea, const void* ps, unsigned o0, unsigned o1, unsigned o2, unsigned o3,
                 unsigned oe, unsigned op, unsigned l) {
  unsigned keep;
  asm volatile("s_mov_b32 %0, m0\n\t"
               "s_add_u32 m0, %10, 0x1000\n\ts_nop 0\n\tglobal_load_lds_dwordx4 %5, %8\n\t"
               "s_mov_b32 m0, %10\n\ts_nop 0\n\tglobal_load_lds_dwordx4 %1, %7\n\t"
               "s_add_u32 m0, %10, 0x400\n\ts_nop 0\n\tglobal_load_lds_dwordx4 %2, %7\n\t"
               "s_add_u32 m0, %10, 0x800\n\ts_nop 0\n\tglobal_load_lds_dwordx4 %3, %7\n\t"
               "s_add_u32 m0, %10, 0xc00\n\ts_nop 0\n\tglobal_load_lds_dwordx4 %4, %7\n\t"
               "s_add_u32 m0, %10, 0x1400\n\ts_nop 0\n\tglobal_load_lds_dword %6, %9\n\t"
               "s_mov_b32 m0, %0"
               : "=&s"(keep) : "v"(o0), "v"(o1), "v"(o2), "v"(o3), "v"(oe), "v"(op), "s"(hm), "s"(ea), "s"(ps), "s"(l)
               : "memory", "scc");
}
template <int CTRL, int ROWMASK> DI unsigned dpp_add(unsigned x) {
  return x + (unsigned)__builtin_amdgcn_update_dpp(0, (int)x, CTRL, ROWMASK, 0xf, false);
}
DI unsigned wave_scan_incl(unsigned x) {
  x = dpp_add<0x111, 0xf>(x); x = dpp_add<0x112, 0xf>(x); x = dpp_add<0x114, 0xf>(x); x = dpp_add<0x118, 0xf>(x);
  x = dpp_add<0x142, 0xa>(x); x = dpp_add<0x143, 0xc>(x);
  return x;
}
#define STAMP(i)
#define STAMP_ARG

#define CLOBBER_V "v100", "v101", "v102", "v103", "v104", "v105", "v106", "v107", "v108", "v109", "v110", "v111", "v112", "v113", "v114", "v115", "v116", "v117", "v118", "v119", "v120", "v121", "v122", "v123", "v124", "v125", "v126", "v127", "v128", "v129", "v130", "v131", "v132", "v133", "v134", "v135", "v148", "v149", "v150", "v151", "v152", "v153", "v154", "v155", "v156", "v157", "v158", "v159", "v160", "v161", "v162", "v163", "v164", "v165", "v166", "v167", "v168", "v169", "v170", "v171", "v172", "v173", "v174", "v175", "v176", "v177", "v178", "v179", "v180", "v181", "v182", "v183", "v184", "v185", "v186", "v187", "v188", "v189", "v190", "v191", "v192", "v193", "v194", "v195", "v196", "v197", "v198", "v199", "v200", "v201", "v202", "v203", "v204", "v205", "v206", "v207", "v208", "v209", "v210", "v211", "v212", "v213", "v214", "v215", "v216", "v217", "v218", "v219", "v220", "v221", "v222", "v223", "v224", "v225", "v226", "v227", "v228", "v229", "v230", "v231"
#define LOOP_ASM_FOLD \
  "s_mov_b32 %[keep], m0\n\t" \
  "s_mov_b64 vcc, %[hsel]\n\t" \
  "ds_read2_b32 v[220:221], v252 offset0:0 offset1:2\n\t" \
  "ds_read2_b32 v[222:223], v252 offset0:4 offset1:6\n\t" \
  "ds_read_b64 v[226:227], v253\n\t" \
  "ds_read_b32 v232, v254\n\t" \
  "v_mov_b32 v176, 0\n\t" \
  "v_mov_b32 v177, 0\n\t" \
  "v_mov_b32 v178, 0\n\t" \
  "v_mov_b32 v179, 0\n\t" \
  "v_mov_b32 v180, 0\n\t" \
  "v_mov_b32 v181, 0\n\t" \
  "v_mov_b32 v182, 0\n\t" \
  "v_mov_b32 v183, 0\n\t" \
  "v_mov_b32 v184, 0\n\t" \
  "v_mov_b32 v185, 0\n\t" \
  "v_mov_b32 v186, 0\n\t" \
  "v_mov_b32 v187, 0\n\t" \
  "v_mov_b32 v188, 0\n\t" \
  "v_mov_b32 v189, 0\n\t" \
  "v_mov_b32 v190, 0\n\t" \
  "v_mov_b32 v191, 0\n\t" \
  "v_mov_b32 v230, 0\n\t" \
  "v_mov_b32 v231, 0\n\t" \
  "v_mov_b32 v208, 0\n\t" \
  "v_mov_b32 v209, 0\n\t" \
  "v_mov_b32 v210, 0\n\t" \
  "v_mov_b32 v211, 0\n\t" \
  "ds_write_b128 v243, v[184:187] offset:0\n\t" \
  "s_nop 0\n\t" \
  "v_mfma_f32_16x16x32_bf16 v[132:135], v[184:187], v[188:191], 0\n\t" \
  "v_add_u32 v252, 0x100, v252\n\t" \
  "v_add_u32 v253, 0x100, v253\n\t" \
  "v_add_u32 v254, 0x100, v254\n\t" \
  "s_waitcnt lgkmcnt(0)\n\t" \
  "v_lshrrev_b32 v232, 20, v232\n\t" \
  "v_add_u32 v233, %[posd], v232\n\t" \
  "ds_read_b128 v[216:219], v233\n\t" \
  "v_add_u32 v233, v240, v232\n\t" \
  "ds_read_b128 v[172:175], v233 offset:32\n\t" \
  "ds_read_b128 v[168:171], v233\n\t" \
  "v_add_u32 v233, v241, v232\n\t" \
  "ds_write_b16_d16_hi v233, v255 offset:5376\n\t" \
  "v_mad_u32_u16 v220, v220, %[c128], v248\n\t" \
  "v_mad_u32_u16 v221, v221, %[c128], v249\n\t" \
  "v_mad_u32_u16 v222, v222, %[c128], v248\n\t" \
  "v_mad_u32_u16 v223, v223, %[c128], v249\n\t" \
  "v_or_b32 v226, v226, v250\n\t" \
  "v_mad_u32_u16 v227, v227, 6, v251\n\t" \
  "s_waitcnt vmcnt(0)\n\t" \
  "ds_read_b128 v[148:151], v234\n\t" \
  "ds_read_b128 v[152:155], v235\n\t" \
  "ds_read_b128 v[156:159], v236\n\t" \
  "ds_read_b128 v[160:163], v237\n\t" \
  "ds_read_b128 v[164:167], v238 offset:4096\n\t" \
  "ds_read_b64 v[224:225], v239 offset:5120\n\t" \
  "s_waitcnt lgkmcnt(0)\n\t" \
  "s_cmp_lt_i32 %[nt], 1\n\t" \
  "s_cbranch_scc1 7f\n\t" \
  "s_mov_b32 m0, %[b2]\n\t" \
  "s_nop 0\n\t" \
  "global_load_lds_dwordx4 v226, %[ea]\n\t" \
  "global_load_lds_dword v227, %[pos] offset:1024\n\t" \
  "s_mov_b32 m0, %[b1]\n\t" \
  "s_nop 0\n\t" \
  "global_load_lds_dwordx4 v220, %[h0]\n\t" \
  "global_load_lds_dwordx4 v221, %[h1] offset:1024\n\t" \
  "global_load_lds_dwordx4 v222, %[h2] offset:2048\n\t" \
  "global_load_lds_dwordx4 v223, %[h3] offset:3072\n\t" \
  "7:\n\t" \
  "ds_read2_b32 v[220:221], v252 offset0:0 offset1:2\n\t" \
  "ds_read2_b32 v[222:223], v252 offset0:4 offset1:6\n\t" \
  "ds_read_b64 v[226:227], v253\n\t" \
  "ds_read_b32 v232, v254\n\t" \
  "v_add_u32 v252, 0x100, v252\n\t" \
  "v_add_u32 v253, 0x100, v253\n\t" \
  "v_add_u32 v254, 0x100, v254\n\t" \
  "v_and_b32 v225, 0xffff0000, v225\n\t" \
  "v_lshlrev_b32 v219, 16, v224\n\t" \
  "v_and_b32 v224, 0xffff0000, v224\n\t" \
  "v_sub_f32 v229, v218, v225\n\t" \
  "v_sub_f32 v216, v216, v219\n\t" \
  "v_sub_f32 v217, v217, v224\n\t" \
  "v_mul_f32 v218, v229, v229\n\t" \
  "v_cndmask_b32 v228, v216, v217, vcc\n\t" \
  "v_fmac_f32 v218, v216, v216\n\t" \
  "v_fmac_f32 v218, v217, v217\n\t" \
  "v_cvt_pk_bf16_f32 v218, v218, v218\n\t" \
  "v_cndmask_b32_sdwa v175, v175, v218, vcc dst_sel:WORD_1 dst_unused:UNUSED_PRESERVE src0_sel:WORD_1 src1_sel:WORD_1\n\t" \
  "v_mfma_f32_32x32x16_bf16 v[100:115], v[4:7], v[148:151], 0\n\t" \
  "v_mfma_f32_32x32x16_bf16 v[100:115], v[12:15], v[152:155], v[100:115]\n\t" \
  "v_mfma_f32_32x32x16_bf16 v[100:115], v[20:23], v[156:159], v[100:115]\n\t" \
  "v_mfma_f32_32x32x16_bf16 v[100:115], v[28:31], v[160:163], v[100:115]\n\t" \
  "v_mfma_f32_32x32x16_bf16 v[100:115], v[36:39], v[168:171], v[100:115]\n\t" \
  "v_mfma_f32_32x32x16_bf16 v[100:115], v[88:91], v[164:167], v[100:115]\n\t" \
  "v_mfma_f32_32x32x16_bf16 v[100:115], v[44:47], v[172:175], v[100:115]\n\t" \
  "s_cmp_eq_u32 %[nt], 0\n\t" \
  "s_nop 10\n\t" \
  "v_exp_f32 v192, v100\n\t" \
  "v_exp_f32 v193, v101\n\t" \
  "v_exp_f32 v194, v102\n\t" \
  "v_exp_f32 v195, v103\n\t" \
  "s_nop 0\n\t" \
  "v_add_f32 v192, 1.0, v192\n\t" \
  "v_add_f32 v193, 1.0, v193\n\t" \
  "v_add_f32 v194, 1.0, v194\n\t" \
  "v_add_f32 v195, 1.0, v195\n\t" \
  "s_cbranch_scc1 6f\n\t" \
  "s_cmp_lt_i32 %[thr], 0\n\t" \
  "s_cbranch_scc1 1f\n\t" \
  "s_setprio 1\n\t" \
  "1:\n\t" \
  "ds_write_b128 v243, v[188:191] offset:16\n\t" \
  "ds_read_b64_tr_b16 v[200:201], v244 offset:0\n\t" \
  "ds_read_b64_tr_b16 v[202:203], v244 offset:320\n\t" \
  "ds_read_b64_tr_b16 v[204:205], v244 offset:1280\n\t" \
  "ds_read_b64_tr_b16 v[206:207], v244 offset:1600\n\t" \
  "v_mfma_f32_16x16x32_bf16 v[132:135], v[144:147], v[188:191], v[132:135]\n\t" \
  "v_rcp_f32 v192, v192\n\t" \
  "v_rcp_f32 v193, v193\n\t" \
  "v_rcp_f32 v194, v194\n\t" \
  "v_rcp_f32 v195, v195\n\t" \
  "v_mfma_f32_32x32x16_bf16 v[116:131], v[8:11], v[148:151], 0\n\t" \
  "v_exp_f32 v196, v104\n\t" \
  "v_exp_f32 v197, v105\n\t" \
  "v_exp_f32 v198, v106\n\t" \
  "v_exp_f32 v199, v107\n\t" \
  "v_mfma_f32_32x32x16_bf16 v[116:131], v[16:19], v[152:155], v[116:131]\n\t" \
  "v_mul_f32 v192, v100, v192\n\t" \
  "v_mul_f32 v193, v101, v193\n\t" \
  "v_mul_f32 v194, v102, v194\n\t" \
  "v_mul_f32 v195, v103, v195\n\t" \
  "v_add_f32 v196, 1.0, v196\n\t" \
  "v_add_f32 v197, 1.0, v197\n\t" \
  "v_add_f32 v198, 1.0, v198\n\t" \
  "v_add_f32 v199, 1.0, v199\n\t" \
  "v_cvt_pk_bf16_f32 v184, v192, v193\n\t" \
  "v_cvt_pk_bf16_f32 v185, v194, v195\n\t" \
  "v_mfma_f32_32x32x16_bf16 v[116:131], v[24:27], v[156:159], v[116:131]\n\t" \
  "v_rcp_f32 v196, v196\n\t" \
  "v_rcp_f32 v197, v197\n\t" \
  "v_rcp_f32 v198, v198\n\t" \
  "v_rcp_f32 v199, v199\n\t" \
  "s_waitcnt lgkmcnt(2)\n\t" \
  "v_mfma_f32_32x32x16_bf16 v[68:83], v[200:203], v[176:179], v[68:83]\n\t" \
  "v_exp_f32 v192, v108\n\t" \
  "v_exp_f32 v193, v109\n\t" \
  "v_exp_f32 v194, v110\n\t" \
  "v_exp_f32 v195, v111\n\t" \
  "v_mfma_f32_32x32x16_bf16 v[116:131], v[32:35], v[160:163], v[116:131]\n\t" \
  "v_mul_f32 v196, v104, v196\n\t" \
  "v_mul_f32 v197, v105, v197\n\t" \
  "v_mul_f32 v198, v106, v198\n\t" \
  "v_mul_f32 v199, v107, v199\n\t" \
  "v_mul_f32 v230, v230, v132\n\t" \
  "v_mul_f32 v231, v231, v132\n\t" \
  "v_cvt_pk_bf16_f32 v230, v230, v231\n\t" \
  "ds_write_b16 v245, v230 offset:7456\n\t" \
  "ds_write_b16_d16_hi v241, v230 offset:7584\n\t" \
  "ds_read_b128 v[200:203], v246 offset:7456\n\t" \
  "ds_read_b128 v[208:211], v246 offset:7488\n\t" \
  "v_mov_b32 v230, v228\n\t" \
  "v_mov_b32 v231, v229\n\t" \
  "v_add_f32 v192, 1.0, v192\n\t" \
  "v_add_f32 v193, 1.0, v193\n\t" \
  "v_add_f32 v194, 1.0, v194\n\t" \
  "v_add_f32 v195, 1.0, v195\n\t" \
  "v_cvt_pk_bf16_f32 v186, v196, v197\n\t" \
  "v_cvt_pk_bf16_f32 v187, v198, v199\n\t" \
  "v_mfma_f32_32x32x16_bf16 v[116:131], v[40:43], v[168:171], v[116:131]\n\t" \
  "v_rcp_f32 v192, v192\n\t" \
  "v_rcp_f32 v193, v193\n\t" \
  "v_rcp_f32 v194, v194\n\t" \
  "v_rcp_f32 v195, v195\n\t" \
  "s_sub_u32 %[nt], %[nt], 1\n\t" \
  "s_cmp_lg_u32 %[nt], %[thr]\n\t" \
  "s_cbranch_scc1 3f\n\t" \
  "s_setprio 0\n\t" \
  "3:\n\t" \
  "v_mfma_f32_32x32x16_bf16 v[116:131], v[92:95], v[164:167], v[116:131]\n\t" \
  "v_exp_f32 v196, v112\n\t" \
  "v_exp_f32 v197, v113\n\t" \
  "v_exp_f32 v198, v114\n\t" \
  "v_exp_f32 v199, v115\n\t" \
  "v_mfma_f32_32x32x16_bf16 v[116:131], v[48:51], v[172:175], v[116:131]\n\t" \
  "v_mul_f32 v192, v108, v192\n\t" \
  "v_mul_f32 v193, v109, v193\n\t" \
  "v_mul_f32 v194, v110, v194\n\t" \
  "v_mul_f32 v195, v111, v195\n\t" \
  "s_waitcnt vmcnt(0)\n\t" \
  "ds_read_b128 v[148:151], v234\n\t" \
  "ds_read_b128 v[152:155], v235\n\t" \
  "ds_read_b128 v[156:159], v236\n\t" \
  "ds_read_b128 v[160:163], v237\n\t" \
  "ds_read_b128 v[164:167], v238 offset:4096\n\t" \
  "ds_read_b64 v[224:225], v239 offset:5120\n\t" \
  "ds_write_b128 v243, v[184:187] offset:0\n\t" \
  "v_add_f32 v196, 1.0, v196\n\t" \
  "v_add_f32 v197, 1.0, v197\n\t" \
  "v_add_f32 v198, 1.0, v198\n\t" \
  "v_add_f32 v199, 1.0, v199\n\t" \
  "v_cvt_pk_bf16_f32 v188, v192, v193\n\t" \
  "v_cvt_pk_bf16_f32 v189, v194, v195\n\t" \
  "v_mad_u32_u16 v220, v220, %[c128], v248\n\t" \
  "v_mad_u32_u16 v221, v221, %[c128], v249\n\t" \
  "v_mad_u32_u16 v222, v222, %[c128], v248\n\t" \
  "s_waitcnt lgkmcnt(11)\n\t" \
  "v_mfma_f32_32x32x16_bf16 v[68:83], v[204:207], v[180:183], v[68:83]\n\t" \
  "v_rcp_f32 v196, v196\n\t" \
  "v_rcp_f32 v197, v197\n\t" \
  "v_rcp_f32 v198, v198\n\t" \
  "v_rcp_f32 v199, v199\n\t" \
  "v_mad_u32_u16 v223, v223, %[c128], v249\n\t" \
  "v_or_b32 v226, v226, v250\n\t" \
  "v_mad_u32_u16 v227, v227, 6, v251\n\t" \
  "s_waitcnt lgkmcnt(8)\n\t" \
  "v_mfma_f32_16x16x32_bf16 v[84:87], v[200:203], v[176:179], v[84:87]\n\t" \
  "v_exp_f32 v192, v116\n\t" \
  "v_exp_f32 v193, v117\n\t" \
  "v_exp_f32 v194, v118\n\t" \
  "v_exp_f32 v195, v119\n\t" \
  "s_waitcnt lgkmcnt(7)\n\t" \
  "v_mfma_f32_16x16x32_bf16 v[84:87], v[208:211], v[180:183], v[84:87]\n\t" \
  "v_mul_f32 v196, v112, v196\n\t" \
  "v_mul_f32 v197, v113, v197\n\t" \
  "v_mul_f32 v198, v114, v198\n\t" \
  "v_mul_f32 v199, v115, v199\n\t" \
  "s_waitcnt lgkmcnt(1)\n\t" \
  "s_cmp_lt_i32 %[nt], 1\n\t" \
  "s_cbranch_scc1 4f\n\t" \
  "s_mov_b32 m0, %[b2]\n\t" \
  "s_nop 0\n\t" \
  "global_load_lds_dwordx4 v226, %[ea]\n\t" \
  "global_load_lds_dword v227, %[pos] offset:1024\n\t" \
  "s_mov_b32 m0, %[b1]\n\t" \
  "s_nop 0\n\t" \
  "global_load_lds_dwordx4 v220, %[h0]\n\t" \
  "global_load_lds_dwordx4 v221, %[h1] offset:1024\n\t" \
  "global_load_lds_dwordx4 v222, %[h2] offset:2048\n\t" \
  "global_load_lds_dwordx4 v223, %[h3] offset:3072\n\t" \
  "4:\n\t" \
  "v_add_f32 v192, 1.0, v192\n\t" \
  "v_add_f32 v193, 1.0, v193\n\t" \
  "v_add_f32 v194, 1.0, v194\n\t" \
  "v_add_f32 v195, 1.0, v195\n\t" \
  "v_cvt_pk_bf16_f32 v190, v196, v197\n\t" \
  "v_cvt_pk_bf16_f32 v191, v198, v199\n\t" \
  "v_lshrrev_b32 v232, 20, v232\n\t" \
  "v_add_u32 v220, %[posd], v232\n\t" \
  "v_add_u32 v221, v240, v232\n\t" \
  "ds_write_b128 v243, v[188:191] offset:16\n\t" \
  "ds_read_b128 v[176:179], v242 offset:5376\n\t" \
  "ds_read_b128 v[180:183], v242 offset:5408\n\t" \
  "ds_write_b16 v233, v255 offset:5376\n\t" \
  "ds_read_b128 v[216:219], v220\n\t" \
  "ds_read_b128 v[172:175], v221 offset:32\n\t" \
  "ds_read_b128 v[168:171], v221\n\t" \
  "ds_read_b64_tr_b16 v[200:201], v244 offset:0\n\t" \
  "ds_read_b64_tr_b16 v[202:203], v244 offset:320\n\t" \
  "ds_read_b64_tr_b16 v[204:205], v244 offset:1280\n\t" \
  "ds_read_b64_tr_b16 v[206:207], v244 offset:1600\n\t" \
  "v_add_u32 v233, v241, v232\n\t" \
  "ds_write_b16_d16_hi v233, v255 offset:5376\n\t" \
  "v_mfma_f32_16x16x32_bf16 v[132:135], v[96:99], v[184:187], 0\n\t" \
  "v_rcp_f32 v192, v192\n\t" \
  "v_rcp_f32 v193, v193\n\t" \
  "v_rcp_f32 v194, v194\n\t" \
  "v_rcp_f32 v195, v195\n\t" \
  "v_mfma_f32_32x32x16_bf16 v[100:115], v[4:7], v[148:151], 0\n\t" \
  "v_exp_f32 v196, v120\n\t" \
  "v_exp_f32 v197, v121\n\t" \
  "v_exp_f32 v198, v122\n\t" \
  "v_exp_f32 v199, v123\n\t" \
  "v_mfma_f32_32x32x16_bf16 v[100:115], v[12:15], v[152:155], v[100:115]\n\t" \
  "v_mul_f32 v192, v116, v192\n\t" \
  "v_mul_f32 v193, v117, v193\n\t" \
  "v_mul_f32 v194, v118, v194\n\t" \
  "v_mul_f32 v195, v119, v195\n\t" \
  "v_add_f32 v196, 1.0, v196\n\t" \
  "v_add_f32 v197, 1.0, v197\n\t" \
  "v_add_f32 v198, 1.0, v198\n\t" \
  "v_add_f32 v199, 1.0, v199\n\t" \
  "v_cvt_pk_bf16_f32 v184, v192, v193\n\t" \
  "v_cvt_pk_bf16_f32 v185, v194, v195\n\t" \
  "v_and_b32 v225, 0xffff0000, v225\n\t" \
  "s_waitcnt lgkmcnt(7)\n\t" \
  "v_lshlrev_b32 v219, 16, v224\n\t" \
  "v_and_b32 v224, 0xffff0000, v224\n\t" \
  "v_sub_f32 v229, v218, v225\n\t" \
  "v_sub_f32 v216, v216, v219\n\t" \
  "v_sub_f32 v217, v217, v224\n\t" \
  "v_mfma_f32_32x32x16_bf16 v[100:115], v[20:23], v[156:159], v[100:115]\n\t" \
  "v_rcp_f32 v196, v196\n\t" \
  "v_rcp_f32 v197, v197\n\t" \
  "v_rcp_f32 v198, v198\n\t" \
  "v_rcp_f32 v199, v199\n\t" \
  "v_mul_f32 v218, v229, v229\n\t" \
  "v_cndmask_b32 v228, v216, v217, vcc\n\t" \
  "v_fmac_f32 v218, v216, v216\n\t" \
  "v_fmac_f32 v218, v217, v217\n\t" \
  "v_cvt_pk_bf16_f32 v218, v218, v218\n\t" \
  "s_waitcnt lgkmcnt(6)\n\t" \
  "v_cndmask_b32_sdwa v175, v175, v218, vcc dst_sel:WORD_1 dst_unused:UNUSED_PRESERVE src0_sel:WORD_1 src1_sel:WORD_1\n\t" \
  "v_mfma_f32_16x16x32_bf16 v[132:135], v[136:139], v[188:191], v[132:135]\n\t" \
  "v_exp_f32 v192, v124\n\t" \
  "v_exp_f32 v193, v125\n\t" \
  "v_exp_f32 v194, v126\n\t" \
  "v_exp_f32 v195, v127\n\t" \
  "ds_read2_b32 v[220:221], v252 offset0:0 offset1:2\n\t" \
  "ds_read2_b32 v[222:223], v252 offset0:4 offset1:6\n\t" \
  "ds_read_b64 v[226:227], v253\n\t" \
  "ds_read_b32 v232, v254\n\t" \
  "v_mfma_f32_32x32x16_bf16 v[100:115], v[28:31], v[160:163], v[100:115]\n\t" \
  "v_mul_f32 v196, v120, v196\n\t" \
  "v_mul_f32 v197, v121, v197\n\t" \
  "v_mul_f32 v198, v122, v198\n\t" \
  "v_mul_f32 v199, v123, v199\n\t" \
  "v_add_u32 v252, 0x100, v252\n\t" \
  "v_add_u32 v253, 0x100, v253\n\t" \
  "v_add_u32 v254, 0x100, v254\n\t" \
  "v_add_f32 v192, 1.0, v192\n\t" \
  "v_add_f32 v193, 1.0, v193\n\t" \
  "v_add_f32 v194, 1.0, v194\n\t" \
  "v_add_f32 v195, 1.0, v195\n\t" \
  "v_cvt_pk_bf16_f32 v186, v196, v197\n\t" \
  "v_cvt_pk_bf16_f32 v187, v198, v199\n\t" \
  "s_waitcnt lgkmcnt(9)\n\t" \
  "v_mfma_f32_32x32x16_bf16 v[100:115], v[36:39], v[168:171], v[100:115]\n\t" \
  "v_rcp_f32 v192, v192\n\t" \
  "v_rcp_f32 v193, v193\n\t" \
  "v_rcp_f32 v194, v194\n\t" \
  "v_rcp_f32 v195, v195\n\t" \
  "v_mfma_f32_32x32x16_bf16 v[100:115], v[44:47], v[172:175], v[100:115]\n\t" \
  "v_exp_f32 v196, v128\n\t" \
  "v_exp_f32 v197, v129\n\t" \
  "v_exp_f32 v198, v130\n\t" \
  "v_exp_f32 v199, v131\n\t" \
  "v_mfma_f32_32x32x16_bf16 v[100:115], v[88:91], v[164:167], v[100:115]\n\t" \
  "v_mul_f32 v192, v124, v192\n\t" \
  "v_mul_f32 v193, v125, v193\n\t" \
  "v_mul_f32 v194, v126, v194\n\t" \
  "v_mul_f32 v195, v127, v195\n\t" \
  "ds_write_b128 v243, v[184:187] offset:0\n\t" \
  "v_add_f32 v196, 1.0, v196\n\t" \
  "v_add_f32 v197, 1.0, v197\n\t" \
  "v_add_f32 v198, 1.0, v198\n\t" \
  "v_add_f32 v199, 1.0, v199\n\t" \
  "v_cvt_pk_bf16_f32 v188, v192, v193\n\t" \
  "v_cvt_pk_bf16_f32 v189, v194, v195\n\t" \
  "s_waitcnt lgkmcnt(8)\n\t" \
  "v_mfma_f32_32x32x16_bf16 v[52:67], v[200:203], v[176:179], v[52:67]\n\t" \
  "v_rcp_f32 v196, v196\n\t" \
  "v_rcp_f32 v197, v197\n\t" \
  "v_rcp_f32 v198, v198\n\t" \
  "v_rcp_f32 v199, v199\n\t" \
  "s_waitcnt lgkmcnt(6)\n\t" \
  "v_mfma_f32_32x32x16_bf16 v[52:67], v[204:207], v[180:183], v[52:67]\n\t" \
  "v_exp_f32 v192, v100\n\t" \
  "v_exp_f32 v193, v101\n\t" \
  "v_exp_f32 v194, v102\n\t" \
  "v_exp_f32 v195, v103\n\t" \
  "s_cmp_eq_u32 %[nt], 0\n\t" \
  "v_mfma_f32_16x16x32_bf16 v[132:135], v[140:143], v[184:187], v[132:135]\n\t" \
  "v_mul_f32 v196, v128, v196\n\t" \
  "v_mul_f32 v197, v129, v197\n\t" \
  "v_mul_f32 v198, v130, v198\n\t" \
  "v_mul_f32 v199, v131, v199\n\t" \
  "v_add_f32 v192, 1.0, v192\n\t" \
  "v_add_f32 v193, 1.0, v193\n\t" \
  "v_add_f32 v194, 1.0, v194\n\t" \
  "v_add_f32 v195, 1.0, v195\n\t" \
  "v_cvt_pk_bf16_f32 v190, v196, v197\n\t" \
  "v_cvt_pk_bf16_f32 v191, v198, v199\n\t" \
  "s_cbranch_scc0 1b\n\t" \
  "s_waitcnt lgkmcnt(0)\n\t" \
  "s_cmp_lg_u32 %[last], 0\n\t" \
  "s_cbranch_scc1 8f\n\t" \
  "ds_write_b128 v243, v[188:191] offset:16\n\t" \
  "ds_read_b64_tr_b16 v[200:201], v244 offset:0\n\t" \
  "ds_read_b64_tr_b16 v[202:203], v244 offset:320\n\t" \
  "ds_read_b64_tr_b16 v[204:205], v244 offset:1280\n\t" \
  "ds_read_b64_tr_b16 v[206:207], v244 offset:1600\n\t" \
  "v_mfma_f32_16x16x32_bf16 v[132:135], v[144:147], v[188:191], v[132:135]\n\t" \
  "s_waitcnt lgkmcnt(0)\n\t" \
  "v_mfma_f32_32x32x16_bf16 v[68:83], v[200:203], v[176:179], v[68:83]\n\t" \
  "v_mfma_f32_32x32x16_bf16 v[68:83], v[204:207], v[180:183], v[68:83]\n\t" \
  "s_nop 8\n\t" \
  "v_mul_f32 v230, v230, v132\n\t" \
  "v_mul_f32 v231, v231, v132\n\t" \
  "v_cvt_pk_bf16_f32 v230, v230, v231\n\t" \
  "ds_write_b16 v245, v230 offset:7456\n\t" \
  "ds_write_b16_d16_hi v241, v230 offset:7584\n\t" \
  "ds_read_b128 v[200:203], v246 offset:7456\n\t" \
  "ds_read_b128 v[208:211], v246 offset:7488\n\t" \
  "s_waitcnt lgkmcnt(0)\n\t" \
  "v_mfma_f32_16x16x32_bf16 v[84:87], v[200:203], v[176:179], v[84:87]\n\t" \
  "v_mfma_f32_16x16x32_bf16 v[84:87], v[208:211], v[180:183], v[84:87]\n\t" \
  "s_branch 9f\n\t" \
  "6:\n\t" \
  "s_cmp_lg_u32 %[last], 0\n\t" \
  "s_cbranch_scc0 9f\n\t" \
  "s_waitcnt lgkmcnt(0)\n\t" \
  "v_cndmask_b32 v224, 0, 24, vcc\n\t" \
  "v_xor_b32 v220, v234, v224\n\t" \
  "v_xor_b32 v221, v235, v224\n\t" \
  "v_xor_b32 v222, v236, v224\n\t" \
  "v_xor_b32 v223, v237, v224\n\t" \
  "v_xor_b32 v216, 16, v220\n\t" \
  "v_xor_b32 v217, 16, v221\n\t" \
  "v_xor_b32 v218, 16, v222\n\t" \
  "v_xor_b32 v219, 16, v223\n\t" \
  "ds_read_b64 v[148:149], v220\n\t" \
  "ds_read_b64 v[150:151], v216\n\t" \
  "ds_read_b64 v[152:153], v221\n\t" \
  "ds_read_b64 v[154:155], v217\n\t" \
  "ds_read_b64 v[156:157], v222\n\t" \
  "ds_read_b64 v[158:159], v218\n\t" \
  "ds_read_b64 v[160:161], v223\n\t" \
  "ds_read_b64 v[162:163], v219\n\t" \
  "ds_read_b128 v[4:7], v247 offset:6144\n\t" \
  "ds_read_b128 v[8:11], v247 offset:7168\n\t" \
  "ds_read_b128 v[12:15], v247 offset:8192\n\t" \
  "ds_read_b128 v[16:19], v247 offset:9216\n\t" \
  "ds_read_b128 v[20:23], v247 offset:10240\n\t" \
  "ds_read_b128 v[24:27], v247 offset:11264\n\t" \
  "v_subrev_u32 v225, %[b1], v241\n\t" \
  "v_lshl_add_u32 v225, v225, 5, v243\n\t" \
  "v_sub_u32 v225, v225, v224\n\t" \
  "s_nop 4\n\t" \
  "v_cvt_pk_bf16_f32 v184, v52, v53\n\t" \
  "v_cvt_pk_bf16_f32 v185, v54, v55\n\t" \
  "v_cvt_pk_bf16_f32 v186, v56, v57\n\t" \
  "v_cvt_pk_bf16_f32 v187, v58, v59\n\t" \
  "v_cvt_pk_bf16_f32 v188, v60, v61\n\t" \
  "v_cvt_pk_bf16_f32 v189, v62, v63\n\t" \
  "v_cvt_pk_bf16_f32 v190, v64, v65\n\t" \
  "v_cvt_pk_bf16_f32 v191, v66, v67\n\t" \
  "v_cvt_pk_bf16_f32 v36, v68, v69\n\t" \
  "v_cvt_pk_bf16_f32 v37, v70, v71\n\t" \
  "v_cvt_pk_bf16_f32 v38, v72, v73\n\t" \
  "v_cvt_pk_bf16_f32 v39, v74, v75\n\t" \
  "v_cvt_pk_bf16_f32 v40, v76, v77\n\t" \
  "v_cvt_pk_bf16_f32 v41, v78, v79\n\t" \
  "v_cvt_pk_bf16_f32 v42, v80, v81\n\t" \
  "v_cvt_pk_bf16_f32 v43, v82, v83\n\t" \
  "s_waitcnt lgkmcnt(5)\n\t" \
  "v_mfma_f32_32x32x16_bf16 v[100:115], v[4:7], v[148:151], 0\n\t" \
  "s_waitcnt lgkmcnt(4)\n\t" \
  "v_mfma_f32_32x32x16_bf16 v[116:131], v[8:11], v[148:151], 0\n\t" \
  "ds_read_b128 v[28:31], v247 offset:12288\n\t" \
  "ds_read_b128 v[32:35], v247 offset:13312\n\t" \
  "s_waitcnt lgkmcnt(5)\n\t" \
  "v_mfma_f32_32x32x16_bf16 v[100:115], v[12:15], v[152:155], v[100:115]\n\t" \
  "s_waitcnt lgkmcnt(4)\n\t" \
  "v_mfma_f32_32x32x16_bf16 v[116:131], v[16:19], v[152:155], v[116:131]\n\t" \
  "ds_read_b128 v[4:7], v247 offset:14336\n\t" \
  "ds_read_b128 v[8:11], v247 offset:15360\n\t" \
  "s_waitcnt lgkmcnt(5)\n\t" \
  "v_mfma_f32_32x32x16_bf16 v[100:115], v[20:23], v[156:159], v[100:115]\n\t" \
  "s_waitcnt lgkmcnt(4)\n\t" \
  "v_mfma_f32_32x32x16_bf16 v[116:131], v[24:27], v[156:159], v[116:131]\n\t" \
  "ds_read_b128 v[12:15], v247 offset:16384\n\t" \
  "ds_read_b128 v[16:19], v247 offset:17408\n\t" \
  "s_waitcnt lgkmcnt(5)\n\t" \
  "v_mfma_f32_32x32x16_bf16 v[100:115], v[28:31], v[160:163], v[100:115]\n\t" \
  "s_waitcnt lgkmcnt(4)\n\t" \
  "v_mfma_f32_32x32x16_bf16 v[116:131], v[32:35], v[160:163], v[116:131]\n\t" \
  "ds_read_b128 v[20:23], v247 offset:18432\n\t" \
  "ds_read_b128 v[24:27], v247 offset:19456\n\t" \
  "s_waitcnt lgkmcnt(5)\n\t" \
  "v_mfma_f32_32x32x16_bf16 v[100:115], v[4:7], v[184:187], v[100:115]\n\t" \
  "ds_read_b128 v[28:31], v247 offset:20480\n\t" \
  "ds_read_b128 v[32:35], v247 offset:21504\n\t" \
  "s_waitcnt lgkmcnt(5)\n\t" \
  "v_mfma_f32_32x32x16_bf16 v[100:115], v[12:15], v[188:191], v[100:115]\n\t" \
  "s_waitcnt lgkmcnt(3)\n\t" \
  "v_mfma_f32_32x32x16_bf16 v[100:115], v[20:23], v[36:39], v[100:115]\n\t" \
  "s_waitcnt lgkmcnt(1)\n\t" \
  "v_mfma_f32_32x32x16_bf16 v[100:115], v[28:31], v[40:43], v[100:115]\n\t" \
  "s_waitcnt lgkmcnt(0)\n\t" \
  "s_branch 10f\n\t" \
  "8:\n\t" \
  "ds_write_b128 v243, v[188:191] offset:16\n\t" \
  "ds_read_b64_tr_b16 v[200:201], v244 offset:0\n\t" \
  "ds_read_b64_tr_b16 v[202:203], v244 offset:320\n\t" \
  "ds_read_b64_tr_b16 v[204:205], v244 offset:1280\n\t" \
  "ds_read_b64_tr_b16 v[206:207], v244 offset:1600\n\t" \
  "v_mfma_f32_16x16x32_bf16 v[132:135], v[144:147], v[188:191], v[132:135]\n\t" \
  "v_cndmask_b32 v224, 0, 24, vcc\n\t" \
  "v_xor_b32 v220, v234, v224\n\t" \
  "v_xor_b32 v221, v235, v224\n\t" \
  "v_xor_b32 v222, v236, v224\n\t" \
  "v_xor_b32 v223, v237, v224\n\t" \
  "v_xor_b32 v216, 16, v220\n\t" \
  "v_xor_b32 v217, 16, v221\n\t" \
  "v_xor_b32 v218, 16, v222\n\t" \
  "v_xor_b32 v219, 16, v223\n\t" \
  "ds_read_b64 v[148:149], v220\n\t" \
  "ds_read_b64 v[150:151], v216\n\t" \
  "ds_read_b64 v[152:153], v221\n\t" \
  "ds_read_b64 v[154:155], v217\n\t" \
  "ds_read_b64 v[156:157], v222\n\t" \
  "ds_read_b64 v[158:159], v218\n\t" \
  "ds_read_b64 v[160:161], v223\n\t" \
  "ds_read_b64 v[162:163], v219\n\t" \
  "ds_read_b128 v[4:7], v247 offset:6144\n\t" \
  "ds_read_b128 v[8:11], v247 offset:7168\n\t" \
  "s_waitcnt lgkmcnt(11)\n\t" \
  "ds_read_b128 v[12:15], v247 offset:8192\n\t" \
  "ds_read_b128 v[16:19], v247 offset:9216\n\t" \
  "ds_read_b128 v[20:23], v247 offset:10240\n\t" \
  "ds_read_b128 v[24:27], v247 offset:11264\n\t" \
  "v_subrev_u32 v225, %[b1], v241\n\t" \
  "v_lshl_add_u32 v225, v225, 5, v243\n\t" \
  "v_sub_u32 v225, v225, v224\n\t" \
  "v_mfma_f32_32x32x16_bf16 v[68:83], v[200:203], v[176:179], v[68:83]\n\t" \
  "s_waitcnt lgkmcnt(5)\n\t" \
  "v_mfma_f32_32x32x16_bf16 v[100:115], v[4:7], v[148:151], 0\n\t" \
  "v_mfma_f32_32x32x16_bf16 v[68:83], v[204:207], v[180:183], v[68:83]\n\t" \
  "s_waitcnt lgkmcnt(4)\n\t" \
  "v_mfma_f32_32x32x16_bf16 v[116:131], v[8:11], v[148:151], 0\n\t" \
  "ds_read_b128 v[28:31], v247 offset:12288\n\t" \
  "ds_read_b128 v[32:35], v247 offset:13312\n\t" \
  "s_waitcnt lgkmcnt(5)\n\t" \
  "v_mfma_f32_32x32x16_bf16 v[100:115], v[12:15], v[152:155], v[100:115]\n\t" \
  "v_mul_f32 v230, v230, v132\n\t" \
  "v_mul_f32 v231, v231, v132\n\t" \
  "v_cvt_pk_bf16_f32 v230, v230, v231\n\t" \
  "ds_write_b16 v245, v230 offset:7456\n\t" \
  "ds_write_b16_d16_hi v241, v230 offset:7584\n\t" \
  "ds_read_b128 v[200:203], v246 offset:7456\n\t" \
  "ds_read_b128 v[208:211], v246 offset:7488\n\t" \
  "v_cvt_pk_bf16_f32 v184, v52, v53\n\t" \
  "v_cvt_pk_bf16_f32 v185, v54, v55\n\t" \
  "v_cvt_pk_bf16_f32 v186, v56, v57\n\t" \
  "v_cvt_pk_bf16_f32 v187, v58, v59\n\t" \
  "s_waitcnt lgkmcnt(8)\n\t" \
  "v_mfma_f32_32x32x16_bf16 v[116:131], v[16:19], v[152:155], v[116:131]\n\t" \
  "ds_read_b128 v[4:7], v247 offset:14336\n\t" \
  "ds_read_b128 v[8:11], v247 offset:15360\n\t" \
  "s_waitcnt lgkmcnt(9)\n\t" \
  "v_mfma_f32_32x32x16_bf16 v[100:115], v[20:23], v[156:159], v[100:115]\n\t" \
  "v_cvt_pk_bf16_f32 v188, v60, v61\n\t" \
  "v_cvt_pk_bf16_f32 v189, v62, v63\n\t" \
  "v_cvt_pk_bf16_f32 v190, v64, v65\n\t" \
  "v_cvt_pk_bf16_f32 v191, v66, v67\n\t" \
  "s_waitcnt lgkmcnt(8)\n\t" \
  "v_mfma_f32_32x32x16_bf16 v[116:131], v[24:27], v[156:159], v[116:131]\n\t" \
  "ds_read_b128 v[12:15], v247 offset:16384\n\t" \
  "ds_read_b128 v[16:19], v247 offset:17408\n\t" \
  "s_waitcnt lgkmcnt(9)\n\t" \
  "v_mfma_f32_32x32x16_bf16 v[100:115], v[28:31], v[160:163], v[100:115]\n\t" \
  "v_cvt_pk_bf16_f32 v36, v68, v69\n\t" \
  "v_cvt_pk_bf16_f32 v37, v70, v71\n\t" \
  "v_cvt_pk_bf16_f32 v38, v72, v73\n\t" \
  "v_cvt_pk_bf16_f32 v39, v74, v75\n\t" \
  "s_waitcnt lgkmcnt(5)\n\t" \
  "v_mfma_f32_16x16x32_bf16 v[84:87], v[200:203], v[176:179], v[84:87]\n\t" \
  "v_cvt_pk_bf16_f32 v40, v76, v77\n\t" \
  "v_cvt_pk_bf16_f32 v41, v78, v79\n\t" \
  "v_cvt_pk_bf16_f32 v42, v80, v81\n\t" \
  "v_cvt_pk_bf16_f32 v43, v82, v83\n\t" \
  "v_mfma_f32_32x32x16_bf16 v[116:131], v[32:35], v[160:163], v[116:131]\n\t" \
  "ds_read_b128 v[20:23], v247 offset:18432\n\t" \
  "ds_read_b128 v[24:27], v247 offset:19456\n\t" \
  "s_waitcnt lgkmcnt(6)\n\t" \
  "v_mfma_f32_16x16x32_bf16 v[84:87], v[208:211], v[180:183], v[84:87]\n\t" \
  "s_waitcnt lgkmcnt(5)\n\t" \
  "v_mfma_f32_32x32x16_bf16 v[100:115], v[4:7], v[184:187], v[100:115]\n\t" \
  "ds_read_b128 v[28:31], v247 offset:20480\n\t" \
  "ds_read_b128 v[32:35], v247 offset:21504\n\t" \
  "s_waitcnt lgkmcnt(5)\n\t" \
  "v_mfma_f32_32x32x16_bf16 v[100:115], v[12:15], v[188:191], v[100:115]\n\t" \
  "s_waitcnt lgkmcnt(3)\n\t" \
  "v_mfma_f32_32x32x16_bf16 v[100:115], v[20:23], v[36:39], v[100:115]\n\t" \
  "s_waitcnt lgkmcnt(1)\n\t" \
  "v_mfma_f32_32x32x16_bf16 v[100:115], v[28:31], v[40:43], v[100:115]\n\t" \
  "s_waitcnt lgkmcnt(0)\n\t" \
  "10:\n\t" \
  "v_mov_b32 v212, 0xbfb8aa3b\n\t" \
  "v_mfma_f32_32x32x16_bf16 v[116:131], v[8:11], v[184:187], v[116:131]\n\t" \
  "v_mfma_f32_32x32x16_bf16 v[116:131], v[16:19], v[188:191], v[116:131]\n\t" \
  "s_nop 8\n\t" \
  "v_exp_f32 v192, v100\n\t" \
  "v_exp_f32 v193, v101\n\t" \
  "v_exp_f32 v194, v102\n\t" \
  "v_exp_f32 v195, v103\n\t" \
  "v_lshlrev_b32 v200, 16, v148\n\t" \
  "v_and_b32 v201, 0xffff0000, v148\n\t" \
  "v_lshlrev_b32 v202, 16, v149\n\t" \
  "v_and_b32 v203, 0xffff0000, v149\n\t" \
  "v_mfma_f32_32x32x16_bf16 v[116:131], v[24:27], v[36:39], v[116:131]\n\t" \
  "v_fmaak_f32 v192, v192, v212, 0xbfb8aa3b\n\t" \
  "v_fmaak_f32 v193, v193, v212, 0xbfb8aa3b\n\t" \
  "v_fmaak_f32 v194, v194, v212, 0xbfb8aa3b\n\t" \
  "v_fmaak_f32 v195, v195, v212, 0xbfb8aa3b\n\t" \
  "v_rcp_f32 v192, v192\n\t" \
  "v_rcp_f32 v193, v193\n\t" \
  "v_rcp_f32 v194, v194\n\t" \
  "v_rcp_f32 v195, v195\n\t" \
  "v_mfma_f32_32x32x16_bf16 v[116:131], v[32:35], v[40:43], v[116:131]\n\t" \
  "v_exp_f32 v196, v104\n\t" \
  "v_exp_f32 v197, v105\n\t" \
  "v_exp_f32 v198, v106\n\t" \
  "v_exp_f32 v199, v107\n\t" \
  "v_fmac_f32 v200, v100, v192\n\t" \
  "v_fmac_f32 v201, v101, v193\n\t" \
  "v_fmac_f32 v202, v102, v194\n\t" \
  "v_fmac_f32 v203, v103, v195\n\t" \
  "v_lshlrev_b32 v204, 16, v150\n\t" \
  "v_and_b32 v205, 0xffff0000, v150\n\t" \
  "v_lshlrev_b32 v206, 16, v151\n\t" \
  "v_and_b32 v207, 0xffff0000, v151\n\t" \
  "v_fmaak_f32 v196, v196, v212, 0xbfb8aa3b\n\t" \
  "v_fmaak_f32 v197, v197, v212, 0xbfb8aa3b\n\t" \
  "v_fmaak_f32 v198, v198, v212, 0xbfb8aa3b\n\t" \
  "v_fmaak_f32 v199, v199, v212, 0xbfb8aa3b\n\t" \
  "v_cvt_pk_bf16_f32 v176, v200, v201\n\t" \
  "v_cvt_pk_bf16_f32 v177, v202, v203\n\t" \
  "ds_write_b64 v225, v[176:177] offset:0\n\t" \
  "v_rcp_f32 v196, v196\n\t" \
  "v_rcp_f32 v197, v197\n\t" \
  "v_rcp_f32 v198, v198\n\t" \
  "v_rcp_f32 v199, v199\n\t" \
  "v_exp_f32 v192, v108\n\t" \
  "v_exp_f32 v193, v109\n\t" \
  "v_exp_f32 v194, v110\n\t" \
  "v_exp_f32 v195, v111\n\t" \
  "v_fmac_f32 v204, v104, v196\n\t" \
  "v_fmac_f32 v205, v105, v197\n\t" \
  "v_fmac_f32 v206, v106, v198\n\t" \
  "v_fmac_f32 v207, v107, v199\n\t" \
  "v_lshlrev_b32 v200, 16, v152\n\t" \
  "v_and_b32 v201, 0xffff0000, v152\n\t" \
  "v_lshlrev_b32 v202, 16, v153\n\t" \
  "v_and_b32 v203, 0xffff0000, v153\n\t" \
  "v_fmaak_f32 v192, v192, v212, 0xbfb8aa3b\n\t" \
  "v_fmaak_f32 v193, v193, v212, 0xbfb8aa3b\n\t" \
  "v_fmaak_f32 v194, v194, v212, 0xbfb8aa3b\n\t" \
  "v_fmaak_f32 v195, v195, v212, 0xbfb8aa3b\n\t" \
  "v_cvt_pk_bf16_f32 v178, v204, v205\n\t" \
  "v_cvt_pk_bf16_f32 v179, v206, v207\n\t" \
  "ds_write_b64 v225, v[178:179] offset:16\n\t" \
  "v_rcp_f32 v192, v192\n\t" \
  "v_rcp_f32 v193, v193\n\t" \
  "v_rcp_f32 v194, v194\n\t" \
  "v_rcp_f32 v195, v195\n\t" \
  "v_exp_f32 v196, v112\n\t" \
  "v_exp_f32 v197, v113\n\t" \
  "v_exp_f32 v198, v114\n\t" \
  "v_exp_f32 v199, v115\n\t" \
  "v_fmac_f32 v200, v108, v192\n\t" \
  "v_fmac_f32 v201, v109, v193\n\t" \
  "v_fmac_f32 v202, v110, v194\n\t" \
  "v_fmac_f32 v203, v111, v195\n\t" \
  "v_lshlrev_b32 v204, 16, v154\n\t" \
  "v_and_b32 v205, 0xffff0000, v154\n\t" \
  "v_lshlrev_b32 v206, 16, v155\n\t" \
  "v_and_b32 v207, 0xffff0000, v155\n\t" \
  "v_fmaak_f32 v196, v196, v212, 0xbfb8aa3b\n\t" \
  "v_fmaak_f32 v197, v197, v212, 0xbfb8aa3b\n\t" \
  "v_fmaak_f32 v198, v198, v212, 0xbfb8aa3b\n\t" \
  "v_fmaak_f32 v199, v199, v212, 0xbfb8aa3b\n\t" \
  "v_cvt_pk_bf16_f32 v180, v200, v201\n\t" \
  "v_cvt_pk_bf16_f32 v181, v202, v203\n\t" \
  "ds_write_b64 v225, v[180:181] offset:32\n\t" \
  "v_rcp_f32 v196, v196\n\t" \
  "v_rcp_f32 v197, v197\n\t" \
  "v_rcp_f32 v198, v198\n\t" \
  "v_rcp_f32 v199, v199\n\t" \
  "v_exp_f32 v192, v116\n\t" \
  "v_exp_f32 v193, v117\n\t" \
  "v_exp_f32 v194, v118\n\t" \
  "v_exp_f32 v195, v119\n\t" \
  "v_fmac_f32 v204, v112, v196\n\t" \
  "v_fmac_f32 v205, v113, v197\n\t" \
  "v_fmac_f32 v206, v114, v198\n\t" \
  "v_fmac_f32 v207, v115, v199\n\t" \
  "v_lshlrev_b32 v200, 16, v156\n\t" \
  "v_and_b32 v201, 0xffff0000, v156\n\t" \
  "v_lshlrev_b32 v202, 16, v157\n\t" \
  "v_and_b32 v203, 0xffff0000, v157\n\t" \
  "v_fmaak_f32 v192, v192, v212, 0xbfb8aa3b\n\t" \
  "v_fmaak_f32 v193, v193, v212, 0xbfb8aa3b\n\t" \
  "v_fmaak_f32 v194, v194, v212, 0xbfb8aa3b\n\t" \
  "v_fmaak_f32 v195, v195, v212, 0xbfb8aa3b\n\t" \
  "v_cvt_pk_bf16_f32 v182, v204, v205\n\t" \
  "v_cvt_pk_bf16_f32 v183, v206, v207\n\t" \
  "ds_write_b64 v225, v[182:183] offset:48\n\t" \
  "v_rcp_f32 v192, v192\n\t" \
  "v_rcp_f32 v193, v193\n\t" \
  "v_rcp_f32 v194, v194\n\t" \
  "v_rcp_f32 v195, v195\n\t" \
  "v_exp_f32 v196, v120\n\t" \
  "v_exp_f32 v197, v121\n\t" \
  "v_exp_f32 v198, v122\n\t" \
  "v_exp_f32 v199, v123\n\t" \
  "v_fmac_f32 v200, v116, v192\n\t" \
  "v_fmac_f32 v201, v117, v193\n\t" \
  "v_fmac_f32 v202, v118, v194\n\t" \
  "v_fmac_f32 v203, v119, v195\n\t" \
  "v_lshlrev_b32 v204, 16, v158\n\t" \
  "v_and_b32 v205, 0xffff0000, v158\n\t" \
  "v_lshlrev_b32 v206, 16, v159\n\t" \
  "v_and_b32 v207, 0xffff0000, v159\n\t" \
  "v_fmaak_f32 v196, v196, v212, 0xbfb8aa3b\n\t" \
  "v_fmaak_f32 v197, v197, v212, 0xbfb8aa3b\n\t" \
  "v_fmaak_f32 v198, v198, v212, 0xbfb8aa3b\n\t" \
  "v_fmaak_f32 v199, v199, v212, 0xbfb8aa3b\n\t" \
  "v_cvt_pk_bf16_f32 v176, v200, v201\n\t" \
  "v_cvt_pk_bf16_f32 v177, v202, v203\n\t" \
  "ds_write_b64 v225, v[176:177] offset:64\n\t" \
  "v_rcp_f32 v196, v196\n\t" \
  "v_rcp_f32 v197, v197\n\t" \
  "v_rcp_f32 v198, v198\n\t" \
  "v_rcp_f32 v199, v199\n\t" \
  "v_exp_f32 v192, v124\n\t" \
  "v_exp_f32 v193, v125\n\t" \
  "v_exp_f32 v194, v126\n\t" \
  "v_exp_f32 v195, v127\n\t" \
  "v_fmac_f32 v204, v120, v196\n\t" \
  "v_fmac_f32 v205, v121, v197\n\t" \
  "v_fmac_f32 v206, v122, v198\n\t" \
  "v_fmac_f32 v207, v123, v199\n\t" \
  "v_lshlrev_b32 v200, 16, v160\n\t" \
  "v_and_b32 v201, 0xffff0000, v160\n\t" \
  "v_lshlrev_b32 v202, 16, v161\n\t" \
  "v_and_b32 v203, 0xffff0000, v161\n\t" \
  "v_fmaak_f32 v192, v192, v212, 0xbfb8aa3b\n\t" \
  "v_fmaak_f32 v193, v193, v212, 0xbfb8aa3b\n\t" \
  "v_fmaak_f32 v194, v194, v212, 0xbfb8aa3b\n\t" \
  "v_fmaak_f32 v195, v195, v212, 0xbfb8aa3b\n\t" \
  "v_cvt_pk_bf16_f32 v178, v204, v205\n\t" \
  "v_cvt_pk_bf16_f32 v179, v206, v207\n\t" \
  "ds_write_b64 v225, v[178:179] offset:80\n\t" \
  "v_rcp_f32 v192, v192\n\t" \
  "v_rcp_f32 v193, v193\n\t" \
  "v_rcp_f32 v194, v194\n\t" \
  "v_rcp_f32 v195, v195\n\t" \
  "v_exp_f32 v196, v128\n\t" \
  "v_exp_f32 v197, v129\n\t" \
  "v_exp_f32 v198, v130\n\t" \
  "v_exp_f32 v199, v131\n\t" \
  "v_fmac_f32 v200, v124, v192\n\t" \
  "v_fmac_f32 v201, v125, v193\n\t" \
  "v_fmac_f32 v202, v126, v194\n\t" \
  "v_fmac_f32 v203, v127, v195\n\t" \
  "v_lshlrev_b32 v204, 16, v162\n\t" \
  "v_and_b32 v205, 0xffff0000, v162\n\t" \
  "v_lshlrev_b32 v206, 16, v163\n\t" \
  "v_and_b32 v207, 0xffff0000, v163\n\t" \
  "v_fmaak_f32 v196, v196, v212, 0xbfb8aa3b\n\t" \
  "v_fmaak_f32 v197, v197, v212, 0xbfb8aa3b\n\t" \
  "v_fmaak_f32 v198, v198, v212, 0xbfb8aa3b\n\t" \
  "v_fmaak_f32 v199, v199, v212, 0xbfb8aa3b\n\t" \
  "v_cvt_pk_bf16_f32 v180, v200, v201\n\t" \
  "v_cvt_pk_bf16_f32 v181, v202, v203\n\t" \
  "ds_write_b64 v225, v[180:181] offset:96\n\t" \
  "v_rcp_f32 v196, v196\n\t" \
  "v_rcp_f32 v197, v197\n\t" \
  "v_rcp_f32 v198, v198\n\t" \
  "v_rcp_f32 v199, v199\n\t" \
  "s_nop 0\n\t" \
  "v_fmac_f32 v204, v128, v196\n\t" \
  "v_fmac_f32 v205, v129, v197\n\t" \
  "v_fmac_f32 v206, v130, v198\n\t" \
  "v_fmac_f32 v207, v131, v199\n\t" \
  "v_cvt_pk_bf16_f32 v182, v204, v205\n\t" \
  "v_cvt_pk_bf16_f32 v183, v206, v207\n\t" \
  "ds_write_b64 v225, v[182:183] offset:112\n\t" \
  "9:\n\t" \
  "ds_write_b16 v233, v255 offset:5376\n\t" \
  "s_mov_b32 m0, %[keep]\n\t" \
  "s_setprio 0\n\t" \
  "s_nop 11\n\t" \
  ""
#define LOOP_ASM_PLAIN \
  "s_mov_b32 %[keep], m0\n\t" \
  "s_mov_b64 vcc, %[hsel]\n\t" \
  "ds_read2_b32 v[220:221], v252 offset0:0 offset1:2\n\t" \
  "ds_read2_b32 v[222:223], v252 offset0:4 offset1:6\n\t" \
  "ds_read_b64 v[226:227], v253\n\t" \
  "ds_read_b32 v232, v254\n\t" \
  "v_mov_b32 v176, 0\n\t" \
  "v_mov_b32 v177, 0\n\t" \
  "v_mov_b32 v178, 0\n\t" \
  "v_mov_b32 v179, 0\n\t" \
  "v_mov_b32 v180, 0\n\t" \
  "v_mov_b32 v181, 0\n\t" \
  "v_mov_b32 v182, 0\n\t" \
  "v_mov_b32 v183, 0\n\t" \
  "v_mov_b32 v184, 0\n\t" \
  "v_mov_b32 v185, 0\n\t" \
  "v_mov_b32 v186, 0\n\t" \
  "v_mov_b32 v187, 0\n\t" \
  "v_mov_b32 v188, 0\n\t" \
  "v_mov_b32 v189, 0\n\t" \
  "v_mov_b32 v190, 0\n\t" \
  "v_mov_b32 v191, 0\n\t" \
  "v_mov_b32 v230, 0\n\t" \
  "v_mov_b32 v231, 0\n\t" \
  "v_mov_b32 v208, 0\n\t" \
  "v_mov_b32 v209, 0\n\t" \
  "v_mov_b32 v210, 0\n\t" \
  "v_mov_b32 v211, 0\n\t" \
  "ds_write_b128 v243, v[184:187] offset:0\n\t" \
  "s_nop 0\n\t" \
  "v_mfma_f32_16x16x32_bf16 v[132:135], v[184:187], v[188:191], 0\n\t" \
  "v_add_u32 v252, 0x100, v252\n\t" \
  "v_add_u32 v253, 0x100, v253\n\t" \
  "v_add_u32 v254, 0x100, v254\n\t" \
  "s_waitcnt lgkmcnt(0)\n\t" \
  "v_lshrrev_b32 v232, 20, v232\n\t" \
  "v_add_u32 v233, %[posd], v232\n\t" \
  "ds_read_b128 v[216:219], v233\n\t" \
  "v_add_u32 v233, v240, v232\n\t" \
  "ds_read_b128 v[172:175], v233 offset:32\n\t" \
  "ds_read_b128 v[168:171], v233\n\t" \
  "v_add_u32 v233, v241, v232\n\t" \
  "ds_write_b16_d16_hi v233, v255 offset:5376\n\t" \
  "v_mad_u32_u16 v220, v220, %[c128], v248\n\t" \
  "v_mad_u32_u16 v221, v221, %[c128], v249\n\t" \
  "v_mad_u32_u16 v222, v222, %[c128], v248\n\t" \
  "v_mad_u32_u16 v223, v223, %[c128], v249\n\t" \
  "v_or_b32 v226, v226, v250\n\t" \
  "v_mad_u32_u16 v227, v227, 6, v251\n\t" \
  "s_waitcnt vmcnt(0)\n\t" \
  "ds_read_b128 v[148:151], v234\n\t" \
  "ds_read_b128 v[152:155], v235\n\t" \
  "ds_read_b128 v[156:159], v236\n\t" \
  "ds_read_b128 v[160:163], v237\n\t" \
  "ds_read_b128 v[164:167], v238 offset:4096\n\t" \
  "ds_read_b64 v[224:225], v239 offset:5120\n\t" \
  "s_waitcnt lgkmcnt(0)\n\t" \
  "s_cmp_lt_i32 %[nt], 1\n\t" \
  "s_cbranch_scc1 7f\n\t" \
  "s_mov_b32 m0, %[b2]\n\t" \
  "s_nop 0\n\t" \
  "global_load_lds_dwordx4 v226, %[ea]\n\t" \
  "global_load_lds_dword v227, %[pos] offset:1024\n\t" \
  "s_mov_b32 m0, %[b1]\n\t" \
  "s_nop 0\n\t" \
  "global_load_lds_dwordx4 v220, %[h0]\n\t" \
  "global_load_lds_dwordx4 v221, %[h1] offset:1024\n\t" \
  "global_load_lds_dwordx4 v222, %[h2] offset:2048\n\t" \
  "global_load_lds_dwordx4 v223, %[h3] offset:3072\n\t" \
  "7:\n\t" \
  "ds_read2_b32 v[220:221], v252 offset0:0 offset1:2\n\t" \
  "ds_read2_b32 v[222:223], v252 offset0:4 offset1:6\n\t" \
  "ds_read_b64 v[226:227], v253\n\t" \
  "ds_read_b32 v232, v254\n\t" \
  "v_add_u32 v252, 0x100, v252\n\t" \
  "v_add_u32 v253, 0x100, v253\n\t" \
  "v_add_u32 v254, 0x100, v254\n\t" \
  "v_and_b32 v225, 0xffff0000, v225\n\t" \
  "v_lshlrev_b32 v219, 16, v224\n\t" \
  "v_and_b32 v224, 0xffff0000, v224\n\t" \
  "v_sub_f32 v229, v218, v225\n\t" \
  "v_sub_f32 v216, v216, v219\n\t" \
  "v_sub_f32 v217, v217, v224\n\t" \
  "v_mul_f32 v218, v229, v229\n\t" \
  "v_cndmask_b32 v228, v216, v217, vcc\n\t" \
  "v_fmac_f32 v218, v216, v216\n\t" \
  "v_fmac_f32 v218, v217, v217\n\t" \
  "v_cvt_pk_bf16_f32 v218, v218, v218\n\t" \
  "v_cndmask_b32_sdwa v175, v175, v218, vcc dst_sel:WORD_1 dst_unused:UNUSED_PRESERVE src0_sel:WORD_1 src1_sel:WORD_1\n\t" \
  "v_mfma_f32_32x32x16_bf16 v[100:115], v[4:7], v[148:151], 0\n\t" \
  "v_mfma_f32_32x32x16_bf16 v[100:115], v[12:15], v[152:155], v[100:115]\n\t" \
  "v_mfma_f32_32x32x16_bf16 v[100:115], v[20:23], v[156:159], v[100:115]\n\t" \
  "v_mfma_f32_32x32x16_bf16 v[100:115], v[28:31], v[160:163], v[100:115]\n\t" \
  "v_mfma_f32_32x32x16_bf16 v[100:115], v[36:39], v[168:171], v[100:115]\n\t" \
  "v_mfma_f32_32x32x16_bf16 v[100:115], v[88:91], v[164:167], v[100:115]\n\t" \
  "v_mfma_f32_32x32x16_bf16 v[100:115], v[44:47], v[172:175], v[100:115]\n\t" \
  "s_cmp_eq_u32 %[nt], 0\n\t" \
  "s_nop 10\n\t" \
  "v_mul_f32 v192, 0xbfb8aa3b, v100\n\t" \
  "v_mul_f32 v193, 0xbfb8aa3b, v101\n\t" \
  "v_mul_f32 v194, 0xbfb8aa3b, v102\n\t" \
  "v_mul_f32 v195, 0xbfb8aa3b, v103\n\t" \
  "v_exp_f32 v192, v192\n\t" \
  "v_exp_f32 v193, v193\n\t" \
  "v_exp_f32 v194, v194\n\t" \
  "v_exp_f32 v195, v195\n\t" \
  "s_nop 0\n\t" \
  "v_add_f32 v192, 1.0, v192\n\t" \
  "v_add_f32 v193, 1.0, v193\n\t" \
  "v_add_f32 v194, 1.0, v194\n\t" \
  "v_add_f32 v195, 1.0, v195\n\t" \
  "s_cbranch_scc1 6f\n\t" \
  "s_cmp_lt_i32 %[thr], 0\n\t" \
  "s_cbranch_scc1 1f\n\t" \
  "s_setprio 1\n\t" \
  "1:\n\t" \
  "ds_write_b128 v243, v[188:191] offset:16\n\t" \
  "ds_read_b64_tr_b16 v[200:201], v244 offset:0\n\t" \
  "ds_read_b64_tr_b16 v[202:203], v244 offset:320\n\t" \
  "ds_read_b64_tr_b16 v[204:205], v244 offset:1280\n\t" \
  "ds_read_b64_tr_b16 v[206:207], v244 offset:1600\n\t" \
  "v_mfma_f32_16x16x32_bf16 v[132:135], v[144:147], v[188:191], v[132:135]\n\t" \
  "v_rcp_f32 v192, v192\n\t" \
  "v_rcp_f32 v193, v193\n\t" \
  "v_rcp_f32 v194, v194\n\t" \
  "v_rcp_f32 v195, v195\n\t" \
  "v_mfma_f32_32x32x16_bf16 v[116:131], v[8:11], v[148:151], 0\n\t" \
  "v_mul_f32 v196, 0xbfb8aa3b, v104\n\t" \
  "v_mul_f32 v197, 0xbfb8aa3b, v105\n\t" \
  "v_mul_f32 v198, 0xbfb8aa3b, v106\n\t" \
  "v_mul_f32 v199, 0xbfb8aa3b, v107\n\t" \
  "v_exp_f32 v196, v196\n\t" \
  "v_exp_f32 v197, v197\n\t" \
  "v_exp_f32 v198, v198\n\t" \
  "v_exp_f32 v199, v199\n\t" \
  "v_mfma_f32_32x32x16_bf16 v[116:131], v[16:19], v[152:155], v[116:131]\n\t" \
  "v_mul_f32 v192, v100, v192\n\t" \
  "v_mul_f32 v193, v101, v193\n\t" \
  "v_mul_f32 v194, v102, v194\n\t" \
  "v_mul_f32 v195, v103, v195\n\t" \
  "v_add_f32 v196, 1.0, v196\n\t" \
  "v_add_f32 v197, 1.0, v197\n\t" \
  "v_add_f32 v198, 1.0, v198\n\t" \
  "v_add_f32 v199, 1.0, v199\n\t" \
  "v_cvt_pk_bf16_f32 v184, v192, v193\n\t" \
  "v_cvt_pk_bf16_f32 v185, v194, v195\n\t" \
  "v_mfma_f32_32x32x16_bf16 v[116:131], v[24:27], v[156:159], v[116:131]\n\t" \
  "v_rcp_f32 v196, v196\n\t" \
  "v_rcp_f32 v197, v197\n\t" \
  "v_rcp_f32 v198, v198\n\t" \
  "v_rcp_f32 v199, v199\n\t" \
  "s_waitcnt lgkmcnt(2)\n\t" \
  "v_mfma_f32_32x32x16_bf16 v[68:83], v[200:203], v[176:179], v[68:83]\n\t" \
  "v_mul_f32 v192, 0xbfb8aa3b, v108\n\t" \
  "v_mul_f32 v193, 0xbfb8aa3b, v109\n\t" \
  "v_mul_f32 v194, 0xbfb8aa3b, v110\n\t" \
  "v_mul_f32 v195, 0xbfb8aa3b, v111\n\t" \
  "v_exp_f32 v192, v192\n\t" \
  "v_exp_f32 v193, v193\n\t" \
  "v_exp_f32 v194, v194\n\t" \
  "v_exp_f32 v195, v195\n\t" \
  "v_mfma_f32_32x32x16_bf16 v[116:131], v[32:35], v[160:163], v[116:131]\n\t" \
  "v_mul_f32 v196, v104, v196\n\t" \
  "v_mul_f32 v197, v105, v197\n\t" \
  "v_mul_f32 v198, v106, v198\n\t" \
  "v_mul_f32 v199, v107, v199\n\t" \
  "v_mul_f32 v230, v230, v132\n\t" \
  "v_mul_f32 v231, v231, v132\n\t" \
  "v_cvt_pk_bf16_f32 v230, v230, v231\n\t" \
  "ds_write_b16 v245, v230 offset:7456\n\t" \
  "ds_write_b16_d16_hi v241, v230 offset:7584\n\t" \
  "ds_read_b128 v[200:203], v246 offset:7456\n\t" \
  "ds_read_b128 v[208:211], v246 offset:7488\n\t" \
  "v_mov_b32 v230, v228\n\t" \
  "v_mov_b32 v231, v229\n\t" \
  "v_add_f32 v192, 1.0, v192\n\t" \
  "v_add_f32 v193, 1.0, v193\n\t" \
  "v_add_f32 v194, 1.0, v194\n\t" \
  "v_add_f32 v195, 1.0, v195\n\t" \
  "v_cvt_pk_bf16_f32 v186, v196, v197\n\t" \
  "v_cvt_pk_bf16_f32 v187, v198, v199\n\t" \
  "v_mfma_f32_32x32x16_bf16 v[116:131], v[40:43], v[168:171], v[116:131]\n\t" \
  "v_rcp_f32 v192, v192\n\t" \
  "v_rcp_f32 v193, v193\n\t" \
  "v_rcp_f32 v194, v194\n\t" \
  "v_rcp_f32 v195, v195\n\t" \
  "s_sub_u32 %[nt], %[nt], 1\n\t" \
  "s_cmp_lg_u32 %[nt], %[thr]\n\t" \
  "s_cbranch_scc1 3f\n\t" \
  "s_setprio 0\n\t" \
  "3:\n\t" \
  "v_mfma_f32_32x32x16_bf16 v[116:131], v[92:95], v[164:167], v[116:131]\n\t" \
  "v_mul_f32 v196, 0xbfb8aa3b, v112\n\t" \
  "v_mul_f32 v197, 0xbfb8aa3b, v113\n\t" \
  "v_mul_f32 v198, 0xbfb8aa3b, v114\n\t" \
  "v_mul_f32 v199, 0xbfb8aa3b, v115\n\t" \
  "v_exp_f32 v196, v196\n\t" \
  "v_exp_f32 v197, v197\n\t" \
  "v_exp_f32 v198, v198\n\t" \
  "v_exp_f32 v199, v199\n\t" \
  "v_mfma_f32_32x32x16_bf16 v[116:131], v[48:51], v[172:175], v[116:131]\n\t" \
  "v_mul_f32 v192, v108, v192\n\t" \
  "v_mul_f32 v193, v109, v193\n\t" \
  "v_mul_f32 v194, v110, v194\n\t" \
  "v_mul_f32 v195, v111, v195\n\t" \
  "s_waitcnt vmcnt(0)\n\t" \
  "ds_read_b128 v[148:151], v234\n\t" \
  "ds_read_b128 v[152:155], v235\n\t" \
  "ds_read_b128 v[156:159], v236\n\t" \
  "ds_read_b128 v[160:163], v237\n\t" \
  "ds_read_b128 v[164:167], v238 offset:4096\n\t" \
  "ds_read_b64 v[224:225], v239 offset:5120\n\t" \
  "ds_write_b128 v243, v[184:187] offset:0\n\t" \
  "v_add_f32 v196, 1.0, v196\n\t" \
  "v_add_f32 v197, 1.0, v197\n\t" \
  "v_add_f32 v198, 1.0, v198\n\t" \
  "v_add_f32 v199, 1.0, v199\n\t" \
  "v_cvt_pk_bf16_f32 v188, v192, v193\n\t" \
  "v_cvt_pk_bf16_f32 v189, v194, v195\n\t" \
  "v_mad_u32_u16 v220, v220, %[c128], v248\n\t" \
  "v_mad_u32_u16 v221, v221, %[c128], v249\n\t" \
  "v_mad_u32_u16 v222, v222, %[c128], v248\n\t" \
  "s_waitcnt lgkmcnt(11)\n\t" \
  "v_mfma_f32_32x32x16_bf16 v[68:83], v[204:207], v[180:183], v[68:83]\n\t" \
  "v_rcp_f32 v196, v196\n\t" \
  "v_rcp_f32 v197, v197\n\t" \
  "v_rcp_f32 v198, v198\n\t" \
  "v_rcp_f32 v199, v199\n\t" \
  "v_mad_u32_u16 v223, v223, %[c128], v249\n\t" \
  "v_or_b32 v226, v226, v250\n\t" \
  "v_mad_u32_u16 v227, v227, 6, v251\n\t" \
  "s_waitcnt lgkmcnt(8)\n\t" \
  "v_mfma_f32_16x16x32_bf16 v[84:87], v[200:203], v[176:179], v[84:87]\n\t" \
  "v_mul_f32 v192, 0xbfb8aa3b, v116\n\t" \
  "v_mul_f32 v193, 0xbfb8aa3b, v117\n\t" \
  "v_mul_f32 v194, 0xbfb8aa3b, v118\n\t" \
  "v_mul_f32 v195, 0xbfb8aa3b, v119\n\t" \
  "v_exp_f32 v192, v192\n\t" \
  "v_exp_f32 v193, v193\n\t" \
  "v_exp_f32 v194, v194\n\t" \
  "v_exp_f32 v195, v195\n\t" \
  "s_waitcnt lgkmcnt(7)\n\t" \
  "v_mfma_f32_16x16x32_bf16 v[84:87], v[208:211], v[180:183], v[84:87]\n\t" \
  "v_mul_f32 v196, v112, v196\n\t" \
  "v_mul_f32 v197, v113, v197\n\t" \
  "v_mul_f32 v198, v114, v198\n\t" \
  "v_mul_f32 v199, v115, v199\n\t" \
  "s_waitcnt lgkmcnt(1)\n\t" \
  "s_cmp_lt_i32 %[nt], 1\n\t" \
  "s_cbranch_scc1 4f\n\t" \
  "s_mov_b32 m0, %[b2]\n\t" \
  "s_nop 0\n\t" \
  "global_load_lds_dwordx4 v226, %[ea]\n\t" \
  "global_load_lds_dword v227, %[pos] offset:1024\n\t" \
  "s_mov_b32 m0, %[b1]\n\t" \
  "s_nop 0\n\t" \
  "global_load_lds_dwordx4 v220, %[h0]\n\t" \
  "global_load_lds_dwordx4 v221, %[h1] offset:1024\n\t" \
  "global_load_lds_dwordx4 v222, %[h2] offset:2048\n\t" \
  "global_load_lds_dwordx4 v223, %[h3] offset:3072\n\t" \
  "4:\n\t" \
  "v_add_f32 v192, 1.0, v192\n\t" \
  "v_add_f32 v193, 1.0, v193\n\t" \
  "v_add_f32 v194, 1.0, v194\n\t" \
  "v_add_f32 v195, 1.0, v195\n\t" \
  "v_cvt_pk_bf16_f32 v190, v196, v197\n\t" \
  "v_cvt_pk_bf16_f32 v191, v198, v199\n\t" \
  "v_lshrrev_b32 v232, 20, v232\n\t" \
  "v_add_u32 v220, %[posd], v232\n\t" \
  "v_add_u32 v221, v240, v232\n\t" \
  "ds_write_b128 v243, v[188:191] offset:16\n\t" \
  "ds_read_b128 v[176:179], v242 offset:5376\n\t" \
  "ds_read_b128 v[180:183], v242 offset:5408\n\t" \
  "ds_write_b16 v233, v255 offset:5376\n\t" \
  "ds_read_b128 v[216:219], v220\n\t" \
  "ds_read_b128 v[172:175], v221 offset:32\n\t" \
  "ds_read_b128 v[168:171], v221\n\t" \
  "ds_read_b64_tr_b16 v[200:201], v244 offset:0\n\t" \
  "ds_read_b64_tr_b16 v[202:203], v244 offset:320\n\t" \
  "ds_read_b64_tr_b16 v[204:205], v244 offset:1280\n\t" \
  "ds_read_b64_tr_b16 v[206:207], v244 offset:1600\n\t" \
  "v_add_u32 v233, v241, v232\n\t" \
  "ds_write_b16_d16_hi v233, v255 offset:5376\n\t" \
  "v_mfma_f32_16x16x32_bf16 v[132:135], v[96:99], v[184:187], 0\n\t" \
  "v_rcp_f32 v192, v192\n\t" \
  "v_rcp_f32 v193, v193\n\t" \
  "v_rcp_f32 v194, v194\n\t" \
  "v_rcp_f32 v195, v195\n\t" \
  "v_mfma_f32_32x32x16_bf16 v[100:115], v[4:7], v[148:151], 0\n\t" \
  "v_mul_f32 v196, 0xbfb8aa3b, v120\n\t" \
  "v_mul_f32 v197, 0xbfb8aa3b, v121\n\t" \
  "v_mul_f32 v198, 0xbfb8aa3b, v122\n\t" \
  "v_mul_f32 v199, 0xbfb8aa3b, v123\n\t" \
  "v_exp_f32 v196, v196\n\t" \
  "v_exp_f32 v197, v197\n\t" \
  "v_exp_f32 v198, v198\n\t" \
  "v_exp_f32 v199, v199\n\t" \
  "v_mfma_f32_32x32x16_bf16 v[100:115], v[12:15], v[152:155], v[100:115]\n\t" \
  "v_mul_f32 v192, v116, v192\n\t" \
  "v_mul_f32 v193, v117, v193\n\t" \
  "v_mul_f32 v194, v118, v194\n\t" \
  "v_mul_f32 v195, v119, v195\n\t" \
  "v_add_f32 v196, 1.0, v196\n\t" \
  "v_add_f32 v197, 1.0, v197\n\t" \
  "v_add_f32 v198, 1.0, v198\n\t" \
  "v_add_f32 v199, 1.0, v199\n\t" \
  "v_cvt_pk_bf16_f32 v184, v192, v193\n\t" \
  "v_cvt_pk_bf16_f32 v185, v194, v195\n\t" \
  "v_and_b32 v225, 0xffff0000, v225\n\t" \
  "s_waitcnt lgkmcnt(7)\n\t" \
  "v_lshlrev_b32 v219, 16, v224\n\t" \
  "v_and_b32 v224, 0xffff0000, v224\n\t" \
  "v_sub_f32 v229, v218, v225\n\t" \
  "v_sub_f32 v216, v216, v219\n\t" \
  "v_sub_f32 v217, v217, v224\n\t" \
  "v_mfma_f32_32x32x16_bf16 v[100:115], v[20:23], v[156:159], v[100:115]\n\t" \
  "v_rcp_f32 v196, v196\n\t" \
  "v_rcp_f32 v197, v197\n\t" \
  "v_rcp_f32 v198, v198\n\t" \
  "v_rcp_f32 v199, v199\n\t" \
  "v_mul_f32 v218, v229, v229\n\t" \
  "v_cndmask_b32 v228, v216, v217, vcc\n\t" \
  "v_fmac_f32 v218, v216, v216\n\t" \
  "v_fmac_f32 v218, v217, v217\n\t" \
  "v_cvt_pk_bf16_f32 v218, v218, v218\n\t" \
  "s_waitcnt lgkmcnt(6)\n\t" \
  "v_cndmask_b32_sdwa v175, v175, v218, vcc dst_sel:WORD_1 dst_unused:UNUSED_PRESERVE src0_sel:WORD_1 src1_sel:WORD_1\n\t" \
  "v_mfma_f32_16x16x32_bf16 v[132:135], v[136:139], v[188:191], v[132:135]\n\t" \
  "v_mul_f32 v192, 0xbfb8aa3b, v124\n\t" \
  "v_mul_f32 v193, 0xbfb8aa3b, v125\n\t" \
  "v_mul_f32 v194, 0xbfb8aa3b, v126\n\t" \
  "v_mul_f32 v195, 0xbfb8aa3b, v127\n\t" \
  "v_exp_f32 v192, v192\n\t" \
  "v_exp_f32 v193, v193\n\t" \
  "v_exp_f32 v194, v194\n\t" \
  "v_exp_f32 v195, v195\n\t" \
  "ds_read2_b32 v[220:221], v252 offset0:0 offset1:2\n\t" \
  "ds_read2_b32 v[222:223], v252 offset0:4 offset1:6\n\t" \
  "ds_read_b64 v[226:227], v253\n\t" \
  "ds_read_b32 v232, v254\n\t" \
  "v_mfma_f32_32x32x16_bf16 v[100:115], v[28:31], v[160:163], v[100:115]\n\t" \
  "v_mul_f32 v196, v120, v196\n\t" \
  "v_mul_f32 v197, v121, v197\n\t" \
  "v_mul_f32 v198, v122, v198\n\t" \
  "v_mul_f32 v199, v123, v199\n\t" \
  "v_add_u32 v252, 0x100, v252\n\t" \
  "v_add_u32 v253, 0x100, v253\n\t" \
  "v_add_u32 v254, 0x100, v254\n\t" \
  "v_add_f32 v192, 1.0, v192\n\t" \
  "v_add_f32 v193, 1.0, v193\n\t" \
  "v_add_f32 v194, 1.0, v194\n\t" \
  "v_add_f32 v195, 1.0, v195\n\t" \
  "v_cvt_pk_bf16_f32 v186, v196, v197\n\t" \
  "v_cvt_pk_bf16_f32 v187, v198, v199\n\t" \
  "s_waitcnt lgkmcnt(9)\n\t" \
  "v_mfma_f32_32x32x16_bf16 v[100:115], v[36:39], v[168:171], v[100:115]\n\t" \
  "v_rcp_f32 v192, v192\n\t" \
  "v_rcp_f32 v193, v193\n\t" \
  "v_rcp_f32 v194, v194\n\t" \
  "v_rcp_f32 v195, v195\n\t" \
  "v_mfma_f32_32x32x16_bf16 v[100:115], v[44:47], v[172:175], v[100:115]\n\t" \
  "v_mul_f32 v196, 0xbfb8aa3b, v128\n\t" \
  "v_mul_f32 v197, 0xbfb8aa3b, v129\n\t" \
  "v_mul_f32 v198, 0xbfb8aa3b, v130\n\t" \
  "v_mul_f32 v199, 0xbfb8aa3b, v131\n\t" \
  "v_exp_f32 v196, v196\n\t" \
  "v_exp_f32 v197, v197\n\t" \
  "v_exp_f32 v198, v198\n\t" \
  "v_exp_f32 v199, v199\n\t" \
  "v_mfma_f32_32x32x16_bf16 v[100:115], v[88:91], v[164:167], v[100:115]\n\t" \
  "v_mul_f32 v192, v124, v192\n\t" \
  "v_mul_f32 v193, v125, v193\n\t" \
  "v_mul_f32 v194, v126, v194\n\t" \
  "v_mul_f32 v195, v127, v195\n\t" \
  "ds_write_b128 v243, v[184:187] offset:0\n\t" \
  "v_add_f32 v196, 1.0, v196\n\t" \
  "v_add_f32 v197, 1.0, v197\n\t" \
  "v_add_f32 v198, 1.0, v198\n\t" \
  "v_add_f32 v199, 1.0, v199\n\t" \
  "v_cvt_pk_bf16_f32 v188, v192, v193\n\t" \
  "v_cvt_pk_bf16_f32 v189, v194, v195\n\t" \
  "s_waitcnt lgkmcnt(8)\n\t" \
  "v_mfma_f32_32x32x16_bf16 v[52:67], v[200:203], v[176:179], v[52:67]\n\t" \
  "v_rcp_f32 v196, v196\n\t" \
  "v_rcp_f32 v197, v197\n\t" \
  "v_rcp_f32 v198, v198\n\t" \
  "v_rcp_f32 v199, v199\n\t" \
  "s_waitcnt lgkmcnt(6)\n\t" \
  "v_mfma_f32_32x32x16_bf16 v[52:67], v[204:207], v[180:183], v[52:67]\n\t" \
  "v_mul_f32 v192, 0xbfb8aa3b, v100\n\t" \
  "v_mul_f32 v193, 0xbfb8aa3b, v101\n\t" \
  "v_mul_f32 v194, 0xbfb8aa3b, v102\n\t" \
  "v_mul_f32 v195, 0xbfb8aa3b, v103\n\t" \
  "v_exp_f32 v192, v192\n\t" \
  "v_exp_f32 v193, v193\n\t" \
  "v_exp_f32 v194, v194\n\t" \
  "v_exp_f32 v195, v195\n\t" \
  "s_cmp_eq_u32 %[nt], 0\n\t" \
  "v_mfma_f32_16x16x32_bf16 v[132:135], v[140:143], v[184:187], v[132:135]\n\t" \
  "v_mul_f32 v196, v128, v196\n\t" \
  "v_mul_f32 v197, v129, v197\n\t" \
  "v_mul_f32 v198, v130, v198\n\t" \
  "v_mul_f32 v199, v131, v199\n\t" \
  "v_add_f32 v192, 1.0, v192\n\t" \
  "v_add_f32 v193, 1.0, v193\n\t" \
  "v_add_f32 v194, 1.0, v194\n\t" \
  "v_add_f32 v195, 1.0, v195\n\t" \
  "v_cvt_pk_bf16_f32 v190, v196, v197\n\t" \
  "v_cvt_pk_bf16_f32 v191, v198, v199\n\t" \
  "s_cbranch_scc0 1b\n\t" \
  "s_waitcnt lgkmcnt(0)\n\t" \
  "s_cmp_lg_u32 %[last], 0\n\t" \
  "s_cbranch_scc1 8f\n\t" \
  "ds_write_b128 v243, v[188:191] offset:16\n\t" \
  "ds_read_b64_tr_b16 v[200:201], v244 offset:0\n\t" \
  "ds_read_b64_tr_b16 v[202:203], v244 offset:320\n\t" \
  "ds_read_b64_tr_b16 v[204:205], v244 offset:1280\n\t" \
  "ds_read_b64_tr_b16 v[206:207], v244 offset:1600\n\t" \
  "v_mfma_f32_16x16x32_bf16 v[132:135], v[144:147], v[188:191], v[132:135]\n\t" \
  "s_waitcnt lgkmcnt(0)\n\t" \
  "v_mfma_f32_32x32x16_bf16 v[68:83], v[200:203], v[176:179], v[68:83]\n\t" \
  "v_mfma_f32_32x32x16_bf16 v[68:83], v[204:207], v[180:183], v[68:83]\n\t" \
  "s_nop 8\n\t" \
  "v_mul_f32 v230, v230, v132\n\t" \
  "v_mul_f32 v231, v231, v132\n\t" \
  "v_cvt_pk_bf16_f32 v230, v230, v231\n\t" \
  "ds_write_b16 v245, v230 offset:7456\n\t" \
  "ds_write_b16_d16_hi v241, v230 offset:7584\n\t" \
  "ds_read_b128 v[200:203], v246 offset:7456\n\t" \
  "ds_read_b128 v[208:211], v246 offset:7488\n\t" \
  "s_waitcnt lgkmcnt(0)\n\t" \
  "v_mfma_f32_16x16x32_bf16 v[84:87], v[200:203], v[176:179], v[84:87]\n\t" \
  "v_mfma_f32_16x16x32_bf16 v[84:87], v[208:211], v[180:183], v[84:87]\n\t" \
  "s_branch 9f\n\t" \
  "6:\n\t" \
  "s_cmp_lg_u32 %[last], 0\n\t" \
  "s_cbranch_scc0 9f\n\t" \
  "s_waitcnt lgkmcnt(0)\n\t" \
  "v_cndmask_b32 v224, 0, 24, vcc\n\t" \
  "v_xor_b32 v220, v234, v224\n\t" \
  "v_xor_b32 v221, v235, v224\n\t" \
  "v_xor_b32 v222, v236, v224\n\t" \
  "v_xor_b32 v223, v237, v224\n\t" \
  "v_xor_b32 v216, 16, v220\n\t" \
  "v_xor_b32 v217, 16, v221\n\t" \
  "v_xor_b32 v218, 16, v222\n\t" \
  "v_xor_b32 v219, 16, v223\n\t" \
  "ds_read_b64 v[148:149], v220\n\t" \
  "ds_read_b64 v[150:151], v216\n\t" \
  "ds_read_b64 v[152:153], v221\n\t" \
  "ds_read_b64 v[154:155], v217\n\t" \
  "ds_read_b64 v[156:157], v222\n\t" \
  "ds_read_b64 v[158:159], v218\n\t" \
  "ds_read_b64 v[160:161], v223\n\t" \
  "ds_read_b64 v[162:163], v219\n\t" \
  "ds_read_b128 v[4:7], v247 offset:6144\n\t" \
  "ds_read_b128 v[8:11], v247 offset:7168\n\t" \
  "ds_read_b128 v[12:15], v247 offset:8192\n\t" \
  "ds_read_b128 v[16:19], v247 offset:9216\n\t" \
  "ds_read_b128 v[20:23], v247 offset:10240\n\t" \
  "ds_read_b128 v[24:27], v247 offset:11264\n\t" \
  "v_subrev_u32 v225, %[b1], v241\n\t" \
  "v_lshl_add_u32 v225, v225, 5, v243\n\t" \
  "v_sub_u32 v225, v225, v224\n\t" \
  "s_nop 4\n\t" \
  "v_cvt_pk_bf16_f32 v184, v52, v53\n\t" \
  "v_cvt_pk_bf16_f32 v185, v54, v55\n\t" \
  "v_cvt_pk_bf16_f32 v186, v56, v57\n\t" \
  "v_cvt_pk_bf16_f32 v187, v58, v59\n\t" \
  "v_cvt_pk_bf16_f32 v188, v60, v61\n\t" \
  "v_cvt_pk_bf16_f32 v189, v62, v63\n\t" \
  "v_cvt_pk_bf16_f32 v190, v64, v65\n\t" \
  "v_cvt_pk_bf16_f32 v191, v66, v67\n\t" \
  "v_cvt_pk_bf16_f32 v36, v68, v69\n\t" \
  "v_cvt_pk_bf16_f32 v37, v70, v71\n\t" \
  "v_cvt_pk_bf16_f32 v38, v72, v73\n\t" \
  "v_cvt_pk_bf16_f32 v39, v74, v75\n\t" \
  "v_cvt_pk_bf16_f32 v40, v76, v77\n\t" \
  "v_cvt_pk_bf16_f32 v41, v78, v79\n\t" \
  "v_cvt_pk_bf16_f32 v42, v80, v81\n\t" \
  "v_cvt_pk_bf16_f32 v43, v82, v83\n\t" \
  "s_waitcnt lgkmcnt(5)\n\t" \
  "v_mfma_f32_32x32x16_bf16 v[100:115], v[4:7], v[148:151], 0\n\t" \
  "s_waitcnt lgkmcnt(4)\n\t" \
  "v_mfma_f32_32x32x16_bf16 v[116:131], v[8:11], v[148:151], 0\n\t" \
  "ds_read_b128 v[28:31], v247 offset:12288\n\t" \
  "ds_read_b128 v[32:35], v247 offset:13312\n\t" \
  "s_waitcnt lgkmcnt(5)\n\t" \
  "v_mfma_f32_32x32x16_bf16 v[100:115], v[12:15], v[152:155], v[100:115]\n\t" \
  "s_waitcnt lgkmcnt(4)\n\t" \
  "v_mfma_f32_32x32x16_bf16 v[116:131], v[16:19], v[152:155], v[116:131]\n\t" \
  "ds_read_b128 v[4:7], v247 offset:14336\n\t" \
  "ds_read_b128 v[8:11], v247 offset:15360\n\t" \
  "s_waitcnt lgkmcnt(5)\n\t" \
  "v_mfma_f32_32x32x16_bf16 v[100:115], v[20:23], v[156:159], v[100:115]\n\t" \
  "s_waitcnt lgkmcnt(4)\n\t" \
  "v_mfma_f32_32x32x16_bf16 v[116:131], v[24:27], v[156:159], v[116:131]\n\t" \
  "ds_read_b128 v[12:15], v247 offset:16384\n\t" \
  "ds_read_b128 v[16:19], v247 offset:17408\n\t" \
  "s_waitcnt lgkmcnt(5)\n\t" \
  "v_mfma_f32_32x32x16_bf16 v[100:115], v[28:31], v[160:163], v[100:115]\n\t" \
  "s_waitcnt lgkmcnt(4)\n\t" \
  "v_mfma_f32_32x32x16_bf16 v[116:131], v[32:35], v[160:163], v[116:131]\n\t" \
  "ds_read_b128 v[20:23], v247 offset:18432\n\t" \
  "ds_read_b128 v[24:27], v247 offset:19456\n\t" \
  "s_waitcnt lgkmcnt(5)\n\t" \
  "v_mfma_f32_32x32x16_bf16 v[100:115], v[4:7], v[184:187], v[100:115]\n\t" \
  "ds_read_b128 v[28:31], v247 offset:20480\n\t" \
  "ds_read_b128 v[32:35], v247 offset:21504\n\t" \
  "s_waitcnt lgkmcnt(5)\n\t" \
  "v_mfma_f32_32x32x16_bf16 v[100:115], v[12:15], v[188:191], v[100:115]\n\t" \
  "s_waitcnt lgkmcnt(3)\n\t" \
  "v_mfma_f32_32x32x16_bf16 v[100:115], v[20:23], v[36:39], v[100:115]\n\t" \
  "s_waitcnt lgkmcnt(1)\n\t" \
  "v_mfma_f32_32x32x16_bf16 v[100:115], v[28:31], v[40:43], v[100:115]\n\t" \
  "s_waitcnt lgkmcnt(0)\n\t" \
  "s_branch 10f\n\t" \
  "8:\n\t" \
  "ds_write_b128 v243, v[188:191] offset:16\n\t" \
  "ds_read_b64_tr_b16 v[200:201], v244 offset:0\n\t" \
  "ds_read_b64_tr_b16 v[202:203], v244 offset:320\n\t" \
  "ds_read_b64_tr_b16 v[204:205], v244 offset:1280\n\t" \
  "ds_read_b64_tr_b16 v[206:207], v244 offset:1600\n\t" \
  "v_mfma_f32_16x16x32_bf16 v[132:135], v[144:147], v[188:191], v[132:135]\n\t" \
  "v_cndmask_b32 v224, 0, 24, vcc\n\t" \
  "v_xor_b32 v220, v234, v224\n\t" \
  "v_xor_b32 v221, v235, v224\n\t" \
  "v_xor_b32 v222, v236, v224\n\t" \
  "v_xor_b32 v223, v237, v224\n\t" \
  "v_xor_b32 v216, 16, v220\n\t" \
  "v_xor_b32 v217, 16, v221\n\t" \
  "v_xor_b32 v218, 16, v222\n\t" \
  "v_xor_b32 v219, 16, v223\n\t" \
  "ds_read_b64 v[148:149], v220\n\t" \
  "ds_read_b64 v[150:151], v216\n\t" \
  "ds_read_b64 v[152:153], v221\n\t" \
  "ds_read_b64 v[154:155], v217\n\t" \
  "ds_read_b64 v[156:157], v222\n\t" \
  "ds_read_b64 v[158:159], v218\n\t" \
  "ds_read_b64 v[160:161], v223\n\t" \
  "ds_read_b64 v[162:163], v219\n\t" \
  "ds_read_b128 v[4:7], v247 offset:6144\n\t" \
  "ds_read_b128 v[8:11], v247 offset:7168\n\t" \
  "s_waitcnt lgkmcnt(11)\n\t" \
  "ds_read_b128 v[12:15], v247 offset:8192\n\t" \
  "ds_read_b128 v[16:19], v247 offset:9216\n\t" \
  "ds_read_b128 v[20:23], v247 offset:10240\n\t" \
  "ds_read_b128 v[24:27], v247 offset:11264\n\t" \
  "v_subrev_u32 v225, %[b1], v241\n\t" \
  "v_lshl_add_u32 v225, v225, 5, v243\n\t" \
  "v_sub_u32 v225, v225, v224\n\t" \
  "v_mfma_f32_32x32x16_bf16 v[68:83], v[200:203], v[176:179], v[68:83]\n\t" \
  "s_waitcnt lgkmcnt(5)\n\t" \
  "v_mfma_f32_32x32x16_bf16 v[100:115], v[4:7], v[148:151], 0\n\t" \
  "v_mfma_f32_32x32x16_bf16 v[68:83], v[204:207], v[180:183], v[68:83]\n\t" \
  "s_waitcnt lgkmcnt(4)\n\t" \
  "v_mfma_f32_32x32x16_bf16 v[116:131], v[8:11], v[148:151], 0\n\t" \
  "ds_read_b128 v[28:31], v247 offset:12288\n\t" \
  "ds_read_b128 v[32:35], v247 offset:13312\n\t" \
  "s_waitcnt lgkmcnt(5)\n\t" \
  "v_mfma_f32_32x32x16_bf16 v[100:115], v[12:15], v[152:155], v[100:115]\n\t" \
  "v_mul_f32 v230, v230, v132\n\t" \
  "v_mul_f32 v231, v231, v132\n\t" \
  "v_cvt_pk_bf16_f32 v230, v230, v231\n\t" \
  "ds_write_b16 v245, v230 offset:7456\n\t" \
  "ds_write_b16_d16_hi v241, v230 offset:7584\n\t" \
  "ds_read_b128 v[200:203], v246 offset:7456\n\t" \
  "ds_read_b128 v[208:211], v246 offset:7488\n\t" \
  "v_cvt_pk_bf16_f32 v184, v52, v53\n\t" \
  "v_cvt_pk_bf16_f32 v185, v54, v55\n\t" \
  "v_cvt_pk_bf16_f32 v186, v56, v57\n\t" \
  "v_cvt_pk_bf16_f32 v187, v58, v59\n\t" \
  "s_waitcnt lgkmcnt(8)\n\t" \
  "v_mfma_f32_32x32x16_bf16 v[116:131], v[16:19], v[152:155], v[116:131]\n\t" \
  "ds_read_b128 v[4:7], v247 offset:14336\n\t" \
  "ds_read_b128 v[8:11], v247 offset:15360\n\t" \
  "s_waitcnt lgkmcnt(9)\n\t" \
  "v_mfma_f32_32x32x16_bf16 v[100:115], v[20:23], v[156:159], v[100:115]\n\t" \
  "v_cvt_pk_bf16_f32 v188, v60, v61\n\t" \
  "v_cvt_pk_bf16_f32 v189, v62, v63\n\t" \
  "v_cvt_pk_bf16_f32 v190, v64, v65\n\t" \
  "v_cvt_pk_bf16_f32 v191, v66, v67\n\t" \
  "s_waitcnt lgkmcnt(8)\n\t" \
  "v_mfma_f32_32x32x16_bf16 v[116:131], v[24:27], v[156:159], v[116:131]\n\t" \
  "ds_read_b128 v[12:15], v247 offset:16384\n\t" \
  "ds_read_b128 v[16:19], v247 offset:17408\n\t" \
  "s_waitcnt lgkmcnt(9)\n\t" \
  "v_mfma_f32_32x32x16_bf16 v[100:115], v[28:31], v[160:163], v[100:115]\n\t" \
  "v_cvt_pk_bf16_f32 v36, v68, v69\n\t" \
  "v_cvt_pk_bf16_f32 v37, v70, v71\n\t" \
  "v_cvt_pk_bf16_f32 v38, v72, v73\n\t" \
  "v_cvt_pk_bf16_f32 v39, v74, v75\n\t" \
  "s_waitcnt lgkmcnt(5)\n\t" \
  "v_mfma_f32_16x16x32_bf16 v[84:87], v[200:203], v[176:179], v[84:87]\n\t" \
  "v_cvt_pk_bf16_f32 v40, v76, v77\n\t" \
  "v_cvt_pk_bf16_f32 v41, v78, v79\n\t" \
  "v_cvt_pk_bf16_f32 v42, v80, v81\n\t" \
  "v_cvt_pk_bf16_f32 v43, v82, v83\n\t" \
  "v_mfma_f32_32x32x16_bf16 v[116:131], v[32:35], v[160:163], v[116:131]\n\t" \
  "ds_read_b128 v[20:23], v247 offset:18432\n\t" \
  "ds_read_b128 v[24:27], v247 offset:19456\n\t" \
  "s_waitcnt lgkmcnt(6)\n\t" \
  "v_mfma_f32_16x16x32_bf16 v[84:87], v[208:211], v[180:183], v[84:87]\n\t" \
  "s_waitcnt lgkmcnt(5)\n\t" \
  "v_mfma_f32_32x32x16_bf16 v[100:115], v[4:7], v[184:187], v[100:115]\n\t" \
  "ds_read_b128 v[28:31], v247 offset:20480\n\t" \
  "ds_read_b128 v[32:35], v247 offset:21504\n\t" \
  "s_waitcnt lgkmcnt(5)\n\t" \
  "v_mfma_f32_32x32x16_bf16 v[100:115], v[12:15], v[188:191], v[100:115]\n\t" \
  "s_waitcnt lgkmcnt(3)\n\t" \
  "v_mfma_f32_32x32x16_bf16 v[100:115], v[20:23], v[36:39], v[100:115]\n\t" \
  "s_waitcnt lgkmcnt(1)\n\t" \
  "v_mfma_f32_32x32x16_bf16 v[100:115], v[28:31], v[40:43], v[100:115]\n\t" \
  "s_waitcnt lgkmcnt(0)\n\t" \
  "10:\n\t" \
  "v_mov_b32 v212, 0xbfb8aa3b\n\t" \
  "v_mfma_f32_32x32x16_bf16 v[116:131], v[8:11], v[184:187], v[116:131]\n\t" \
  "v_mfma_f32_32x32x16_bf16 v[116:131], v[16:19], v[188:191], v[116:131]\n\t" \
  "s_nop 8\n\t" \
  "v_mul_f32 v192, 0xbfb8aa3b, v100\n\t" \
  "v_mul_f32 v193, 0xbfb8aa3b, v101\n\t" \
  "v_mul_f32 v194, 0xbfb8aa3b, v102\n\t" \
  "v_mul_f32 v195, 0xbfb8aa3b, v103\n\t" \
  "v_exp_f32 v192, v192\n\t" \
  "v_exp_f32 v193, v193\n\t" \
  "v_exp_f32 v194, v194\n\t" \
  "v_exp_f32 v195, v195\n\t" \
  "v_lshlrev_b32 v200, 16, v148\n\t" \
  "v_and_b32 v201, 0xffff0000, v148\n\t" \
  "v_lshlrev_b32 v202, 16, v149\n\t" \
  "v_and_b32 v203, 0xffff0000, v149\n\t" \
  "v_mfma_f32_32x32x16_bf16 v[116:131], v[24:27], v[36:39], v[116:131]\n\t" \
  "v_add_f32 v192, 1.0, v192\n\t" \
  "v_add_f32 v193, 1.0, v193\n\t" \
  "v_add_f32 v194, 1.0, v194\n\t" \
  "v_add_f32 v195, 1.0, v195\n\t" \
  "v_rcp_f32 v192, v192\n\t" \
  "v_rcp_f32 v193, v193\n\t" \
  "v_rcp_f32 v194, v194\n\t" \
  "v_rcp_f32 v195, v195\n\t" \
  "v_mfma_f32_32x32x16_bf16 v[116:131], v[32:35], v[40:43], v[116:131]\n\t" \
  "v_mul_f32 v196, 0xbfb8aa3b, v104\n\t" \
  "v_mul_f32 v197, 0xbfb8aa3b, v105\n\t" \
  "v_mul_f32 v198, 0xbfb8aa3b, v106\n\t" \
  "v_mul_f32 v199, 0xbfb8aa3b, v107\n\t" \
  "v_exp_f32 v196, v196\n\t" \
  "v_exp_f32 v197, v197\n\t" \
  "v_exp_f32 v198, v198\n\t" \
  "v_exp_f32 v199, v199\n\t" \
  "v_fmac_f32 v200, v100, v192\n\t" \
  "v_fmac_f32 v201, v101, v193\n\t" \
  "v_fmac_f32 v202, v102, v194\n\t" \
  "v_fmac_f32 v203, v103, v195\n\t" \
  "v_lshlrev_b32 v204, 16, v150\n\t" \
  "v_and_b32 v205, 0xffff0000, v150\n\t" \
  "v_lshlrev_b32 v206, 16, v151\n\t" \
  "v_and_b32 v207, 0xffff0000, v151\n\t" \
  "v_add_f32 v196, 1.0, v196\n\t" \
  "v_add_f32 v197, 1.0, v197\n\t" \
  "v_add_f32 v198, 1.0, v198\n\t" \
  "v_add_f32 v199, 1.0, v199\n\t" \
  "v_cvt_pk_bf16_f32 v176, v200, v201\n\t" \
  "v_cvt_pk_bf16_f32 v177, v202, v203\n\t" \
  "ds_write_b64 v225, v[176:177] offset:0\n\t" \
  "v_rcp_f32 v196, v196\n\t" \
  "v_rcp_f32 v197, v197\n\t" \
  "v_rcp_f32 v198, v198\n\t" \
  "v_rcp_f32 v199, v199\n\t" \
  "v_mul_f32 v192, 0xbfb8aa3b, v108\n\t" \
  "v_mul_f32 v193, 0xbfb8aa3b, v109\n\t" \
  "v_mul_f32 v194, 0xbfb8aa3b, v110\n\t" \
  "v_mul_f32 v195, 0xbfb8aa3b, v111\n\t" \
  "v_exp_f32 v192, v192\n\t" \
  "v_exp_f32 v193, v193\n\t" \
  "v_exp_f32 v194, v194\n\t" \
  "v_exp_f32 v195, v195\n\t" \
  "v_fmac_f32 v204, v104, v196\n\t" \
  "v_fmac_f32 v205, v105, v197\n\t" \
  "v_fmac_f32 v206, v106, v198\n\t" \
  "v_fmac_f32 v207, v107, v199\n\t" \
  "v_lshlrev_b32 v200, 16, v152\n\t" \
  "v_and_b32 v201, 0xffff0000, v152\n\t" \
  "v_lshlrev_b32 v202, 16, v153\n\t" \
  "v_and_b32 v203, 0xffff0000, v153\n\t" \
  "v_add_f32 v192, 1.0, v192\n\t" \
  "v_add_f32 v193, 1.0, v193\n\t" \
  "v_add_f32 v194, 1.0, v194\n\t" \
  "v_add_f32 v195, 1.0, v195\n\t" \
  "v_cvt_pk_bf16_f32 v178, v204, v205\n\t" \
  "v_cvt_pk_bf16_f32 v179, v206, v207\n\t" \
  "ds_write_b64 v225, v[178:179] offset:16\n\t" \
  "v_rcp_f32 v192, v192\n\t" \
  "v_rcp_f32 v193, v193\n\t" \
  "v_rcp_f32 v194, v194\n\t" \
  "v_rcp_f32 v195, v195\n\t" \
  "v_mul_f32 v196, 0xbfb8aa3b, v112\n\t" \
  "v_mul_f32 v197, 0xbfb8aa3b, v113\n\t" \
  "v_mul_f32 v198, 0xbfb8aa3b, v114\n\t" \
  "v_mul_f32 v199, 0xbfb8aa3b, v115\n\t" \
  "v_exp_f32 v196, v196\n\t" \
  "v_exp_f32 v197, v197\n\t" \
  "v_exp_f32 v198, v198\n\t" \
  "v_exp_f32 v199, v199\n\t" \
  "v_fmac_f32 v200, v108, v192\n\t" \
  "v_fmac_f32 v201, v109, v193\n\t" \
  "v_fmac_f32 v202, v110, v194\n\t" \
  "v_fmac_f32 v203, v111, v195\n\t" \
  "v_lshlrev_b32 v204, 16, v154\n\t" \
  "v_and_b32 v205, 0xffff0000, v154\n\t" \
  "v_lshlrev_b32 v206, 16, v155\n\t" \
  "v_and_b32 v207, 0xffff0000, v155\n\t" \
  "v_add_f32 v196, 1.0, v196\n\t" \
  "v_add_f32 v197, 1.0, v197\n\t" \
  "v_add_f32 v198, 1.0, v198\n\t" \
  "v_add_f32 v199, 1.0, v199\n\t" \
  "v_cvt_pk_bf16_f32 v180, v200, v201\n\t" \
  "v_cvt_pk_bf16_f32 v181, v202, v203\n\t" \
  "ds_write_b64 v225, v[180:181] offset:32\n\t" \
  "v_rcp_f32 v196, v196\n\t" \
  "v_rcp_f32 v197, v197\n\t" \
  "v_rcp_f32 v198, v198\n\t" \
  "v_rcp_f32 v199, v199\n\t" \
  "v_mul_f32 v192, 0xbfb8aa3b, v116\n\t" \
  "v_mul_f32 v193, 0xbfb8aa3b, v117\n\t" \
  "v_mul_f32 v194, 0xbfb8aa3b, v118\n\t" \
  "v_mul_f32 v195, 0xbfb8aa3b, v119\n\t" \
  "v_exp_f32 v192, v192\n\t" \
  "v_exp_f32 v193, v193\n\t" \
  "v_exp_f32 v194, v194\n\t" \
  "v_exp_f32 v195, v195\n\t" \
  "v_fmac_f32 v204, v112, v196\n\t" \
  "v_fmac_f32 v205, v113, v197\n\t" \
  "v_fmac_f32 v206, v114, v198\n\t" \
  "v_fmac_f32 v207, v115, v199\n\t" \
  "v_lshlrev_b32 v200, 16, v156\n\t" \
  "v_and_b32 v201, 0xffff0000, v156\n\t" \
  "v_lshlrev_b32 v202, 16, v157\n\t" \
  "v_and_b32 v203, 0xffff0000, v157\n\t" \
  "v_add_f32 v192, 1.0, v192\n\t" \
  "v_add_f32 v193, 1.0, v193\n\t" \
  "v_add_f32 v194, 1.0, v194\n\t" \
  "v_add_f32 v195, 1.0, v195\n\t" \
  "v_cvt_pk_bf16_f32 v182, v204, v205\n\t" \
  "v_cvt_pk_bf16_f32 v183, v206, v207\n\t" \
  "ds_write_b64 v225, v[182:183] offset:48\n\t" \
  "v_rcp_f32 v192, v192\n\t" \
  "v_rcp_f32 v193, v193\n\t" \
  "v_rcp_f32 v194, v194\n\t" \
  "v_rcp_f32 v195, v195\n\t" \
  "v_mul_f32 v196, 0xbfb8aa3b, v120\n\t" \
  "v_mul_f32 v197, 0xbfb8aa3b, v121\n\t" \
  "v_mul_f32 v198, 0xbfb8aa3b, v122\n\t" \
  "v_mul_f32 v199, 0xbfb8aa3b, v123\n\t" \
  "v_exp_f32 v196, v196\n\t" \
  "v_exp_f32 v197, v197\n\t" \
  "v_exp_f32 v198, v198\n\t" \
  "v_exp_f32 v199, v199\n\t" \
  "v_fmac_f32 v200, v116, v192\n\t" \
  "v_fmac_f32 v201, v117, v193\n\t" \
  "v_fmac_f32 v202, v118, v194\n\t" \
  "v_fmac_f32 v203, v119, v195\n\t" \
  "v_lshlrev_b32 v204, 16, v158\n\t" \
  "v_and_b32 v205, 0xffff0000, v158\n\t" \
  "v_lshlrev_b32 v206, 16, v159\n\t" \
  "v_and_b32 v207, 0xffff0000, v159\n\t" \
  "v_add_f32 v196, 1.0, v196\n\t" \
  "v_add_f32 v197, 1.0, v197\n\t" \
  "v_add_f32 v198, 1.0, v198\n\t" \
  "v_add_f32 v199, 1.0, v199\n\t" \
  "v_cvt_pk_bf16_f32 v176, v200, v201\n\t" \
  "v_cvt_pk_bf16_f32 v177, v202, v203\n\t" \
  "ds_write_b64 v225, v[176:177] offset:64\n\t" \
  "v_rcp_f32 v196, v196\n\t" \
  "v_rcp_f32 v197, v197\n\t" \
  "v_rcp_f32 v198, v198\n\t" \
  "v_rcp_f32 v199, v199\n\t" \
  "v_mul_f32 v192, 0xbfb8aa3b, v124\n\t" \
  "v_mul_f32 v193, 0xbfb8aa3b, v125\n\t" \
  "v_mul_f32 v194, 0xbfb8aa3b, v126\n\t" \
  "v_mul_f32 v195, 0xbfb8aa3b, v127\n\t" \
  "v_exp_f32 v192, v192\n\t" \
  "v_exp_f32 v193, v193\n\t" \
  "v_exp_f32 v194, v194\n\t" \
  "v_exp_f32 v195, v195\n\t" \
  "v_fmac_f32 v204, v120, v196\n\t" \
  "v_fmac_f32 v205, v121, v197\n\t" \
  "v_fmac_f32 v206, v122, v198\n\t" \
  "v_fmac_f32 v207, v123, v199\n\t" \
  "v_lshlrev_b32 v200, 16, v160\n\t" \
  "v_and_b32 v201, 0xffff0000, v160\n\t" \
  "v_lshlrev_b32 v202, 16, v161\n\t" \
  "v_and_b32 v203, 0xffff0000, v161\n\t" \
  "v_add_f32 v192, 1.0, v192\n\t" \
  "v_add_f32 v193, 1.0, v193\n\t" \
  "v_add_f32 v194, 1.0, v194\n\t" \
  "v_add_f32 v195, 1.0, v195\n\t" \
  "v_cvt_pk_bf16_f32 v178, v204, v205\n\t" \
  "v_cvt_pk_bf16_f32 v179, v206, v207\n\t" \
  "ds_write_b64 v225, v[178:179] offset:80\n\t" \
  "v_rcp_f32 v192, v192\n\t" \
  "v_rcp_f32 v193, v193\n\t" \
  "v_rcp_f32 v194, v194\n\t" \
  "v_rcp_f32 v195, v195\n\t" \
  "v_mul_f32 v196, 0xbfb8aa3b, v128\n\t" \
  "v_mul_f32 v197, 0xbfb8aa3b, v129\n\t" \
  "v_mul_f32 v198, 0xbfb8aa3b, v130\n\t" \
  "v_mul_f32 v199, 0xbfb8aa3b, v131\n\t" \
  "v_exp_f32 v196, v196\n\t" \
  "v_exp_f32 v197, v197\n\t" \
  "v_exp_f32 v198, v198\n\t" \
  "v_exp_f32 v199, v199\n\t" \
  "v_fmac_f32 v200, v124, v192\n\t" \
  "v_fmac_f32 v201, v125, v193\n\t" \
  "v_fmac_f32 v202, v126, v194\n\t" \
  "v_fmac_f32 v203, v127, v195\n\t" \
  "v_lshlrev_b32 v204, 16, v162\n\t" \
  "v_and_b32 v205, 0xffff0000, v162\n\t" \
  "v_lshlrev_b32 v206, 16, v163\n\t" \
  "v_and_b32 v207, 0xffff0000, v163\n\t" \
  "v_add_f32 v196, 1.0, v196\n\t" \
  "v_add_f32 v197, 1.0, v197\n\t" \
  "v_add_f32 v198, 1.0, v198\n\t" \
  "v_add_f32 v199, 1.0, v199\n\t" \
  "v_cvt_pk_bf16_f32 v180, v200, v201\n\t" \
  "v_cvt_pk_bf16_f32 v181, v202, v203\n\t" \
  "ds_write_b64 v225, v[180:181] offset:96\n\t" \
  "v_rcp_f32 v196, v196\n\t" \
  "v_rcp_f32 v197, v197\n\t" \
  "v_rcp_f32 v198, v198\n\t" \
  "v_rcp_f32 v199, v199\n\t" \
  "s_nop 0\n\t" \
  "v_fmac_f32 v204, v128, v196\n\t" \
  "v_fmac_f32 v205, v129, v197\n\t" \
  "v_fmac_f32 v206, v130, v198\n\t" \
  "v_fmac_f32 v207, v131, v199\n\t" \
  "v_cvt_pk_bf16_f32 v182, v204, v205\n\t" \
  "v_cvt_pk_bf16_f32 v183, v206, v207\n\t" \
  "ds_write_b64 v225, v[182:183] offset:112\n\t" \
  "9:\n\t" \
  "ds_write_b16 v233, v255 offset:5376\n\t" \
  "s_mov_b32 m0, %[keep]\n\t" \
  "s_setprio 0\n\t" \
  "s_nop 11\n\t" \
  ""
#define LOOP_ASM LOOP_ASM_FOLD

DI u32x4 tr_frag(unsigned p, int half) {
  const s16x4 lo = __builtin_amdgcn_ds_read_tr16_b64_v4i16((lds_s16x4_p)p);
  const s16x4 hi = __builtin_amdgcn_ds_read_tr16_b64_v4i16((lds_s16x4_p)(p + half));
  return __builtin_bit_cast(u32x4, __builtin_shufflevector(lo, hi, 0, 1, 2, 3, 4, 5, 6, 7));
}
DI u32x4 fold(u32x4 v) {
#pragma unroll
  for (int i = 0; i < 4; i++) v[i] = pk_bf16(bf2f(v[i] & 0xffffu) * -LOG2E, __uint_as_float(v[i] & 0xffff0000u) * -LOG2E);
  return v;
}

__global__ __launch_bounds__(512, 2) void egnn_kernel(const char* __restrict__ hmat, const char* __restrict__ pos,
                                                      const char* __restrict__ ea, const int* __restrict__ src,
                                                      const int* __restrict__ dst, const char* __restrict__ we,
                                                      const char* __restrict__ wx, const char* __restrict__ wh,
                                                      char* __restrict__ ws, uint16_t* __restrict__ out, int N, int E STAMP_ARG) {
  const int w = __builtin_amdgcn_readfirstlane(threadIdx.x >> 6);
  const int bkt = (blockIdx.x & 7) * 32 + (blockIdx.x >> 3);
  const int n0 = bkt * NB;
  STAMP(0);
  unsigned wl = L_WAVE + w * WSZ, mi = L_MI + w * MISZ;
  const auto rs_tab = __builtin_amdgcn_make_buffer_rsrc((void*)(ws + WS_TAB), 0, NBK * NBK * 8, 0x00020000);
  const auto rs_rec = __builtin_amdgcn_make_buffer_rsrc((void*)(ws + WS_REC), 0, NBK * CHS, 0x00020000);
  const auto rs_ea = __builtin_amdgcn_make_buffer_rsrc((void*)ea, 0, E * 32, 0x00020000);
  unsigned tag_lo, tag_hi;
  unsigned wdv0, wdv1;

  {
    const int lane = lane_id(), tid = w * 64 + lane, r = lane & 31, h = lane >> 5;
    const int e0 = bkt * CH;
    int ne = max(E - e0, 0);
    asm("" : "+s"(ne));
    ne = min(ne, CH);
    const auto rs_d = __builtin_amdgcn_make_buffer_rsrc((void*)(dst + e0), 0, ne * 4, 0x00020000);
    const auto rs_s = __builtin_amdgcn_make_buffer_rsrc((void*)(src + e0), 0, ne * 4, 0x00020000);
    unsigned d[7], sv[7];
    u32x2 xa, xb;
    unsigned long long ep;
    asm volatile("s_load_dwordx2 %0, %1, 0x0" : "=s"(ep) : "s"(ws + WS_EPOCH) : "memory");
#pragma unroll
    for (int i = 0; i < 7; i++)
      asm volatile("buffer_load_dword %0, %2, %3, %5 offen nt\n\tbuffer_load_dword %1, %2, %4, %5 offen nt"
                   : "=&v"(d[i]), "=&v"(sv[i]) : "v"(tid * 4), "s"(rs_d), "s"(rs_s), "s"(i * 2048) : "memory");
    LDS(unsigned, L_CNT + tid * 4) = 0;
    asm volatile("s_waitcnt lgkmcnt(0)\n\ts_barrier" : "+s"(ep) :: "memory");
    asm volatile("global_load_ushort %0, %4, %5\n\tglobal_load_ushort %1, %4, %5 offset:64\n\t"
                 "global_load_dwordx2 %2, %6, %7\n\tglobal_load_dwordx2 %3, %6, %7 offset:16"
                 : "=&v"(wdv0), "=&v"(wdv1), "=&v"(xa), "=&v"(xb)
                 : "v"(r * 2), "s"(we + 128 * 128), "v"(h * 8), "s"(wx + ((w + 2) & 3) * 32) : "memory");
    {
      const int j = lane >> 3, jl = (j & 3) * 128 + (lane & 7) * 16, jh = (j >> 2) * 128;
      const char* const h0 = wh + (w < 4 ? 16 * w : 64 + 32 * ((w - 4) >> 1) + 4 * (w & 1)) * 128;
      const char* const e0p = we + (w < 4 ? 64 + 16 * w : 16 * (w - 4)) * 128;
      const unsigned oh = jl + jh * (w < 4 ? 8 : 16), oe = jl + jh * (w < 4 ? 4 : 8);
      unsigned keep;
      asm volatile("s_mov_b32 %0, m0\n\ts_mov_b32 m0, %9\n\ts_nop 0\n\t"
                   "global_load_lds_dwordx4 %1, %4\n\tglobal_load_lds_dwordx4 %1, %5 offset:1024\n\t"
                   "global_load_lds_dwordx4 %2, %6 offset:2048\n\tglobal_load_lds_dwordx4 %2, %7 offset:3072\n\t"
                   "s_add_u32 m0, %9, 0x1000\n\ts_nop 0\n\tglobal_load_lds_dwordx4 %3, %8\n\ts_mov_b32 m0, %0"
                   : "=&s"(keep)
                   : "v"(oh), "v"(oe), "v"(w < 2 ? (lane >> 2) * 128 + (lane & 3) * 16 : 0),
                     "s"(h0), "s"(h0 + (w < 4 ? 4 : 8) * 128 - 1024), "s"(e0p - 2048), "s"(e0p + (w < 4 ? 8 : 4) * 128 - 3072),
                     "s"(we + 129 * 128 + (w & 1) * 64), "s"(wl)
                   : "memory", "scc");
    }
    unsigned ra[5], keep;
    {
      const unsigned cc0 = ((lane & 7) ^ (lane >> 4)) * 16, cc1 = ((lane & 7) ^ (4 + (lane >> 4))) * 16;
      const int nr = n0 + w + (lane & 56);
      ra[0] = min(nr, N - 1) * 128 + cc0; ra[1] = min(nr + 64, N - 1) * 128 + cc1; ra[2] = min(nr + 128, N - 1) * 128 + cc0;
      ra[3] = min(n0 + w + 192, N - 1) * 128 + cc1; ra[4] = mul24(min(n0 + w + min(lane >> 1, 24) * 8, N - 1), 6) + (lane & 1) * 2;
      asm volatile("" : "+v"(ra[0]), "+v"(ra[1]), "+v"(ra[2]), "+v"(ra[3]), "+v"(ra[4]));
    }
    asm volatile("s_waitcnt vmcnt(9)" : "+v"(d[0]), "+v"(d[1]), "+v"(d[2]), "+v"(d[3]), "+v"(d[4]), "+v"(d[5]), "+v"(d[6]),
                 "+v"(sv[0]), "+v"(sv[1]), "+v"(sv[2]), "+v"(sv[3]), "+v"(sv[4]), "+v"(sv[5]), "+v"(sv[6]) :: "memory");
    STAMP(10);
    {
      asm volatile("s_mov_b32 %0, m0\n\t"
                   "s_mov_b32 m0, %8\n\ts_nop 0\n\tglobal_load_lds_dwordx4 %1, %6\n\t"
                   "s_add_u32 m0, %8, 0x400\n\ts_nop 0\n\tglobal_load_lds_dwordx4 %2, %6\n\t"
                   "s_add_u32 m0, %8, 0x800\n\ts_nop 0\n\tglobal_load_lds_dwordx4 %3, %6\n\t"
                   "s_add_u32 m0, %8, 0xc00\n\ts_nop 0\n\tglobal_load_lds_dwordx4 %4, %6\n\t"
                   "s_add_u32 m0, %8, 0x1000\n\ts_nop 0\n\tglobal_load_lds_dword %5, %7\n\ts_mov_b32 m0, %0"
                   : "=&s"(keep)
                   : "v"(ra[0]), "v"(ra[1]), "v"(ra[2]), "v"(ra[3]), "v"(ra[4]), "s"(hmat), "s"(pos), "s"(mi)
                   : "memory", "scc");
    }
    unsigned ent, total;
    auto sort = [&](auto NS_, auto FULL_) __attribute__((always_inline)) {
      constexpr int NS = decltype(NS_)::value;
      constexpr bool FULL = decltype(FULL_)::value;
      unsigned bk[NS], rank[NS];
      u32x2 rec[NS];
#pragma unroll
      for (int i = 0; i < NS; i++) {
        const int el = i * 512 + tid;
        const unsigned b = mul24(d[i], 85599u) >> 24;
        const unsigned dl = d[i] - mul24(b, NB);
        bk[i] = (FULL || el < ne ? b : NBK + lane) * 4;
        rank[i] = __hip_atomic_fetch_add(&LDS(unsigned, L_CNT + bk[i]), 8u, __ATOMIC_RELAXED, __HIP_MEMORY_SCOPE_WORKGROUP);
        rec[i][0] = el;
        rec[i][1] = ((mul24(dl >> 3, 1272u) + dl) << 16) | sv[i] | 0x80000u;
      }
      LDS(unsigned, L_BASE + (NBK + lane) * 4) = 0x40000000u;
      barrier_lds();
      u32x4 cv = LDS(u32x4, L_CNT + lane * 16);
#pragma unroll
      for (int i = 0; i < 4; i++) cv[i] = (cv[i] + 8) & ~15u;
      const unsigned sum = cv[0] + cv[1] + cv[2] + cv[3], incl = wave_scan_incl(sum);
      const unsigned ex = incl - sum + L_REC;
      total = __builtin_amdgcn_readlane(incl, 63);
      const u32x4 bv = {ex, ex + cv[0], ex + cv[0] + cv[1], ex + cv[0] + cv[1] + cv[2]};
      LDS(u32x4, L_BASE + lane * 16) = bv;
      unsigned bs[NS];
#pragma unroll
      for (int i = 0; i < NS; i++) bs[i] = LDS(unsigned, L_BASE + bk[i]);
      ent = ((LDS(unsigned, L_BASE + (tid & 255) * 4) - L_REC) >> 4) | (LDS(unsigned, L_CNT + (tid & 255) * 4) << 9);
      const unsigned lim = L_REC + 8 * SEG + lane * 8;
#pragma unroll
      for (int i = 0; i < NS; i++) LDS(u32x2, FULL ? bs[i] + rank[i] : min(bs[i] + rank[i], lim)) = rec[i];
      barrier_lds();
    };
    if (ne == CH && w != 0) sort(std::integral_constant<int, 6>(), std::true_type());
    else sort(std::integral_constant<int, 7>(), std::false_type());
    LDS(unsigned, L_CNT + tid * 4) = 0;
    STAMP(11);
    tag_lo = (unsigned)(ep + 1) & 0xffu;
    tag_hi = (unsigned)((ep + 1) >> 8);
    {
      const u32x2 g = {ent | (tag_lo << 24), tag_hi};
      __builtin_amdgcn_raw_buffer_store_b64(g, rs_tab, tid < NBK ? bkt * (NBK * 8) + tid * 8 : 0x80000000u, 0, 16);
      const auto rs_out = __builtin_amdgcn_make_buffer_rsrc((void*)(ws + WS_REC + (size_t)bkt * CHS), 0, __builtin_amdgcn_readfirstlane(total), 0x00020000);
#pragma unroll
      for (int i = 0; i < 4; i++) {
        const u32x4 v = LDS(u32x4, L_REC + (i * 512 + tid) * 16);
        const u32x4 o = {v[1], v[3], v[0] | ((v[2] & 0xfffu) << 12) | (tag_lo << 24), tag_hi};
        __builtin_amdgcn_raw_buffer_store_b128(o, rs_out, (i * 512 + tid) * 16, 0, 16);
      }
    }
    STAMP(12);
    asm volatile("s_waitcnt vmcnt(10)" : "+v"(wdv0), "+v"(wdv1), "+v"(xa), "+v"(xb) :: "memory");
    {
      const unsigned tp = wl + (8 * h + ((lane >> 2) & 3)) * 128 + ((lane >> 4) & 1) * 32 + (lane & 3) * 8;
      u32x4 f[2], g[2];
#pragma unroll
      for (int t = 0; t < 2; t++) { f[t] = tr_frag(tp + 64 * t, 512); g[t] = tr_frag(tp + 2048 + 64 * t, 512); }
#pragma unroll
      for (int t = 0; t < 2; t++) {
        if (w < 4) { f[t] = fold(f[t]); g[t] = fold(g[t]); }
        LDS(u32x4, L_WH + (2 * w + t) * 1024 + lane * 16) = f[t];
        LDS(u32x4, wl + 2048 + t * 1024 + lane * 16) = g[t];
      }
      u32x4 x;
      if (w < 2) x = fold(tr_frag(wl + W_STE + (8 * h + ((lane >> 2) & 3)) * 64 + ((lane >> 4) & 1) * 32 + (lane & 3) * 8, 256));
      else {
        const unsigned m = (lane & 3) == 0 && ((lane >> 2) & 1) == ((lane >> 4) & 1) ? ~0u : 0u;
        x = (u32x4){xa[0] & m, xa[1] & m, xb[0] & m, xb[1] & m};
      }
      if (w < 6) LDS(u32x4, L_WJ + w * 1024 + lane * 16) = x;
    }
    if (tid >= 256 && tid < 256 + 26 * 4) {
      const int t = tid - 256, row = t >> 2, sx = (t >> 1) & 1, hh = t & 1;
      const int x = row < DUMMY ? row - 16 * sx - 4 * hh : -1;
      u32x4 v;
      v[0] = x == 0 ? 0x3f80u : x == 1 ? 0x3f800000u : 0u;  v[1] = x == 2 ? 0x3f80u : x == 3 ? 0x3f800000u : 0u;
      v[2] = x == 8 ? 0x3f80u : x == 9 ? 0x3f800000u : 0u;  v[3] = x == 10 ? 0x3f80u : x == 11 ? 0x3f800000u : 0u;
      LDS(u32x4, L_LUT + row * 80 + (t & 3) * 16) = v;
    }
    {
      const u32x4 z4 = {0u, 0u, 0u, 0u};
#pragma unroll
      for (int i = 0; i < 3; i++) { const int o = i * 1024 + lane * 16; if (o < 26 * 80 + 256) LDS(u32x4, wl + W_SI + o) = z4; }
    }
    barrier_lds();
    STAMP(14);
  }

  f32x16 Y0, Y1; f32x4 Yp = {0.f, 0.f, 0.f, 0.f};
#pragma unroll
  for (int i = 0; i < 16; i++) { Y0[i] = 0.f; Y1[i] = 0.f; }
  u32x32 W2; u32x16 PA; u32x8 W3; u32x4 WX0, WX1, WX2, WX3;
  int jbase = 0, part = -1, grp = w, gother = w;

  auto round = [&](bool first) __attribute__((always_inline)) -> bool {
    const int lane = lane_id(), tid = w * 64 + lane, r = lane & 31, h = lane >> 5;
    const unsigned zr = opaque(0);
    if (!first) { LDS(unsigned, L_CNT + tid * 4) = zr; barrier_lds(); }
    LDS(unsigned, L_BASE + lane * 4) = 0x40000000u;
    unsigned tb[4];
    {
      u32x2 t;
      for (int spin = 0;; spin++) {
        asm volatile("buffer_load_dwordx2 %0, %1, %2, %3 offen sc1\n\ts_waitcnt vmcnt(0)"
                     : "=&v"(t) : "v"((w * 32 + r) * (NBK * 8)), "s"(rs_tab), "s"(bkt * 8) : "memory");
        if (__builtin_amdgcn_ballot_w64(t[1] != tag_hi || (t[0] >> 24) != tag_lo) == 0) break;
        if (spin > (1 << 20)) { t[0] = 0; break; }
        __builtin_amdgcn_s_sleep(POLL_SLEEP);
      }
      LDS(unsigned, L_WAVE + w * WSZ + W_STE + r * 4) = t[0];
      const u32x4 t0 = LDS(u32x4, L_WAVE + w * WSZ + W_STE + (lane >> 3) * 16);
#pragma unroll
      for (int p = 0; p < 4; p++) tb[p] = t0[p];
    }
    STAMP(8);
    u32x4 gr[6];
    unsigned ad[6], left[6], eb[6];
    unsigned lmax = 0, nov = 0;
    bool big;
    const int ovmax = part >= 0 ? LG : 2 * LG, cap = 2 * LG + 2 * ovmax;
    {
      const unsigned jj = lane & (LG - 1), c0 = (unsigned)tid / LG * 4, oob = 0x80000000u + zr;
      const unsigned o0 = mul24(c0, CHS) + (jbase / 2 + jj) * 16, eb0 = mul24(c0, CH);
      unsigned off[4], len[4];
#pragma unroll
      for (int p = 0; p < 4; p++) {
        len[p] = (tb[p] >> 12) & 0xfffu;
        if (part >= 0 && part != 4 * p + (w & 3)) len[p] = 0;
        off[p] = o0 + (tb[p] & 0xfffu) * 16 + p * CHS;
        left[p] = len[p] - (jbase + 2 * jj);
        ad[p] = (int)left[p] > 0 ? off[p] : oob;
        eb[p] = eb0 + p * CH;
        asm volatile("buffer_load_dwordx4 %0, %1, %2, 0 offen sc1" : "=v"(gr[p]) : "v"(ad[p]), "s"(rs_rec) : "memory");
        lmax = max(lmax, tb[p] & 0xfff000u);
      }
      asm volatile("" : "+v"(len[0]), "+v"(len[1]), "+v"(len[2]), "+v"(len[3]));
      ad[4] = oob; ad[5] = oob; left[4] = zr; left[5] = zr; eb[4] = zr; eb[5] = zr;
      unsigned ov[4], s1 = 0;
#pragma unroll
      for (int p = 0; p < 4; p++) {
        ov[p] = (unsigned)min(max(((int)len[p] - (jbase + 2 * LG) + 1) >> 1, 0), ovmax);
        const bool sel = jj - nov < ov[p];
        ad[4] = sel ? off[p] + (LG - nov) * 16 : ad[4];
        left[4] = sel ? len[p] - (jbase + 2 * LG) - 2 * (jj - nov) : left[4];
        eb[4] = sel ? eb0 + p * CH : eb[4];
        nov += ov[p];
      }
      asm volatile("buffer_load_dwordx4 %0, %1, %2, 0 offen sc1" : "=v"(gr[4]) : "v"(ad[4]), "s"(rs_rec) : "memory");
      gr[5] = (u32x4){zr, zr, zr, zr};
      big = __builtin_amdgcn_ballot_w64(nov > LG) != 0;
      if (big) {
#pragma unroll
        for (int p = 0; p < 4; p++) {
          const bool sel = jj + LG - s1 < ov[p];
          ad[5] = sel ? off[p] + (2 * LG - s1) * 16 : ad[5];
          left[5] = sel ? len[p] - (jbase + 2 * LG) - 2 * (jj + LG - s1) : left[5];
          eb[5] = sel ? eb0 + p * CH : eb[5];
          s1 += ov[p];
        }
        asm volatile("buffer_load_dwordx4 %0, %1, %2, 0 offen sc1" : "+v"(gr[5]) : "v"(ad[5]), "s"(rs_rec) : "memory");
      }
    }
    unsigned sink = zr;
    STAMP(7);
    u32x4 w1f[8];
    if (first) {
#pragma unroll
      for (int i = 0; i < 8; i++) w1f[i] = LDS(u32x4, L_WAVE + (4 + (i >> 1)) * WSZ + 2048 + (i & 1) * 1024 + lane * 16);
#pragma unroll
      for (int i = 0; i < 8; i++) {
        const u32x4 v = LDS(u32x4, L_WAVE + (i >> 1) * WSZ + 2048 + (i & 1) * 1024 + lane * 16);
#pragma unroll
        for (int j = 0; j < 4; j++) W2[4 * i + j] = v[j];
      }
#pragma unroll
      for (int i = 0; i < 2; i++) {
        const u32x4 v = LDS(u32x4, L_WJ + i * 1024 + lane * 16);
#pragma unroll
        for (int j = 0; j < 4; j++) W3[4 * i + j] = v[j];
      }
      WX0 = LDS(u32x4, L_WJ + 2048 + lane * 16); WX1 = LDS(u32x4, L_WJ + 3072 + lane * 16);
      WX2 = LDS(u32x4, L_WJ + 4096 + lane * 16); WX3 = LDS(u32x4, L_WJ + 5120 + lane * 16);
    }
    STAMP(13);
    if ((lmax >> 12) > (unsigned)(jbase + cap) || nov > 2 * LG) __hip_atomic_fetch_or(&LDS(unsigned, L_FLAG), nov > 2 * LG ? 3u : 1u, __ATOMIC_RELAXED, __HIP_MEMORY_SCOPE_WORKGROUP);
    unsigned mk1 = (lane & 7) >= 1 ? ~zr : zr, mk2 = (lane & 7) >= 2 ? ~zr : zr, sg0 = L_REC + (lane >> 3) * SEG;
    unsigned lim = L_REC + 8 * SEG + lane * 8, own = n0 + min(8 * (r & 3) + (r >> 2), 24) * 8;
    unsigned ce = ((lane & 1) ^ ((lane >> 4) & 1)) * 16, cp = (lane & 1) * 2;
    unsigned cc0 = ((lane & 7) ^ (lane >> 4)) * 16, cc1 = ((lane & 7) ^ (4 + (lane >> 4))) * 16;
    unsigned q_s4 = L_REC + (lane >> 3) * 32 + 4;
    unsigned q_r2 = L_REC + (4 * ((lane >> 1) & 7) + (lane >> 4)) * 8;
    unsigned q_r1 = L_REC + (4 * (r & 7) + (r >> 3)) * 8 + 4;
    asm volatile("" : "+v"(mk1), "+v"(mk2), "+v"(sg0), "+v"(lim), "+v"(own), "+v"(ce), "+v"(cp), "+v"(cc0), "+v"(cc1), "+v"(q_s4),
                 "+v"(q_r2), "+v"(q_r1));
    asm volatile("s_waitcnt vmcnt(0)" : "+v"(gr[0]), "+v"(gr[1]), "+v"(gr[2]), "+v"(gr[3]), "+v"(gr[4]), "+v"(gr[5]) :: "memory");
    for (int spin = 0;; spin++) {
      bool bad = false;
#pragma unroll
      for (int p = 0; p < 6; p++) bad |= ((int)left[p] > 0) & ((gr[p][3] != tag_hi) | ((gr[p][2] >> 24) != tag_lo));
      if (__builtin_amdgcn_ballot_w64(bad) == 0 || spin > (1 << 20)) break;
      asm volatile("buffer_load_dwordx4 %0, %6, %12, 0 offen sc1\n\tbuffer_load_dwordx4 %1, %7, %12, 0 offen sc1\n\t"
                   "buffer_load_dwordx4 %2, %8, %12, 0 offen sc1\n\tbuffer_load_dwordx4 %3, %9, %12, 0 offen sc1\n\t"
                   "buffer_load_dwordx4 %4, %10, %12, 0 offen sc1\n\tbuffer_load_dwordx4 %5, %11, %12, 0 offen sc1\n\ts_waitcnt vmcnt(0)"
                   : "=&v"(gr[0]), "=&v"(gr[1]), "=&v"(gr[2]), "=&v"(gr[3]), "=&v"(gr[4]), "=&v"(gr[5])
                   : "v"(ad[0]), "v"(ad[1]), "v"(ad[2]), "v"(ad[3]), "v"(ad[4]), "v"(ad[5]), "s"(rs_rec) : "memory");
    }
    STAMP(1);
    u32x2 rc[12];
#pragma unroll
    for (int p = 0; p < 6; p++) {
      rc[2 * p] = (u32x2){(eb[p] + (gr[p][2] & 0xfffu)) << 5, (int)left[p] > 0 ? gr[p][0] : 0u};
      rc[2 * p + 1] = (u32x2){(eb[p] + ((gr[p][2] >> 12) & 0xfffu)) << 5, (int)left[p] > 1 ? gr[p][1] : 0u};
    }
    unsigned rk[12], ky[12];
#pragma unroll
    for (int p = 0; p < 12; p++) {
      if (p == 10 && !big) { ky[10] = ky[11] = 0; rk[10] = rk[11] = 0; break; }
      ky[p] = max((rc[p][1] >> 9) & 0x7fcu, (unsigned)lane * 4);
      rk[p] = __hip_atomic_fetch_add(&LDS(unsigned, L_CNT + ky[p]), 8u + zr, __ATOMIC_RELAXED, __HIP_MEMORY_SCOPE_WORKGROUP);
    }
    if (EPF) {
#pragma unroll
      for (int p = 0; p < 10; p++) {
        const unsigned a = (ky[p] & 0x47cu) - 0x400u < (unsigned)EPF * 4 ? rc[p][0] : 0x80000000u + zr;
        asm volatile("buffer_load_dword %0, %1, %2, 0 offen" : "+{v3}"(sink) : "v"(a), "s"(rs_ea) : "memory");
      }
    }
    barrier_lds();
    STAMP(2);
    unsigned g;
    const unsigned flagv = LDS(unsigned, L_FLAG);
    {
      const u32x4 cv = LDS(u32x4, L_CNT + 1024 + lane * 16);
      const unsigned sum = cv[0] + cv[1] + cv[2] + cv[3];
      g = sum + (__builtin_amdgcn_update_dpp(0, (int)sum, 0x111, 0xf, 0xf, true) & mk1);
      g += __builtin_amdgcn_update_dpp(0, (int)g, 0x112, 0xf, 0xf, true) & mk2;
      g += __builtin_amdgcn_update_dpp(0, (int)g, 0x114, 0xf, 0xa, false);
      const unsigned ex = g - sum + sg0;
      const u32x4 bv = {ex, ex + cv[0], ex + cv[0] + cv[1], ex + cv[0] + cv[1] + cv[2]};
      LDS(u32x4, L_BASE + 1024 + lane * 16) = bv;
    }
    unsigned bs[10];
#pragma unroll
    for (int p = 0; p < 10; p++) bs[p] = LDS(unsigned, L_BASE + ky[p]);
    const unsigned flag = __builtin_amdgcn_readfirstlane(flagv);
    const bool again = flag & 1;
    const bool over = (flag & 2) || __builtin_amdgcn_ballot_w64(g > (unsigned)(SEG - 256)) != 0;
    if (!over) {
#pragma unroll
      for (int p = 0; p < 10; p++) LDS(u32x2, min(bs[p] + rk[p], lim)) = rc[p];
      if (big) {
        LDS(u32x2, min(LDS(unsigned, L_BASE + ky[10]) + rk[10], lim)) = rc[10];
        LDS(u32x2, min(LDS(unsigned, L_BASE + ky[11]) + rk[11], lim)) = rc[11];
      }
    }
    if (first) {
      const unsigned key = (g << 3) | (lane >> 3);
      unsigned ks[8], rank = 0;
#pragma unroll
      for (int k = 0; k < 8; k++) ks[k] = __builtin_amdgcn_readlane(key, 8 * k + 7);
#pragma unroll
      for (int k = 0; k < 8; k++) rank += ks[k] > key;
      const unsigned long long sel = 0x8080808080808080ull;
      grp = __builtin_ctzll(__builtin_amdgcn_ballot_w64(rank == (unsigned)(w < 4 ? w : 11 - w)) & sel) >> 3;
      gother = __builtin_ctzll(__builtin_amdgcn_ballot_w64(rank == (unsigned)(w < 4 ? 7 - w : w - 4)) & sel) >> 3;
      wl = L_WAVE + grp * WSZ; mi = L_MI + grp * MISZ;
    }
    unsigned segn = over ? 0u : (unsigned)__builtin_amdgcn_readlane(g, 8 * grp + 7);
    const unsigned seg0 = grp * SEG;
    const int nt = (segn + 255) >> 8;
    {
      const u32x2 pad = {zr, DUMMY * (80u << 20)}, row = {zr, min(own + grp, (unsigned)N - 1) | (DUMMY * (80u << 20))};
      LDS(u32x2, L_REC + seg0 + segn + r * 8) = pad;
      LDS(u32x2, L_REC + seg0 + nt * 256 + r * 8) = row;
    }
    barrier_lds();
    STAMP(3);

    const unsigned o_s4 = q_s4 + seg0, o_r2 = q_r2 + seg0, o_r1 = q_r1 + seg0;
    {
      unsigned ns[4];
#pragma unroll
      for (int i = 0; i < 4; i++) ns[i] = LDS(uint16_t, o_s4 + i * 8);
      const u32x2 nr2 = LDS(u32x2, o_r2);
      dma_tile(hmat, ea, pos, ns[0] * 128 + cc0, ns[1] * 128 + cc1, ns[2] * 128 + cc0, ns[3] * 128 + cc1,
               nr2[0] | ce, (nr2[1] & 0xffffu) * 6 + cp, wl);
    }
    if (first) {
      u32x2 hb[8];
#pragma unroll
      for (int i = 0; i < 8; i++) hb[i] = LDS(u32x2, mi + r * 128 + ((i ^ ((r >> 1) & 7)) * 16) + h * 8);
      const u32x2 pg = LDS(u32x2, mi + 4096 + r * 8);
#pragma unroll
      for (int t = 0; t < 2; t++) {
        f32x16 P;
#pragma unroll
        for (int i = 0; i < 16; i++) P[i] = 0.f;
#pragma unroll
        for (int s = 0; s < 4; s++) {
          const u32x4 a = {hb[2 * s][0], hb[2 * s][1], hb[2 * s + 1][0], hb[2 * s + 1][1]};
          P = MFMA(as_bf(a), as_bf(w1f[2 * s + t]), P);
        }
        if (h) P[15] = bf2f(t ? wdv1 : wdv0);
#pragma unroll
        for (int i = 0; i < 16; i++) P[i] *= -LOG2E;
#pragma unroll
        for (int s = 0; s < 2; s++)
#pragma unroll
          for (int j = 0; j < 4; j++) PA[4 * (2 * s + t) + j] = pk_bf16(P[8 * s + 2 * j], P[8 * s + 2 * j + 1]);
      }
      const f32x4 pv = {bf2f(pg[0] & 0xffffu), __uint_as_float(pg[0] & 0xffff0000u), __uint_as_float(pg[1] & 0xffff0000u), 0.f};
      if (lane < DUMMY) LDS(f32x4, wl + W_SI + lane * 80 + 64) = pv;
      if (bkt == 0 && tid == 0) {
        const u32x2 e1 = {tag_lo | (tag_hi << 8), tag_hi >> 24};
        asm volatile("global_store_dwordx2 %0, %1, %2 sc1" :: "v"(zr), "v"(e1), "s"(ws + WS_EPOCH) : "memory");
      }
    }
    const unsigned nother = (__builtin_amdgcn_readlane(g, 8 * gother + 7) + 255) >> 8;
    const bool last_round = !over && !again && (part < 0 || part == 15);
    STAMP(4);

    {
      u32x16 A0; u32x8 A1;
      A0[0] = zr; A0[1] = zr;
#pragma unroll
      for (int s = 0; s < 4; s++) A0[2 + s] = wl + W_STH + r * 128 + (((2 * s + h) ^ ((r >> 1) & 7)) * 16);
      A0[6] = wl + r * 32 + ((h ^ ((r >> 3) & 1)) * 16);
      A0[7] = wl + r * 8;
      A0[8] = L_LUT + h * 16;
      A0[9] = wl + r * 2;
      A0[10] = wl + min(r, DUMMY) * 80 + h * 16;
      A0[11] = mi + r * MIL + h * 32;
      A0[12] = mi + (8 * h + ((lane >> 2) & 3)) * MIL + (4 * ((lane >> 4) & 1) + (lane & 3)) * 8;
      A0[13] = wl + h * 64 + r * 2;
      {
        const int row = (lane & 15) - 4 * ((lane >> 4) & 1);
        A0[14] = wl + ((unsigned)row < 3 ? row : 3) * 64 + h * 16;
      }
      A0[15] = L_WJ + lane * 16;
      A1[0] = cc0; A1[1] = cc1; A1[2] = ce; A1[3] = cp;
      A1[4] = o_s4 + 256; A1[5] = o_r2 + 256; A1[6] = o_r1;
      A1[7] = 0x3f800000u + zr;
      unsigned cnt = nt, keep;
#define PRIO_A 14
#define PRIO_B 7
      const int lastv = __builtin_amdgcn_readfirstlane(opaque(last_round ? 1 : 0));
      const int thr = __builtin_amdgcn_readfirstlane(w >= 4 ? max((int)(nother * PRIO_A - cnt * PRIO_B) >> 5, 0) : -1);
      asm volatile(LOOP_ASM
                   : "+{v[52:67]}"(Y0), "+{v[68:83]}"(Y1), "+{v[84:87]}"(Yp), "+{v[232:247]}"(A0), "+{v[248:255]}"(A1), "+{v3}"(sink),
                     [nt] "+s"(cnt), [keep] "=&s"(keep)
                   : "{v[4:35]}"(W2), "{v[36:51]}"(PA), "{v[88:95]}"(W3), "{v[96:99]}"(WX0), "{v[136:139]}"(WX1), "{v[140:143]}"(WX2), "{v[144:147]}"(WX3),
                     [h0] "s"(hmat), [h1] "s"(hmat - 1024), [h2] "s"(hmat - 2048), [h3] "s"(hmat - 3072), [ea] "s"(ea), [pos] "s"(pos - 1024),
                     [b1] "s"(wl), [b2] "s"(wl + W_STE), [posd] "s"(wl + W_SI + 64), [c128] "s"(128u), [thr] "s"(thr), [last] "s"(lastv),
                     [hsel] "s"(0xffffffff00000000ull)
                   : "memory", "scc", "vcc", CLOBBER_V);
    }
    if (over) part = 0;
    else if (part >= 0 && part < 15) part++;
    else { part = -1; jbase += cap; }
    return last_round;
  };
  if (!round(true)) {
    do {
      wait_vm0();
      barrier_lds();
      jbase = __builtin_amdgcn_readfirstlane(jbase); part = __builtin_amdgcn_readfirstlane(part);
    } while (!round(false));
  }
  STAMP(5);

  {
    const int lane = lane_id(), r = lane & 31, h = lane >> 5;
    Yp[0] *= -LN2; Yp[1] *= -LN2; Yp[2] *= -LN2;
    const auto rs_o = __builtin_amdgcn_make_buffer_rsrc((void*)out, 0, N * 128, 0x00020000);
#pragma unroll
    for (int i = 0; i < 4; i++) {
      const int q = 8 * i + (lane >> 3), dl = q * 8 + grp;
      const u32x4 v = LDS(u32x4, mi + q * MIP + (lane & 7) * 16);
      if (dl < NB) __builtin_amdgcn_raw_buffer_store_b128(v, rs_o, (n0 + dl) * 128 + (lane & 7) * 16, 0, OUT_AUX);
    }
    const int dl = r * 8 + grp;
    if (h == 0 && dl < NB && n0 + dl < N) {
      const f32x4 pd = LDS(f32x4, wl + W_SI + r * 80 + 64);
      uint16_t* po = out + (size_t)N * 64 + (size_t)(n0 + dl) * 3;
      const unsigned xy = pk_bf16(pd[0] + Yp[0], pd[1] + Yp[1]), zz = pk_bf16(pd[2] + Yp[2], 0.f);
      po[0] = (uint16_t)xy; po[1] = (uint16_t)(xy >> 16); po[2] = (uint16_t)zz;
    }
  }
  STAMP(6);
}

}

#define STAMP_PASS1
#define STAMP_PASS2

extern "C" void kernel_launch(void* const* d_in, const int* in_sizes, int n_in, void* d_out, int out_size,
                              void* d_ws, size_t ws_size, hipStream_t stream) {
  const int N = in_sizes[0] / 64, E = in_sizes[3];
  if (N > NBK * NB || E > NBK * CH || N < 1 || E < 1) return;
  (void)hipFuncSetAttribute((const void*)egnn_kernel, hipFuncAttributeMaxDynamicSharedMemorySize, L_TOTAL);
  egnn_kernel<<<NBK, 512, L_TOTAL, stream>>>((const char*)d_in[0], (const char*)d_in[1], (const char*)d_in[2], (const int*)d_in[3],
                                             (const int*)d_in[4], (const char*)d_in[5], (const char*)d_in[6], (const char*)d_in[7],
                                             (char*)d_ws, (uint16_t*)d_out, N, E STAMP_PASS2);
}
```

```cpp
#include <hip/hip_runtime.h>
#include <stdint.h>
#include <type_traits>

#define FOLD_LOG2E 1

namespace {

typedef __bf16 bf16x8 __attribute__((ext_vector_type(8)));
typedef __bf16 bf16x2 __attribute__((ext_vector_type(2)));
typedef float f32x16 __attribute__((ext_vector_type(16)));
typedef float f32x4 __attribute__((ext_vector_type(4)));
typedef unsigned u32x4 __attribute__((ext_vector_type(4)));
typedef unsigned u32x2 __attribute__((ext_vector_type(2)));
typedef unsigned u32x8 __attribute__((ext_vector_type(8)));
typedef unsigned u32x16 __attribute__((ext_vector_type(16)));
typedef unsigned u32x32 __attribute__((ext_vector_type(32)));
typedef short s16x4 __attribute__((ext_vector_type(4)));
typedef __attribute__((address_space(3))) s16x4* lds_s16x4_p;

#define DI __device__ __forceinline__
#define MFMA(a, b, c) __builtin_amdgcn_mfma_f32_32x32x16_bf16((a), (b), (c), 0, 0, 0)

#define POLL_SLEEP 2
#define EPF 2
#define OUT_AUX 16
constexpr int NBK = 256;
constexpr int LG = 8;
constexpr int NB = 196;
constexpr int CH = 3136;
constexpr float LOG2E = 1.4426950408889634f;
constexpr float LN2 = 0.6931471805599453f;

constexpr size_t WS_EPOCH = 0;
constexpr size_t WS_TAB = 256;
constexpr size_t WS_REC = WS_TAB + (size_t)NBK * NBK * 8;
constexpr int CHS = (CH + NBK) * 8;

constexpr int W_STH = 0, W_STE = 4096, W_STP = 5120, W_SI = 5376, W_TI = W_SI + 26 * 80, WSZ = W_TI + 256;
constexpr int DUMMY = 25;
constexpr int MIP = 144, MISZ = 32 * MIP;
constexpr int MIL = 80;
constexpr int SEG = 17 * 256;
constexpr int L_CNT = 0;
constexpr int L_BASE = L_CNT + 2048;
constexpr int L_FLAG = L_CNT + 512;
constexpr int L_REC = L_BASE + 2048;
constexpr int L_LUT = L_REC + 8 * SEG + 512;
constexpr int L_WAVE = L_LUT + 26 * 80;
constexpr int L_MI = L_WAVE + 8 * WSZ;
constexpr int L_WJ = L_MI + 8 * MISZ;
constexpr int L_WH = L_WJ + 6 * 1024;
constexpr int L_TOTAL = L_WH + 16 * 1024;
static_assert(L_TOTAL <= 163840, "LDS");
static_assert(WSZ % 16 == 0 && L_MI % 16 == 0 && L_LUT % 16 == 0, "align");

#pragma clang diagnostic ignored "-Wint-to-pointer-cast"
#define LDS(T, off) (*(__attribute__((address_space(3))) T*)(unsigned)(off))
DI unsigned mul24(unsigned a, unsigned b) { return (unsigned)__umul24(a, b); }
DI float bf2f(unsigned lo16) { return __uint_as_float(lo16 << 16); }
DI unsigned pk_bf16(float a, float b) { bf16x2 v = {(__bf16)a, (__bf16)b}; return __builtin_bit_cast(unsigned, v); }
DI bf16x8 as_bf(u32x4 v) { return __builtin_bit_cast(bf16x8, v); }
DI int opaque(int x) { asm volatile("" : "+v"(x)); return x; }
DI int lane_id() {
  int x; asm volatile("v_mbcnt_lo_u32_b32 %0, -1, 0\n\tv_mbcnt_hi_u32_b32 %0, -1, %0" : "=v"(x)); return x;
}
DI void wait_vm0() { asm volatile("s_waitcnt vmcnt(0)" ::: "memory"); }
DI void barrier_lds() { asm volatile("s_waitcnt lgkmcnt(0)\n\ts_barrier" ::: "memory"); }
DI void dma_tile(const void* hm, const void* ea, const void* ps, unsigned o0, unsigned o1, unsigned o2, unsigned o3,
                 unsigned oe, unsigned op, unsigned l) {
  unsigned keep;
  asm volatile("s_mov_b32 %0, m0\n\t"
               "s_add_u32 m0, %10, 0x1000\n\ts_nop 0\n\tglobal_load_lds_dwordx4 %5, %8\n\t"
               "s_mov_b32 m0, %10\n\ts_nop 0\n\tglobal_load_lds_dwordx4 %1, %7\n\t"
               "s_add_u32 m0, %10, 0x400\n\ts_nop 0\n\tglobal_load_lds_dwordx4 %2, %7\n\t"
               "s_add_u32 m0, %10, 0x800\n\ts_nop 0\n\tglobal_load_lds_dwordx4 %3, %7\n\t"
               "s_add_u32 m0, %10, 0xc00\n\ts_nop 0\n\tglobal_load_lds_dwordx4 %4, %7\n\t"
               "s_add_u32 m0, %10, 0x1400\n\ts_nop 0\n\tglobal_load_lds_dword %6, %9\n\t"
               "s_mov_b32 m0, %0"
               : "=&s"(keep) : "v"(o0), "v"(o1), "v"(o2), "v"(o3), "v"(oe), "v"(op), "s"(hm), "s"(ea), "s"(ps), "s"(l)
               : "memory", "scc");
}
template <int CTRL, int ROWMASK> DI unsigned dpp_add(unsigned x) {
  return x + (unsigned)__builtin_amdgcn_update_dpp(0, (int)x, CTRL, ROWMASK, 0xf, false);
}
DI unsigned wave_scan_incl(unsigned x) {
  x = dpp_add<0x111, 0xf>(x); x = dpp_add<0x112, 0xf>(x); x = dpp_add<0x114, 0xf>(x); x = dpp_add<0x118, 0xf>(x);
  x = dpp_add<0x142, 0xa>(x); x = dpp_add<0x143, 0xc>(x);
  return x;
}
#define STAMP(i)
#define STAMP_ARG

#define CLOBBER_V "v100", "v101", "v102", "v103", "v104", "v105", "v106", "v107", "v108", "v109", "v110", "v111", "v112", "v113", "v114", "v115", "v116", "v117", "v118", "v119", "v120", "v121", "v122", "v123", "v124", "v125", "v126", "v127", "v128", "v129", "v130", "v131", "v132", "v133", "v134", "v135", "v148", "v149", "v150", "v151", "v152", "v153", "v154", "v155", "v156", "v157", "v158", "v159", "v160", "v161", "v162", "v163", "v164", "v165", "v166", "v167", "v168", "v169", "v170", "v171", "v172", "v173", "v174", "v175", "v176", "v177", "v178", "v179", "v180", "v181", "v182", "v183", "v184", "v185", "v186", "v187", "v188", "v189", "v190", "v191", "v192", "v193", "v194", "v195", "v196", "v197", "v198", "v199", "v200", "v201", "v202", "v203", "v204", "v205", "v206", "v207", "v208", "v209", "v210", "v211", "v214", "v215", "v216", "v217", "v218", "v219", "v220", "v221", "v222", "v223", "v224", "v225", "v226", "v227", "v228", "v229", "v230", "v231"
#define LOOP_ASM_FOLD \
  "s_mov_b32 %[keep], m0\n\t" \
  "s_mov_b64 vcc, %[hsel]\n\t" \
  "ds_read2_b32 v[220:221], v252 offset0:0 offset1:2\n\t" \
  "ds_read2_b32 v[222:223], v252 offset0:4 offset1:6\n\t" \
  "ds_read_b64 v[226:227], v253\n\t" \
  "ds_read_b32 v232, v254\n\t" \
  "v_mov_b32 v176, 0\n\t" \
  "v_mov_b32 v177, 0\n\t" \
  "v_mov_b32 v178, 0\n\t" \
  "v_mov_b32 v179, 0\n\t" \
  "v_mov_b32 v180, 0\n\t" \
  "v_mov_b32 v181, 0\n\t" \
  "v_mov_b32 v182, 0\n\t" \
  "v_mov_b32 v183, 0\n\t" \
  "v_mov_b32 v184, 0\n\t" \
  "v_mov_b32 v185, 0\n\t" \
  "v_mov_b32 v186, 0\n\t" \
  "v_mov_b32 v187, 0\n\t" \
  "v_mov_b32 v188, 0\n\t" \
  "v_mov_b32 v189, 0\n\t" \
  "v_mov_b32 v190, 0\n\t" \
  "v_mov_b32 v191, 0\n\t" \
  "v_mov_b32 v230, 0\n\t" \
  "v_mov_b32 v231, 0\n\t" \
  "v_mov_b32 v208, 0\n\t" \
  "v_mov_b32 v209, 0\n\t" \
  "v_mov_b32 v210, 0\n\t" \
  "v_mov_b32 v211, 0\n\t" \
  "ds_write_b128 v243, v[184:187] offset:0\n\t" \
  "s_nop 0\n\t" \
  "v_mfma_f32_16x16x32_bf16 v[132:135], v[184:187], v[188:191], 0\n\t" \
  "v_add_u32 v252, 0x100, v252\n\t" \
  "v_add_u32 v253, 0x100, v253\n\t" \
  "v_add_u32 v254, 0x100, v254\n\t" \
  "s_waitcnt lgkmcnt(0)\n\t" \
  "v_lshrrev_b32 v232, 20, v232\n\t" \
  "v_add_u32 v233, %[posd], v232\n\t" \
  "ds_read_b128 v[216:219], v233\n\t" \
  "v_add_u32 v233, v240, v232\n\t" \
  "ds_read_b128 v[172:175], v233 offset:32\n\t" \
  "ds_read_b128 v[168:171], v233\n\t" \
  "v_add_u32 v233, v241, v232\n\t" \
  "ds_write_b16_d16_hi v233, v255 offset:5376\n\t" \
  "v_mad_u32_u16 v220, v220, %[c128], v248\n\t" \
  "v_mad_u32_u16 v221, v221, %[c128], v249\n\t" \
  "v_mad_u32_u16 v222, v222, %[c128], v248\n\t" \
  "v_mad_u32_u16 v223, v223, %[c128], v249\n\t" \
  "v_or_b32 v226, v226, v250\n\t" \
  "v_mad_u32_u16 v227, v227, 6, v251\n\t" \
  "s_waitcnt vmcnt(0)\n\t" \
  "ds_read_b128 v[148:151], v234\n\t" \
  "ds_read_b128 v[152:155], v235\n\t" \
  "ds_read_b128 v[156:159], v236\n\t" \
  "ds_read_b128 v[160:163], v237\n\t" \
  "ds_read_b128 v[164:167], v238 offset:4096\n\t" \
  "ds_read_b64 v[224:225], v239 offset:5120\n\t" \
  "s_waitcnt lgkmcnt(0)\n\t" \
  "s_cmp_lt_i32 %[nt], 1\n\t" \
  "s_cbranch_scc1 7f\n\t" \
  "s_mov_b32 m0, %[b2]\n\t" \
  "s_nop 0\n\t" \
  "global_load_lds_dwordx4 v226, %[ea]\n\t" \
  "global_load_lds_dword v227, %[pos] offset:1024\n\t" \
  "s_mov_b32 m0, %[b1]\n\t" \
  "s_nop 0\n\t" \
  "global_load_lds_dwordx4 v220, %[h0]\n\t" \
  "global_load_lds_dwordx4 v221, %[h1] offset:1024\n\t" \
  "global_load_lds_dwordx4 v222, %[h2] offset:2048\n\t" \
  "global_load_lds_dwordx4 v223, %[h3] offset:3072\n\t" \
  "7:\n\t" \
  "ds_read2_b32 v[220:221], v252 offset0:0 offset1:2\n\t" \
  "ds_read2_b32 v[222:223], v252 offset0:4 offset1:6\n\t" \
  "ds_read_b64 v[226:227], v253\n\t" \
  "ds_read_b32 v232, v254\n\t" \
  "v_add_u32 v252, 0x100, v252\n\t" \
  "v_add_u32 v253, 0x100, v253\n\t" \
  "v_add_u32 v254, 0x100, v254\n\t" \
  "v_and_b32 v225, 0xffff0000, v225\n\t" \
  "v_lshlrev_b32 v219, 16, v224\n\t" \
  "v_and_b32 v224, 0xffff0000, v224\n\t" \
  "v_sub_f32 v229, v218, v225\n\t" \
  "v_sub_f32 v216, v216, v219\n\t" \
  "v_sub_f32 v217, v217, v224\n\t" \
  "v_mul_f32 v218, v229, v229\n\t" \
  "v_cndmask_b32 v228, v216, v217, vcc\n\t" \
  "v_fmac_f32 v218, v216, v216\n\t" \
  "v_fmac_f32 v218, v217, v217\n\t" \
  "v_cvt_pk_bf16_f32 v218, v218, v218\n\t" \
  "v_cndmask_b32_sdwa v175, v175, v218, vcc dst_sel:WORD_1 dst_unused:UNUSED_PRESERVE src0_sel:WORD_1 src1_sel:WORD_1\n\t" \
  "v_mfma_f32_32x32x16_bf16 v[100:115], v[4:7], v[148:151], 0\n\t" \
  "v_mfma_f32_32x32x16_bf16 v[100:115], v[12:15], v[152:155], v[100:115]\n\t" \
  "v_mfma_f32_32x32x16_bf16 v[100:115], v[20:23], v[156:159], v[100:115]\n\t" \
  "v_mfma_f32_32x32x16_bf16 v[100:115], v[28:31], v[160:163], v[100:115]\n\t" \
  "v_mfma_f32_32x32x16_bf16 v[100:115], v[36:39], v[168:171], v[100:115]\n\t" \
  "v_mfma_f32_32x32x16_bf16 v[100:115], v[88:91], v[164:167], v[100:115]\n\t" \
  "v_mfma_f32_32x32x16_bf16 v[100:115], v[44:47], v[172:175], v[100:115]\n\t" \
  "s_cmp_eq_u32 %[nt], 0\n\t" \
  "s_nop 10\n\t" \
  "v_exp_f32 v192, v100\n\t" \
  "v_exp_f32 v193, v101\n\t" \
  "v_exp_f32 v194, v102\n\t" \
  "v_exp_f32 v195, v103\n\t" \
  "s_nop 0\n\t" \
  "v_add_f32 v192, 1.0, v192\n\t" \
  "v_add_f32 v193, 1.0, v193\n\t" \
  "v_add_f32 v194, 1.0, v194\n\t" \
  "v_add_f32 v195, 1.0, v195\n\t" \
  "s_cbranch_scc1 6f\n\t" \
  "s_cmp_lt_i32 %[thr], 1\n\t" \
  "s_cbranch_scc1 1f\n\t" \
  "s_setprio 1\n\t" \
  "1:\n\t" \
  "ds_write_b128 v243, v[188:191] offset:16\n\t" \
  "ds_read_b32 v214, v213\n\t" \
  "ds_read_b64_tr_b16 v[200:201], v244 offset:0\n\t" \
  "ds_read_b64_tr_b16 v[202:203], v244 offset:320\n\t" \
  "ds_read_b64_tr_b16 v[204:205], v244 offset:1280\n\t" \
  "ds_read_b64_tr_b16 v[206:207], v244 offset:1600\n\t" \
  "v_mfma_f32_16x16x32_bf16 v[132:135], v[144:147], v[188:191], v[132:135]\n\t" \
  "v_rcp_f32 v192, v192\n\t" \
  "v_rcp_f32 v193, v193\n\t" \
  "v_rcp_f32 v194, v194\n\t" \
  "v_rcp_f32 v195, v195\n\t" \
  "v_mfma_f32_32x32x16_bf16 v[116:131], v[8:11], v[148:151], 0\n\t" \
  "v_exp_f32 v196, v104\n\t" \
  "v_exp_f32 v197, v105\n\t" \
  "v_exp_f32 v198, v106\n\t" \
  "v_exp_f32 v199, v107\n\t" \
  "v_mfma_f32_32x32x16_bf16 v[116:131], v[16:19], v[152:155], v[116:131]\n\t" \
  "v_mul_f32 v192, v100, v192\n\t" \
  "v_mul_f32 v193, v101, v193\n\t" \
  "v_mul_f32 v194, v102, v194\n\t" \
  "v_mul_f32 v195, v103, v195\n\t" \
  "v_add_f32 v196, 1.0, v196\n\t" \
  "v_add_f32 v197, 1.0, v197\n\t" \
  "v_add_f32 v198, 1.0, v198\n\t" \
  "v_add_f32 v199, 1.0, v199\n\t" \
  "v_cvt_pk_bf16_f32 v184, v192, v193\n\t" \
  "v_cvt_pk_bf16_f32 v185, v194, v195\n\t" \
  "v_mfma_f32_32x32x16_bf16 v[116:131], v[24:27], v[156:159], v[116:131]\n\t" \
  "v_rcp_f32 v196, v196\n\t" \
  "v_rcp_f32 v197, v197\n\t" \
  "v_rcp_f32 v198, v198\n\t" \
  "v_rcp_f32 v199, v199\n\t" \
  "s_waitcnt lgkmcnt(2)\n\t" \
  "v_mfma_f32_32x32x16_bf16 v[68:83], v[200:203], v[176:179], v[68:83]\n\t" \
  "v_exp_f32 v192, v108\n\t" \
  "v_exp_f32 v193, v109\n\t" \
  "v_exp_f32 v194, v110\n\t" \
  "v_exp_f32 v195, v111\n\t" \
  "v_mfma_f32_32x32x16_bf16 v[116:131], v[32:35], v[160:163], v[116:131]\n\t" \
  "v_mul_f32 v196, v104, v196\n\t" \
  "v_mul_f32 v197, v105, v197\n\t" \
  "v_mul_f32 v198, v106, v198\n\t" \
  "v_mul_f32 v199, v107, v199\n\t" \
  "v_mul_f32 v230, v230, v132\n\t" \
  "v_mul_f32 v231, v231, v132\n\t" \
  "v_cvt_pk_bf16_f32 v230, v230, v231\n\t" \
  "ds_write_b16 v245, v230 offset:7456\n\t" \
  "ds_write_b16_d16_hi v241, v230 offset:7584\n\t" \
  "ds_read_b128 v[200:203], v246 offset:7456\n\t" \
  "ds_read_b128 v[208:211], v246 offset:7488\n\t" \
  "v_mov_b32 v230, v228\n\t" \
  "v_mov_b32 v231, v229\n\t" \
  "v_add_f32 v192, 1.0, v192\n\t" \
  "v_add_f32 v193, 1.0, v193\n\t" \
  "v_add_f32 v194, 1.0, v194\n\t" \
  "v_add_f32 v195, 1.0, v195\n\t" \
  "v_cvt_pk_bf16_f32 v186, v196, v197\n\t" \
  "v_cvt_pk_bf16_f32 v187, v198, v199\n\t" \
  "v_mfma_f32_32x32x16_bf16 v[116:131], v[40:43], v[168:171], v[116:131]\n\t" \
  "v_rcp_f32 v192, v192\n\t" \
  "v_rcp_f32 v193, v193\n\t" \
  "v_rcp_f32 v194, v194\n\t" \
  "v_rcp_f32 v195, v195\n\t" \
  "s_sub_u32 %[nt], %[nt], 1\n\t" \
  "v_mov_b32 v215, %[nt]\n\t" \
  "v_readfirstlane_b32 %[tmp], v214\n\t" \
  "s_mov_b64 exec, 1\n\t" \
  "ds_write_b32 v212, v215\n\t" \
  "s_mov_b64 exec, -1\n\t" \
  "s_sub_i32 %[tmp], %[nt], %[tmp]\n\t" \
  "s_add_i32 %[tmp], %[tmp], %[thr]\n\t" \
  "s_cmp_gt_i32 %[tmp], 0\n\t" \
  "s_setprio 0\n\t" \
  "s_cbranch_scc0 3f\n\t" \
  "s_setprio 1\n\t" \
  "3:\n\t" \
  "v_mfma_f32_32x32x16_bf16 v[116:131], v[92:95], v[164:167], v[116:131]\n\t" \
  "v_exp_f32 v196, v112\n\t" \
  "v_exp_f32 v197, v113\n\t" \
  "v_exp_f32 v198, v114\n\t" \
  "v_exp_f32 v199, v115\n\t" \
  "v_mfma_f32_32x32x16_bf16 v[116:131], v[48:51], v[172:175], v[116:131]\n\t" \
  "v_mul_f32 v192, v108, v192\n\t" \
  "v_mul_f32 v193, v109, v193\n\t" \
  "v_mul_f32 v194, v110, v194\n\t" \
  "v_mul_f32 v195, v111, v195\n\t" \
  "s_waitcnt vmcnt(0)\n\t" \
  "ds_read_b128 v[148:151], v234\n\t" \
  "ds_read_b128 v[152:155], v235\n\t" \
  "ds_read_b128 v[156:159], v236\n\t" \
  "ds_read_b128 v[160:163], v237\n\t" \
  "ds_read_b128 v[164:167], v238 offset:4096\n\t" \
  "ds_read_b64 v[224:225], v239 offset:5120\n\t" \
  "ds_write_b128 v243, v[184:187] offset:0\n\t" \
  "v_add_f32 v196, 1.0, v196\n\t" \
  "v_add_f32 v197, 1.0, v197\n\t" \
  "v_add_f32 v198, 1.0, v198\n\t" \
  "v_add_f32 v199, 1.0, v199\n\t" \
  "v_cvt_pk_bf16_f32 v188, v192, v193\n\t" \
  "v_cvt_pk_bf16_f32 v189, v194, v195\n\t" \
  "v_mad_u32_u16 v220, v220, %[c128], v248\n\t" \
  "v_mad_u32_u16 v221, v221, %[c128], v249\n\t" \
  "v_mad_u32_u16 v222, v222, %[c128], v248\n\t" \
  "s_waitcnt lgkmcnt(12)\n\t" \
  "v_mfma_f32_32x32x16_bf16 v[68:83], v[204:207], v[180:183], v[68:83]\n\t" \
  "v_rcp_f32 v196, v196\n\t" \
  "v_rcp_f32 v197, v197\n\t" \
  "v_rcp_f32 v198, v198\n\t" \
  "v_rcp_f32 v199, v199\n\t" \
  "v_mad_u32_u16 v223, v223, %[c128], v249\n\t" \
  "v_or_b32 v226, v226, v250\n\t" \
  "v_mad_u32_u16 v227, v227, 6, v251\n\t" \
  "s_waitcnt lgkmcnt(9)\n\t" \
  "v_mfma_f32_16x16x32_bf16 v[84:87], v[200:203], v[176:179], v[84:87]\n\t" \
  "v_exp_f32 v192, v116\n\t" \
  "v_exp_f32 v193, v117\n\t" \
  "v_exp_f32 v194, v118\n\t" \
  "v_exp_f32 v195, v119\n\t" \
  "s_waitcnt lgkmcnt(8)\n\t" \
  "v_mfma_f32_16x16x32_bf16 v[84:87], v[208:211], v[180:183], v[84:87]\n\t" \
  "v_mul_f32 v196, v112, v196\n\t" \
  "v_mul_f32 v197, v113, v197\n\t" \
  "v_mul_f32 v198, v114, v198\n\t" \
  "v_mul_f32 v199, v115, v199\n\t" \
  "s_waitcnt lgkmcnt(1)\n\t" \
  "s_cmp_lt_i32 %[nt], 1\n\t" \
  "s_cbranch_scc1 4f\n\t" \
  "s_mov_b32 m0, %[b2]\n\t" \
  "s_nop 0\n\t" \
  "global_load_lds_dwordx4 v226, %[ea]\n\t" \
  "global_load_lds_dword v227, %[pos] offset:1024\n\t" \
  "s_mov_b32 m0, %[b1]\n\t" \
  "s_nop 0\n\t" \
  "global_load_lds_dwordx4 v220, %[h0]\n\t" \
  "global_load_lds_dwordx4 v221, %[h1] offset:1024\n\t" \
  "global_load_lds_dwordx4 v222, %[h2] offset:2048\n\t" \
  "global_load_lds_dwordx4 v223, %[h3] offset:3072\n\t" \
  "4:\n\t" \
  "v_add_f32 v192, 1.0, v192\n\t" \
  "v_add_f32 v193, 1.0, v193\n\t" \
  "v_add_f32 v194, 1.0, v194\n\t" \
  "v_add_f32 v195, 1.0, v195\n\t" \
  "v_cvt_pk_bf16_f32 v190, v196, v197\n\t" \
  "v_cvt_pk_bf16_f32 v191, v198, v199\n\t" \
  "v_lshrrev_b32 v232, 20, v232\n\t" \
  "v_add_u32 v220, %[posd], v232\n\t" \
  "v_add_u32 v221, v240, v232\n\t" \
  "ds_write_b128 v243, v[188:191] offset:16\n\t" \
  "ds_read_b128 v[176:179], v242 offset:5376\n\t" \
  "ds_read_b128 v[180:183], v242 offset:5408\n\t" \
  "ds_write_b16 v233, v255 offset:5376\n\t" \
  "ds_read_b128 v[216:219], v220\n\t" \
  "ds_read_b128 v[172:175], v221 offset:32\n\t" \
  "ds_read_b128 v[168:171], v221\n\t" \
  "ds_read_b64_tr_b16 v[200:201], v244 offset:0\n\t" \
  "ds_read_b64_tr_b16 v[202:203], v244 offset:320\n\t" \
  "ds_read_b64_tr_b16 v[204:205], v244 offset:1280\n\t" \
  "ds_read_b64_tr_b16 v[206:207], v244 offset:1600\n\t" \
  "v_add_u32 v233, v241, v232\n\t" \
  "ds_write_b16_d16_hi v233, v255 offset:5376\n\t" \
  "v_mfma_f32_16x16x32_bf16 v[132:135], v[96:99], v[184:187], 0\n\t" \
  "v_rcp_f32 v192, v192\n\t" \
  "v_rcp_f32 v193, v193\n\t" \
  "v_rcp_f32 v194, v194\n\t" \
  "v_rcp_f32 v195, v195\n\t" \
  "v_mfma_f32_32x32x16_bf16 v[100:115], v[4:7], v[148:151], 0\n\t" \
  "v_exp_f32 v196, v120\n\t" \
  "v_exp_f32 v197, v121\n\t" \
  "v_exp_f32 v198, v122\n\t" \
  "v_exp_f32 v199, v123\n\t" \
  "v_mfma_f32_32x32x16_bf16 v[100:115], v[12:15], v[152:155], v[100:115]\n\t" \
  "v_mul_f32 v192, v116, v192\n\t" \
  "v_mul_f32 v193, v117, v193\n\t" \
  "v_mul_f32 v194, v118, v194\n\t" \
  "v_mul_f32 v195, v119, v195\n\t" \
  "v_add_f32 v196, 1.0, v196\n\t" \
  "v_add_f32 v197, 1.0, v197\n\t" \
  "v_add_f32 v198, 1.0, v198\n\t" \
  "v_add_f32 v199, 1.0, v199\n\t" \
  "v_cvt_pk_bf16_f32 v184, v192, v193\n\t" \
  "v_cvt_pk_bf16_f32 v185, v194, v195\n\t" \
  "v_and_b32 v225, 0xffff0000, v225\n\t" \
  "s_waitcnt lgkmcnt(7)\n\t" \
  "v_lshlrev_b32 v219, 16, v224\n\t" \
  "v_and_b32 v224, 0xffff0000, v224\n\t" \
  "v_sub_f32 v229, v218, v225\n\t" \
  "v_sub_f32 v216, v216, v219\n\t" \
  "v_sub_f32 v217, v217, v224\n\t" \
  "v_mfma_f32_32x32x16_bf16 v[100:115], v[20:23], v[156:159], v[100:115]\n\t" \
  "v_rcp_f32 v196, v196\n\t" \
  "v_rcp_f32 v197, v197\n\t" \
  "v_rcp_f32 v198, v198\n\t" \
  "v_rcp_f32 v199, v199\n\t" \
  "v_mul_f32 v218, v229, v229\n\t" \
  "v_cndmask_b32 v228, v216, v217, vcc\n\t" \
  "v_fmac_f32 v218, v216, v216\n\t" \
  "v_fmac_f32 v218, v217, v217\n\t" \
  "v_cvt_pk_bf16_f32 v218, v218, v218\n\t" \
  "s_waitcnt lgkmcnt(6)\n\t" \
  "v_cndmask_b32_sdwa v175, v175, v218, vcc dst_sel:WORD_1 dst_unused:UNUSED_PRESERVE src0_sel:WORD_1 src1_sel:WORD_1\n\t" \
  "v_mfma_f32_16x16x32_bf16 v[132:135], v[136:139], v[188:191], v[132:135]\n\t" \
  "v_exp_f32 v192, v124\n\t" \
  "v_exp_f32 v193, v125\n\t" \
  "v_exp_f32 v194, v126\n\t" \
  "v_exp_f32 v195, v127\n\t" \
  "ds_read2_b32 v[220:221], v252 offset0:0 offset1:2\n\t" \
  "ds_read2_b32 v[222:223], v252 offset0:4 offset1:6\n\t" \
  "ds_read_b64 v[226:227], v253\n\t" \
  "ds_read_b32 v232, v254\n\t" \
  "v_mfma_f32_32x32x16_bf16 v[100:115], v[28:31], v[160:163], v[100:115]\n\t" \
  "v_mul_f32 v196, v120, v196\n\t" \
  "v_mul_f32 v197, v121, v197\n\t" \
  "v_mul_f32 v198, v122, v198\n\t" \
  "v_mul_f32 v199, v123, v199\n\t" \
  "v_add_u32 v252, 0x100, v252\n\t" \
  "v_add_u32 v253, 0x100, v253\n\t" \
  "v_add_u32 v254, 0x100, v254\n\t" \
  "v_add_f32 v192, 1.0, v192\n\t" \
  "v_add_f32 v193, 1.0, v193\n\t" \
  "v_add_f32 v194, 1.0, v194\n\t" \
  "v_add_f32 v195, 1.0, v195\n\t" \
  "v_cvt_pk_bf16_f32 v186, v196, v197\n\t" \
  "v_cvt_pk_bf16_f32 v187, v198, v199\n\t" \
  "s_waitcnt lgkmcnt(9)\n\t" \
  "v_mfma_f32_32x32x16_bf16 v[100:115], v[36:39], v[168:171], v[100:115]\n\t" \
  "v_rcp_f32 v192, v192\n\t" \
  "v_rcp_f32 v193, v193\n\t" \
  "v_rcp_f32 v194, v194\n\t" \
  "v_rcp_f32 v195, v195\n\t" \
  "v_mfma_f32_32x32x16_bf16 v[100:115], v[44:47], v[172:175], v[100:115]\n\t" \
  "v_exp_f32 v196, v128\n\t" \
  "v_exp_f32 v197, v129\n\t" \
  "v_exp_f32 v198, v130\n\t" \
  "v_exp_f32 v199, v131\n\t" \
  "v_mfma_f32_32x32x16_bf16 v[100:115], v[88:91], v[164:167], v[100:115]\n\t" \
  "v_mul_f32 v192, v124, v192\n\t" \
  "v_mul_f32 v193, v125, v193\n\t" \
  "v_mul_f32 v194, v126, v194\n\t" \
  "v_mul_f32 v195, v127, v195\n\t" \
  "ds_write_b128 v243, v[184:187] offset:0\n\t" \
  "v_add_f32 v196, 1.0, v196\n\t" \
  "v_add_f32 v197, 1.0, v197\n\t" \
  "v_add_f32 v198, 1.0, v198\n\t" \
  "v_add_f32 v199, 1.0, v199\n\t" \
  "v_cvt_pk_bf16_f32 v188, v192, v193\n\t" \
  "v_cvt_pk_bf16_f32 v189, v194, v195\n\t" \
  "s_waitcnt lgkmcnt(8)\n\t" \
  "v_mfma_f32_32x32x16_bf16 v[52:67], v[200:203], v[176:179], v[52:67]\n\t" \
  "v_rcp_f32 v196, v196\n\t" \
  "v_rcp_f32 v197, v197\n\t" \
  "v_rcp_f32 v198, v198\n\t" \
  "v_rcp_f32 v199, v199\n\t" \
  "s_waitcnt lgkmcnt(6)\n\t" \
  "v_mfma_f32_32x32x16_bf16 v[52:67], v[204:207], v[180:183], v[52:67]\n\t" \
  "v_exp_f32 v192, v100\n\t" \
  "v_exp_f32 v193, v101\n\t" \
  "v_exp_f32 v194, v102\n\t" \
  "v_exp_f32 v195, v103\n\t" \
  "s_cmp_eq_u32 %[nt], 0\n\t" \
  "v_mfma_f32_16x16x32_bf16 v[132:135], v[140:143], v[184:187], v[132:135]\n\t" \
  "v_mul_f32 v196, v128, v196\n\t" \
  "v_mul_f32 v197, v129, v197\n\t" \
  "v_mul_f32 v198, v130, v198\n\t" \
  "v_mul_f32 v199, v131, v199\n\t" \
  "v_add_f32 v192, 1.0, v192\n\t" \
  "v_add_f32 v193, 1.0, v193\n\t" \
  "v_add_f32 v194, 1.0, v194\n\t" \
  "v_add_f32 v195, 1.0, v195\n\t" \
  "v_cvt_pk_bf16_f32 v190, v196, v197\n\t" \
  "v_cvt_pk_bf16_f32 v191, v198, v199\n\t" \
  "s_cbranch_scc0 1b\n\t" \
  "s_waitcnt lgkmcnt(0)\n\t" \
  "s_cmp_lg_u32 %[last], 0\n\t" \
  "s_cbranch_scc1 8f\n\t" \
  "ds_write_b128 v243, v[188:191] offset:16\n\t" \
  "ds_read_b64_tr_b16 v[200:201], v244 offset:0\n\t" \
  "ds_read_b64_tr_b16 v[202:203], v244 offset:320\n\t" \
  "ds_read_b64_tr_b16 v[204:205], v244 offset:1280\n\t" \
  "ds_read_b64_tr_b16 v[206:207], v244 offset:1600\n\t" \
  "v_mfma_f32_16x16x32_bf16 v[132:135], v[144:147], v[188:191], v[132:135]\n\t" \
  "s_waitcnt lgkmcnt(0)\n\t" \
  "v_mfma_f32_32x32x16_bf16 v[68:83], v[200:203], v[176:179], v[68:83]\n\t" \
  "v_mfma_f32_32x32x16_bf16 v[68:83], v[204:207], v[180:183], v[68:83]\n\t" \
  "s_nop 8\n\t" \
  "v_mul_f32 v230, v230, v132\n\t" \
  "v_mul_f32 v231, v231, v132\n\t" \
  "v_cvt_pk_bf16_f32 v230, v230, v231\n\t" \
  "ds_write_b16 v245, v230 offset:7456\n\t" \
  "ds_write_b16_d16_hi v241, v230 offset:7584\n\t" \
  "ds_read_b128 v[200:203], v246 offset:7456\n\t" \
  "ds_read_b128 v[208:211], v246 offset:7488\n\t" \
  "s_waitcnt lgkmcnt(0)\n\t" \
  "v_mfma_f32_16x16x32_bf16 v[84:87], v[200:203], v[176:179], v[84:87]\n\t" \
  "v_mfma_f32_16x16x32_bf16 v[84:87], v[208:211], v[180:183], v[84:87]\n\t" \
  "s_branch 9f\n\t" \
  "6:\n\t" \
  "s_cmp_lg_u32 %[last], 0\n\t" \
  "s_cbranch_scc0 9f\n\t" \
  "s_waitcnt lgkmcnt(0)\n\t" \
  "v_cndmask_b32 v224, 0, 24, vcc\n\t" \
  "v_xor_b32 v220, v234, v224\n\t" \
  "v_xor_b32 v221, v235, v224\n\t" \
  "v_xor_b32 v222, v236, v224\n\t" \
  "v_xor_b32 v223, v237, v224\n\t" \
  "v_xor_b32 v216, 16, v220\n\t" \
  "v_xor_b32 v217, 16, v221\n\t" \
  "v_xor_b32 v218, 16, v222\n\t" \
  "v_xor_b32 v219, 16, v223\n\t" \
  "ds_read_b64 v[148:149], v220\n\t" \
  "ds_read_b64 v[150:151], v216\n\t" \
  "ds_read_b64 v[152:153], v221\n\t" \
  "ds_read_b64 v[154:155], v217\n\t" \
  "ds_read_b64 v[156:157], v222\n\t" \
  "ds_read_b64 v[158:159], v218\n\t" \
  "ds_read_b64 v[160:161], v223\n\t" \
  "ds_read_b64 v[162:163], v219\n\t" \
  "ds_read_b128 v[4:7], v247 offset:6144\n\t" \
  "ds_read_b128 v[8:11], v247 offset:7168\n\t" \
  "ds_read_b128 v[12:15], v247 offset:8192\n\t" \
  "ds_read_b128 v[16:19], v247 offset:9216\n\t" \
  "ds_read_b128 v[20:23], v247 offset:10240\n\t" \
  "ds_read_b128 v[24:27], v247 offset:11264\n\t" \
  "v_subrev_u32 v225, %[b1], v241\n\t" \
  "v_lshl_add_u32 v225, v225, 5, v243\n\t" \
  "v_sub_u32 v225, v225, v224\n\t" \
  "s_nop 4\n\t" \
  "v_cvt_pk_bf16_f32 v184, v52, v53\n\t" \
  "v_cvt_pk_bf16_f32 v185, v54, v55\n\t" \
  "v_cvt_pk_bf16_f32 v186, v56, v57\n\t" \
  "v_cvt_pk_bf16_f32 v187, v58, v59\n\t" \
  "v_cvt_pk_bf16_f32 v188, v60, v61\n\t" \
  "v_cvt_pk_bf16_f32 v189, v62, v63\n\t" \
  "v_cvt_pk_bf16_f32 v190, v64, v65\n\t" \
  "v_cvt_pk_bf16_f32 v191, v66, v67\n\t" \
  "v_cvt_pk_bf16_f32 v36, v68, v69\n\t" \
  "v_cvt_pk_bf16_f32 v37, v70, v71\n\t" \
  "v_cvt_pk_bf16_f32 v38, v72, v73\n\t" \
  "v_cvt_pk_bf16_f32 v39, v74, v75\n\t" \
  "v_cvt_pk_bf16_f32 v40, v76, v77\n\t" \
  "v_cvt_pk_bf16_f32 v41, v78, v79\n\t" \
  "v_cvt_pk_bf16_f32 v42, v80, v81\n\t" \
  "v_cvt_pk_bf16_f32 v43, v82, v83\n\t" \
  "s_waitcnt lgkmcnt(5)\n\t" \
  "v_mfma_f32_32x32x16_bf16 v[100:115], v[4:7], v[148:151], 0\n\t" \
  "s_waitcnt lgkmcnt(4)\n\t" \
  "v_mfma_f32_32x32x16_bf16 v[116:131], v[8:11], v[148:151], 0\n\t" \
  "ds_read_b128 v[28:31], v247 offset:12288\n\t" \
  "ds_read_b128 v[32:35], v247 offset:13312\n\t" \
  "s_waitcnt lgkmcnt(5)\n\t" \
  "v_mfma_f32_32x32x16_bf16 v[100:115], v[12:15], v[152:155], v[100:115]\n\t" \
  "s_waitcnt lgkmcnt(4)\n\t" \
  "v_mfma_f32_32x32x16_bf16 v[116:131], v[16:19], v[152:155], v[116:131]\n\t" \
  "ds_read_b128 v[4:7], v247 offset:14336\n\t" \
  "ds_read_b128 v[8:11], v247 offset:15360\n\t" \
  "s_waitcnt lgkmcnt(5)\n\t" \
  "v_mfma_f32_32x32x16_bf16 v[100:115], v[20:23], v[156:159], v[100:115]\n\t" \
  "s_waitcnt lgkmcnt(4)\n\t" \
  "v_mfma_f32_32x32x16_bf16 v[116:131], v[24:27], v[156:159], v[116:131]\n\t" \
  "ds_read_b128 v[12:15], v247 offset:16384\n\t" \
  "ds_read_b128 v[16:19], v247 offset:17408\n\t" \
  "s_waitcnt lgkmcnt(5)\n\t" \
  "v_mfma_f32_32x32x16_bf16 v[100:115], v[28:31], v[160:163], v[100:115]\n\t" \
  "s_waitcnt lgkmcnt(4)\n\t" \
  "v_mfma_f32_32x32x16_bf16 v[116:131], v[32:35], v[160:163], v[116:131]\n\t" \
  "ds_read_b128 v[20:23], v247 offset:18432\n\t" \
  "ds_read_b128 v[24:27], v247 offset:19456\n\t" \
  "s_waitcnt lgkmcnt(5)\n\t" \
  "v_mfma_f32_32x32x16_bf16 v[100:115], v[4:7], v[184:187], v[100:115]\n\t" \
  "ds_read_b128 v[28:31], v247 offset:20480\n\t" \
  "ds_read_b128 v[32:35], v247 offset:21504\n\t" \
  "s_waitcnt lgkmcnt(5)\n\t" \
  "v_mfma_f32_32x32x16_bf16 v[100:115], v[12:15], v[188:191], v[100:115]\n\t" \
  "s_waitcnt lgkmcnt(3)\n\t" \
  "v_mfma_f32_32x32x16_bf16 v[100:115], v[20:23], v[36:39], v[100:115]\n\t" \
  "s_waitcnt lgkmcnt(1)\n\t" \
  "v_mfma_f32_32x32x16_bf16 v[100:115], v[28:31], v[40:43], v[100:115]\n\t" \
  "s_waitcnt lgkmcnt(0)\n\t" \
  "s_branch 10f\n\t" \
  "8:\n\t" \
  "ds_write_b128 v243, v[188:191] offset:16\n\t" \
  "ds_read_b64_tr_b16 v[200:201], v244 offset:0\n\t" \
  "ds_read_b64_tr_b16 v[202:203], v244 offset:320\n\t" \
  "ds_read_b64_tr_b16 v[204:205], v244 offset:1280\n\t" \
  "ds_read_b64_tr_b16 v[206:207], v244 offset:1600\n\t" \
  "v_mfma_f32_16x16x32_bf16 v[132:135], v[144:147], v[188:191], v[132:135]\n\t" \
  "v_cndmask_b32 v224, 0, 24, vcc\n\t" \
  "v_xor_b32 v220, v234, v224\n\t" \
  "v_xor_b32 v221, v235, v224\n\t" \
  "v_xor_b32 v222, v236, v224\n\t" \
  "v_xor_b32 v223, v237, v224\n\t" \
  "v_xor_b32 v216, 16, v220\n\t" \
  "v_xor_b32 v217, 16, v221\n\t" \
  "v_xor_b32 v218, 16, v222\n\t" \
  "v_xor_b32 v219, 16, v223\n\t" \
  "ds_read_b64 v[148:149], v220\n\t" \
  "ds_read_b64 v[150:151], v216\n\t" \
  "ds_read_b64 v[152:153], v221\n\t" \
  "ds_read_b64 v[154:155], v217\n\t" \
  "ds_read_b64 v[156:157], v222\n\t" \
  "ds_read_b64 v[158:159], v218\n\t" \
  "ds_read_b64 v[160:161], v223\n\t" \
  "ds_read_b64 v[162:163], v219\n\t" \
  "ds_read_b128 v[4:7], v247 offset:6144\n\t" \
  "ds_read_b128 v[8:11], v247 offset:7168\n\t" \
  "s_waitcnt lgkmcnt(11)\n\t" \
  "ds_read_b128 v[12:15], v247 offset:8192\n\t" \
  "ds_read_b128 v[16:19], v247 offset:9216\n\t" \
  "ds_read_b128 v[20:23], v247 offset:10240\n\t" \
  "ds_read_b128 v[24:27], v247 offset:11264\n\t" \
  "v_subrev_u32 v225, %[b1], v241\n\t" \
  "v_lshl_add_u32 v225, v225, 5, v243\n\t" \
  "v_sub_u32 v225, v225, v224\n\t" \
  "v_mfma_f32_32x32x16_bf16 v[68:83], v[200:203], v[176:179], v[68:83]\n\t" \
  "s_waitcnt lgkmcnt(5)\n\t" \
  "v_mfma_f32_32x32x16_bf16 v[100:115], v[4:7], v[148:151], 0\n\t" \
  "v_mfma_f32_32x32x16_bf16 v[68:83], v[204:207], v[180:183], v[68:83]\n\t" \
  "s_waitcnt lgkmcnt(4)\n\t" \
  "v_mfma_f32_32x32x16_bf16 v[116:131], v[8:11], v[148:151], 0\n\t" \
  "ds_read_b128 v[28:31], v247 offset:12288\n\t" \
  "ds_read_b128 v[32:35], v247 offset:13312\n\t" \
  "s_waitcnt lgkmcnt(5)\n\t" \
  "v_mfma_f32_32x32x16_bf16 v[100:115], v[12:15], v[152:155], v[100:115]\n\t" \
  "v_mul_f32 v230, v230, v132\n\t" \
  "v_mul_f32 v231, v231, v132\n\t" \
  "v_cvt_pk_bf16_f32 v230, v230, v231\n\t" \
  "ds_write_b16 v245, v230 offset:7456\n\t" \
  "ds_write_b16_d16_hi v241, v230 offset:7584\n\t" \
  "ds_read_b128 v[200:203], v246 offset:7456\n\t" \
  "ds_read_b128 v[208:211], v246 offset:7488\n\t" \
  "v_cvt_pk_bf16_f32 v184, v52, v53\n\t" \
  "v_cvt_pk_bf16_f32 v185, v54, v55\n\t" \
  "v_cvt_pk_bf16_f32 v186, v56, v57\n\t" \
  "v_cvt_pk_bf16_f32 v187, v58, v59\n\t" \
  "s_waitcnt lgkmcnt(8)\n\t" \
  "v_mfma_f32_32x32x16_bf16 v[116:131], v[16:19], v[152:155], v[116:131]\n\t" \
  "ds_read_b128 v[4:7], v247 offset:14336\n\t" \
  "ds_read_b128 v[8:11], v247 offset:15360\n\t" \
  "s_waitcnt lgkmcnt(9)\n\t" \
  "v_mfma_f32_32x32x16_bf16 v[100:115], v[20:23], v[156:159], v[100:115]\n\t" \
  "v_cvt_pk_bf16_f32 v188, v60, v61\n\t" \
  "v_cvt_pk_bf16_f32 v189, v62, v63\n\t" \
  "v_cvt_pk_bf16_f32 v190, v64, v65\n\t" \
  "v_cvt_pk_bf16_f32 v191, v66, v67\n\t" \
  "s_waitcnt lgkmcnt(8)\n\t" \
  "v_mfma_f32_32x32x16_bf16 v[116:131], v[24:27], v[156:159], v[116:131]\n\t" \
  "ds_read_b128 v[12:15], v247 offset:16384\n\t" \
  "ds_read_b128 v[16:19], v247 offset:17408\n\t" \
  "s_waitcnt lgkmcnt(9)\n\t" \
  "v_mfma_f32_32x32x16_bf16 v[100:115], v[28:31], v[160:163], v[100:115]\n\t" \
  "v_cvt_pk_bf16_f32 v36, v68, v69\n\t" \
  "v_cvt_pk_bf16_f32 v37, v70, v71\n\t" \
  "v_cvt_pk_bf16_f32 v38, v72, v73\n\t" \
  "v_cvt_pk_bf16_f32 v39, v74, v75\n\t" \
  "s_waitcnt lgkmcnt(5)\n\t" \
  "v_mfma_f32_16x16x32_bf16 v[84:87], v[200:203], v[176:179], v[84:87]\n\t" \
  "v_cvt_pk_bf16_f32 v40, v76, v77\n\t" \
  "v_cvt_pk_bf16_f32 v41, v78, v79\n\t" \
  "v_cvt_pk_bf16_f32 v42, v80, v81\n\t" \
  "v_cvt_pk_bf16_f32 v43, v82, v83\n\t" \
  "v_mfma_f32_32x32x16_bf16 v[116:131], v[32:35], v[160:163], v[116:131]\n\t" \
  "ds_read_b128 v[20:23], v247 offset:18432\n\t" \
  "ds_read_b128 v[24:27], v247 offset:19456\n\t" \
  "s_waitcnt lgkmcnt(6)\n\t" \
  "v_mfma_f32_16x16x32_bf16 v[84:87], v[208:211], v[180:183], v[84:87]\n\t" \
  "s_waitcnt lgkmcnt(5)\n\t" \
  "v_mfma_f32_32x32x16_bf16 v[100:115], v[4:7], v[184:187], v[100:115]\n\t" \
  "ds_read_b128 v[28:31], v247 offset:20480\n\t" \
  "ds_read_b128 v[32:35], v247 offset:21504\n\t" \
  "s_waitcnt lgkmcnt(5)\n\t" \
  "v_mfma_f32_32x32x16_bf16 v[100:115], v[12:15], v[188:191], v[100:115]\n\t" \
  "s_waitcnt lgkmcnt(3)\n\t" \
  "v_mfma_f32_32x32x16_bf16 v[100:115], v[20:23], v[36:39], v[100:115]\n\t" \
  "s_waitcnt lgkmcnt(1)\n\t" \
  "v_mfma_f32_32x32x16_bf16 v[100:115], v[28:31], v[40:43], v[100:115]\n\t" \
  "s_waitcnt lgkmcnt(0)\n\t" \
  "10:\n\t" \
  "v_mov_b32 v212, 0xbfb8aa3b\n\t" \
  "v_mfma_f32_32x32x16_bf16 v[116:131], v[8:11], v[184:187], v[116:131]\n\t" \
  "v_mfma_f32_32x32x16_bf16 v[116:131], v[16:19], v[188:191], v[116:131]\n\t" \
  "s_nop 8\n\t" \
  "v_exp_f32 v192, v100\n\t" \
  "v_exp_f32 v193, v101\n\t" \
  "v_exp_f32 v194, v102\n\t" \
  "v_exp_f32 v195, v103\n\t" \
  "v_lshlrev_b32 v200, 16, v148\n\t" \
  "v_and_b32 v201, 0xffff0000, v148\n\t" \
  "v_lshlrev_b32 v202, 16, v149\n\t" \
  "v_and_b32 v203, 0xffff0000, v149\n\t" \
  "v_mfma_f32_32x32x16_bf16 v[116:131], v[24:27], v[36:39], v[116:131]\n\t" \
  "v_fmaak_f32 v192, v192, v212, 0xbfb8aa3b\n\t" \
  "v_fmaak_f32 v193, v193, v212, 0xbfb8aa3b\n\t" \
  "v_fmaak_f32 v194, v194, v212, 0xbfb8aa3b\n\t" \
  "v_fmaak_f32 v195, v195, v212, 0xbfb8aa3b\n\t" \
  "v_rcp_f32 v192, v192\n\t" \
  "v_rcp_f32 v193, v193\n\t" \
  "v_rcp_f32 v194, v194\n\t" \
  "v_rcp_f32 v195, v195\n\t" \
  "v_mfma_f32_32x32x16_bf16 v[116:131], v[32:35], v[40:43], v[116:131]\n\t" \
  "v_exp_f32 v196, v104\n\t" \
  "v_exp_f32 v197, v105\n\t" \
  "v_exp_f32 v198, v106\n\t" \
  "v_exp_f32 v199, v107\n\t" \
  "v_fmac_f32 v200, v100, v192\n\t" \
  "v_fmac_f32 v201, v101, v193\n\t" \
  "v_fmac_f32 v202, v102, v194\n\t" \
  "v_fmac_f32 v203, v103, v195\n\t" \
  "v_lshlrev_b32 v204, 16, v150\n\t" \
  "v_and_b32 v205, 0xffff0000, v150\n\t" \
  "v_lshlrev_b32 v206, 16, v151\n\t" \
  "v_and_b32 v207, 0xffff0000, v151\n\t" \
  "v_fmaak_f32 v196, v196, v212, 0xbfb8aa3b\n\t" \
  "v_fmaak_f32 v197, v197, v212, 0xbfb8aa3b\n\t" \
  "v_fmaak_f32 v198, v198, v212, 0xbfb8aa3b\n\t" \
  "v_fmaak_f32 v199, v199, v212, 0xbfb8aa3b\n\t" \
  "v_cvt_pk_bf16_f32 v176, v200, v201\n\t" \
  "v_cvt_pk_bf16_f32 v177, v202, v203\n\t" \
  "ds_write_b64 v225, v[176:177] offset:0\n\t" \
  "v_rcp_f32 v196, v196\n\t" \
  "v_rcp_f32 v197, v197\n\t" \
  "v_rcp_f32 v198, v198\n\t" \
  "v_rcp_f32 v199, v199\n\t" \
  "v_exp_f32 v192, v108\n\t" \
  "v_exp_f32 v193, v109\n\t" \
  "v_exp_f32 v194, v110\n\t" \
  "v_exp_f32 v195, v111\n\t" \
  "v_fmac_f32 v204, v104, v196\n\t" \
  "v_fmac_f32 v205, v105, v197\n\t" \
  "v_fmac_f32 v206, v106, v198\n\t" \
  "v_fmac_f32 v207, v107, v199\n\t" \
  "v_lshlrev_b32 v200, 16, v152\n\t" \
  "v_and_b32 v201, 0xffff0000, v152\n\t" \
  "v_lshlrev_b32 v202, 16, v153\n\t" \
  "v_and_b32 v203, 0xffff0000, v153\n\t" \
  "v_fmaak_f32 v192, v192, v212, 0xbfb8aa3b\n\t" \
  "v_fmaak_f32 v193, v193, v212, 0xbfb8aa3b\n\t" \
  "v_fmaak_f32 v194, v194, v212, 0xbfb8aa3b\n\t" \
  "v_fmaak_f32 v195, v195, v212, 0xbfb8aa3b\n\t" \
  "v_cvt_pk_bf16_f32 v178, v204, v205\n\t" \
  "v_cvt_pk_bf16_f32 v179, v206, v207\n\t" \
  "ds_write_b64 v225, v[178:179] offset:16\n\t" \
  "v_rcp_f32 v192, v192\n\t" \
  "v_rcp_f32 v193, v193\n\t" \
  "v_rcp_f32 v194, v194\n\t" \
  "v_rcp_f32 v195, v195\n\t" \
  "v_exp_f32 v196, v112\n\t" \
  "v_exp_f32 v197, v113\n\t" \
  "v_exp_f32 v198, v114\n\t" \
  "v_exp_f32 v199, v115\n\t" \
  "v_fmac_f32 v200, v108, v192\n\t" \
  "v_fmac_f32 v201, v109, v193\n\t" \
  "v_fmac_f32 v202, v110, v194\n\t" \
  "v_fmac_f32 v203, v111, v195\n\t" \
  "v_lshlrev_b32 v204, 16, v154\n\t" \
  "v_and_b32 v205, 0xffff0000, v154\n\t" \
  "v_lshlrev_b32 v206, 16, v155\n\t" \
  "v_and_b32 v207, 0xffff0000, v155\n\t" \
  "v_fmaak_f32 v196, v196, v212, 0xbfb8aa3b\n\t" \
  "v_fmaak_f32 v197, v197, v212, 0xbfb8aa3b\n\t" \
  "v_fmaak_f32 v198, v198, v212, 0xbfb8aa3b\n\t" \
  "v_fmaak_f32 v199, v199, v212, 0xbfb8aa3b\n\t" \
  "v_cvt_pk_bf16_f32 v180, v200, v201\n\t" \
  "v_cvt_pk_bf16_f32 v181, v202, v203\n\t" \
  "ds_write_b64 v225, v[180:181] offset:32\n\t" \
  "v_rcp_f32 v196, v196\n\t" \
  "v_rcp_f32 v197, v197\n\t" \
  "v_rcp_f32 v198, v198\n\t" \
  "v_rcp_f32 v199, v199\n\t" \
  "v_exp_f32 v192, v116\n\t" \
  "v_exp_f32 v193, v117\n\t" \
  "v_exp_f32 v194, v118\n\t" \
  "v_exp_f32 v195, v119\n\t" \
  "v_fmac_f32 v204, v112, v196\n\t" \
  "v_fmac_f32 v205, v113, v197\n\t" \
  "v_fmac_f32 v206, v114, v198\n\t" \
  "v_fmac_f32 v207, v115, v199\n\t" \
  "v_lshlrev_b32 v200, 16, v156\n\t" \
  "v_and_b32 v201, 0xffff0000, v156\n\t" \
  "v_lshlrev_b32 v202, 16, v157\n\t" \
  "v_and_b32 v203, 0xffff0000, v157\n\t" \
  "v_fmaak_f32 v192, v192, v212, 0xbfb8aa3b\n\t" \
  "v_fmaak_f32 v193, v193, v212, 0xbfb8aa3b\n\t" \
  "v_fmaak_f32 v194, v194, v212, 0xbfb8aa3b\n\t" \
  "v_fmaak_f32 v195, v195, v212, 0xbfb8aa3b\n\t" \
  "v_cvt_pk_bf16_f32 v182, v204, v205\n\t" \
  "v_cvt_pk_bf16_f32 v183, v206, v207\n\t" \
  "ds_write_b64 v225, v[182:183] offset:48\n\t" \
  "v_rcp_f32 v192, v192\n\t" \
  "v_rcp_f32 v193, v193\n\t" \
  "v_rcp_f32 v194, v194\n\t" \
  "v_rcp_f32 v195, v195\n\t" \
  "v_exp_f32 v196, v120\n\t" \
  "v_exp_f32 v197, v121\n\t" \
  "v_exp_f32 v198, v122\n\t" \
  "v_exp_f32 v199, v123\n\t" \
  "v_fmac_f32 v200, v116, v192\n\t" \
  "v_fmac_f32 v201, v117, v193\n\t" \
  "v_fmac_f32 v202, v118, v194\n\t" \
  "v_fmac_f32 v203, v119, v195\n\t" \
  "v_lshlrev_b32 v204, 16, v158\n\t" \
  "v_and_b32 v205, 0xffff0000, v158\n\t" \
  "v_lshlrev_b32 v206, 16, v159\n\t" \
  "v_and_b32 v207, 0xffff0000, v159\n\t" \
  "v_fmaak_f32 v196, v196, v212, 0xbfb8aa3b\n\t" \
  "v_fmaak_f32 v197, v197, v212, 0xbfb8aa3b\n\t" \
  "v_fmaak_f32 v198, v198, v212, 0xbfb8aa3b\n\t" \
  "v_fmaak_f32 v199, v199, v212, 0xbfb8aa3b\n\t" \
  "v_cvt_pk_bf16_f32 v176, v200, v201\n\t" \
  "v_cvt_pk_bf16_f32 v177, v202, v203\n\t" \
  "ds_write_b64 v225, v[176:177] offset:64\n\t" \
  "v_rcp_f32 v196, v196\n\t" \
  "v_rcp_f32 v197, v197\n\t" \
  "v_rcp_f32 v198, v198\n\t" \
  "v_rcp_f32 v199, v199\n\t" \
  "v_exp_f32 v192, v124\n\t" \
  "v_exp_f32 v193, v125\n\t" \
  "v_exp_f32 v194, v126\n\t" \
  "v_exp_f32 v195, v127\n\t" \
  "v_fmac_f32 v204, v120, v196\n\t" \
  "v_fmac_f32 v205, v121, v197\n\t" \
  "v_fmac_f32 v206, v122, v198\n\t" \
  "v_fmac_f32 v207, v123, v199\n\t" \
  "v_lshlrev_b32 v200, 16, v160\n\t" \
  "v_and_b32 v201, 0xffff0000, v160\n\t" \
  "v_lshlrev_b32 v202, 16, v161\n\t" \
  "v_and_b32 v203, 0xffff0000, v161\n\t" \
  "v_fmaak_f32 v192, v192, v212, 0xbfb8aa3b\n\t" \
  "v_fmaak_f32 v193, v193, v212, 0xbfb8aa3b\n\t" \
  "v_fmaak_f32 v194, v194, v212, 0xbfb8aa3b\n\t" \
  "v_fmaak_f32 v195, v195, v212, 0xbfb8aa3b\n\t" \
  "v_cvt_pk_bf16_f32 v178, v204, v205\n\t" \
  "v_cvt_pk_bf16_f32 v179, v206, v207\n\t" \
  "ds_write_b64 v225, v[178:179] offset:80\n\t" \
  "v_rcp_f32 v192, v192\n\t" \
  "v_rcp_f32 v193, v193\n\t" \
  "v_rcp_f32 v194, v194\n\t" \
  "v_rcp_f32 v195, v195\n\t" \
  "v_exp_f32 v196, v128\n\t" \
  "v_exp_f32 v197, v129\n\t" \
  "v_exp_f32 v198, v130\n\t" \
  "v_exp_f32 v199, v131\n\t" \
  "v_fmac_f32 v200, v124, v192\n\t" \
  "v_fmac_f32 v201, v125, v193\n\t" \
  "v_fmac_f32 v202, v126, v194\n\t" \
  "v_fmac_f32 v203, v127, v195\n\t" \
  "v_lshlrev_b32 v204, 16, v162\n\t" \
  "v_and_b32 v205, 0xffff0000, v162\n\t" \
  "v_lshlrev_b32 v206, 16, v163\n\t" \
  "v_and_b32 v207, 0xffff0000, v163\n\t" \
  "v_fmaak_f32 v196, v196, v212, 0xbfb8aa3b\n\t" \
  "v_fmaak_f32 v197, v197, v212, 0xbfb8aa3b\n\t" \
  "v_fmaak_f32 v198, v198, v212, 0xbfb8aa3b\n\t" \
  "v_fmaak_f32 v199, v199, v212, 0xbfb8aa3b\n\t" \
  "v_cvt_pk_bf16_f32 v180, v200, v201\n\t" \
  "v_cvt_pk_bf16_f32 v181, v202, v203\n\t" \
  "ds_write_b64 v225, v[180:181] offset:96\n\t" \
  "v_rcp_f32 v196, v196\n\t" \
  "v_rcp_f32 v197, v197\n\t" \
  "v_rcp_f32 v198, v198\n\t" \
  "v_rcp_f32 v199, v199\n\t" \
  "s_nop 0\n\t" \
  "v_fmac_f32 v204, v128, v196\n\t" \
  "v_fmac_f32 v205, v129, v197\n\t" \
  "v_fmac_f32 v206, v130, v198\n\t" \
  "v_fmac_f32 v207, v131, v199\n\t" \
  "v_cvt_pk_bf16_f32 v182, v204, v205\n\t" \
  "v_cvt_pk_bf16_f32 v183, v206, v207\n\t" \
  "ds_write_b64 v225, v[182:183] offset:112\n\t" \
  "9:\n\t" \
  "ds_write_b16 v233, v255 offset:5376\n\t" \
  "s_mov_b32 m0, %[keep]\n\t" \
  "s_setprio 0\n\t" \
  "s_nop 11\n\t" \
  ""
#define LOOP_ASM_PLAIN \
  "s_mov_b32 %[keep], m0\n\t" \
  "s_mov_b64 vcc, %[hsel]\n\t" \
  "ds_read2_b32 v[220:221], v252 offset0:0 offset1:2\n\t" \
  "ds_read2_b32 v[222:223], v252 offset0:4 offset1:6\n\t" \
  "ds_read_b64 v[226:227], v253\n\t" \
  "ds_read_b32 v232, v254\n\t" \
  "v_mov_b32 v176, 0\n\t" \
  "v_mov_b32 v177, 0\n\t" \
  "v_mov_b32 v178, 0\n\t" \
  "v_mov_b32 v179, 0\n\t" \
  "v_mov_b32 v180, 0\n\t" \
  "v_mov_b32 v181, 0\n\t" \
  "v_mov_b32 v182, 0\n\t" \
  "v_mov_b32 v183, 0\n\t" \
  "v_mov_b32 v184, 0\n\t" \
  "v_mov_b32 v185, 0\n\t" \
  "v_mov_b32 v186, 0\n\t" \
  "v_mov_b32 v187, 0\n\t" \
  "v_mov_b32 v188, 0\n\t" \
  "v_mov_b32 v189, 0\n\t" \
  "v_mov_b32 v190, 0\n\t" \
  "v_mov_b32 v191, 0\n\t" \
  "v_mov_b32 v230, 0\n\t" \
  "v_mov_b32 v231, 0\n\t" \
  "v_mov_b32 v208, 0\n\t" \
  "v_mov_b32 v209, 0\n\t" \
  "v_mov_b32 v210, 0\n\t" \
  "v_mov_b32 v211, 0\n\t" \
  "ds_write_b128 v243, v[184:187] offset:0\n\t" \
  "s_nop 0\n\t" \
  "v_mfma_f32_16x16x32_bf16 v[132:135], v[184:187], v[188:191], 0\n\t" \
  "v_add_u32 v252, 0x100, v252\n\t" \
  "v_add_u32 v253, 0x100, v253\n\t" \
  "v_add_u32 v254, 0x100, v254\n\t" \
  "s_waitcnt lgkmcnt(0)\n\t" \
  "v_lshrrev_b32 v232, 20, v232\n\t" \
  "v_add_u32 v233, %[posd], v232\n\t" \
  "ds_read_b128 v[216:219], v233\n\t" \
  "v_add_u32 v233, v240, v232\n\t" \
  "ds_read_b128 v[172:175], v233 offset:32\n\t" \
  "ds_read_b128 v[168:171], v233\n\t" \
  "v_add_u32 v233, v241, v232\n\t" \
  "ds_write_b16_d16_hi v233, v255 offset:5376\n\t" \
  "v_mad_u32_u16 v220, v220, %[c128], v248\n\t" \
  "v_mad_u32_u16 v221, v221, %[c128], v249\n\t" \
  "v_mad_u32_u16 v222, v222, %[c128], v248\n\t" \
  "v_mad_u32_u16 v223, v223, %[c128], v249\n\t" \
  "v_or_b32 v226, v226, v250\n\t" \
  "v_mad_u32_u16 v227, v227, 6, v251\n\t" \
  "s_waitcnt vmcnt(0)\n\t" \
  "ds_read_b128 v[148:151], v234\n\t" \
  "ds_read_b128 v[152:155], v235\n\t" \
  "ds_read_b128 v[156:159], v236\n\t" \
  "ds_read_b128 v[160:163], v237\n\t" \
  "ds_read_b128 v[164:167], v238 offset:4096\n\t" \
  "ds_read_b64 v[224:225], v239 offset:5120\n\t" \
  "s_waitcnt lgkmcnt(0)\n\t" \
  "s_cmp_lt_i32 %[nt], 1\n\t" \
  "s_cbranch_scc1 7f\n\t" \
  "s_mov_b32 m0, %[b2]\n\t" \
  "s_nop 0\n\t" \
  "global_load_lds_dwordx4 v226, %[ea]\n\t" \
  "global_load_lds_dword v227, %[pos] offset:1024\n\t" \
  "s_mov_b32 m0, %[b1]\n\t" \
  "s_nop 0\n\t" \
  "global_load_lds_dwordx4 v220, %[h0]\n\t" \
  "global_load_lds_dwordx4 v221, %[h1] offset:1024\n\t" \
  "global_load_lds_dwordx4 v222, %[h2] offset:2048\n\t" \
  "global_load_lds_dwordx4 v223, %[h3] offset:3072\n\t" \
  "7:\n\t" \
  "ds_read2_b32 v[220:221], v252 offset0:0 offset1:2\n\t" \
  "ds_read2_b32 v[222:223], v252 offset0:4 offset1:6\n\t" \
  "ds_read_b64 v[226:227], v253\n\t" \
  "ds_read_b32 v232, v254\n\t" \
  "v_add_u32 v252, 0x100, v252\n\t" \
  "v_add_u32 v253, 0x100, v253\n\t" \
  "v_add_u32 v254, 0x100, v254\n\t" \
  "v_and_b32 v225, 0xffff0000, v225\n\t" \
  "v_lshlrev_b32 v219, 16, v224\n\t" \
  "v_and_b32 v224, 0xffff0000, v224\n\t" \
  "v_sub_f32 v229, v218, v225\n\t" \
  "v_sub_f32 v216, v216, v219\n\t" \
  "v_sub_f32 v217, v217, v224\n\t" \
  "v_mul_f32 v218, v229, v229\n\t" \
  "v_cndmask_b32 v228, v216, v217, vcc\n\t" \
  "v_fmac_f32 v218, v216, v216\n\t" \
  "v_fmac_f32 v218, v217, v217\n\t" \
  "v_cvt_pk_bf16_f32 v218, v218, v218\n\t" \
  "v_cndmask_b32_sdwa v175, v175, v218, vcc dst_sel:WORD_1 dst_unused:UNUSED_PRESERVE src0_sel:WORD_1 src1_sel:WORD_1\n\t" \
  "v_mfma_f32_32x32x16_bf16 v[100:115], v[4:7], v[148:151], 0\n\t" \
  "v_mfma_f32_32x32x16_bf16 v[100:115], v[12:15], v[152:155], v[100:115]\n\t" \
  "v_mfma_f32_32x32x16_bf16 v[100:115], v[20:23], v[156:159], v[100:115]\n\t" \
  "v_mfma_f32_32x32x16_bf16 v[100:115], v[28:31], v[160:163], v[100:115]\n\t" \
  "v_mfma_f32_32x32x16_bf16 v[100:115], v[36:39], v[168:171], v[100:115]\n\t" \
  "v_mfma_f32_32x32x16_bf16 v[100:115], v[88:91], v[164:167], v[100:115]\n\t" \
  "v_mfma_f32_32x32x16_bf16 v[100:115], v[44:47], v[172:175], v[100:115]\n\t" \
  "s_cmp_eq_u32 %[nt], 0\n\t" \
  "s_nop 10\n\t" \
  "v_mul_f32 v192, 0xbfb8aa3b, v100\n\t" \
  "v_mul_f32 v193, 0xbfb8aa3b, v101\n\t" \
  "v_mul_f32 v194, 0xbfb8aa3b, v102\n\t" \
  "v_mul_f32 v195, 0xbfb8aa3b, v103\n\t" \
  "v_exp_f32 v192, v192\n\t" \
  "v_exp_f32 v193, v193\n\t" \
  "v_exp_f32 v194, v194\n\t" \
  "v_exp_f32 v195, v195\n\t" \
  "s_nop 0\n\t" \
  "v_add_f32 v192, 1.0, v192\n\t" \
  "v_add_f32 v193, 1.0, v193\n\t" \
  "v_add_f32 v194, 1.0, v194\n\t" \
  "v_add_f32 v195, 1.0, v195\n\t" \
  "s_cbranch_scc1 6f\n\t" \
  "s_cmp_lt_i32 %[thr], 1\n\t" \
  "s_cbranch_scc1 1f\n\t" \
  "s_setprio 1\n\t" \
  "1:\n\t" \
  "ds_write_b128 v243, v[188:191] offset:16\n\t" \
  "ds_read_b32 v214, v213\n\t" \
  "ds_read_b64_tr_b16 v[200:201], v244 offset:0\n\t" \
  "ds_read_b64_tr_b16 v[202:203], v244 offset:320\n\t" \
  "ds_read_b64_tr_b16 v[204:205], v244 offset:1280\n\t" \
  "ds_read_b64_tr_b16 v[206:207], v244 offset:1600\n\t" \
  "v_mfma_f32_16x16x32_bf16 v[132:135], v[144:147], v[188:191], v[132:135]\n\t" \
  "v_rcp_f32 v192, v192\n\t" \
  "v_rcp_f32 v193, v193\n\t" \
  "v_rcp_f32 v194, v194\n\t" \
  "v_rcp_f32 v195, v195\n\t" \
  "v_mfma_f32_32x32x16_bf16 v[116:131], v[8:11], v[148:151], 0\n\t" \
  "v_mul_f32 v196, 0xbfb8aa3b, v104\n\t" \
  "v_mul_f32 v197, 0xbfb8aa3b, v105\n\t" \
  "v_mul_f32 v198, 0xbfb8aa3b, v106\n\t" \
  "v_mul_f32 v199, 0xbfb8aa3b, v107\n\t" \
  "v_exp_f32 v196, v196\n\t" \
  "v_exp_f32 v197, v197\n\t" \
  "v_exp_f32 v198, v198\n\t" \
  "v_exp_f32 v199, v199\n\t" \
  "v_mfma_f32_32x32x16_bf16 v[116:131], v[16:19], v[152:155], v[116:131]\n\t" \
  "v_mul_f32 v192, v100, v192\n\t" \
  "v_mul_f32 v193, v101, v193\n\t" \
  "v_mul_f32 v194, v102, v194\n\t" \
  "v_mul_f32 v195, v103, v195\n\t" \
  "v_add_f32 v196, 1.0, v196\n\t" \
  "v_add_f32 v197, 1.0, v197\n\t" \
  "v_add_f32 v198, 1.0, v198\n\t" \
  "v_add_f32 v199, 1.0, v199\n\t" \
  "v_cvt_pk_bf16_f32 v184, v192, v193\n\t" \
  "v_cvt_pk_bf16_f32 v185, v194, v195\n\t" \
  "v_mfma_f32_32x32x16_bf16 v[116:131], v[24:27], v[156:159], v[116:131]\n\t" \
  "v_rcp_f32 v196, v196\n\t" \
  "v_rcp_f32 v197, v197\n\t" \
  "v_rcp_f32 v198, v198\n\t" \
  "v_rcp_f32 v199, v199\n\t" \
  "s_waitcnt lgkmcnt(2)\n\t" \
  "v_mfma_f32_32x32x16_bf16 v[68:83], v[200:203], v[176:179], v[68:83]\n\t" \
  "v_mul_f32 v192, 0xbfb8aa3b, v108\n\t" \
  "v_mul_f32 v193, 0xbfb8aa3b, v109\n\t" \
  "v_mul_f32 v194, 0xbfb8aa3b, v110\n\t" \
  "v_mul_f32 v195, 0xbfb8aa3b, v111\n\t" \
  "v_exp_f32 v192, v192\n\t" \
  "v_exp_f32 v193, v193\n\t" \
  "v_exp_f32 v194, v194\n\t" \
  "v_exp_f32 v195, v195\n\t" \
  "v_mfma_f32_32x32x16_bf16 v[116:131], v[32:35], v[160:163], v[116:131]\n\t" \
  "v_mul_f32 v196, v104, v196\n\t" \
  "v_mul_f32 v197, v105, v197\n\t" \
  "v_mul_f32 v198, v106, v198\n\t" \
  "v_mul_f32 v199, v107, v199\n\t" \
  "v_mul_f32 v230, v230, v132\n\t" \
  "v_mul_f32 v231, v231, v132\n\t" \
  "v_cvt_pk_bf16_f32 v230, v230, v231\n\t" \
  "ds_write_b16 v245, v230 offset:7456\n\t" \
  "ds_write_b16_d16_hi v241, v230 offset:7584\n\t" \
  "ds_read_b128 v[200:203], v246 offset:7456\n\t" \
  "ds_read_b128 v[208:211], v246 offset:7488\n\t" \
  "v_mov_b32 v230, v228\n\t" \
  "v_mov_b32 v231, v229\n\t" \
  "v_add_f32 v192, 1.0, v192\n\t" \
  "v_add_f32 v193, 1.0, v193\n\t" \
  "v_add_f32 v194, 1.0, v194\n\t" \
  "v_add_f32 v195, 1.0, v195\n\t" \
  "v_cvt_pk_bf16_f32 v186, v196, v197\n\t" \
  "v_cvt_pk_bf16_f32 v187, v198, v199\n\t" \
  "v_mfma_f32_32x32x16_bf16 v[116:131], v[40:43], v[168:171], v[116:131]\n\t" \
  "v_rcp_f32 v192, v192\n\t" \
  "v_rcp_f32 v193, v193\n\t" \
  "v_rcp_f32 v194, v194\n\t" \
  "v_rcp_f32 v195, v195\n\t" \
  "s_sub_u32 %[nt], %[nt], 1\n\t" \
  "v_mov_b32 v215, %[nt]\n\t" \
  "v_readfirstlane_b32 %[tmp], v214\n\t" \
  "s_mov_b64 exec, 1\n\t" \
  "ds_write_b32 v212, v215\n\t" \
  "s_mov_b64 exec, -1\n\t" \
  "s_sub_i32 %[tmp], %[nt], %[tmp]\n\t" \
  "s_add_i32 %[tmp], %[tmp], %[thr]\n\t" \
  "s_cmp_gt_i32 %[tmp], 0\n\t" \
  "s_setprio 0\n\t" \
  "s_cbranch_scc0 3f\n\t" \
  "s_setprio 1\n\t" \
  "3:\n\t" \
  "v_mfma_f32_32x32x16_bf16 v[116:131], v[92:95], v[164:167], v[116:131]\n\t" \
  "v_mul_f32 v196, 0xbfb8aa3b, v112\n\t" \
  "v_mul_f32 v197, 0xbfb8aa3b, v113\n\t" \
  "v_mul_f32 v198, 0xbfb8aa3b, v114\n\t" \
  "v_mul_f32 v199, 0xbfb8aa3b, v115\n\t" \
  "v_exp_f32 v196, v196\n\t" \
  "v_exp_f32 v197, v197\n\t" \
  "v_exp_f32 v198, v198\n\t" \
  "v_exp_f32 v199, v199\n\t" \
  "v_mfma_f32_32x32x16_bf16 v[116:131], v[48:51], v[172:175], v[116:131]\n\t" \
  "v_mul_f32 v192, v108, v192\n\t" \
  "v_mul_f32 v193, v109, v193\n\t" \
  "v_mul_f32 v194, v110, v194\n\t" \
  "v_mul_f32 v195, v111, v195\n\t" \
  "s_waitcnt vmcnt(0)\n\t" \
  "ds_read_b128 v[148:151], v234\n\t" \
  "ds_read_b128 v[152:155], v235\n\t" \
  "ds_read_b128 v[156:159], v236\n\t" \
  "ds_read_b128 v[160:163], v237\n\t" \
  "ds_read_b128 v[164:167], v238 offset:4096\n\t" \
  "ds_read_b64 v[224:225], v239 offset:5120\n\t" \
  "ds_write_b128 v243, v[184:187] offset:0\n\t" \
  "v_add_f32 v196, 1.0, v196\n\t" \
  "v_add_f32 v197, 1.0, v197\n\t" \
  "v_add_f32 v198, 1.0, v198\n\t" \
  "v_add_f32 v199, 1.0, v199\n\t" \
  "v_cvt_pk_bf16_f32 v188, v192, v193\n\t" \
  "v_cvt_pk_bf16_f32 v189, v194, v195\n\t" \
  "v_mad_u32_u16 v220, v220, %[c128], v248\n\t" \
  "v_mad_u32_u16 v221, v221, %[c128], v249\n\t" \
  "v_mad_u32_u16 v222, v222, %[c128], v248\n\t" \
  "s_waitcnt lgkmcnt(12)\n\t" \
  "v_mfma_f32_32x32x16_bf16 v[68:83], v[204:207], v[180:183], v[68:83]\n\t" \
  "v_rcp_f32 v196, v196\n\t" \
  "v_rcp_f32 v197, v197\n\t" \
  "v_rcp_f32 v198, v198\n\t" \
  "v_rcp_f32 v199, v199\n\t" \
  "v_mad_u32_u16 v223, v223, %[c128], v249\n\t" \
  "v_or_b32 v226, v226, v250\n\t" \
  "v_mad_u32_u16 v227, v227, 6, v251\n\t" \
  "s_waitcnt lgkmcnt(9)\n\t" \
  "v_mfma_f32_16x16x32_bf16 v[84:87], v[200:203], v[176:179], v[84:87]\n\t" \
  "v_mul_f32 v192, 0xbfb8aa3b, v116\n\t" \
  "v_mul_f32 v193, 0xbfb8aa3b, v117\n\t" \
  "v_mul_f32 v194, 0xbfb8aa3b, v118\n\t" \
  "v_mul_f32 v195, 0xbfb8aa3b, v119\n\t" \
  "v_exp_f32 v192, v192\n\t" \
  "v_exp_f32 v193, v193\n\t" \
  "v_exp_f32 v194, v194\n\t" \
  "v_exp_f32 v195, v195\n\t" \
  "s_waitcnt lgkmcnt(8)\n\t" \
  "v_mfma_f32_16x16x32_bf16 v[84:87], v[208:211], v[180:183], v[84:87]\n\t" \
  "v_mul_f32 v196, v112, v196\n\t" \
  "v_mul_f32 v197, v113, v197\n\t" \
  "v_mul_f32 v198, v114, v198\n\t" \
  "v_mul_f32 v199, v115, v199\n\t" \
  "s_waitcnt lgkmcnt(1)\n\t" \
  "s_cmp_lt_i32 %[nt], 1\n\t" \
  "s_cbranch_scc1 4f\n\t" \
  "s_mov_b32 m0, %[b2]\n\t" \
  "s_nop 0\n\t" \
  "global_load_lds_dwordx4 v226, %[ea]\n\t" \
  "global_load_lds_dword v227, %[pos] offset:1024\n\t" \
  "s_mov_b32 m0, %[b1]\n\t" \
  "s_nop 0\n\t" \
  "global_load_lds_dwordx4 v220, %[h0]\n\t" \
  "global_load_lds_dwordx4 v221, %[h1] offset:1024\n\t" \
  "global_load_lds_dwordx4 v222, %[h2] offset:2048\n\t" \
  "global_load_lds_dwordx4 v223, %[h3] offset:3072\n\t" \
  "4:\n\t" \
  "v_add_f32 v192, 1.0, v192\n\t" \
  "v_add_f32 v193, 1.0, v193\n\t" \
  "v_add_f32 v194, 1.0, v194\n\t" \
  "v_add_f32 v195, 1.0, v195\n\t" \
  "v_cvt_pk_bf16_f32 v190, v196, v197\n\t" \
  "v_cvt_pk_bf16_f32 v191, v198, v199\n\t" \
  "v_lshrrev_b32 v232, 20, v232\n\t" \
  "v_add_u32 v220, %[posd], v232\n\t" \
  "v_add_u32 v221, v240, v232\n\t" \
  "ds_write_b128 v243, v[188:191] offset:16\n\t" \
  "ds_read_b128 v[176:179], v242 offset:5376\n\t" \
  "ds_read_b128 v[180:183], v242 offset:5408\n\t" \
  "ds_write_b16 v233, v255 offset:5376\n\t" \
  "ds_read_b128 v[216:219], v220\n\t" \
  "ds_read_b128 v[172:175], v221 offset:32\n\t" \
  "ds_read_b128 v[168:171], v221\n\t" \
  "ds_read_b64_tr_b16 v[200:201], v244 offset:0\n\t" \
  "ds_read_b64_tr_b16 v[202:203], v244 offset:320\n\t" \
  "ds_read_b64_tr_b16 v[204:205], v244 offset:1280\n\t" \
  "ds_read_b64_tr_b16 v[206:207], v244 offset:1600\n\t" \
  "v_add_u32 v233, v241, v232\n\t" \
  "ds_write_b16_d16_hi v233, v255 offset:5376\n\t" \
  "v_mfma_f32_16x16x32_bf16 v[132:135], v[96:99], v[184:187], 0\n\t" \
  "v_rcp_f32 v192, v192\n\t" \
  "v_rcp_f32 v193, v193\n\t" \
  "v_rcp_f32 v194, v194\n\t" \
  "v_rcp_f32 v195, v195\n\t" \
  "v_mfma_f32_32x32x16_bf16 v[100:115], v[4:7], v[148:151], 0\n\t" \
  "v_mul_f32 v196, 0xbfb8aa3b, v120\n\t" \
  "v_mul_f32 v197, 0xbfb8aa3b, v121\n\t" \
  "v_mul_f32 v198, 0xbfb8aa3b, v122\n\t" \
  "v_mul_f32 v199, 0xbfb8aa3b, v123\n\t" \
  "v_exp_f32 v196, v196\n\t" \
  "v_exp_f32 v197, v197\n\t" \
  "v_exp_f32 v198, v198\n\t" \
  "v_exp_f32 v199, v199\n\t" \
  "v_mfma_f32_32x32x16_bf16 v[100:115], v[12:15], v[152:155], v[100:115]\n\t" \
  "v_mul_f32 v192, v116, v192\n\t" \
  "v_mul_f32 v193, v117, v193\n\t" \
  "v_mul_f32 v194, v118, v194\n\t" \
  "v_mul_f32 v195, v119, v195\n\t" \
  "v_add_f32 v196, 1.0, v196\n\t" \
  "v_add_f32 v197, 1.0, v197\n\t" \
  "v_add_f32 v198, 1.0, v198\n\t" \
  "v_add_f32 v199, 1.0, v199\n\t" \
  "v_cvt_pk_bf16_f32 v184, v192, v193\n\t" \
  "v_cvt_pk_bf16_f32 v185, v194, v195\n\t" \
  "v_and_b32 v225, 0xffff0000, v225\n\t" \
  "s_waitcnt lgkmcnt(7)\n\t" \
  "v_lshlrev_b32 v219, 16, v224\n\t" \
  "v_and_b32 v224, 0xffff0000, v224\n\t" \
  "v_sub_f32 v229, v218, v225\n\t" \
  "v_sub_f32 v216, v216, v219\n\t" \
  "v_sub_f32 v217, v217, v224\n\t" \
  "v_mfma_f32_32x32x16_bf16 v[100:115], v[20:23], v[156:159], v[100:115]\n\t" \
  "v_rcp_f32 v196, v196\n\t" \
  "v_rcp_f32 v197, v197\n\t" \
  "v_rcp_f32 v198, v198\n\t" \
  "v_rcp_f32 v199, v199\n\t" \
  "v_mul_f32 v218, v229, v229\n\t" \
  "v_cndmask_b32 v228, v216, v217, vcc\n\t" \
  "v_fmac_f32 v218, v216, v216\n\t" \
  "v_fmac_f32 v218, v217, v217\n\t" \
  "v_cvt_pk_bf16_f32 v218, v218, v218\n\t" \
  "s_waitcnt lgkmcnt(6)\n\t" \
  "v_cndmask_b32_sdwa v175, v175, v218, vcc dst_sel:WORD_1 dst_unused:UNUSED_PRESERVE src0_sel:WORD_1 src1_sel:WORD_1\n\t" \
  "v_mfma_f32_16x16x32_bf16 v[132:135], v[136:139], v[188:191], v[132:135]\n\t" \
  "v_mul_f32 v192, 0xbfb8aa3b, v124\n\t" \
  "v_mul_f32 v193, 0xbfb8aa3b, v125\n\t" \
  "v_mul_f32 v194, 0xbfb8aa3b, v126\n\t" \
  "v_mul_f32 v195, 0xbfb8aa3b, v127\n\t" \
  "v_exp_f32 v192, v192\n\t" \
  "v_exp_f32 v193, v193\n\t" \
  "v_exp_f32 v194, v194\n\t" \
  "v_exp_f32 v195, v195\n\t" \
  "ds_read2_b32 v[220:221], v252 offset0:0 offset1:2\n\t" \
  "ds_read2_b32 v[222:223], v252 offset0:4 offset1:6\n\t" \
  "ds_read_b64 v[226:227], v253\n\t" \
  "ds_read_b32 v232, v254\n\t" \
  "v_mfma_f32_32x32x16_bf16 v[100:115], v[28:31], v[160:163], v[100:115]\n\t" \
  "v_mul_f32 v196, v120, v196\n\t" \
  "v_mul_f32 v197, v121, v197\n\t" \
  "v_mul_f32 v198, v122, v198\n\t" \
  "v_mul_f32 v199, v123, v199\n\t" \
  "v_add_u32 v252, 0x100, v252\n\t" \
  "v_add_u32 v253, 0x100, v253\n\t" \
  "v_add_u32 v254, 0x100, v254\n\t" \
  "v_add_f32 v192, 1.0, v192\n\t" \
  "v_add_f32 v193, 1.0, v193\n\t" \
  "v_add_f32 v194, 1.0, v194\n\t" \
  "v_add_f32 v195, 1.0, v195\n\t" \
  "v_cvt_pk_bf16_f32 v186, v196, v197\n\t" \
  "v_cvt_pk_bf16_f32 v187, v198, v199\n\t" \
  "s_waitcnt lgkmcnt(9)\n\t" \
  "v_mfma_f32_32x32x16_bf16 v[100:115], v[36:39], v[168:171], v[100:115]\n\t" \
  "v_rcp_f32 v192, v192\n\t" \
  "v_rcp_f32 v193, v193\n\t" \
  "v_rcp_f32 v194, v194\n\t" \
  "v_rcp_f32 v195, v195\n\t" \
  "v_mfma_f32_32x32x16_bf16 v[100:115], v[44:47], v[172:175], v[100:115]\n\t" \
  "v_mul_f32 v196, 0xbfb8aa3b, v128\n\t" \
  "v_mul_f32 v197, 0xbfb8aa3b, v129\n\t" \
  "v_mul_f32 v198, 0xbfb8aa3b, v130\n\t" \
  "v_mul_f32 v199, 0xbfb8aa3b, v131\n\t" \
  "v_exp_f32 v196, v196\n\t" \
  "v_exp_f32 v197, v197\n\t" \
  "v_exp_f32 v198, v198\n\t" \
  "v_exp_f32 v199, v199\n\t" \
  "v_mfma_f32_32x32x16_bf16 v[100:115], v[88:91], v[164:167], v[100:115]\n\t" \
  "v_mul_f32 v192, v124, v192\n\t" \
  "v_mul_f32 v193, v125, v193\n\t" \
  "v_mul_f32 v194, v126, v194\n\t" \
  "v_mul_f32 v195, v127, v195\n\t" \
  "ds_write_b128 v243, v[184:187] offset:0\n\t" \
  "v_add_f32 v196, 1.0, v196\n\t" \
  "v_add_f32 v197, 1.0, v197\n\t" \
  "v_add_f32 v198, 1.0, v198\n\t" \
  "v_add_f32 v199, 1.0, v199\n\t" \
  "v_cvt_pk_bf16_f32 v188, v192, v193\n\t" \
  "v_cvt_pk_bf16_f32 v189, v194, v195\n\t" \
  "s_waitcnt lgkmcnt(8)\n\t" \
  "v_mfma_f32_32x32x16_bf16 v[52:67], v[200:203], v[176:179], v[52:67]\n\t" \
  "v_rcp_f32 v196, v196\n\t" \
  "v_rcp_f32 v197, v197\n\t" \
  "v_rcp_f32 v198, v198\n\t" \
  "v_rcp_f32 v199, v199\n\t" \
  "s_waitcnt lgkmcnt(6)\n\t" \
  "v_mfma_f32_32x32x16_bf16 v[52:67], v[204:207], v[180:183], v[52:67]\n\t" \
  "v_mul_f32 v192, 0xbfb8aa3b, v100\n\t" \
  "v_mul_f32 v193, 0xbfb8aa3b, v101\n\t" \
  "v_mul_f32 v194, 0xbfb8aa3b, v102\n\t" \
  "v_mul_f32 v195, 0xbfb8aa3b, v103\n\t" \
  "v_exp_f32 v192, v192\n\t" \
  "v_exp_f32 v193, v193\n\t" \
  "v_exp_f32 v194, v194\n\t" \
  "v_exp_f32 v195, v195\n\t" \
  "s_cmp_eq_u32 %[nt], 0\n\t" \
  "v_mfma_f32_16x16x32_bf16 v[132:135], v[140:143], v[184:187], v[132:135]\n\t" \
  "v_mul_f32 v196, v128, v196\n\t" \
  "v_mul_f32 v197, v129, v197\n\t" \
  "v_mul_f32 v198, v130, v198\n\t" \
  "v_mul_f32 v199, v131, v199\n\t" \
  "v_add_f32 v192, 1.0, v192\n\t" \
  "v_add_f32 v193, 1.0, v193\n\t" \
  "v_add_f32 v194, 1.0, v194\n\t" \
  "v_add_f32 v195, 1.0, v195\n\t" \
  "v_cvt_pk_bf16_f32 v190, v196, v197\n\t" \
  "v_cvt_pk_bf16_f32 v191, v198, v199\n\t" \
  "s_cbranch_scc0 1b\n\t" \
  "s_waitcnt lgkmcnt(0)\n\t" \
  "s_cmp_lg_u32 %[last], 0\n\t" \
  "s_cbranch_scc1 8f\n\t" \
  "ds_write_b128 v243, v[188:191] offset:16\n\t" \
  "ds_read_b64_tr_b16 v[200:201], v244 offset:0\n\t" \
  "ds_read_b64_tr_b16 v[202:203], v244 offset:320\n\t" \
  "ds_read_b64_tr_b16 v[204:205], v244 offset:1280\n\t" \
  "ds_read_b64_tr_b16 v[206:207], v244 offset:1600\n\t" \
  "v_mfma_f32_16x16x32_bf16 v[132:135], v[144:147], v[188:191], v[132:135]\n\t" \
  "s_waitcnt lgkmcnt(0)\n\t" \
  "v_mfma_f32_32x32x16_bf16 v[68:83], v[200:203], v[176:179], v[68:83]\n\t" \
  "v_mfma_f32_32x32x16_bf16 v[68:83], v[204:207], v[180:183], v[68:83]\n\t" \
  "s_nop 8\n\t" \
  "v_mul_f32 v230, v230, v132\n\t" \
  "v_mul_f32 v231, v231, v132\n\t" \
  "v_cvt_pk_bf16_f32 v230, v230, v231\n\t" \
  "ds_write_b16 v245, v230 offset:7456\n\t" \
  "ds_write_b16_d16_hi v241, v230 offset:7584\n\t" \
  "ds_read_b128 v[200:203], v246 offset:7456\n\t" \
  "ds_read_b128 v[208:211], v246 offset:7488\n\t" \
  "s_waitcnt lgkmcnt(0)\n\t" \
  "v_mfma_f32_16x16x32_bf16 v[84:87], v[200:203], v[176:179], v[84:87]\n\t" \
  "v_mfma_f32_16x16x32_bf16 v[84:87], v[208:211], v[180:183], v[84:87]\n\t" \
  "s_branch 9f\n\t" \
  "6:\n\t" \
  "s_cmp_lg_u32 %[last], 0\n\t" \
  "s_cbranch_scc0 9f\n\t" \
  "s_waitcnt lgkmcnt(0)\n\t" \
  "v_cndmask_b32 v224, 0, 24, vcc\n\t" \
  "v_xor_b32 v220, v234, v224\n\t" \
  "v_xor_b32 v221, v235, v224\n\t" \
  "v_xor_b32 v222, v236, v224\n\t" \
  "v_xor_b32 v223, v237, v224\n\t" \
  "v_xor_b32 v216, 16, v220\n\t" \
  "v_xor_b32 v217, 16, v221\n\t" \
  "v_xor_b32 v218, 16, v222\n\t" \
  "v_xor_b32 v219, 16, v223\n\t" \
  "ds_read_b64 v[148:149], v220\n\t" \
  "ds_read_b64 v[150:151], v216\n\t" \
  "ds_read_b64 v[152:153], v221\n\t" \
  "ds_read_b64 v[154:155], v217\n\t" \
  "ds_read_b64 v[156:157], v222\n\t" \
  "ds_read_b64 v[158:159], v218\n\t" \
  "ds_read_b64 v[160:161], v223\n\t" \
  "ds_read_b64 v[162:163], v219\n\t" \
  "ds_read_b128 v[4:7], v247 offset:6144\n\t" \
  "ds_read_b128 v[8:11], v247 offset:7168\n\t" \
  "ds_read_b128 v[12:15], v247 offset:8192\n\t" \
  "ds_read_b128 v[16:19], v247 offset:9216\n\t" \
  "ds_read_b128 v[20:23], v247 offset:10240\n\t" \
  "ds_read_b128 v[24:27], v247 offset:11264\n\t" \
  "v_subrev_u32 v225, %[b1], v241\n\t" \
  "v_lshl_add_u32 v225, v225, 5, v243\n\t" \
  "v_sub_u32 v225, v225, v224\n\t" \
  "s_nop 4\n\t" \
  "v_cvt_pk_bf16_f32 v184, v52, v53\n\t" \
  "v_cvt_pk_bf16_f32 v185, v54, v55\n\t" \
  "v_cvt_pk_bf16_f32 v186, v56, v57\n\t" \
  "v_cvt_pk_bf16_f32 v187, v58, v59\n\t" \
  "v_cvt_pk_bf16_f32 v188, v60, v61\n\t" \
  "v_cvt_pk_bf16_f32 v189, v62, v63\n\t" \
  "v_cvt_pk_bf16_f32 v190, v64, v65\n\t" \
  "v_cvt_pk_bf16_f32 v191, v66, v67\n\t" \
  "v_cvt_pk_bf16_f32 v36, v68, v69\n\t" \
  "v_cvt_pk_bf16_f32 v37, v70, v71\n\t" \
  "v_cvt_pk_bf16_f32 v38, v72, v73\n\t" \
  "v_cvt_pk_bf16_f32 v39, v74, v75\n\t" \
  "v_cvt_pk_bf16_f32 v40, v76, v77\n\t" \
  "v_cvt_pk_bf16_f32 v41, v78, v79\n\t" \
  "v_cvt_pk_bf16_f32 v42, v80, v81\n\t" \
  "v_cvt_pk_bf16_f32 v43, v82, v83\n\t" \
  "s_waitcnt lgkmcnt(5)\n\t" \
  "v_mfma_f32_32x32x16_bf16 v[100:115], v[4:7], v[148:151], 0\n\t" \
  "s_waitcnt lgkmcnt(4)\n\t" \
  "v_mfma_f32_32x32x16_bf16 v[116:131], v[8:11], v[148:151], 0\n\t" \
  "ds_read_b128 v[28:31], v247 offset:12288\n\t" \
  "ds_read_b128 v[32:35], v247 offset:13312\n\t" \
  "s_waitcnt lgkmcnt(5)\n\t" \
  "v_mfma_f32_32x32x16_bf16 v[100:115], v[12:15], v[152:155], v[100:115]\n\t" \
  "s_waitcnt lgkmcnt(4)\n\t" \
  "v_mfma_f32_32x32x16_bf16 v[116:131], v[16:19], v[152:155], v[116:131]\n\t" \
  "ds_read_b128 v[4:7], v247 offset:14336\n\t" \
  "ds_read_b128 v[8:11], v247 offset:15360\n\t" \
  "s_waitcnt lgkmcnt(5)\n\t" \
  "v_mfma_f32_32x32x16_bf16 v[100:115], v[20:23], v[156:159], v[100:115]\n\t" \
  "s_waitcnt lgkmcnt(4)\n\t" \
  "v_mfma_f32_32x32x16_bf16 v[116:131], v[24:27], v[156:159], v[116:131]\n\t" \
  "ds_read_b128 v[12:15], v247 offset:16384\n\t" \
  "ds_read_b128 v[16:19], v247 offset:17408\n\t" \
  "s_waitcnt lgkmcnt(5)\n\t" \
  "v_mfma_f32_32x32x16_bf16 v[100:115], v[28:31], v[160:163], v[100:115]\n\t" \
  "s_waitcnt lgkmcnt(4)\n\t" \
  "v_mfma_f32_32x32x16_bf16 v[116:131], v[32:35], v[160:163], v[116:131]\n\t" \
  "ds_read_b128 v[20:23], v247 offset:18432\n\t" \
  "ds_read_b128 v[24:27], v247 offset:19456\n\t" \
  "s_waitcnt lgkmcnt(5)\n\t" \
  "v_mfma_f32_32x32x16_bf16 v[100:115], v[4:7], v[184:187], v[100:115]\n\t" \
  "ds_read_b128 v[28:31], v247 offset:20480\n\t" \
  "ds_read_b128 v[32:35], v247 offset:21504\n\t" \
  "s_waitcnt lgkmcnt(5)\n\t" \
  "v_mfma_f32_32x32x16_bf16 v[100:115], v[12:15], v[188:191], v[100:115]\n\t" \
  "s_waitcnt lgkmcnt(3)\n\t" \
  "v_mfma_f32_32x32x16_bf16 v[100:115], v[20:23], v[36:39], v[100:115]\n\t" \
  "s_waitcnt lgkmcnt(1)\n\t" \
  "v_mfma_f32_32x32x16_bf16 v[100:115], v[28:31], v[40:43], v[100:115]\n\t" \
  "s_waitcnt lgkmcnt(0)\n\t" \
  "s_branch 10f\n\t" \
  "8:\n\t" \
  "ds_write_b128 v243, v[188:191] offset:16\n\t" \
  "ds_read_b64_tr_b16 v[200:201], v244 offset:0\n\t" \
  "ds_read_b64_tr_b16 v[202:203], v244 offset:320\n\t" \
  "ds_read_b64_tr_b16 v[204:205], v244 offset:1280\n\t" \
  "ds_read_b64_tr_b16 v[206:207], v244 offset:1600\n\t" \
  "v_mfma_f32_16x16x32_bf16 v[132:135], v[144:147], v[188:191], v[132:135]\n\t" \
  "v_cndmask_b32 v224, 0, 24, vcc\n\t" \
  "v_xor_b32 v220, v234, v224\n\t" \
  "v_xor_b32 v221, v235, v224\n\t" \
  "v_xor_b32 v222, v236, v224\n\t" \
  "v_xor_b32 v223, v237, v224\n\t" \
  "v_xor_b32 v216, 16, v220\n\t" \
  "v_xor_b32 v217, 16, v221\n\t" \
  "v_xor_b32 v218, 16, v222\n\t" \
  "v_xor_b32 v219, 16, v223\n\t" \
  "ds_read_b64 v[148:149], v220\n\t" \
  "ds_read_b64 v[150:151], v216\n\t" \
  "ds_read_b64 v[152:153], v221\n\t" \
  "ds_read_b64 v[154:155], v217\n\t" \
  "ds_read_b64 v[156:157], v222\n\t" \
  "ds_read_b64 v[158:159], v218\n\t" \
  "ds_read_b64 v[160:161], v223\n\t" \
  "ds_read_b64 v[162:163], v219\n\t" \
  "ds_read_b128 v[4:7], v247 offset:6144\n\t" \
  "ds_read_b128 v[8:11], v247 offset:7168\n\t" \
  "s_waitcnt lgkmcnt(11)\n\t" \
  "ds_read_b128 v[12:15], v247 offset:8192\n\t" \
  "ds_read_b128 v[16:19], v247 offset:9216\n\t" \
  "ds_read_b128 v[20:23], v247 offset:10240\n\t" \
  "ds_read_b128 v[24:27], v247 offset:11264\n\t" \
  "v_subrev_u32 v225, %[b1], v241\n\t" \
  "v_lshl_add_u32 v225, v225, 5, v243\n\t" \
  "v_sub_u32 v225, v225, v224\n\t" \
  "v_mfma_f32_32x32x16_bf16 v[68:83], v[200:203], v[176:179], v[68:83]\n\t" \
  "s_waitcnt lgkmcnt(5)\n\t" \
  "v_mfma_f32_32x32x16_bf16 v[100:115], v[4:7], v[148:151], 0\n\t" \
  "v_mfma_f32_32x32x16_bf16 v[68:83], v[204:207], v[180:183], v[68:83]\n\t" \
  "s_waitcnt lgkmcnt(4)\n\t" \
  "v_mfma_f32_32x32x16_bf16 v[116:131], v[8:11], v[148:151], 0\n\t" \
  "ds_read_b128 v[28:31], v247 offset:12288\n\t" \
  "ds_read_b128 v[32:35], v247 offset:13312\n\t" \
  "s_waitcnt lgkmcnt(5)\n\t" \
  "v_mfma_f32_32x32x16_bf16 v[100:115], v[12:15], v[152:155], v[100:115]\n\t" \
  "v_mul_f32 v230, v230, v132\n\t" \
  "v_mul_f32 v231, v231, v132\n\t" \
  "v_cvt_pk_bf16_f32 v230, v230, v231\n\t" \
  "ds_write_b16 v245, v230 offset:7456\n\t" \
  "ds_write_b16_d16_hi v241, v230 offset:7584\n\t" \
  "ds_read_b128 v[200:203], v246 offset:7456\n\t" \
  "ds_read_b128 v[208:211], v246 offset:7488\n\t" \
  "v_cvt_pk_bf16_f32 v184, v52, v53\n\t" \
  "v_cvt_pk_bf16_f32 v185, v54, v55\n\t" \
  "v_cvt_pk_bf16_f32 v186, v56, v57\n\t" \
  "v_cvt_pk_bf16_f32 v187, v58, v59\n\t" \
  "s_waitcnt lgkmcnt(8)\n\t" \
  "v_mfma_f32_32x32x16_bf16 v[116:131], v[16:19], v[152:155], v[116:131]\n\t" \
  "ds_read_b128 v[4:7], v247 offset:14336\n\t" \
  "ds_read_b128 v[8:11], v247 offset:15360\n\t" \
  "s_waitcnt lgkmcnt(9)\n\t" \
  "v_mfma_f32_32x32x16_bf16 v[100:115], v[20:23], v[156:159], v[100:115]\n\t" \
  "v_cvt_pk_bf16_f32 v188, v60, v61\n\t" \
  "v_cvt_pk_bf16_f32 v189, v62, v63\n\t" \
  "v_cvt_pk_bf16_f32 v190, v64, v65\n\t" \
  "v_cvt_pk_bf16_f32 v191, v66, v67\n\t" \
  "s_waitcnt lgkmcnt(8)\n\t" \
  "v_mfma_f32_32x32x16_bf16 v[116:131], v[24:27], v[156:159], v[116:131]\n\t" \
  "ds_read_b128 v[12:15], v247 offset:16384\n\t" \
  "ds_read_b128 v[16:19], v247 offset:17408\n\t" \
  "s_waitcnt lgkmcnt(9)\n\t" \
  "v_mfma_f32_32x32x16_bf16 v[100:115], v[28:31], v[160:163], v[100:115]\n\t" \
  "v_cvt_pk_bf16_f32 v36, v68, v69\n\t" \
  "v_cvt_pk_bf16_f32 v37, v70, v71\n\t" \
  "v_cvt_pk_bf16_f32 v38, v72, v73\n\t" \
  "v_cvt_pk_bf16_f32 v39, v74, v75\n\t" \
  "s_waitcnt lgkmcnt(5)\n\t" \
  "v_mfma_f32_16x16x32_bf16 v[84:87], v[200:203], v[176:179], v[84:87]\n\t" \
  "v_cvt_pk_bf16_f32 v40, v76, v77\n\t" \
  "v_cvt_pk_bf16_f32 v41, v78, v79\n\t" \
  "v_cvt_pk_bf16_f32 v42, v80, v81\n\t" \
  "v_cvt_pk_bf16_f32 v43, v82, v83\n\t" \
  "v_mfma_f32_32x32x16_bf16 v[116:131], v[32:35], v[160:163], v[116:131]\n\t" \
  "ds_read_b128 v[20:23], v247 offset:18432\n\t" \
  "ds_read_b128 v[24:27], v247 offset:19456\n\t" \
  "s_waitcnt lgkmcnt(6)\n\t" \
  "v_mfma_f32_16x16x32_bf16 v[84:87], v[208:211], v[180:183], v[84:87]\n\t" \
  "s_waitcnt lgkmcnt(5)\n\t" \
  "v_mfma_f32_32x32x16_bf16 v[100:115], v[4:7], v[184:187], v[100:115]\n\t" \
  "ds_read_b128 v[28:31], v247 offset:20480\n\t" \
  "ds_read_b128 v[32:35], v247 offset:21504\n\t" \
  "s_waitcnt lgkmcnt(5)\n\t" \
  "v_mfma_f32_32x32x16_bf16 v[100:115], v[12:15], v[188:191], v[100:115]\n\t" \
  "s_waitcnt lgkmcnt(3)\n\t" \
  "v_mfma_f32_32x32x16_bf16 v[100:115], v[20:23], v[36:39], v[100:115]\n\t" \
  "s_waitcnt lgkmcnt(1)\n\t" \
  "v_mfma_f32_32x32x16_bf16 v[100:115], v[28:31], v[40:43], v[100:115]\n\t" \
  "s_waitcnt lgkmcnt(0)\n\t" \
  "10:\n\t" \
  "v_mov_b32 v212, 0xbfb8aa3b\n\t" \
  "v_mfma_f32_32x32x16_bf16 v[116:131], v[8:11], v[184:187], v[116:131]\n\t" \
  "v_mfma_f32_32x32x16_bf16 v[116:131], v[16:19], v[188:191], v[116:131]\n\t" \
  "s_nop 8\n\t" \
  "v_mul_f32 v192, 0xbfb8aa3b, v100\n\t" \
  "v_mul_f32 v193, 0xbfb8aa3b, v101\n\t" \
  "v_mul_f32 v194, 0xbfb8aa3b, v102\n\t" \
  "v_mul_f32 v195, 0xbfb8aa3b, v103\n\t" \
  "v_exp_f32 v192, v192\n\t" \
  "v_exp_f32 v193, v193\n\t" \
  "v_exp_f32 v194, v194\n\t" \
  "v_exp_f32 v195, v195\n\t" \
  "v_lshlrev_b32 v200, 16, v148\n\t" \
  "v_and_b32 v201, 0xffff0000, v148\n\t" \
  "v_lshlrev_b32 v202, 16, v149\n\t" \
  "v_and_b32 v203, 0xffff0000, v149\n\t" \
  "v_mfma_f32_32x32x16_bf16 v[116:131], v[24:27], v[36:39], v[116:131]\n\t" \
  "v_add_f32 v192, 1.0, v192\n\t" \
  "v_add_f32 v193, 1.0, v193\n\t" \
  "v_add_f32 v194, 1.0, v194\n\t" \
  "v_add_f32 v195, 1.0, v195\n\t" \
  "v_rcp_f32 v192, v192\n\t" \
  "v_rcp_f32 v193, v193\n\t" \
  "v_rcp_f32 v194, v194\n\t" \
  "v_rcp_f32 v195, v195\n\t" \
  "v_mfma_f32_32x32x16_bf16 v[116:131], v[32:35], v[40:43], v[116:131]\n\t" \
  "v_mul_f32 v196, 0xbfb8aa3b, v104\n\t" \
  "v_mul_f32 v197, 0xbfb8aa3b, v105\n\t" \
  "v_mul_f32 v198, 0xbfb8aa3b, v106\n\t" \
  "v_mul_f32 v199, 0xbfb8aa3b, v107\n\t" \
  "v_exp_f32 v196, v196\n\t" \
  "v_exp_f32 v197, v197\n\t" \
  "v_exp_f32 v198, v198\n\t" \
  "v_exp_f32 v199, v199\n\t" \
  "v_fmac_f32 v200, v100, v192\n\t" \
  "v_fmac_f32 v201, v101, v193\n\t" \
  "v_fmac_f32 v202, v102, v194\n\t" \
  "v_fmac_f32 v203, v103, v195\n\t" \
  "v_lshlrev_b32 v204, 16, v150\n\t" \
  "v_and_b32 v205, 0xffff0000, v150\n\t" \
  "v_lshlrev_b32 v206, 16, v151\n\t" \
  "v_and_b32 v207, 0xffff0000, v151\n\t" \
  "v_add_f32 v196, 1.0, v196\n\t" \
  "v_add_f32 v197, 1.0, v197\n\t" \
  "v_add_f32 v198, 1.0, v198\n\t" \
  "v_add_f32 v199, 1.0, v199\n\t" \
  "v_cvt_pk_bf16_f32 v176, v200, v201\n\t" \
  "v_cvt_pk_bf16_f32 v177, v202, v203\n\t" \
  "ds_write_b64 v225, v[176:177] offset:0\n\t" \
  "v_rcp_f32 v196, v196\n\t" \
  "v_rcp_f32 v197, v197\n\t" \
  "v_rcp_f32 v198, v198\n\t" \
  "v_rcp_f32 v199, v199\n\t" \
  "v_mul_f32 v192, 0xbfb8aa3b, v108\n\t" \
  "v_mul_f32 v193, 0xbfb8aa3b, v109\n\t" \
  "v_mul_f32 v194, 0xbfb8aa3b, v110\n\t" \
  "v_mul_f32 v195, 0xbfb8aa3b, v111\n\t" \
  "v_exp_f32 v192, v192\n\t" \
  "v_exp_f32 v193, v193\n\t" \
  "v_exp_f32 v194, v194\n\t" \
  "v_exp_f32 v195, v195\n\t" \
  "v_fmac_f32 v204, v104, v196\n\t" \
  "v_fmac_f32 v205, v105, v197\n\t" \
  "v_fmac_f32 v206, v106, v198\n\t" \
  "v_fmac_f32 v207, v107, v199\n\t" \
  "v_lshlrev_b32 v200, 16, v152\n\t" \
  "v_and_b32 v201, 0xffff0000, v152\n\t" \
  "v_lshlrev_b32 v202, 16, v153\n\t" \
  "v_and_b32 v203, 0xffff0000, v153\n\t" \
  "v_add_f32 v192, 1.0, v192\n\t" \
  "v_add_f32 v193, 1.0, v193\n\t" \
  "v_add_f32 v194, 1.0, v194\n\t" \
  "v_add_f32 v195, 1.0, v195\n\t" \
  "v_cvt_pk_bf16_f32 v178, v204, v205\n\t" \
  "v_cvt_pk_bf16_f32 v179, v206, v207\n\t" \
  "ds_write_b64 v225, v[178:179] offset:16\n\t" \
  "v_rcp_f32 v192, v192\n\t" \
  "v_rcp_f32 v193, v193\n\t" \
  "v_rcp_f32 v194, v194\n\t" \
  "v_rcp_f32 v195, v195\n\t" \
  "v_mul_f32 v196, 0xbfb8aa3b, v112\n\t" \
  "v_mul_f32 v197, 0xbfb8aa3b, v113\n\t" \
  "v_mul_f32 v198, 0xbfb8aa3b, v114\n\t" \
  "v_mul_f32 v199, 0xbfb8aa3b, v115\n\t" \
  "v_exp_f32 v196, v196\n\t" \
  "v_exp_f32 v197, v197\n\t" \
  "v_exp_f32 v198, v198\n\t" \
  "v_exp_f32 v199, v199\n\t" \
  "v_fmac_f32 v200, v108, v192\n\t" \
  "v_fmac_f32 v201, v109, v193\n\t" \
  "v_fmac_f32 v202, v110, v194\n\t" \
  "v_fmac_f32 v203, v111, v195\n\t" \
  "v_lshlrev_b32 v204, 16, v154\n\t" \
  "v_and_b32 v205, 0xffff0000, v154\n\t" \
  "v_lshlrev_b32 v206, 16, v155\n\t" \
  "v_and_b32 v207, 0xffff0000, v155\n\t" \
  "v_add_f32 v196, 1.0, v196\n\t" \
  "v_add_f32 v197, 1.0, v197\n\t" \
  "v_add_f32 v198, 1.0, v198\n\t" \
  "v_add_f32 v199, 1.0, v199\n\t" \
  "v_cvt_pk_bf16_f32 v180, v200, v201\n\t" \
  "v_cvt_pk_bf16_f32 v181, v202, v203\n\t" \
  "ds_write_b64 v225, v[180:181] offset:32\n\t" \
  "v_rcp_f32 v196, v196\n\t" \
  "v_rcp_f32 v197, v197\n\t" \
  "v_rcp_f32 v198, v198\n\t" \
  "v_rcp_f32 v199, v199\n\t" \
  "v_mul_f32 v192, 0xbfb8aa3b, v116\n\t" \
  "v_mul_f32 v193, 0xbfb8aa3b, v117\n\t" \
  "v_mul_f32 v194, 0xbfb8aa3b, v118\n\t" \
  "v_mul_f32 v195, 0xbfb8aa3b, v119\n\t" \
  "v_exp_f32 v192, v192\n\t" \
  "v_exp_f32 v193, v193\n\t" \
  "v_exp_f32 v194, v194\n\t" \
  "v_exp_f32 v195, v195\n\t" \
  "v_fmac_f32 v204, v112, v196\n\t" \
  "v_fmac_f32 v205, v113, v197\n\t" \
  "v_fmac_f32 v206, v114, v198\n\t" \
  "v_fmac_f32 v207, v115, v199\n\t" \
  "v_lshlrev_b32 v200, 16, v156\n\t" \
  "v_and_b32 v201, 0xffff0000, v156\n\t" \
  "v_lshlrev_b32 v202, 16, v157\n\t" \
  "v_and_b32 v203, 0xffff0000, v157\n\t" \
  "v_add_f32 v192, 1.0, v192\n\t" \
  "v_add_f32 v193, 1.0, v193\n\t" \
  "v_add_f32 v194, 1.0, v194\n\t" \
  "v_add_f32 v195, 1.0, v195\n\t" \
  "v_cvt_pk_bf16_f32 v182, v204, v205\n\t" \
  "v_cvt_pk_bf16_f32 v183, v206, v207\n\t" \
  "ds_write_b64 v225, v[182:183] offset:48\n\t" \
  "v_rcp_f32 v192, v192\n\t" \
  "v_rcp_f32 v193, v193\n\t" \
  "v_rcp_f32 v194, v194\n\t" \
  "v_rcp_f32 v195, v195\n\t" \
  "v_mul_f32 v196, 0xbfb8aa3b, v120\n\t" \
  "v_mul_f32 v197, 0xbfb8aa3b, v121\n\t" \
  "v_mul_f32 v198, 0xbfb8aa3b, v122\n\t" \
  "v_mul_f32 v199, 0xbfb8aa3b, v123\n\t" \
  "v_exp_f32 v196, v196\n\t" \
  "v_exp_f32 v197, v197\n\t" \
  "v_exp_f32 v198, v198\n\t" \
  "v_exp_f32 v199, v199\n\t" \
  "v_fmac_f32 v200, v116, v192\n\t" \
  "v_fmac_f32 v201, v117, v193\n\t" \
  "v_fmac_f32 v202, v118, v194\n\t" \
  "v_fmac_f32 v203, v119, v195\n\t" \
  "v_lshlrev_b32 v204, 16, v158\n\t" \
  "v_and_b32 v205, 0xffff0000, v158\n\t" \
  "v_lshlrev_b32 v206, 16, v159\n\t" \
  "v_and_b32 v207, 0xffff0000, v159\n\t" \
  "v_add_f32 v196, 1.0, v196\n\t" \
  "v_add_f32 v197, 1.0, v197\n\t" \
  "v_add_f32 v198, 1.0, v198\n\t" \
  "v_add_f32 v199, 1.0, v199\n\t" \
  "v_cvt_pk_bf16_f32 v176, v200, v201\n\t" \
  "v_cvt_pk_bf16_f32 v177, v202, v203\n\t" \
  "ds_write_b64 v225, v[176:177] offset:64\n\t" \
  "v_rcp_f32 v196, v196\n\t" \
  "v_rcp_f32 v197, v197\n\t" \
  "v_rcp_f32 v198, v198\n\t" \
  "v_rcp_f32 v199, v199\n\t" \
  "v_mul_f32 v192, 0xbfb8aa3b, v124\n\t" \
  "v_mul_f32 v193, 0xbfb8aa3b, v125\n\t" \
  "v_mul_f32 v194, 0xbfb8aa3b, v126\n\t" \
  "v_mul_f32 v195, 0xbfb8aa3b, v127\n\t" \
  "v_exp_f32 v192, v192\n\t" \
  "v_exp_f32 v193, v193\n\t" \
  "v_exp_f32 v194, v194\n\t" \
  "v_exp_f32 v195, v195\n\t" \
  "v_fmac_f32 v204, v120, v196\n\t" \
  "v_fmac_f32 v205, v121, v197\n\t" \
  "v_fmac_f32 v206, v122, v198\n\t" \
  "v_fmac_f32 v207, v123, v199\n\t" \
  "v_lshlrev_b32 v200, 16, v160\n\t" \
  "v_and_b32 v201, 0xffff0000, v160\n\t" \
  "v_lshlrev_b32 v202, 16, v161\n\t" \
  "v_and_b32 v203, 0xffff0000, v161\n\t" \
  "v_add_f32 v192, 1.0, v192\n\t" \
  "v_add_f32 v193, 1.0, v193\n\t" \
  "v_add_f32 v194, 1.0, v194\n\t" \
  "v_add_f32 v195, 1.0, v195\n\t" \
  "v_cvt_pk_bf16_f32 v178, v204, v205\n\t" \
  "v_cvt_pk_bf16_f32 v179, v206, v207\n\t" \
  "ds_write_b64 v225, v[178:179] offset:80\n\t" \
  "v_rcp_f32 v192, v192\n\t" \
  "v_rcp_f32 v193, v193\n\t" \
  "v_rcp_f32 v194, v194\n\t" \
  "v_rcp_f32 v195, v195\n\t" \
  "v_mul_f32 v196, 0xbfb8aa3b, v128\n\t" \
  "v_mul_f32 v197, 0xbfb8aa3b, v129\n\t" \
  "v_mul_f32 v198, 0xbfb8aa3b, v130\n\t" \
  "v_mul_f32 v199, 0xbfb8aa3b, v131\n\t" \
  "v_exp_f32 v196, v196\n\t" \
  "v_exp_f32 v197, v197\n\t" \
  "v_exp_f32 v198, v198\n\t" \
  "v_exp_f32 v199, v199\n\t" \
  "v_fmac_f32 v200, v124, v192\n\t" \
  "v_fmac_f32 v201, v125, v193\n\t" \
  "v_fmac_f32 v202, v126, v194\n\t" \
  "v_fmac_f32 v203, v127, v195\n\t" \
  "v_lshlrev_b32 v204, 16, v162\n\t" \
  "v_and_b32 v205, 0xffff0000, v162\n\t" \
  "v_lshlrev_b32 v206, 16, v163\n\t" \
  "v_and_b32 v207, 0xffff0000, v163\n\t" \
  "v_add_f32 v196, 1.0, v196\n\t" \
  "v_add_f32 v197, 1.0, v197\n\t" \
  "v_add_f32 v198, 1.0, v198\n\t" \
  "v_add_f32 v199, 1.0, v199\n\t" \
  "v_cvt_pk_bf16_f32 v180, v200, v201\n\t" \
  "v_cvt_pk_bf16_f32 v181, v202, v203\n\t" \
  "ds_write_b64 v225, v[180:181] offset:96\n\t" \
  "v_rcp_f32 v196, v196\n\t" \
  "v_rcp_f32 v197, v197\n\t" \
  "v_rcp_f32 v198, v198\n\t" \
  "v_rcp_f32 v199, v199\n\t" \
  "s_nop 0\n\t" \
  "v_fmac_f32 v204, v128, v196\n\t" \
  "v_fmac_f32 v205, v129, v197\n\t" \
  "v_fmac_f32 v206, v130, v198\n\t" \
  "v_fmac_f32 v207, v131, v199\n\t" \
  "v_cvt_pk_bf16_f32 v182, v204, v205\n\t" \
  "v_cvt_pk_bf16_f32 v183, v206, v207\n\t" \
  "ds_write_b64 v225, v[182:183] offset:112\n\t" \
  "9:\n\t" \
  "ds_write_b16 v233, v255 offset:5376\n\t" \
  "s_mov_b32 m0, %[keep]\n\t" \
  "s_setprio 0\n\t" \
  "s_nop 11\n\t" \
  ""
#define LOOP_ASM LOOP_ASM_FOLD

DI u32x4 tr_frag(unsigned p, int half) {
  const s16x4 lo = __builtin_amdgcn_ds_read_tr16_b64_v4i16((lds_s16x4_p)p);
  const s16x4 hi = __builtin_amdgcn_ds_read_tr16_b64_v4i16((lds_s16x4_p)(p + half));
  return __builtin_bit_cast(u32x4, __builtin_shufflevector(lo, hi, 0, 1, 2, 3, 4, 5, 6, 7));
}
DI u32x4 fold(u32x4 v) {
#pragma unroll
  for (int i = 0; i < 4; i++) v[i] = pk_bf16(bf2f(v[i] & 0xffffu) * -LOG2E, __uint_as_float(v[i] & 0xffff0000u) * -LOG2E);
  return v;
}

__global__ __launch_bounds__(512, 2) void egnn_kernel(const char* __restrict__ hmat, const char* __restrict__ pos,
                                                      const char* __restrict__ ea, const int* __restrict__ src,
                                                      const int* __restrict__ dst, const char* __restrict__ we,
                                                      const char* __restrict__ wx, const char* __restrict__ wh,
                                                      char* __restrict__ ws, uint16_t* __restrict__ out, int N, int E STAMP_ARG) {
  const int w = __builtin_amdgcn_readfirstlane(threadIdx.x >> 6);
  const int bkt = (blockIdx.x & 7) * 32 + (blockIdx.x >> 3);
  const int n0 = bkt * NB;
  STAMP(0);
  unsigned wl = L_WAVE + w * WSZ, mi = L_MI + w * MISZ;
  const auto rs_tab = __builtin_amdgcn_make_buffer_rsrc((void*)(ws + WS_TAB), 0, NBK * NBK * 8, 0x00020000);
  const auto rs_rec = __builtin_amdgcn_make_buffer_rsrc((void*)(ws + WS_REC), 0, NBK * CHS, 0x00020000);
  const auto rs_ea = __builtin_amdgcn_make_buffer_rsrc((void*)ea, 0, E * 32, 0x00020000);
  unsigned tag_lo, tag_hi;
  unsigned wdv0, wdv1;

  {
    const int lane = lane_id(), tid = w * 64 + lane, r = lane & 31, h = lane >> 5;
    const int e0 = bkt * CH;
    int ne = max(E - e0, 0);
    asm("" : "+s"(ne));
    ne = min(ne, CH);
    const auto rs_d = __builtin_amdgcn_make_buffer_rsrc((void*)(dst + e0), 0, ne * 4, 0x00020000);
    const auto rs_s = __builtin_amdgcn_make_buffer_rsrc((void*)(src + e0), 0, ne * 4, 0x00020000);
    unsigned d[7], sv[7];
    u32x2 xa, xb;
    unsigned long long ep;
    asm volatile("s_load_dwordx2 %0, %1, 0x0" : "=s"(ep) : "s"(ws + WS_EPOCH) : "memory");
#pragma unroll
    for (int i = 0; i < 7; i++)
      asm volatile("buffer_load_dword %0, %2, %3, %5 offen nt\n\tbuffer_load_dword %1, %2, %4, %5 offen nt"
                   : "=&v"(d[i]), "=&v"(sv[i]) : "v"(tid * 4), "s"(rs_d), "s"(rs_s), "s"(i * 2048) : "memory");
    LDS(unsigned, L_CNT + tid * 4) = 0;
    asm volatile("s_waitcnt lgkmcnt(0)\n\ts_barrier" : "+s"(ep) :: "memory");
    asm volatile("global_load_ushort %0, %4, %5\n\tglobal_load_ushort %1, %4, %5 offset:64\n\t"
                 "global_load_dwordx2 %2, %6, %7\n\tglobal_load_dwordx2 %3, %6, %7 offset:16"
                 : "=&v"(wdv0), "=&v"(wdv1), "=&v"(xa), "=&v"(xb)
                 : "v"(r * 2), "s"(we + 128 * 128), "v"(h * 8), "s"(wx + ((w + 2) & 3) * 32) : "memory");
    {
      const int j = lane >> 3, jl = (j & 3) * 128 + (lane & 7) * 16, jh = (j >> 2) * 128;
      const char* const h0 = wh + (w < 4 ? 16 * w : 64 + 32 * ((w - 4) >> 1) + 4 * (w & 1)) * 128;
      const char* const e0p = we + (w < 4 ? 64 + 16 * w : 16 * (w - 4)) * 128;
      const unsigned oh = jl + jh * (w < 4 ? 8 : 16), oe = jl + jh * (w < 4 ? 4 : 8);
      unsigned keep;
      asm volatile("s_mov_b32 %0, m0\n\ts_mov_b32 m0, %9\n\ts_nop 0\n\t"
                   "global_load_lds_dwordx4 %1, %4\n\tglobal_load_lds_dwordx4 %1, %5 offset:1024\n\t"
                   "global_load_lds_dwordx4 %2, %6 offset:2048\n\tglobal_load_lds_dwordx4 %2, %7 offset:3072\n\t"
                   "s_add_u32 m0, %9, 0x1000\n\ts_nop 0\n\tglobal_load_lds_dwordx4 %3, %8\n\ts_mov_b32 m0, %0"
                   : "=&s"(keep)
                   : "v"(oh), "v"(oe), "v"(w < 2 ? (lane >> 2) * 128 + (lane & 3) * 16 : 0),
                     "s"(h0), "s"(h0 + (w < 4 ? 4 : 8) * 128 - 1024), "s"(e0p - 2048), "s"(e0p + (w < 4 ? 8 : 4) * 128 - 3072),
                     "s"(we + 129 * 128 + (w & 1) * 64), "s"(wl)
                   : "memory", "scc");
    }
    unsigned ra[5], keep;
    {
      const unsigned cc0 = ((lane & 7) ^ (lane >> 4)) * 16, cc1 = ((lane & 7) ^ (4 + (lane >> 4))) * 16;
      const int nr = n0 + w + (lane & 56);
      ra[0] = min(nr, N - 1) * 128 + cc0; ra[1] = min(nr + 64, N - 1) * 128 + cc1; ra[2] = min(nr + 128, N - 1) * 128 + cc0;
      ra[3] = min(n0 + w + 192, N - 1) * 128 + cc1; ra[4] = mul24(min(n0 + w + min(lane >> 1, 24) * 8, N - 1), 6) + (lane & 1) * 2;
      asm volatile("" : "+v"(ra[0]), "+v"(ra[1]), "+v"(ra[2]), "+v"(ra[3]), "+v"(ra[4]));
    }
    asm volatile("s_waitcnt vmcnt(9)" : "+v"(d[0]), "+v"(d[1]), "+v"(d[2]), "+v"(d[3]), "+v"(d[4]), "+v"(d[5]), "+v"(d[6]),
                 "+v"(sv[0]), "+v"(sv[1]), "+v"(sv[2]), "+v"(sv[3]), "+v"(sv[4]), "+v"(sv[5]), "+v"(sv[6]) :: "memory");
    STAMP(10);
    {
      asm volatile("s_mov_b32 %0, m0\n\t"
                   "s_mov_b32 m0, %8\n\ts_nop 0\n\tglobal_load_lds_dwordx4 %1, %6\n\t"
                   "s_add_u32 m0, %8, 0x400\n\ts_nop 0\n\tglobal_load_lds_dwordx4 %2, %6\n\t"
                   "s_add_u32 m0, %8, 0x800\n\ts_nop 0\n\tglobal_load_lds_dwordx4 %3, %6\n\t"
                   "s_add_u32 m0, %8, 0xc00\n\ts_nop 0\n\tglobal_load_lds_dwordx4 %4, %6\n\t"
                   "s_add_u32 m0, %8, 0x1000\n\ts_nop 0\n\tglobal_load_lds_dword %5, %7\n\ts_mov_b32 m0, %0"
                   : "=&s"(keep)
                   : "v"(ra[0]), "v"(ra[1]), "v"(ra[2]), "v"(ra[3]), "v"(ra[4]), "s"(hmat), "s"(pos), "s"(mi)
                   : "memory", "scc");
    }
    unsigned ent, total;
    auto sort = [&](auto NS_, auto FULL_) __attribute__((always_inline)) {
      constexpr int NS = decltype(NS_)::value;
      constexpr bool FULL = decltype(FULL_)::value;
      unsigned bk[NS], rank[NS];
      u32x2 rec[NS];
#pragma unroll
      for (int i = 0; i < NS; i++) {
        const int el = i * 512 + tid;
        const unsigned b = mul24(d[i], 85599u) >> 24;
        const unsigned dl = d[i] - mul24(b, NB);
        bk[i] = (FULL || el < ne ? b : NBK + lane) * 4;
        rank[i] = __hip_atomic_fetch_add(&LDS(unsigned, L_CNT + bk[i]), 8u, __ATOMIC_RELAXED, __HIP_MEMORY_SCOPE_WORKGROUP);
        rec[i][0] = el;
        rec[i][1] = ((mul24(dl >> 3, 1272u) + dl) << 16) | sv[i] | 0x80000u;
      }
      LDS(unsigned, L_BASE + (NBK + lane) * 4) = 0x40000000u;
      barrier_lds();
      u32x4 cv = LDS(u32x4, L_CNT + lane * 16);
#pragma unroll
      for (int i = 0; i < 4; i++) cv[i] = (cv[i] + 8) & ~15u;
      const unsigned sum = cv[0] + cv[1] + cv[2] + cv[3], incl = wave_scan_incl(sum);
      const unsigned ex = incl - sum + L_REC;
      total = __builtin_amdgcn_readlane(incl, 63);
      const u32x4 bv = {ex, ex + cv[0], ex + cv[0] + cv[1], ex + cv[0] + cv[1] + cv[2]};
      LDS(u32x4, L_BASE + lane * 16) = bv;
      unsigned bs[NS];
#pragma unroll
      for (int i = 0; i < NS; i++) bs[i] = LDS(unsigned, L_BASE + bk[i]);
      ent = ((LDS(unsigned, L_BASE + (tid & 255) * 4) - L_REC) >> 4) | (LDS(unsigned, L_CNT + (tid & 255) * 4) << 9);
      const unsigned lim = L_REC + 8 * SEG + lane * 8;
#pragma unroll
      for (int i = 0; i < NS; i++) LDS(u32x2, FULL ? bs[i] + rank[i] : min(bs[i] + rank[i], lim)) = rec[i];
      barrier_lds();
    };
    if (ne == CH && w != 0) sort(std::integral_constant<int, 6>(), std::true_type());
    else sort(std::integral_constant<int, 7>(), std::false_type());
    LDS(unsigned, L_CNT + tid * 4) = 0;
    STAMP(11);
    tag_lo = (unsigned)(ep + 1) & 0xffu;
    tag_hi = (unsigned)((ep + 1) >> 8);
    {
      const u32x2 g = {ent | (tag_lo << 24), tag_hi};
      __builtin_amdgcn_raw_buffer_store_b64(g, rs_tab, tid < NBK ? bkt * (NBK * 8) + tid * 8 : 0x80000000u, 0, 16);
      const auto rs_out = __builtin_amdgcn_make_buffer_rsrc((void*)(ws + WS_REC + (size_t)bkt * CHS), 0, __builtin_amdgcn_readfirstlane(total), 0x00020000);
#pragma unroll
      for (int i = 0; i < 4; i++) {
        const u32x4 v = LDS(u32x4, L_REC + (i * 512 + tid) * 16);
        const u32x4 o = {v[1], v[3], v[0] | ((v[2] & 0xfffu) << 12) | (tag_lo << 24), tag_hi};
        __builtin_amdgcn_raw_buffer_store_b128(o, rs_out, (i * 512 + tid) * 16, 0, 16);
      }
    }
    STAMP(12);
    asm volatile("s_waitcnt vmcnt(10)" : "+v"(wdv0), "+v"(wdv1), "+v"(xa), "+v"(xb) :: "memory");
    {
      const unsigned tp = wl + (8 * h + ((lane >> 2) & 3)) * 128 + ((lane >> 4) & 1) * 32 + (lane & 3) * 8;
      u32x4 f[2], g[2];
#pragma unroll
      for (int t = 0; t < 2; t++) { f[t] = tr_frag(tp + 64 * t, 512); g[t] = tr_frag(tp + 2048 + 64 * t, 512); }
#pragma unroll
      for (int t = 0; t < 2; t++) {
        if (w < 4) { f[t] = fold(f[t]); g[t] = fold(g[t]); }
        LDS(u32x4, L_WH + (2 * w + t) * 1024 + lane * 16) = f[t];
        LDS(u32x4, wl + 2048 + t * 1024 + lane * 16) = g[t];
      }
      u32x4 x;
      if (w < 2) x = fold(tr_frag(wl + W_STE + (8 * h + ((lane >> 2) & 3)) * 64 + ((lane >> 4) & 1) * 32 + (lane & 3) * 8, 256));
      else {
        const unsigned m = (lane & 3) == 0 && ((lane >> 2) & 1) == ((lane >> 4) & 1) ? ~0u : 0u;
        x = (u32x4){xa[0] & m, xa[1] & m, xb[0] & m, xb[1] & m};
      }
      if (w < 6) LDS(u32x4, L_WJ + w * 1024 + lane * 16) = x;
    }
    if (tid >= 256 && tid < 256 + 26 * 4) {
      const int t = tid - 256, row = t >> 2, sx = (t >> 1) & 1, hh = t & 1;
      const int x = row < DUMMY ? row - 16 * sx - 4 * hh : -1;
      u32x4 v;
      v[0] = x == 0 ? 0x3f80u : x == 1 ? 0x3f800000u : 0u;  v[1] = x == 2 ? 0x3f80u : x == 3 ? 0x3f800000u : 0u;
      v[2] = x == 8 ? 0x3f80u : x == 9 ? 0x3f800000u : 0u;  v[3] = x == 10 ? 0x3f80u : x == 11 ? 0x3f800000u : 0u;
      LDS(u32x4, L_LUT + row * 80 + (t & 3) * 16) = v;
    }
    {
      const u32x4 z4 = {0u, 0u, 0u, 0u};
#pragma unroll
      for (int i = 0; i < 3; i++) { const int o = i * 1024 + lane * 16; if (o < 26 * 80 + 256) LDS(u32x4, wl + W_SI + o) = z4; }
    }
    barrier_lds();
    STAMP(14);
  }

  f32x16 Y0, Y1; f32x4 Yp = {0.f, 0.f, 0.f, 0.f};
#pragma unroll
  for (int i = 0; i < 16; i++) { Y0[i] = 0.f; Y1[i] = 0.f; }
  u32x32 W2; u32x16 PA; u32x8 W3; u32x4 WX0, WX1, WX2, WX3;
  int jbase = 0, part = -1, grp = w, gother = w;

  auto round = [&](bool first) __attribute__((always_inline)) -> bool {
    const int lane = lane_id(), tid = w * 64 + lane, r = lane & 31, h = lane >> 5;
    const unsigned zr = opaque(0);
    if (!first) { LDS(unsigned, L_CNT + tid * 4) = zr; barrier_lds(); }
    LDS(unsigned, L_BASE + lane * 4) = 0x40000000u;
    unsigned tb[4];
    {
      u32x2 t;
      for (int spin = 0;; spin++) {
        asm volatile("buffer_load_dwordx2 %0, %1, %2, %3 offen sc1\n\ts_waitcnt vmcnt(0)"
                     : "=&v"(t) : "v"((w * 32 + r) * (NBK * 8)), "s"(rs_tab), "s"(bkt * 8) : "memory");
        if (__builtin_amdgcn_ballot_w64(t[1] != tag_hi || (t[0] >> 24) != tag_lo) == 0) break;
        if (spin > (1 << 20)) { t[0] = 0; break; }
        __builtin_amdgcn_s_sleep(POLL_SLEEP);
      }
      LDS(unsigned, L_WAVE + w * WSZ + W_STE + r * 4) = t[0];
      const u32x4 t0 = LDS(u32x4, L_WAVE + w * WSZ + W_STE + (lane >> 3) * 16);
#pragma unroll
      for (int p = 0; p < 4; p++) tb[p] = t0[p];
    }
    STAMP(8);
    u32x4 gr[6];
    unsigned ad[6], left[6], eb[6];
    unsigned lmax = 0, nov = 0;
    bool big;
    const int ovmax = part >= 0 ? LG : 2 * LG, cap = 2 * LG + 2 * ovmax;
    {
      const unsigned jj = lane & (LG - 1), c0 = (unsigned)tid / LG * 4, oob = 0x80000000u + zr;
      const unsigned o0 = mul24(c0, CHS) + (jbase / 2 + jj) * 16, eb0 = mul24(c0, CH);
      unsigned off[4], len[4];
#pragma unroll
      for (int p = 0; p < 4; p++) {
        len[p] = (tb[p] >> 12) & 0xfffu;
        if (part >= 0 && part != 4 * p + (w & 3)) len[p] = 0;
        off[p] = o0 + (tb[p] & 0xfffu) * 16 + p * CHS;
        left[p] = len[p] - (jbase + 2 * jj);
        ad[p] = (int)left[p] > 0 ? off[p] : oob;
        eb[p] = eb0 + p * CH;
        asm volatile("buffer_load_dwordx4 %0, %1, %2, 0 offen sc1" : "=v"(gr[p]) : "v"(ad[p]), "s"(rs_rec) : "memory");
        lmax = max(lmax, tb[p] & 0xfff000u);
      }
      asm volatile("" : "+v"(len[0]), "+v"(len[1]), "+v"(len[2]), "+v"(len[3]));
      ad[4] = oob; ad[5] = oob; left[4] = zr; left[5] = zr; eb[4] = zr; eb[5] = zr;
      unsigned ov[4], s1 = 0;
#pragma unroll
      for (int p = 0; p < 4; p++) {
        ov[p] = (unsigned)min(max(((int)len[p] - (jbase + 2 * LG) + 1) >> 1, 0), ovmax);
        const bool sel = jj - nov < ov[p];
        ad[4] = sel ? off[p] + (LG - nov) * 16 : ad[4];
        left[4] = sel ? len[p] - (jbase + 2 * LG) - 2 * (jj - nov) : left[4];
        eb[4] = sel ? eb0 + p * CH : eb[4];
        nov += ov[p];
      }
      asm volatile("buffer_load_dwordx4 %0, %1, %2, 0 offen sc1" : "=v"(gr[4]) : "v"(ad[4]), "s"(rs_rec) : "memory");
      gr[5] = (u32x4){zr, zr, zr, zr};
      big = __builtin_amdgcn_ballot_w64(nov > LG) != 0;
      if (big) {
#pragma unroll
        for (int p = 0; p < 4; p++) {
          const bool sel = jj + LG - s1 < ov[p];
          ad[5] = sel ? off[p] + (2 * LG - s1) * 16 : ad[5];
          left[5] = sel ? len[p] - (jbase + 2 * LG) - 2 * (jj + LG - s1) : left[5];
          eb[5] = sel ? eb0 + p * CH : eb[5];
          s1 += ov[p];
        }
        asm volatile("buffer_load_dwordx4 %0, %1, %2, 0 offen sc1" : "+v"(gr[5]) : "v"(ad[5]), "s"(rs_rec) : "memory");
      }
    }
    unsigned sink = zr;
    STAMP(7);
    u32x4 w1f[8];
    if (first) {
#pragma unroll
      for (int i = 0; i < 8; i++) w1f[i] = LDS(u32x4, L_WAVE + (4 + (i >> 1)) * WSZ + 2048 + (i & 1) * 1024 + lane * 16);
#pragma unroll
      for (int i = 0; i < 8; i++) {
        const u32x4 v = LDS(u32x4, L_WAVE + (i >> 1) * WSZ + 2048 + (i & 1) * 1024 + lane * 16);
#pragma unroll
        for (int j = 0; j < 4; j++) W2[4 * i + j] = v[j];
      }
#pragma unroll
      for (int i = 0; i < 2; i++) {
        const u32x4 v = LDS(u32x4, L_WJ + i * 1024 + lane * 16);
#pragma unroll
        for (int j = 0; j < 4; j++) W3[4 * i + j] = v[j];
      }
      WX0 = LDS(u32x4, L_WJ + 2048 + lane * 16); WX1 = LDS(u32x4, L_WJ + 3072 + lane * 16);
      WX2 = LDS(u32x4, L_WJ + 4096 + lane * 16); WX3 = LDS(u32x4, L_WJ + 5120 + lane * 16);
    }
    STAMP(13);
    if ((lmax >> 12) > (unsigned)(jbase + cap) || nov > 2 * LG) __hip_atomic_fetch_or(&LDS(unsigned, L_FLAG), nov > 2 * LG ? 3u : 1u, __ATOMIC_RELAXED, __HIP_MEMORY_SCOPE_WORKGROUP);
    unsigned mk1 = (lane & 7) >= 1 ? ~zr : zr, mk2 = (lane & 7) >= 2 ? ~zr : zr, sg0 = L_REC + (lane >> 3) * SEG;
    unsigned lim = L_REC + 8 * SEG + lane * 8, own = n0 + min(8 * (r & 3) + (r >> 2), 24) * 8;
    unsigned ce = ((lane & 1) ^ ((lane >> 4) & 1)) * 16, cp = (lane & 1) * 2;
    unsigned cc0 = ((lane & 7) ^ (lane >> 4)) * 16, cc1 = ((lane & 7) ^ (4 + (lane >> 4))) * 16;
    unsigned q_s4 = L_REC + (lane >> 3) * 32 + 4;
    unsigned q_r2 = L_REC + (4 * ((lane >> 1) & 7) + (lane >> 4)) * 8;
    unsigned q_r1 = L_REC + (4 * (r & 7) + (r >> 3)) * 8 + 4;
    asm volatile("" : "+v"(mk1), "+v"(mk2), "+v"(sg0), "+v"(lim), "+v"(own), "+v"(ce), "+v"(cp), "+v"(cc0), "+v"(cc1), "+v"(q_s4),
                 "+v"(q_r2), "+v"(q_r1));
    asm volatile("s_waitcnt vmcnt(0)" : "+v"(gr[0]), "+v"(gr[1]), "+v"(gr[2]), "+v"(gr[3]), "+v"(gr[4]), "+v"(gr[5]) :: "memory");
    for (int spin = 0;; spin++) {
      bool bad = false;
#pragma unroll
      for (int p = 0; p < 6; p++) bad |= ((int)left[p] > 0) & ((gr[p][3] != tag_hi) | ((gr[p][2] >> 24) != tag_lo));
      if (__builtin_amdgcn_ballot_w64(bad) == 0 || spin > (1 << 20)) break;
      asm volatile("buffer_load_dwordx4 %0, %6, %12, 0 offen sc1\n\tbuffer_load_dwordx4 %1, %7, %12, 0 offen sc1\n\t"
                   "buffer_load_dwordx4 %2, %8, %12, 0 offen sc1\n\tbuffer_load_dwordx4 %3, %9, %12, 0 offen sc1\n\t"
                   "buffer_load_dwordx4 %4, %10, %12, 0 offen sc1\n\tbuffer_load_dwordx4 %5, %11, %12, 0 offen sc1\n\ts_waitcnt vmcnt(0)"
                   : "=&v"(gr[0]), "=&v"(gr[1]), "=&v"(gr[2]), "=&v"(gr[3]), "=&v"(gr[4]), "=&v"(gr[5])
                   : "v"(ad[0]), "v"(ad[1]), "v"(ad[2]), "v"(ad[3]), "v"(ad[4]), "v"(ad[5]), "s"(rs_rec) : "memory");
    }
    STAMP(1);
    u32x2 rc[12];
#pragma unroll
    for (int p = 0; p < 6; p++) {
      rc[2 * p] = (u32x2){(eb[p] + (gr[p][2] & 0xfffu)) << 5, (int)left[p] > 0 ? gr[p][0] : 0u};
      rc[2 * p + 1] = (u32x2){(eb[p] + ((gr[p][2] >> 12) & 0xfffu)) << 5, (int)left[p] > 1 ? gr[p][1] : 0u};
    }
    unsigned rk[12], ky[12];
#pragma unroll
    for (int p = 0; p < 12; p++) {
      if (p == 10 && !big) { ky[10] = ky[11] = 0; rk[10] = rk[11] = 0; break; }
      ky[p] = max((rc[p][1] >> 9) & 0x7fcu, (unsigned)lane * 4);
      rk[p] = __hip_atomic_fetch_add(&LDS(unsigned, L_CNT + ky[p]), 8u + zr, __ATOMIC_RELAXED, __HIP_MEMORY_SCOPE_WORKGROUP);
    }
    if (EPF) {
#pragma unroll
      for (int p = 0; p < 10; p++) {
        const unsigned a = (ky[p] & 0x47cu) - 0x400u < (unsigned)EPF * 4 ? rc[p][0] : 0x80000000u + zr;
        asm volatile("buffer_load_dword %0, %1, %2, 0 offen" : "+{v3}"(sink) : "v"(a), "s"(rs_ea) : "memory");
      }
    }
    barrier_lds();
    STAMP(2);
    unsigned g;
    const unsigned flagv = LDS(unsigned, L_FLAG);
    {
      const u32x4 cv = LDS(u32x4, L_CNT + 1024 + lane * 16);
      const unsigned sum = cv[0] + cv[1] + cv[2] + cv[3];
      g = sum + (__builtin_amdgcn_update_dpp(0, (int)sum, 0x111, 0xf, 0xf, true) & mk1);
      g += __builtin_amdgcn_update_dpp(0, (int)g, 0x112, 0xf, 0xf, true) & mk2;
      g += __builtin_amdgcn_update_dpp(0, (int)g, 0x114, 0xf, 0xa, false);
      const unsigned ex = g - sum + sg0;
      const u32x4 bv = {ex, ex + cv[0], ex + cv[0] + cv[1], ex + cv[0] + cv[1] + cv[2]};
      LDS(u32x4, L_BASE + 1024 + lane * 16) = bv;
    }
    unsigned bs[10];
#pragma unroll
    for (int p = 0; p < 10; p++) bs[p] = LDS(unsigned, L_BASE + ky[p]);
    const unsigned flag = __builtin_amdgcn_readfirstlane(flagv);
    const bool again = flag & 1;
    const bool over = (flag & 2) || __builtin_amdgcn_ballot_w64(g > (unsigned)(SEG - 256)) != 0;
    if (!over) {
#pragma unroll
      for (int p = 0; p < 10; p++) LDS(u32x2, min(bs[p] + rk[p], lim)) = rc[p];
      if (big) {
        LDS(u32x2, min(LDS(unsigned, L_BASE + ky[10]) + rk[10], lim)) = rc[10];
        LDS(u32x2, min(LDS(unsigned, L_BASE + ky[11]) + rk[11], lim)) = rc[11];
      }
    }
    if (first) {
      const unsigned key = (g << 3) | (lane >> 3);
      unsigned ks[8], rank = 0;
#pragma unroll
      for (int k = 0; k < 8; k++) ks[k] = __builtin_amdgcn_readlane(key, 8 * k + 7);
#pragma unroll
      for (int k = 0; k < 8; k++) rank += ks[k] > key;
      const unsigned long long sel = 0x8080808080808080ull;
      grp = __builtin_ctzll(__builtin_amdgcn_ballot_w64(rank == (unsigned)(w < 4 ? w : 11 - w)) & sel) >> 3;
      gother = __builtin_ctzll(__builtin_amdgcn_ballot_w64(rank == (unsigned)(w < 4 ? 7 - w : w - 4)) & sel) >> 3;
      wl = L_WAVE + grp * WSZ; mi = L_MI + grp * MISZ;
    }
    unsigned segn = over ? 0u : (unsigned)__builtin_amdgcn_readlane(g, 8 * grp + 7);
    const unsigned seg0 = grp * SEG;
    const int nt = (segn + 255) >> 8;
    {
      const u32x2 pad = {zr, DUMMY * (80u << 20)}, row = {zr, min(own + grp, (unsigned)N - 1) | (DUMMY * (80u << 20))};
      LDS(u32x2, L_REC + seg0 + segn + r * 8) = pad;
      LDS(u32x2, L_REC + seg0 + nt * 256 + r * 8) = row;
    }
    if (lane == 0) LDS(unsigned, wl + W_SI + DUMMY * 80 + 76) = nt;
    barrier_lds();
    STAMP(3);

    const unsigned o_s4 = q_s4 + seg0, o_r2 = q_r2 + seg0, o_r1 = q_r1 + seg0;
    {
      unsigned ns[4];
#pragma unroll
      for (int i = 0; i < 4; i++) ns[i] = LDS(uint16_t, o_s4 + i * 8);
      const u32x2 nr2 = LDS(u32x2, o_r2);
      dma_tile(hmat, ea, pos, ns[0] * 128 + cc0, ns[1] * 128 + cc1, ns[2] * 128 + cc0, ns[3] * 128 + cc1,
               nr2[0] | ce, (nr2[1] & 0xffffu) * 6 + cp, wl);
    }
    if (first) {
      u32x2 hb[8];
#pragma unroll
      for (int i = 0; i < 8; i++) hb[i] = LDS(u32x2, mi + r * 128 + ((i ^ ((r >> 1) & 7)) * 16) + h * 8);
      const u32x2 pg = LDS(u32x2, mi + 4096 + r * 8);
#pragma unroll
      for (int t = 0; t < 2; t++) {
        f32x16 P;
#pragma unroll
        for (int i = 0; i < 16; i++) P[i] = 0.f;
#pragma unroll
        for (int s = 0; s < 4; s++) {
          const u32x4 a = {hb[2 * s][0], hb[2 * s][1], hb[2 * s + 1][0], hb[2 * s + 1][1]};
          P = MFMA(as_bf(a), as_bf(w1f[2 * s + t]), P);
        }
        if (h) P[15] = bf2f(t ? wdv1 : wdv0);
#pragma unroll
        for (int i = 0; i < 16; i++) P[i] *= -LOG2E;
#pragma unroll
        for (int s = 0; s < 2; s++)
#pragma unroll
          for (int j = 0; j < 4; j++) PA[4 * (2 * s + t) + j] = pk_bf16(P[8 * s + 2 * j], P[8 * s + 2 * j + 1]);
      }
      const f32x4 pv = {bf2f(pg[0] & 0xffffu), __uint_as_float(pg[0] & 0xffff0000u), __uint_as_float(pg[1] & 0xffff0000u), 0.f};
      if (lane < DUMMY) LDS(f32x4, wl + W_SI + lane * 80 + 64) = pv;
      if (bkt == 0 && tid == 0) {
        const u32x2 e1 = {tag_lo | (tag_hi << 8), tag_hi >> 24};
        asm volatile("global_store_dwordx2 %0, %1, %2 sc1" :: "v"(zr), "v"(e1), "s"(ws + WS_EPOCH) : "memory");
      }
    }
    const bool last_round = !over && !again && (part < 0 || part == 15);
    STAMP(4);

    {
      u32x16 A0; u32x8 A1;
      A0[0] = zr; A0[1] = zr;
#pragma unroll
      for (int s = 0; s < 4; s++) A0[2 + s] = wl + W_STH + r * 128 + (((2 * s + h) ^ ((r >> 1) & 7)) * 16);
      A0[6] = wl + r * 32 + ((h ^ ((r >> 3) & 1)) * 16);
      A0[7] = wl + r * 8;
      A0[8] = L_LUT + h * 16;
      A0[9] = wl + r * 2;
      A0[10] = wl + min(r, DUMMY) * 80 + h * 16;
      A0[11] = mi + r * MIL + h * 32;
      A0[12] = mi + (8 * h + ((lane >> 2) & 3)) * MIL + (4 * ((lane >> 4) & 1) + (lane & 3)) * 8;
      A0[13] = wl + h * 64 + r * 2;
      {
        const int row = (lane & 15) - 4 * ((lane >> 4) & 1);
        A0[14] = wl + ((unsigned)row < 3 ? row : 3) * 64 + h * 16;
      }
      A0[15] = L_WJ + lane * 16;
      A1[0] = cc0; A1[1] = cc1; A1[2] = ce; A1[3] = cp;
      A1[4] = o_s4 + 256; A1[5] = o_r2 + 256; A1[6] = o_r1;
      A1[7] = 0x3f800000u + zr;
      unsigned cnt = nt, keep;
      const int lastv = __builtin_amdgcn_readfirstlane(opaque(last_round ? 1 : 0));
      const int thr = w >> 2;
      u32x2 A2 = {wl + W_SI + DUMMY * 80 + 76, (unsigned)(L_WAVE + gother * WSZ + W_SI + DUMMY * 80 + 76)};
      unsigned tmp;
      asm volatile(LOOP_ASM
                   : "+{v[52:67]}"(Y0), "+{v[68:83]}"(Y1), "+{v[84:87]}"(Yp), "+{v[232:247]}"(A0), "+{v[248:255]}"(A1), "+{v3}"(sink),
                     "+{v[212:213]}"(A2), [tmp] "=&s"(tmp),
                     [nt] "+s"(cnt), [keep] "=&s"(keep)
                   : "{v[4:35]}"(W2), "{v[36:51]}"(PA), "{v[88:95]}"(W3), "{v[96:99]}"(WX0), "{v[136:139]}"(WX1), "{v[140:143]}"(WX2), "{v[144:147]}"(WX3),
                     [h0] "s"(hmat), [h1] "s"(hmat - 1024), [h2] "s"(hmat - 2048), [h3] "s"(hmat - 3072), [ea] "s"(ea), [pos] "s"(pos - 1024),
                     [b1] "s"(wl), [b2] "s"(wl + W_STE), [posd] "s"(wl + W_SI + 64), [c128] "s"(128u), [thr] "s"(thr), [last] "s"(lastv),
                     [hsel] "s"(0xffffffff00000000ull)
                   : "memory", "scc", "vcc", CLOBBER_V);
    }
    if (over) part = 0;
    else if (part >= 0 && part < 15) part++;
    else { part = -1; jbase += cap; }
    return last_round;
  };
  if (!round(true)) {
    do {
      wait_vm0();
      barrier_lds();
      jbase = __builtin_amdgcn_readfirstlane(jbase); part = __builtin_amdgcn_readfirstlane(part);
    } while (!round(false));
  }
  STAMP(5);

  {
    const int lane = lane_id(), r = lane & 31, h = lane >> 5;
    Yp[0] *= -LN2; Yp[1] *= -LN2; Yp[2] *= -LN2;
    const auto rs_o = __builtin_amdgcn_make_buffer_rsrc((void*)out, 0, N * 128, 0x00020000);
#pragma unroll
    for (int i = 0; i < 4; i++) {
      const int q = 8 * i + (lane >> 3), dl = q * 8 + grp;
      const u32x4 v = LDS(u32x4, mi + q * MIP + (lane & 7) * 16);
      if (dl < NB) __builtin_amdgcn_raw_buffer_store_b128(v, rs_o, (n0 + dl) * 128 + (lane & 7) * 16, 0, OUT_AUX);
    }
    const int dl = r * 8 + grp;
    if (h == 0 && dl < NB && n0 + dl < N) {
      const f32x4 pd = LDS(f32x4, wl + W_SI + r * 80 + 64);
      uint16_t* po = out + (size_t)N * 64 + (size_t)(n0 + dl) * 3;
      const unsigned xy = pk_bf16(pd[0] + Yp[0], pd[1] + Yp[1]), zz = pk_bf16(pd[2] + Yp[2], 0.f);
      po[0] = (uint16_t)xy; po[1] = (uint16_t)(xy >> 16); po[2] = (uint16_t)zz;
    }
  }
  STAMP(6);
}

}

#define STAMP_PASS1
#define STAMP_PASS2

extern "C" void kernel_launch(void* const* d_in, const int* in_sizes, int n_in, void* d_out, int out_size,
                              void* d_ws, size_t ws_size, hipStream_t stream) {
  const int N = in_sizes[0] / 64, E = in_sizes[3];
  if (N > NBK * NB || E > NBK * CH || N < 1 || E < 1) return;
  (void)hipFuncSetAttribute((const void*)egnn_kernel, hipFuncAttributeMaxDynamicSharedMemorySize, L_TOTAL);
  egnn_kernel<<<NBK, 512, L_TOTAL, stream>>>((const char*)d_in[0], (const char*)d_in[1], (const char*)d_in[2], (const int*)d_in[3],
                                             (const int*)d_in[4], (const char*)d_in[5], (const char*)d_in[6], (const char*)d_in[7],
                                             (char*)d_ws, (uint16_t*)d_out, N, E STAMP_PASS2);
}
```

```cpp
#include <hip/hip_runtime.h>
#include <stdint.h>
#include <type_traits>

#define FOLD_LOG2E 1

namespace {

typedef __bf16 bf16x8 __attribute__((ext_vector_type(8)));
typedef __bf16 bf16x2 __attribute__((ext_vector_type(2)));
typedef float f32x16 __attribute__((ext_vector_type(16)));
typedef float f32x4 __attribute__((ext_vector_type(4)));
typedef unsigned u32x4 __attribute__((ext_vector_type(4)));
typedef unsigned u32x2 __attribute__((ext_vector_type(2)));
typedef unsigned u32x8 __attribute__((ext_vector_type(8)));
typedef unsigned u32x16 __attribute__((ext_vector_type(16)));
typedef unsigned u32x32 __attribute__((ext_vector_type(32)));
typedef short s16x4 __attribute__((ext_vector_type(4)));
typedef __attribute__((address_space(3))) s16x4* lds_s16x4_p;

#define DI __device__ __forceinline__
#define MFMA(a, b, c) __builtin_amdgcn_mfma_f32_32x32x16_bf16((a), (b), (c), 0, 0, 0)

#define POLL_SLEEP 2
#define EPF 2
#define OUT_AUX 16
constexpr int NBK = 256;
constexpr int LG = 8;
constexpr int NB = 196;
constexpr int CH = 3136;
constexpr float LOG2E = 1.4426950408889634f;
constexpr float LN2 = 0.6931471805599453f;

constexpr size_t WS_EPOCH = 0;
constexpr size_t WS_TAB = 256;
constexpr size_t WS_REC = WS_TAB + (size_t)NBK * NBK * 8;
constexpr int CHS = (CH + NBK) * 8;

constexpr int W_STH = 0, W_STE = 4096, W_STP = 5120, W_SI = 5376, W_TI = W_SI + 26 * 80, WSZ = W_TI + 256;
constexpr int DUMMY = 25;
constexpr int MIP = 144, MISZ = 32 * MIP;
constexpr int MIL = 80;
constexpr int SEG = 17 * 256;
constexpr int L_CNT = 0;
constexpr int L_BASE = L_CNT + 2048;
constexpr int L_FLAG = L_CNT + 512;
constexpr int L_REC = L_BASE + 2048;
constexpr int L_LUT = L_REC + 8 * SEG + 512;
constexpr int L_WAVE = L_LUT + 26 * 80;
constexpr int L_MI = L_WAVE + 8 * WSZ;
constexpr int L_WJ = L_MI + 8 * MISZ;
constexpr int L_WH = L_WJ + 6 * 1024;
constexpr int L_TOTAL = L_WH + 16 * 1024;
static_assert(L_TOTAL <= 163840, "LDS");
static_assert(WSZ % 16 == 0 && L_MI % 16 == 0 && L_LUT % 16 == 0, "align");

#pragma clang diagnostic ignored "-Wint-to-pointer-cast"
#define LDS(T, off) (*(__attribute__((address_space(3))) T*)(unsigned)(off))
DI unsigned mul24(unsigned a, unsigned b) { return (unsigned)__umul24(a, b); }
DI float bf2f(unsigned lo16) { return __uint_as_float(lo16 << 16); }
DI unsigned pk_bf16(float a, float b) { bf16x2 v = {(__bf16)a, (__bf16)b}; return __builtin_bit_cast(unsigned, v); }
DI bf16x8 as_bf(u32x4 v) { return __builtin_bit_cast(bf16x8, v); }
DI int opaque(int x) { asm volatile("" : "+v"(x)); return x; }
DI int lane_id() {
  int x; asm volatile("v_mbcnt_lo_u32_b32 %0, -1, 0\n\tv_mbcnt_hi_u32_b32 %0, -1, %0" : "=v"(x)); return x;
}
DI void wait_vm0() { asm volatile("s_waitcnt vmcnt(0)" ::: "memory"); }
DI void barrier_lds() { asm volatile("s_waitcnt lgkmcnt(0)\n\ts_barrier" ::: "memory"); }
DI void dma_tile(const void* hm, const void* ea, const void* ps, unsigned o0, unsigned o1, unsigned o2, unsigned o3,
                 unsigned oe, unsigned op, unsigned l) {
  unsigned keep;
  asm volatile("s_mov_b32 %0, m0\n\t"
               "s_add_u32 m0, %10, 0x1000\n\ts_nop 0\n\tglobal_load_lds_dwordx4 %5, %8\n\t"
               "s_mov_b32 m0, %10\n\ts_nop 0\n\tglobal_load_lds_dwordx4 %1, %7\n\t"
               "s_add_u32 m0, %10, 0x400\n\ts_nop 0\n\tglobal_load_lds_dwordx4 %2, %7\n\t"
               "s_add_u32 m0, %10, 0x800\n\ts_nop 0\n\tglobal_load_lds_dwordx4 %3, %7\n\t"
               "s_add_u32 m0, %10, 0xc00\n\ts_nop 0\n\tglobal_load_lds_dwordx4 %4, %7\n\t"
               "s_add_u32 m0, %10, 0x1400\n\ts_nop 0\n\tglobal_load_lds_dword %6, %9\n\t"
               "s_mov_b32 m0, %0"
               : "=&s"(keep) : "v"(o0), "v"(o1), "v"(o2), "v"(o3), "v"(oe), "v"(op), "s"(hm), "s"(ea), "s"(ps), "s"(l)
               : "memory", "scc");
}
template <int CTRL, int ROWMASK> DI unsigned dpp_add(unsigned x) {
  return x + (unsigned)__builtin_amdgcn_update_dpp(0, (int)x, CTRL, ROWMASK, 0xf, false);
}
DI unsigned wave_scan_incl(unsigned x) {
  x = dpp_add<0x111, 0xf>(x); x = dpp_add<0x112, 0xf>(x); x = dpp_add<0x114, 0xf>(x); x = dpp_add<0x118, 0xf>(x);
  x = dpp_add<0x142, 0xa>(x); x = dpp_add<0x143, 0xc>(x);
  return x;
}
#define STAMP(i)
#define STAMP_ARG

#define CLOBBER_V "v100", "v101", "v102", "v103", "v104", "v105", "v106", "v107", "v108", "v109", "v110", "v111", "v112", "v113", "v114", "v115", "v116", "v117", "v118", "v119", "v120", "v121", "v122", "v123", "v124", "v125", "v126", "v127", "v128", "v129", "v130", "v131", "v132", "v133", "v134", "v135", "v148", "v149", "v150", "v151", "v152", "v153", "v154", "v155", "v156", "v157", "v158", "v159", "v160", "v161", "v162", "v163", "v164", "v165", "v166", "v167", "v168", "v169", "v170", "v171", "v172", "v173", "v174", "v175", "v176", "v177", "v178", "v179", "v180", "v181", "v182", "v183", "v184", "v185", "v186", "v187", "v188", "v189", "v190", "v191", "v192", "v193", "v194", "v195", "v196", "v197", "v198", "v199", "v200", "v201", "v202", "v203", "v204", "v205", "v206", "v207", "v208", "v209", "v210", "v211", "v214", "v215", "v216", "v217", "v218", "v219", "v220", "v221", "v222", "v223", "v224", "v225", "v226", "v227", "v228", "v229", "v230", "v231"
#define LOOP_ASM_FOLD \
  "s_mov_b32 %[keep], m0\n\t" \
  "s_mov_b64 vcc, %[hsel]\n\t" \
  "ds_read2_b32 v[220:221], v252 offset0:0 offset1:2\n\t" \
  "ds_read2_b32 v[222:223], v252 offset0:4 offset1:6\n\t" \
  "ds_read_b64 v[226:227], v253\n\t" \
  "ds_read_b32 v232, v254\n\t" \
  "v_mov_b32 v176, 0\n\t" \
  "v_mov_b32 v177, 0\n\t" \
  "v_mov_b32 v178, 0\n\t" \
  "v_mov_b32 v179, 0\n\t" \
  "v_mov_b32 v180, 0\n\t" \
  "v_mov_b32 v181, 0\n\t" \
  "v_mov_b32 v182, 0\n\t" \
  "v_mov_b32 v183, 0\n\t" \
  "v_mov_b32 v184, 0\n\t" \
  "v_mov_b32 v185, 0\n\t" \
  "v_mov_b32 v186, 0\n\t" \
  "v_mov_b32 v187, 0\n\t" \
  "v_mov_b32 v188, 0\n\t" \
  "v_mov_b32 v189, 0\n\t" \
  "v_mov_b32 v190, 0\n\t" \
  "v_mov_b32 v191, 0\n\t" \
  "v_mov_b32 v230, 0\n\t" \
  "v_mov_b32 v231, 0\n\t" \
  "v_mov_b32 v208, 0\n\t" \
  "v_mov_b32 v209, 0\n\t" \
  "v_mov_b32 v210, 0\n\t" \
  "v_mov_b32 v211, 0\n\t" \
  "ds_write_b128 v243, v[184:187] offset:0\n\t" \
  "s_nop 0\n\t" \
  "v_mfma_f32_16x16x32_bf16 v[132:135], v[184:187], v[188:191], 0\n\t" \
  "v_add_u32 v252, 0x100, v252\n\t" \
  "v_add_u32 v253, 0x100, v253\n\t" \
  "v_add_u32 v254, 0x100, v254\n\t" \
  "s_waitcnt lgkmcnt(0)\n\t" \
  "v_lshrrev_b32 v232, 20, v232\n\t" \
  "v_add_u32 v233, %[posd], v232\n\t" \
  "ds_read_b128 v[216:219], v233\n\t" \
  "v_add_u32 v233, v240, v232\n\t" \
  "ds_read_b128 v[172:175], v233 offset:32\n\t" \
  "ds_read_b128 v[168:171], v233\n\t" \
  "v_add_u32 v233, v241, v232\n\t" \
  "ds_write_b16_d16_hi v233, v255 offset:5376\n\t" \
  "v_mad_u32_u16 v220, v220, %[c128], v248\n\t" \
  "v_mad_u32_u16 v221, v221, %[c128], v249\n\t" \
  "v_mad_u32_u16 v222, v222, %[c128], v248\n\t" \
  "v_mad_u32_u16 v223, v223, %[c128], v249\n\t" \
  "v_or_b32 v226, v226, v250\n\t" \
  "v_mad_u32_u16 v227, v227, 6, v251\n\t" \
  "s_waitcnt vmcnt(0)\n\t" \
  "ds_read_b128 v[148:151], v234\n\t" \
  "ds_read_b128 v[152:155], v235\n\t" \
  "ds_read_b128 v[156:159], v236\n\t" \
  "ds_read_b128 v[160:163], v237\n\t" \
  "ds_read_b128 v[164:167], v238 offset:4096\n\t" \
  "ds_read_b64 v[224:225], v239 offset:5120\n\t" \
  "s_waitcnt lgkmcnt(0)\n\t" \
  "s_cmp_lt_i32 %[nt], 1\n\t" \
  "s_cbranch_scc1 7f\n\t" \
  "s_mov_b32 m0, %[b2]\n\t" \
  "s_nop 0\n\t" \
  "global_load_lds_dwordx4 v226, %[ea]\n\t" \
  "global_load_lds_dword v227, %[pos] offset:1024\n\t" \
  "s_mov_b32 m0, %[b1]\n\t" \
  "s_nop 0\n\t" \
  "global_load_lds_dwordx4 v220, %[h0]\n\t" \
  "global_load_lds_dwordx4 v221, %[h1] offset:1024\n\t" \
  "global_load_lds_dwordx4 v222, %[h2] offset:2048\n\t" \
  "global_load_lds_dwordx4 v223, %[h3] offset:3072\n\t" \
  "7:\n\t" \
  "ds_read2_b32 v[220:221], v252 offset0:0 offset1:2\n\t" \
  "ds_read2_b32 v[222:223], v252 offset0:4 offset1:6\n\t" \
  "ds_read_b64 v[226:227], v253\n\t" \
  "ds_read_b32 v232, v254\n\t" \
  "v_add_u32 v252, 0x100, v252\n\t" \
  "v_add_u32 v253, 0x100, v253\n\t" \
  "v_add_u32 v254, 0x100, v254\n\t" \
  "v_and_b32 v225, 0xffff0000, v225\n\t" \
  "v_lshlrev_b32 v219, 16, v224\n\t" \
  "v_and_b32 v224, 0xffff0000, v224\n\t" \
  "v_sub_f32 v229, v218, v225\n\t" \
  "v_sub_f32 v216, v216, v219\n\t" \
  "v_sub_f32 v217, v217, v224\n\t" \
  "v_mul_f32 v218, v229, v229\n\t" \
  "v_cndmask_b32 v228, v216, v217, vcc\n\t" \
  "v_fmac_f32 v218, v216, v216\n\t" \
  "v_fmac_f32 v218, v217, v217\n\t" \
  "v_cvt_pk_bf16_f32 v218, v218, v218\n\t" \
  "v_cndmask_b32_sdwa v175, v175, v218, vcc dst_sel:WORD_1 dst_unused:UNUSED_PRESERVE src0_sel:WORD_1 src1_sel:WORD_1\n\t" \
  "v_mfma_f32_32x32x16_bf16 v[100:115], v[4:7], v[148:151], 0\n\t" \
  "v_mfma_f32_32x32x16_bf16 v[100:115], v[12:15], v[152:155], v[100:115]\n\t" \
  "v_mfma_f32_32x32x16_bf16 v[100:115], v[20:23], v[156:159], v[100:115]\n\t" \
  "v_mfma_f32_32x32x16_bf16 v[100:115], v[28:31], v[160:163], v[100:115]\n\t" \
  "v_mfma_f32_32x32x16_bf16 v[100:115], v[36:39], v[168:171], v[100:115]\n\t" \
  "v_mfma_f32_32x32x16_bf16 v[100:115], v[88:91], v[164:167], v[100:115]\n\t" \
  "v_mfma_f32_32x32x16_bf16 v[100:115], v[44:47], v[172:175], v[100:115]\n\t" \
  "s_cmp_eq_u32 %[nt], 0\n\t" \
  "s_nop 10\n\t" \
  "v_exp_f32 v192, v100\n\t" \
  "v_exp_f32 v193, v101\n\t" \
  "v_exp_f32 v194, v102\n\t" \
  "v_exp_f32 v195, v103\n\t" \
  "s_nop 0\n\t" \
  "v_add_f32 v192, 1.0, v192\n\t" \
  "v_add_f32 v193, 1.0, v193\n\t" \
  "v_add_f32 v194, 1.0, v194\n\t" \
  "v_add_f32 v195, 1.0, v195\n\t" \
  "s_cbranch_scc1 6f\n\t" \
  "s_cmp_lt_i32 %[thr], 1\n\t" \
  "s_cbranch_scc1 1f\n\t" \
  "s_setprio 1\n\t" \
  "1:\n\t" \
  "ds_write_b128 v243, v[188:191] offset:16\n\t" \
  "ds_read_b32 v214, v213\n\t" \
  "ds_read_b64_tr_b16 v[200:201], v244 offset:0\n\t" \
  "ds_read_b64_tr_b16 v[202:203], v244 offset:320\n\t" \
  "ds_read_b64_tr_b16 v[204:205], v244 offset:1280\n\t" \
  "ds_read_b64_tr_b16 v[206:207], v244 offset:1600\n\t" \
  "v_mfma_f32_16x16x32_bf16 v[132:135], v[144:147], v[188:191], v[132:135]\n\t" \
  "v_rcp_f32 v192, v192\n\t" \
  "v_rcp_f32 v193, v193\n\t" \
  "v_rcp_f32 v194, v194\n\t" \
  "v_rcp_f32 v195, v195\n\t" \
  "v_mfma_f32_32x32x16_bf16 v[116:131], v[8:11], v[148:151], 0\n\t" \
  "v_exp_f32 v196, v104\n\t" \
  "v_exp_f32 v197, v105\n\t" \
  "v_exp_f32 v198, v106\n\t" \
  "v_exp_f32 v199, v107\n\t" \
  "v_mfma_f32_32x32x16_bf16 v[116:131], v[16:19], v[152:155], v[116:131]\n\t" \
  "v_mul_f32 v192, v100, v192\n\t" \
  "v_mul_f32 v193, v101, v193\n\t" \
  "v_mul_f32 v194, v102, v194\n\t" \
  "v_mul_f32 v195, v103, v195\n\t" \
  "v_add_f32 v196, 1.0, v196\n\t" \
  "v_add_f32 v197, 1.0, v197\n\t" \
  "v_add_f32 v198, 1.0, v198\n\t" \
  "v_add_f32 v199, 1.0, v199\n\t" \
  "v_cvt_pk_bf16_f32 v184, v192, v193\n\t" \
  "v_cvt_pk_bf16_f32 v185, v194, v195\n\t" \
  "v_mfma_f32_32x32x16_bf16 v[116:131], v[24:27], v[156:159], v[116:131]\n\t" \
  "v_rcp_f32 v196, v196\n\t" \
  "v_rcp_f32 v197, v197\n\t" \
  "v_rcp_f32 v198, v198\n\t" \
  "v_rcp_f32 v199, v199\n\t" \
  "s_waitcnt lgkmcnt(2)\n\t" \
  "v_mfma_f32_32x32x16_bf16 v[68:83], v[200:203], v[176:179], v[68:83]\n\t" \
  "v_exp_f32 v192, v108\n\t" \
  "v_exp_f32 v193, v109\n\t" \
  "v_exp_f32 v194, v110\n\t" \
  "v_exp_f32 v195, v111\n\t" \
  "v_mfma_f32_32x32x16_bf16 v[116:131], v[32:35], v[160:163], v[116:131]\n\t" \
  "v_mul_f32 v196, v104, v196\n\t" \
  "v_mul_f32 v197, v105, v197\n\t" \
  "v_mul_f32 v198, v106, v198\n\t" \
  "v_mul_f32 v199, v107, v199\n\t" \
  "v_mul_f32 v230, v230, v132\n\t" \
  "v_mul_f32 v231, v231, v132\n\t" \
  "v_cvt_pk_bf16_f32 v230, v230, v231\n\t" \
  "ds_write_b16 v245, v230 offset:7456\n\t" \
  "ds_write_b16_d16_hi v241, v230 offset:7584\n\t" \
  "ds_read_b128 v[200:203], v246 offset:7456\n\t" \
  "ds_read_b128 v[208:211], v246 offset:7488\n\t" \
  "v_mov_b32 v230, v228\n\t" \
  "v_mov_b32 v231, v229\n\t" \
  "v_add_f32 v192, 1.0, v192\n\t" \
  "v_add_f32 v193, 1.0, v193\n\t" \
  "v_add_f32 v194, 1.0, v194\n\t" \
  "v_add_f32 v195, 1.0, v195\n\t" \
  "v_cvt_pk_bf16_f32 v186, v196, v197\n\t" \
  "v_cvt_pk_bf16_f32 v187, v198, v199\n\t" \
  "v_mfma_f32_32x32x16_bf16 v[116:131], v[40:43], v[168:171], v[116:131]\n\t" \
  "v_rcp_f32 v192, v192\n\t" \
  "v_rcp_f32 v193, v193\n\t" \
  "v_rcp_f32 v194, v194\n\t" \
  "v_rcp_f32 v195, v195\n\t" \
  "s_sub_u32 %[nt], %[nt], 1\n\t" \
  "v_mov_b32 v215, %[nt]\n\t" \
  "v_readfirstlane_b32 %[tmp], v214\n\t" \
  "s_mov_b64 exec, 1\n\t" \
  "ds_write_b32 v212, v215\n\t" \
  "s_mov_b64 exec, -1\n\t" \
  "s_sub_i32 %[tmp], %[nt], %[tmp]\n\t" \
  "s_add_i32 %[tmp], %[tmp], %[thr]\n\t" \
  "s_cmp_gt_i32 %[tmp], 0\n\t" \
  "s_setprio 0\n\t" \
  "s_cbranch_scc0 3f\n\t" \
  "s_setprio 1\n\t" \
  "3:\n\t" \
  "v_mfma_f32_32x32x16_bf16 v[116:131], v[92:95], v[164:167], v[116:131]\n\t" \
  "v_exp_f32 v196, v112\n\t" \
  "v_exp_f32 v197, v113\n\t" \
  "v_exp_f32 v198, v114\n\t" \
  "v_exp_f32 v199, v115\n\t" \
  "v_mfma_f32_32x32x16_bf16 v[116:131], v[48:51], v[172:175], v[116:131]\n\t" \
  "v_mul_f32 v192, v108, v192\n\t" \
  "v_mul_f32 v193, v109, v193\n\t" \
  "v_mul_f32 v194, v110, v194\n\t" \
  "v_mul_f32 v195, v111, v195\n\t" \
  "s_waitcnt vmcnt(0)\n\t" \
  "ds_read_b128 v[148:151], v234\n\t" \
  "ds_read_b128 v[152:155], v235\n\t" \
  "ds_read_b128 v[156:159], v236\n\t" \
  "ds_read_b128 v[160:163], v237\n\t" \
  "ds_read_b128 v[164:167], v238 offset:4096\n\t" \
  "ds_read_b64 v[224:225], v239 offset:5120\n\t" \
  "ds_write_b128 v243, v[184:187] offset:0\n\t" \
  "v_add_f32 v196, 1.0, v196\n\t" \
  "v_add_f32 v197, 1.0, v197\n\t" \
  "v_add_f32 v198, 1.0, v198\n\t" \
  "v_add_f32 v199, 1.0, v199\n\t" \
  "v_cvt_pk_bf16_f32 v188, v192, v193\n\t" \
  "v_cvt_pk_bf16_f32 v189, v194, v195\n\t" \
  "v_mad_u32_u16 v220, v220, %[c128], v248\n\t" \
  "v_mad_u32_u16 v221, v221, %[c128], v249\n\t" \
  "v_mad_u32_u16 v222, v222, %[c128], v248\n\t" \
  "s_waitcnt lgkmcnt(12)\n\t" \
  "v_mfma_f32_32x32x16_bf16 v[68:83], v[204:207], v[180:183], v[68:83]\n\t" \
  "v_rcp_f32 v196, v196\n\t" \
  "v_rcp_f32 v197, v197\n\t" \
  "v_rcp_f32 v198, v198\n\t" \
  "v_rcp_f32 v199, v199\n\t" \
  "v_mad_u32_u16 v223, v223, %[c128], v249\n\t" \
  "v_or_b32 v226, v226, v250\n\t" \
  "v_mad_u32_u16 v227, v227, 6, v251\n\t" \
  "s_waitcnt lgkmcnt(9)\n\t" \
  "v_mfma_f32_16x16x32_bf16 v[84:87], v[200:203], v[176:179], v[84:87]\n\t" \
  "v_exp_f32 v192, v116\n\t" \
  "v_exp_f32 v193, v117\n\t" \
  "v_exp_f32 v194, v118\n\t" \
  "v_exp_f32 v195, v119\n\t" \
  "s_waitcnt lgkmcnt(8)\n\t" \
  "v_mfma_f32_16x16x32_bf16 v[84:87], v[208:211], v[180:183], v[84:87]\n\t" \
  "v_mul_f32 v196, v112, v196\n\t" \
  "v_mul_f32 v197, v113, v197\n\t" \
  "v_mul_f32 v198, v114, v198\n\t" \
  "v_mul_f32 v199, v115, v199\n\t" \
  "s_waitcnt lgkmcnt(1)\n\t" \
  "s_cmp_lt_i32 %[nt], 1\n\t" \
  "s_cbranch_scc1 4f\n\t" \
  "s_mov_b32 m0, %[b2]\n\t" \
  "s_nop 0\n\t" \
  "global_load_lds_dwordx4 v226, %[ea]\n\t" \
  "global_load_lds_dword v227, %[pos] offset:1024\n\t" \
  "s_mov_b32 m0, %[b1]\n\t" \
  "s_nop 0\n\t" \
  "global_load_lds_dwordx4 v220, %[h0]\n\t" \
  "global_load_lds_dwordx4 v221, %[h1] offset:1024\n\t" \
  "global_load_lds_dwordx4 v222, %[h2] offset:2048\n\t" \
  "global_load_lds_dwordx4 v223, %[h3] offset:3072\n\t" \
  "4:\n\t" \
  "v_add_f32 v192, 1.0, v192\n\t" \
  "v_add_f32 v193, 1.0, v193\n\t" \
  "v_add_f32 v194, 1.0, v194\n\t" \
  "v_add_f32 v195, 1.0, v195\n\t" \
  "v_cvt_pk_bf16_f32 v190, v196, v197\n\t" \
  "v_cvt_pk_bf16_f32 v191, v198, v199\n\t" \
  "v_lshrrev_b32 v232, 20, v232\n\t" \
  "v_add_u32 v220, %[posd], v232\n\t" \
  "v_add_u32 v221, v240, v232\n\t" \
  "ds_write_b128 v243, v[188:191] offset:16\n\t" \
  "ds_read_b128 v[176:179], v242 offset:5376\n\t" \
  "ds_read_b128 v[180:183], v242 offset:5408\n\t" \
  "ds_write_b16 v233, v255 offset:5376\n\t" \
  "ds_read_b128 v[216:219], v220\n\t" \
  "ds_read_b128 v[172:175], v221 offset:32\n\t" \
  "ds_read_b128 v[168:171], v221\n\t" \
  "ds_read_b64_tr_b16 v[200:201], v244 offset:0\n\t" \
  "ds_read_b64_tr_b16 v[202:203], v244 offset:320\n\t" \
  "ds_read_b64_tr_b16 v[204:205], v244 offset:1280\n\t" \
  "ds_read_b64_tr_b16 v[206:207], v244 offset:1600\n\t" \
  "v_add_u32 v233, v241, v232\n\t" \
  "ds_write_b16_d16_hi v233, v255 offset:5376\n\t" \
  "v_mfma_f32_16x16x32_bf16 v[132:135], v[96:99], v[184:187], 0\n\t" \
  "v_rcp_f32 v192, v192\n\t" \
  "v_rcp_f32 v193, v193\n\t" \
  "v_rcp_f32 v194, v194\n\t" \
  "v_rcp_f32 v195, v195\n\t" \
  "v_mfma_f32_32x32x16_bf16 v[100:115], v[4:7], v[148:151], 0\n\t" \
  "v_exp_f32 v196, v120\n\t" \
  "v_exp_f32 v197, v121\n\t" \
  "v_exp_f32 v198, v122\n\t" \
  "v_exp_f32 v199, v123\n\t" \
  "v_mfma_f32_32x32x16_bf16 v[100:115], v[12:15], v[152:155], v[100:115]\n\t" \
  "v_mul_f32 v192, v116, v192\n\t" \
  "v_mul_f32 v193, v117, v193\n\t" \
  "v_mul_f32 v194, v118, v194\n\t" \
  "v_mul_f32 v195, v119, v195\n\t" \
  "v_add_f32 v196, 1.0, v196\n\t" \
  "v_add_f32 v197, 1.0, v197\n\t" \
  "v_add_f32 v198, 1.0, v198\n\t" \
  "v_add_f32 v199, 1.0, v199\n\t" \
  "v_cvt_pk_bf16_f32 v184, v192, v193\n\t" \
  "v_cvt_pk_bf16_f32 v185, v194, v195\n\t" \
  "v_and_b32 v225, 0xffff0000, v225\n\t" \
  "s_waitcnt lgkmcnt(7)\n\t" \
  "v_lshlrev_b32 v219, 16, v224\n\t" \
  "v_and_b32 v224, 0xffff0000, v224\n\t" \
  "v_sub_f32 v229, v218, v225\n\t" \
  "v_sub_f32 v216, v216, v219\n\t" \
  "v_sub_f32 v217, v217, v224\n\t" \
  "v_mfma_f32_32x32x16_bf16 v[100:115], v[20:23], v[156:159], v[100:115]\n\t" \
  "v_rcp_f32 v196, v196\n\t" \
  "v_rcp_f32 v197, v197\n\t" \
  "v_rcp_f32 v198, v198\n\t" \
  "v_rcp_f32 v199, v199\n\t" \
  "v_mul_f32 v218, v229, v229\n\t" \
  "v_cndmask_b32 v228, v216, v217, vcc\n\t" \
  "v_fmac_f32 v218, v216, v216\n\t" \
  "v_fmac_f32 v218, v217, v217\n\t" \
  "v_cvt_pk_bf16_f32 v218, v218, v218\n\t" \
  "s_waitcnt lgkmcnt(6)\n\t" \
  "v_cndmask_b32_sdwa v175, v175, v218, vcc dst_sel:WORD_1 dst_unused:UNUSED_PRESERVE src0_sel:WORD_1 src1_sel:WORD_1\n\t" \
  "v_mfma_f32_16x16x32_bf16 v[132:135], v[136:139], v[188:191], v[132:135]\n\t" \
  "v_exp_f32 v192, v124\n\t" \
  "v_exp_f32 v193, v125\n\t" \
  "v_exp_f32 v194, v126\n\t" \
  "v_exp_f32 v195, v127\n\t" \
  "ds_read2_b32 v[220:221], v252 offset0:0 offset1:2\n\t" \
  "ds_read2_b32 v[222:223], v252 offset0:4 offset1:6\n\t" \
  "ds_read_b64 v[226:227], v253\n\t" \
  "ds_read_b32 v232, v254\n\t" \
  "v_mfma_f32_32x32x16_bf16 v[100:115], v[28:31], v[160:163], v[100:115]\n\t" \
  "v_mul_f32 v196, v120, v196\n\t" \
  "v_mul_f32 v197, v121, v197\n\t" \
  "v_mul_f32 v198, v122, v198\n\t" \
  "v_mul_f32 v199, v123, v199\n\t" \
  "v_add_u32 v252, 0x100, v252\n\t" \
  "v_add_u32 v253, 0x100, v253\n\t" \
  "v_add_u32 v254, 0x100, v254\n\t" \
  "v_add_f32 v192, 1.0, v192\n\t" \
  "v_add_f32 v193, 1.0, v193\n\t" \
  "v_add_f32 v194, 1.0, v194\n\t" \
  "v_add_f32 v195, 1.0, v195\n\t" \
  "v_cvt_pk_bf16_f32 v186, v196, v197\n\t" \
  "v_cvt_pk_bf16_f32 v187, v198, v199\n\t" \
  "s_waitcnt lgkmcnt(9)\n\t" \
  "v_mfma_f32_32x32x16_bf16 v[100:115], v[36:39], v[168:171], v[100:115]\n\t" \
  "v_rcp_f32 v192, v192\n\t" \
  "v_rcp_f32 v193, v193\n\t" \
  "v_rcp_f32 v194, v194\n\t" \
  "v_rcp_f32 v195, v195\n\t" \
  "v_mfma_f32_32x32x16_bf16 v[100:115], v[44:47], v[172:175], v[100:115]\n\t" \
  "v_exp_f32 v196, v128\n\t" \
  "v_exp_f32 v197, v129\n\t" \
  "v_exp_f32 v198, v130\n\t" \
  "v_exp_f32 v199, v131\n\t" \
  "v_mfma_f32_32x32x16_bf16 v[100:115], v[88:91], v[164:167], v[100:115]\n\t" \
  "v_mul_f32 v192, v124, v192\n\t" \
  "v_mul_f32 v193, v125, v193\n\t" \
  "v_mul_f32 v194, v126, v194\n\t" \
  "v_mul_f32 v195, v127, v195\n\t" \
  "ds_write_b128 v243, v[184:187] offset:0\n\t" \
  "v_add_f32 v196, 1.0, v196\n\t" \
  "v_add_f32 v197, 1.0, v197\n\t" \
  "v_add_f32 v198, 1.0, v198\n\t" \
  "v_add_f32 v199, 1.0, v199\n\t" \
  "v_cvt_pk_bf16_f32 v188, v192, v193\n\t" \
  "v_cvt_pk_bf16_f32 v189, v194, v195\n\t" \
  "s_waitcnt lgkmcnt(8)\n\t" \
  "v_mfma_f32_32x32x16_bf16 v[52:67], v[200:203], v[176:179], v[52:67]\n\t" \
  "v_rcp_f32 v196, v196\n\t" \
  "v_rcp_f32 v197, v197\n\t" \
  "v_rcp_f32 v198, v198\n\t" \
  "v_rcp_f32 v199, v199\n\t" \
  "s_waitcnt lgkmcnt(6)\n\t" \
  "v_mfma_f32_32x32x16_bf16 v[52:67], v[204:207], v[180:183], v[52:67]\n\t" \
  "v_exp_f32 v192, v100\n\t" \
  "v_exp_f32 v193, v101\n\t" \
  "v_exp_f32 v194, v102\n\t" \
  "v_exp_f32 v195, v103\n\t" \
  "s_cmp_eq_u32 %[nt], 0\n\t" \
  "v_mfma_f32_16x16x32_bf16 v[132:135], v[140:143], v[184:187], v[132:135]\n\t" \
  "v_mul_f32 v196, v128, v196\n\t" \
  "v_mul_f32 v197, v129, v197\n\t" \
  "v_mul_f32 v198, v130, v198\n\t" \
  "v_mul_f32 v199, v131, v199\n\t" \
  "v_add_f32 v192, 1.0, v192\n\t" \
  "v_add_f32 v193, 1.0, v193\n\t" \
  "v_add_f32 v194, 1.0, v194\n\t" \
  "v_add_f32 v195, 1.0, v195\n\t" \
  "v_cvt_pk_bf16_f32 v190, v196, v197\n\t" \
  "v_cvt_pk_bf16_f32 v191, v198, v199\n\t" \
  "s_cbranch_scc0 1b\n\t" \
  "s_waitcnt lgkmcnt(0)\n\t" \
  "s_cmp_lg_u32 %[last], 0\n\t" \
  "s_cbranch_scc1 8f\n\t" \
  "ds_write_b128 v243, v[188:191] offset:16\n\t" \
  "ds_read_b64_tr_b16 v[200:201], v244 offset:0\n\t" \
  "ds_read_b64_tr_b16 v[202:203], v244 offset:320\n\t" \
  "ds_read_b64_tr_b16 v[204:205], v244 offset:1280\n\t" \
  "ds_read_b64_tr_b16 v[206:207], v244 offset:1600\n\t" \
  "v_mfma_f32_16x16x32_bf16 v[132:135], v[144:147], v[188:191], v[132:135]\n\t" \
  "s_waitcnt lgkmcnt(0)\n\t" \
  "v_mfma_f32_32x32x16_bf16 v[68:83], v[200:203], v[176:179], v[68:83]\n\t" \
  "v_mfma_f32_32x32x16_bf16 v[68:83], v[204:207], v[180:183], v[68:83]\n\t" \
  "s_nop 8\n\t" \
  "v_mul_f32 v230, v230, v132\n\t" \
  "v_mul_f32 v231, v231, v132\n\t" \
  "v_cvt_pk_bf16_f32 v230, v230, v231\n\t" \
  "ds_write_b16 v245, v230 offset:7456\n\t" \
  "ds_write_b16_d16_hi v241, v230 offset:7584\n\t" \
  "ds_read_b128 v[200:203], v246 offset:7456\n\t" \
  "ds_read_b128 v[208:211], v246 offset:7488\n\t" \
  "s_waitcnt lgkmcnt(0)\n\t" \
  "v_mfma_f32_16x16x32_bf16 v[84:87], v[200:203], v[176:179], v[84:87]\n\t" \
  "v_mfma_f32_16x16x32_bf16 v[84:87], v[208:211], v[180:183], v[84:87]\n\t" \
  "s_branch 9f\n\t" \
  "6:\n\t" \
  "s_cmp_lg_u32 %[last], 0\n\t" \
  "s_cbranch_scc0 9f\n\t" \
  "s_waitcnt lgkmcnt(0)\n\t" \
  "v_cndmask_b32 v224, 0, 24, vcc\n\t" \
  "v_xor_b32 v220, v234, v224\n\t" \
  "v_xor_b32 v221, v235, v224\n\t" \
  "v_xor_b32 v222, v236, v224\n\t" \
  "v_xor_b32 v223, v237, v224\n\t" \
  "v_xor_b32 v216, 16, v220\n\t" \
  "v_xor_b32 v217, 16, v221\n\t" \
  "v_xor_b32 v218, 16, v222\n\t" \
  "v_xor_b32 v219, 16, v223\n\t" \
  "ds_read_b64 v[148:149], v220\n\t" \
  "ds_read_b64 v[150:151], v216\n\t" \
  "ds_read_b64 v[152:153], v221\n\t" \
  "ds_read_b64 v[154:155], v217\n\t" \
  "ds_read_b64 v[156:157], v222\n\t" \
  "ds_read_b64 v[158:159], v218\n\t" \
  "ds_read_b64 v[160:161], v223\n\t" \
  "ds_read_b64 v[162:163], v219\n\t" \
  "ds_read_b128 v[4:7], v247 offset:6144\n\t" \
  "ds_read_b128 v[8:11], v247 offset:7168\n\t" \
  "ds_read_b128 v[12:15], v247 offset:8192\n\t" \
  "ds_read_b128 v[16:19], v247 offset:9216\n\t" \
  "ds_read_b128 v[20:23], v247 offset:10240\n\t" \
  "ds_read_b128 v[24:27], v247 offset:11264\n\t" \
  "v_subrev_u32 v225, %[b1], v241\n\t" \
  "v_lshl_add_u32 v225, v225, 5, v243\n\t" \
  "v_sub_u32 v225, v225, v224\n\t" \
  "s_nop 4\n\t" \
  "v_cvt_pk_bf16_f32 v184, v52, v53\n\t" \
  "v_cvt_pk_bf16_f32 v185, v54, v55\n\t" \
  "v_cvt_pk_bf16_f32 v186, v56, v57\n\t" \
  "v_cvt_pk_bf16_f32 v187, v58, v59\n\t" \
  "v_cvt_pk_bf16_f32 v188, v60, v61\n\t" \
  "v_cvt_pk_bf16_f32 v189, v62, v63\n\t" \
  "v_cvt_pk_bf16_f32 v190, v64, v65\n\t" \
  "v_cvt_pk_bf16_f32 v191, v66, v67\n\t" \
  "v_cvt_pk_bf16_f32 v36, v68, v69\n\t" \
  "v_cvt_pk_bf16_f32 v37, v70, v71\n\t" \
  "v_cvt_pk_bf16_f32 v38, v72, v73\n\t" \
  "v_cvt_pk_bf16_f32 v39, v74, v75\n\t" \
  "v_cvt_pk_bf16_f32 v40, v76, v77\n\t" \
  "v_cvt_pk_bf16_f32 v41, v78, v79\n\t" \
  "v_cvt_pk_bf16_f32 v42, v80, v81\n\t" \
  "v_cvt_pk_bf16_f32 v43, v82, v83\n\t" \
  "s_waitcnt lgkmcnt(5)\n\t" \
  "v_mfma_f32_32x32x16_bf16 v[100:115], v[4:7], v[148:151], 0\n\t" \
  "s_waitcnt lgkmcnt(4)\n\t" \
  "v_mfma_f32_32x32x16_bf16 v[116:131], v[8:11], v[148:151], 0\n\t" \
  "ds_read_b128 v[28:31], v247 offset:12288\n\t" \
  "ds_read_b128 v[32:35], v247 offset:13312\n\t" \
  "s_waitcnt lgkmcnt(5)\n\t" \
  "v_mfma_f32_32x32x16_bf16 v[100:115], v[12:15], v[152:155], v[100:115]\n\t" \
  "s_waitcnt lgkmcnt(4)\n\t" \
  "v_mfma_f32_32x32x16_bf16 v[116:131], v[16:19], v[152:155], v[116:131]\n\t" \
  "ds_read_b128 v[4:7], v247 offset:14336\n\t" \
  "ds_read_b128 v[8:11], v247 offset:15360\n\t" \
  "s_waitcnt lgkmcnt(5)\n\t" \
  "v_mfma_f32_32x32x16_bf16 v[100:115], v[20:23], v[156:159], v[100:115]\n\t" \
  "s_waitcnt lgkmcnt(4)\n\t" \
  "v_mfma_f32_32x32x16_bf16 v[116:131], v[24:27], v[156:159], v[116:131]\n\t" \
  "ds_read_b128 v[12:15], v247 offset:16384\n\t" \
  "ds_read_b128 v[16:19], v247 offset:17408\n\t" \
  "s_waitcnt lgkmcnt(5)\n\t" \
  "v_mfma_f32_32x32x16_bf16 v[100:115], v[28:31], v[160:163], v[100:115]\n\t" \
  "s_waitcnt lgkmcnt(4)\n\t" \
  "v_mfma_f32_32x32x16_bf16 v[116:131], v[32:35], v[160:163], v[116:131]\n\t" \
  "ds_read_b128 v[20:23], v247 offset:18432\n\t" \
  "ds_read_b128 v[24:27], v247 offset:19456\n\t" \
  "s_waitcnt lgkmcnt(5)\n\t" \
  "v_mfma_f32_32x32x16_bf16 v[100:115], v[4:7], v[184:187], v[100:115]\n\t" \
  "ds_read_b128 v[28:31], v247 offset:20480\n\t" \
  "ds_read_b128 v[32:35], v247 offset:21504\n\t" \
  "s_waitcnt lgkmcnt(5)\n\t" \
  "v_mfma_f32_32x32x16_bf16 v[100:115], v[12:15], v[188:191], v[100:115]\n\t" \
  "s_waitcnt lgkmcnt(3)\n\t" \
  "v_mfma_f32_32x32x16_bf16 v[100:115], v[20:23], v[36:39], v[100:115]\n\t" \
  "s_waitcnt lgkmcnt(1)\n\t" \
  "v_mfma_f32_32x32x16_bf16 v[100:115], v[28:31], v[40:43], v[100:115]\n\t" \
  "s_waitcnt lgkmcnt(0)\n\t" \
  "s_branch 10f\n\t" \
  "8:\n\t" \
  "ds_write_b128 v243, v[188:191] offset:16\n\t" \
  "ds_read_b64_tr_b16 v[200:201], v244 offset:0\n\t" \
  "ds_read_b64_tr_b16 v[202:203], v244 offset:320\n\t" \
  "ds_read_b64_tr_b16 v[204:205], v244 offset:1280\n\t" \
  "ds_read_b64_tr_b16 v[206:207], v244 offset:1600\n\t" \
  "v_mfma_f32_16x16x32_bf16 v[132:135], v[144:147], v[188:191], v[132:135]\n\t" \
  "v_cndmask_b32 v224, 0, 24, vcc\n\t" \
  "v_xor_b32 v220, v234, v224\n\t" \
  "v_xor_b32 v221, v235, v224\n\t" \
  "v_xor_b32 v222, v236, v224\n\t" \
  "v_xor_b32 v223, v237, v224\n\t" \
  "v_xor_b32 v216, 16, v220\n\t" \
  "v_xor_b32 v217, 16, v221\n\t" \
  "v_xor_b32 v218, 16, v222\n\t" \
  "v_xor_b32 v219, 16, v223\n\t" \
  "ds_read_b64 v[148:149], v220\n\t" \
  "ds_read_b64 v[150:151], v216\n\t" \
  "ds_read_b64 v[152:153], v221\n\t" \
  "ds_read_b64 v[154:155], v217\n\t" \
  "ds_read_b64 v[156:157], v222\n\t" \
  "ds_read_b64 v[158:159], v218\n\t" \
  "ds_read_b64 v[160:161], v223\n\t" \
  "ds_read_b64 v[162:163], v219\n\t" \
  "ds_read_b128 v[4:7], v247 offset:6144\n\t" \
  "ds_read_b128 v[8:11], v247 offset:7168\n\t" \
  "s_waitcnt lgkmcnt(11)\n\t" \
  "ds_read_b128 v[12:15], v247 offset:8192\n\t" \
  "ds_read_b128 v[16:19], v247 offset:9216\n\t" \
  "ds_read_b128 v[20:23], v247 offset:10240\n\t" \
  "ds_read_b128 v[24:27], v247 offset:11264\n\t" \
  "v_subrev_u32 v225, %[b1], v241\n\t" \
  "v_lshl_add_u32 v225, v225, 5, v243\n\t" \
  "v_sub_u32 v225, v225, v224\n\t" \
  "v_mfma_f32_32x32x16_bf16 v[68:83], v[200:203], v[176:179], v[68:83]\n\t" \
  "s_waitcnt lgkmcnt(5)\n\t" \
  "v_mfma_f32_32x32x16_bf16 v[100:115], v[4:7], v[148:151], 0\n\t" \
  "v_mfma_f32_32x32x16_bf16 v[68:83], v[204:207], v[180:183], v[68:83]\n\t" \
  "s_waitcnt lgkmcnt(4)\n\t" \
  "v_mfma_f32_32x32x16_bf16 v[116:131], v[8:11], v[148:151], 0\n\t" \
  "ds_read_b128 v[28:31], v247 offset:12288\n\t" \
  "ds_read_b128 v[32:35], v247 offset:13312\n\t" \
  "s_waitcnt lgkmcnt(5)\n\t" \
  "v_mfma_f32_32x32x16_bf16 v[100:115], v[12:15], v[152:155], v[100:115]\n\t" \
  "v_mul_f32 v230, v230, v132\n\t" \
  "v_mul_f32 v231, v231, v132\n\t" \
  "v_cvt_pk_bf16_f32 v230, v230, v231\n\t" \
  "ds_write_b16 v245, v230 offset:7456\n\t" \
  "ds_write_b16_d16_hi v241, v230 offset:7584\n\t" \
  "ds_read_b128 v[200:203], v246 offset:7456\n\t" \
  "ds_read_b128 v[208:211], v246 offset:7488\n\t" \
  "v_cvt_pk_bf16_f32 v184, v52, v53\n\t" \
  "v_cvt_pk_bf16_f32 v185, v54, v55\n\t" \
  "v_cvt_pk_bf16_f32 v186, v56, v57\n\t" \
  "v_cvt_pk_bf16_f32 v187, v58, v59\n\t" \
  "s_waitcnt lgkmcnt(8)\n\t" \
  "v_mfma_f32_32x32x16_bf16 v[116:131], v[16:19], v[152:155], v[116:131]\n\t" \
  "ds_read_b128 v[4:7], v247 offset:14336\n\t" \
  "ds_read_b128 v[8:11], v247 offset:15360\n\t" \
  "s_waitcnt lgkmcnt(9)\n\t" \
  "v_mfma_f32_32x32x16_bf16 v[100:115], v[20:23], v[156:159], v[100:115]\n\t" \
  "v_cvt_pk_bf16_f32 v188, v60, v61\n\t" \
  "v_cvt_pk_bf16_f32 v189, v62, v63\n\t" \
  "v_cvt_pk_bf16_f32 v190, v64, v65\n\t" \
  "v_cvt_pk_bf16_f32 v191, v66, v67\n\t" \
  "s_waitcnt lgkmcnt(8)\n\t" \
  "v_mfma_f32_32x32x16_bf16 v[116:131], v[24:27], v[156:159], v[116:131]\n\t" \
  "ds_read_b128 v[12:15], v247 offset:16384\n\t" \
  "ds_read_b128 v[16:19], v247 offset:17408\n\t" \
  "s_waitcnt lgkmcnt(9)\n\t" \
  "v_mfma_f32_32x32x16_bf16 v[100:115], v[28:31], v[160:163], v[100:115]\n\t" \
  "v_cvt_pk_bf16_f32 v36, v68, v69\n\t" \
  "v_cvt_pk_bf16_f32 v37, v70, v71\n\t" \
  "v_cvt_pk_bf16_f32 v38, v72, v73\n\t" \
  "v_cvt_pk_bf16_f32 v39, v74, v75\n\t" \
  "s_waitcnt lgkmcnt(5)\n\t" \
  "v_mfma_f32_16x16x32_bf16 v[84:87], v[200:203], v[176:179], v[84:87]\n\t" \
  "v_cvt_pk_bf16_f32 v40, v76, v77\n\t" \
  "v_cvt_pk_bf16_f32 v41, v78, v79\n\t" \
  "v_cvt_pk_bf16_f32 v42, v80, v81\n\t" \
  "v_cvt_pk_bf16_f32 v43, v82, v83\n\t" \
  "v_mfma_f32_32x32x16_bf16 v[116:131], v[32:35], v[160:163], v[116:131]\n\t" \
  "ds_read_b128 v[20:23], v247 offset:18432\n\t" \
  "ds_read_b128 v[24:27], v247 offset:19456\n\t" \
  "s_waitcnt lgkmcnt(6)\n\t" \
  "v_mfma_f32_16x16x32_bf16 v[84:87], v[208:211], v[180:183], v[84:87]\n\t" \
  "s_waitcnt lgkmcnt(5)\n\t" \
  "v_mfma_f32_32x32x16_bf16 v[100:115], v[4:7], v[184:187], v[100:115]\n\t" \
  "ds_read_b128 v[28:31], v247 offset:20480\n\t" \
  "ds_read_b128 v[32:35], v247 offset:21504\n\t" \
  "s_waitcnt lgkmcnt(5)\n\t" \
  "v_mfma_f32_32x32x16_bf16 v[100:115], v[12:15], v[188:191], v[100:115]\n\t" \
  "s_waitcnt lgkmcnt(3)\n\t" \
  "v_mfma_f32_32x32x16_bf16 v[100:115], v[20:23], v[36:39], v[100:115]\n\t" \
  "s_waitcnt lgkmcnt(1)\n\t" \
  "v_mfma_f32_32x32x16_bf16 v[100:115], v[28:31], v[40:43], v[100:115]\n\t" \
  "s_waitcnt lgkmcnt(0)\n\t" \
  "10:\n\t" \
  "v_mov_b32 v212, 0xbfb8aa3b\n\t" \
  "v_mfma_f32_32x32x16_bf16 v[116:131], v[8:11], v[184:187], v[116:131]\n\t" \
  "v_mfma_f32_32x32x16_bf16 v[116:131], v[16:19], v[188:191], v[116:131]\n\t" \
  "s_nop 8\n\t" \
  "v_exp_f32 v192, v100\n\t" \
  "v_exp_f32 v193, v101\n\t" \
  "v_exp_f32 v194, v102\n\t" \
  "v_exp_f32 v195, v103\n\t" \
  "v_lshlrev_b32 v200, 16, v148\n\t" \
  "v_and_b32 v201, 0xffff0000, v148\n\t" \
  "v_lshlrev_b32 v202, 16, v149\n\t" \
  "v_and_b32 v203, 0xffff0000, v149\n\t" \
  "v_mfma_f32_32x32x16_bf16 v[116:131], v[24:27], v[36:39], v[116:131]\n\t" \
  "v_fmaak_f32 v192, v192, v212, 0xbfb8aa3b\n\t" \
  "v_fmaak_f32 v193, v193, v212, 0xbfb8aa3b\n\t" \
  "v_fmaak_f32 v194, v194, v212, 0xbfb8aa3b\n\t" \
  "v_fmaak_f32 v195, v195, v212, 0xbfb8aa3b\n\t" \
  "v_rcp_f32 v192, v192\n\t" \
  "v_rcp_f32 v193, v193\n\t" \
  "v_rcp_f32 v194, v194\n\t" \
  "v_rcp_f32 v195, v195\n\t" \
  "v_mfma_f32_32x32x16_bf16 v[116:131], v[32:35], v[40:43], v[116:131]\n\t" \
  "v_exp_f32 v196, v104\n\t" \
  "v_exp_f32 v197, v105\n\t" \
  "v_exp_f32 v198, v106\n\t" \
  "v_exp_f32 v199, v107\n\t" \
  "v_fmac_f32 v200, v100, v192\n\t" \
  "v_fmac_f32 v201, v101, v193\n\t" \
  "v_fmac_f32 v202, v102, v194\n\t" \
  "v_fmac_f32 v203, v103, v195\n\t" \
  "v_lshlrev_b32 v204, 16, v150\n\t" \
  "v_and_b32 v205, 0xffff0000, v150\n\t" \
  "v_lshlrev_b32 v206, 16, v151\n\t" \
  "v_and_b32 v207, 0xffff0000, v151\n\t" \
  "v_fmaak_f32 v196, v196, v212, 0xbfb8aa3b\n\t" \
  "v_fmaak_f32 v197, v197, v212, 0xbfb8aa3b\n\t" \
  "v_fmaak_f32 v198, v198, v212, 0xbfb8aa3b\n\t" \
  "v_fmaak_f32 v199, v199, v212, 0xbfb8aa3b\n\t" \
  "v_cvt_pk_bf16_f32 v176, v200, v201\n\t" \
  "v_cvt_pk_bf16_f32 v177, v202, v203\n\t" \
  "ds_write_b64 v225, v[176:177] offset:0\n\t" \
  "v_rcp_f32 v196, v196\n\t" \
  "v_rcp_f32 v197, v197\n\t" \
  "v_rcp_f32 v198, v198\n\t" \
  "v_rcp_f32 v199, v199\n\t" \
  "v_exp_f32 v192, v108\n\t" \
  "v_exp_f32 v193, v109\n\t" \
  "v_exp_f32 v194, v110\n\t" \
  "v_exp_f32 v195, v111\n\t" \
  "v_fmac_f32 v204, v104, v196\n\t" \
  "v_fmac_f32 v205, v105, v197\n\t" \
  "v_fmac_f32 v206, v106, v198\n\t" \
  "v_fmac_f32 v207, v107, v199\n\t" \
  "v_lshlrev_b32 v200, 16, v152\n\t" \
  "v_and_b32 v201, 0xffff0000, v152\n\t" \
  "v_lshlrev_b32 v202, 16, v153\n\t" \
  "v_and_b32 v203, 0xffff0000, v153\n\t" \
  "v_fmaak_f32 v192, v192, v212, 0xbfb8aa3b\n\t" \
  "v_fmaak_f32 v193, v193, v212, 0xbfb8aa3b\n\t" \
  "v_fmaak_f32 v194, v194, v212, 0xbfb8aa3b\n\t" \
  "v_fmaak_f32 v195, v195, v212, 0xbfb8aa3b\n\t" \
  "v_cvt_pk_bf16_f32 v178, v204, v205\n\t" \
  "v_cvt_pk_bf16_f32 v179, v206, v207\n\t" \
  "ds_write_b64 v225, v[178:179] offset:16\n\t" \
  "v_rcp_f32 v192, v192\n\t" \
  "v_rcp_f32 v193, v193\n\t" \
  "v_rcp_f32 v194, v194\n\t" \
  "v_rcp_f32 v195, v195\n\t" \
  "v_exp_f32 v196, v112\n\t" \
  "v_exp_f32 v197, v113\n\t" \
  "v_exp_f32 v198, v114\n\t" \
  "v_exp_f32 v199, v115\n\t" \
  "v_fmac_f32 v200, v108, v192\n\t" \
  "v_fmac_f32 v201, v109, v193\n\t" \
  "v_fmac_f32 v202, v110, v194\n\t" \
  "v_fmac_f32 v203, v111, v195\n\t" \
  "v_lshlrev_b32 v204, 16, v154\n\t" \
  "v_and_b32 v205, 0xffff0000, v154\n\t" \
  "v_lshlrev_b32 v206, 16, v155\n\t" \
  "v_and_b32 v207, 0xffff0000, v155\n\t" \
  "v_fmaak_f32 v196, v196, v212, 0xbfb8aa3b\n\t" \
  "v_fmaak_f32 v197, v197, v212, 0xbfb8aa3b\n\t" \
  "v_fmaak_f32 v198, v198, v212, 0xbfb8aa3b\n\t" \
  "v_fmaak_f32 v199, v199, v212, 0xbfb8aa3b\n\t" \
  "v_cvt_pk_bf16_f32 v180, v200, v201\n\t" \
  "v_cvt_pk_bf16_f32 v181, v202, v203\n\t" \
  "ds_write_b64 v225, v[180:181] offset:32\n\t" \
  "v_rcp_f32 v196, v196\n\t" \
  "v_rcp_f32 v197, v197\n\t" \
  "v_rcp_f32 v198, v198\n\t" \
  "v_rcp_f32 v199, v199\n\t" \
  "v_exp_f32 v192, v116\n\t" \
  "v_exp_f32 v193, v117\n\t" \
  "v_exp_f32 v194, v118\n\t" \
  "v_exp_f32 v195, v119\n\t" \
  "v_fmac_f32 v204, v112, v196\n\t" \
  "v_fmac_f32 v205, v113, v197\n\t" \
  "v_fmac_f32 v206, v114, v198\n\t" \
  "v_fmac_f32 v207, v115, v199\n\t" \
  "v_lshlrev_b32 v200, 16, v156\n\t" \
  "v_and_b32 v201, 0xffff0000, v156\n\t" \
  "v_lshlrev_b32 v202, 16, v157\n\t" \
  "v_and_b32 v203, 0xffff0000, v157\n\t" \
  "v_fmaak_f32 v192, v192, v212, 0xbfb8aa3b\n\t" \
  "v_fmaak_f32 v193, v193, v212, 0xbfb8aa3b\n\t" \
  "v_fmaak_f32 v194, v194, v212, 0xbfb8aa3b\n\t" \
  "v_fmaak_f32 v195, v195, v212, 0xbfb8aa3b\n\t" \
  "v_cvt_pk_bf16_f32 v182, v204, v205\n\t" \
  "v_cvt_pk_bf16_f32 v183, v206, v207\n\t" \
  "ds_write_b64 v225, v[182:183] offset:48\n\t" \
  "v_rcp_f32 v192, v192\n\t" \
  "v_rcp_f32 v193, v193\n\t" \
  "v_rcp_f32 v194, v194\n\t" \
  "v_rcp_f32 v195, v195\n\t" \
  "v_exp_f32 v196, v120\n\t" \
  "v_exp_f32 v197, v121\n\t" \
  "v_exp_f32 v198, v122\n\t" \
  "v_exp_f32 v199, v123\n\t" \
  "v_fmac_f32 v200, v116, v192\n\t" \
  "v_fmac_f32 v201, v117, v193\n\t" \
  "v_fmac_f32 v202, v118, v194\n\t" \
  "v_fmac_f32 v203, v119, v195\n\t" \
  "v_lshlrev_b32 v204, 16, v158\n\t" \
  "v_and_b32 v205, 0xffff0000, v158\n\t" \
  "v_lshlrev_b32 v206, 16, v159\n\t" \
  "v_and_b32 v207, 0xffff0000, v159\n\t" \
  "v_fmaak_f32 v196, v196, v212, 0xbfb8aa3b\n\t" \
  "v_fmaak_f32 v197, v197, v212, 0xbfb8aa3b\n\t" \
  "v_fmaak_f32 v198, v198, v212, 0xbfb8aa3b\n\t" \
  "v_fmaak_f32 v199, v199, v212, 0xbfb8aa3b\n\t" \
  "v_cvt_pk_bf16_f32 v176, v200, v201\n\t" \
  "v_cvt_pk_bf16_f32 v177, v202, v203\n\t" \
  "ds_write_b64 v225, v[176:177] offset:64\n\t" \
  "v_rcp_f32 v196, v196\n\t" \
  "v_rcp_f32 v197, v197\n\t" \
  "v_rcp_f32 v198, v198\n\t" \
  "v_rcp_f32 v199, v199\n\t" \
  "v_exp_f32 v192, v124\n\t" \
  "v_exp_f32 v193, v125\n\t" \
  "v_exp_f32 v194, v126\n\t" \
  "v_exp_f32 v195, v127\n\t" \
  "v_fmac_f32 v204, v120, v196\n\t" \
  "v_fmac_f32 v205, v121, v197\n\t" \
  "v_fmac_f32 v206, v122, v198\n\t" \
  "v_fmac_f32 v207, v123, v199\n\t" \
  "v_lshlrev_b32 v200, 16, v160\n\t" \
  "v_and_b32 v201, 0xffff0000, v160\n\t" \
  "v_lshlrev_b32 v202, 16, v161\n\t" \
  "v_and_b32 v203, 0xffff0000, v161\n\t" \
  "v_fmaak_f32 v192, v192, v212, 0xbfb8aa3b\n\t" \
  "v_fmaak_f32 v193, v193, v212, 0xbfb8aa3b\n\t" \
  "v_fmaak_f32 v194, v194, v212, 0xbfb8aa3b\n\t" \
  "v_fmaak_f32 v195, v195, v212, 0xbfb8aa3b\n\t" \
  "v_cvt_pk_bf16_f32 v178, v204, v205\n\t" \
  "v_cvt_pk_bf16_f32 v179, v206, v207\n\t" \
  "ds_write_b64 v225, v[178:179] offset:80\n\t" \
  "v_rcp_f32 v192, v192\n\t" \
  "v_rcp_f32 v193, v193\n\t" \
  "v_rcp_f32 v194, v194\n\t" \
  "v_rcp_f32 v195, v195\n\t" \
  "v_exp_f32 v196, v128\n\t" \
  "v_exp_f32 v197, v129\n\t" \
  "v_exp_f32 v198, v130\n\t" \
  "v_exp_f32 v199, v131\n\t" \
  "v_fmac_f32 v200, v124, v192\n\t" \
  "v_fmac_f32 v201, v125, v193\n\t" \
  "v_fmac_f32 v202, v126, v194\n\t" \
  "v_fmac_f32 v203, v127, v195\n\t" \
  "v_lshlrev_b32 v204, 16, v162\n\t" \
  "v_and_b32 v205, 0xffff0000, v162\n\t" \
  "v_lshlrev_b32 v206, 16, v163\n\t" \
  "v_and_b32 v207, 0xffff0000, v163\n\t" \
  "v_fmaak_f32 v196, v196, v212, 0xbfb8aa3b\n\t" \
  "v_fmaak_f32 v197, v197, v212, 0xbfb8aa3b\n\t" \
  "v_fmaak_f32 v198, v198, v212, 0xbfb8aa3b\n\t" \
  "v_fmaak_f32 v199, v199, v212, 0xbfb8aa3b\n\t" \
  "v_cvt_pk_bf16_f32 v180, v200, v201\n\t" \
  "v_cvt_pk_bf16_f32 v181, v202, v203\n\t" \
  "ds_write_b64 v225, v[180:181] offset:96\n\t" \
  "v_rcp_f32 v196, v196\n\t" \
  "v_rcp_f32 v197, v197\n\t" \
  "v_rcp_f32 v198, v198\n\t" \
  "v_rcp_f32 v199, v199\n\t" \
  "s_nop 0\n\t" \
  "v_fmac_f32 v204, v128, v196\n\t" \
  "v_fmac_f32 v205, v129, v197\n\t" \
  "v_fmac_f32 v206, v130, v198\n\t" \
  "v_fmac_f32 v207, v131, v199\n\t" \
  "v_cvt_pk_bf16_f32 v182, v204, v205\n\t" \
  "v_cvt_pk_bf16_f32 v183, v206, v207\n\t" \
  "ds_write_b64 v225, v[182:183] offset:112\n\t" \
  "9:\n\t" \
  "ds_write_b16 v233, v255 offset:5376\n\t" \
  "s_mov_b32 m0, %[keep]\n\t" \
  "s_setprio 0\n\t" \
  "s_nop 11\n\t" \
  ""
#define LOOP_ASM_PLAIN \
  "s_mov_b32 %[keep], m0\n\t" \
  "s_mov_b64 vcc, %[hsel]\n\t" \
  "ds_read2_b32 v[220:221], v252 offset0:0 offset1:2\n\t" \
  "ds_read2_b32 v[222:223], v252 offset0:4 offset1:6\n\t" \
  "ds_read_b64 v[226:227], v253\n\t" \
  "ds_read_b32 v232, v254\n\t" \
  "v_mov_b32 v176, 0\n\t" \
  "v_mov_b32 v177, 0\n\t" \
  "v_mov_b32 v178, 0\n\t" \
  "v_mov_b32 v179, 0\n\t" \
  "v_mov_b32 v180, 0\n\t" \
  "v_mov_b32 v181, 0\n\t" \
  "v_mov_b32 v182, 0\n\t" \
  "v_mov_b32 v183, 0\n\t" \
  "v_mov_b32 v184, 0\n\t" \
  "v_mov_b32 v185, 0\n\t" \
  "v_mov_b32 v186, 0\n\t" \
  "v_mov_b32 v187, 0\n\t" \
  "v_mov_b32 v188, 0\n\t" \
  "v_mov_b32 v189, 0\n\t" \
  "v_mov_b32 v190, 0\n\t" \
  "v_mov_b32 v191, 0\n\t" \
  "v_mov_b32 v230, 0\n\t" \
  "v_mov_b32 v231, 0\n\t" \
  "v_mov_b32 v208, 0\n\t" \
  "v_mov_b32 v209, 0\n\t" \
  "v_mov_b32 v210, 0\n\t" \
  "v_mov_b32 v211, 0\n\t" \
  "ds_write_b128 v243, v[184:187] offset:0\n\t" \
  "s_nop 0\n\t" \
  "v_mfma_f32_16x16x32_bf16 v[132:135], v[184:187], v[188:191], 0\n\t" \
  "v_add_u32 v252, 0x100, v252\n\t" \
  "v_add_u32 v253, 0x100, v253\n\t" \
  "v_add_u32 v254, 0x100, v254\n\t" \
  "s_waitcnt lgkmcnt(0)\n\t" \
  "v_lshrrev_b32 v232, 20, v232\n\t" \
  "v_add_u32 v233, %[posd], v232\n\t" \
  "ds_read_b128 v[216:219], v233\n\t" \
  "v_add_u32 v233, v240, v232\n\t" \
  "ds_read_b128 v[172:175], v233 offset:32\n\t" \
  "ds_read_b128 v[168:171], v233\n\t" \
  "v_add_u32 v233, v241, v232\n\t" \
  "ds_write_b16_d16_hi v233, v255 offset:5376\n\t" \
  "v_mad_u32_u16 v220, v220, %[c128], v248\n\t" \
  "v_mad_u32_u16 v221, v221, %[c128], v249\n\t" \
  "v_mad_u32_u16 v222, v222, %[c128], v248\n\t" \
  "v_mad_u32_u16 v223, v223, %[c128], v249\n\t" \
  "v_or_b32 v226, v226, v250\n\t" \
  "v_mad_u32_u16 v227, v227, 6, v251\n\t" \
  "s_waitcnt vmcnt(0)\n\t" \
  "ds_read_b128 v[148:151], v234\n\t" \
  "ds_read_b128 v[152:155], v235\n\t" \
  "ds_read_b128 v[156:159], v236\n\t" \
  "ds_read_b128 v[160:163], v237\n\t" \
  "ds_read_b128 v[164:167], v238 offset:4096\n\t" \
  "ds_read_b64 v[224:225], v239 offset:5120\n\t" \
  "s_waitcnt lgkmcnt(0)\n\t" \
  "s_cmp_lt_i32 %[nt], 1\n\t" \
  "s_cbranch_scc1 7f\n\t" \
  "s_mov_b32 m0, %[b2]\n\t" \
  "s_nop 0\n\t" \
  "global_load_lds_dwordx4 v226, %[ea]\n\t" \
  "global_load_lds_dword v227, %[pos] offset:1024\n\t" \
  "s_mov_b32 m0, %[b1]\n\t" \
  "s_nop 0\n\t" \
  "global_load_lds_dwordx4 v220, %[h0]\n\t" \
  "global_load_lds_dwordx4 v221, %[h1] offset:1024\n\t" \
  "global_load_lds_dwordx4 v222, %[h2] offset:2048\n\t" \
  "global_load_lds_dwordx4 v223, %[h3] offset:3072\n\t" \
  "7:\n\t" \
  "ds_read2_b32 v[220:221], v252 offset0:0 offset1:2\n\t" \
  "ds_read2_b32 v[222:223], v252 offset0:4 offset1:6\n\t" \
  "ds_read_b64 v[226:227], v253\n\t" \
  "ds_read_b32 v232, v254\n\t" \
  "v_add_u32 v252, 0x100, v252\n\t" \
  "v_add_u32 v253, 0x100, v253\n\t" \
  "v_add_u32 v254, 0x100, v254\n\t" \
  "v_and_b32 v225, 0xffff0000, v225\n\t" \
  "v_lshlrev_b32 v219, 16, v224\n\t" \
  "v_and_b32 v224, 0xffff0000, v224\n\t" \
  "v_sub_f32 v229, v218, v225\n\t" \
  "v_sub_f32 v216, v216, v219\n\t" \
  "v_sub_f32 v217, v217, v224\n\t" \
  "v_mul_f32 v218, v229, v229\n\t" \
  "v_cndmask_b32 v228, v216, v217, vcc\n\t" \
  "v_fmac_f32 v218, v216, v216\n\t" \
  "v_fmac_f32 v218, v217, v217\n\t" \
  "v_cvt_pk_bf16_f32 v218, v218, v218\n\t" \
  "v_cndmask_b32_sdwa v175, v175, v218, vcc dst_sel:WORD_1 dst_unused:UNUSED_PRESERVE src0_sel:WORD_1 src1_sel:WORD_1\n\t" \
  "v_mfma_f32_32x32x16_bf16 v[100:115], v[4:7], v[148:151], 0\n\t" \
  "v_mfma_f32_32x32x16_bf16 v[100:115], v[12:15], v[152:155], v[100:115]\n\t" \
  "v_mfma_f32_32x32x16_bf16 v[100:115], v[20:23], v[156:159], v[100:115]\n\t" \
  "v_mfma_f32_32x32x16_bf16 v[100:115], v[28:31], v[160:163], v[100:115]\n\t" \
  "v_mfma_f32_32x32x16_bf16 v[100:115], v[36:39], v[168:171], v[100:115]\n\t" \
  "v_mfma_f32_32x32x16_bf16 v[100:115], v[88:91], v[164:167], v[100:115]\n\t" \
  "v_mfma_f32_32x32x16_bf16 v[100:115], v[44:47], v[172:175], v[100:115]\n\t" \
  "s_cmp_eq_u32 %[nt], 0\n\t" \
  "s_nop 10\n\t" \
  "v_mul_f32 v192, 0xbfb8aa3b, v100\n\t" \
  "v_mul_f32 v193, 0xbfb8aa3b, v101\n\t" \
  "v_mul_f32 v194, 0xbfb8aa3b, v102\n\t" \
  "v_mul_f32 v195, 0xbfb8aa3b, v103\n\t" \
  "v_exp_f32 v192, v192\n\t" \
  "v_exp_f32 v193, v193\n\t" \
  "v_exp_f32 v194, v194\n\t" \
  "v_exp_f32 v195, v195\n\t" \
  "s_nop 0\n\t" \
  "v_add_f32 v192, 1.0, v192\n\t" \
  "v_add_f32 v193, 1.0, v193\n\t" \
  "v_add_f32 v194, 1.0, v194\n\t" \
  "v_add_f32 v195, 1.0, v195\n\t" \
  "s_cbranch_scc1 6f\n\t" \
  "s_cmp_lt_i32 %[thr], 1\n\t" \
  "s_cbranch_scc1 1f\n\t" \
  "s_setprio 1\n\t" \
  "1:\n\t" \
  "ds_write_b128 v243, v[188:191] offset:16\n\t" \
  "ds_read_b32 v214, v213\n\t" \
  "ds_read_b64_tr_b16 v[200:201], v244 offset:0\n\t" \
  "ds_read_b64_tr_b16 v[202:203], v244 offset:320\n\t" \
  "ds_read_b64_tr_b16 v[204:205], v244 offset:1280\n\t" \
  "ds_read_b64_tr_b16 v[206:207], v244 offset:1600\n\t" \
  "v_mfma_f32_16x16x32_bf16 v[132:135], v[144:147], v[188:191], v[132:135]\n\t" \
  "v_rcp_f32 v192, v192\n\t" \
  "v_rcp_f32 v193, v193\n\t" \
  "v_rcp_f32 v194, v194\n\t" \
  "v_rcp_f32 v195, v195\n\t" \
  "v_mfma_f32_32x32x16_bf16 v[116:131], v[8:11], v[148:151], 0\n\t" \
  "v_mul_f32 v196, 0xbfb8aa3b, v104\n\t" \
  "v_mul_f32 v197, 0xbfb8aa3b, v105\n\t" \
  "v_mul_f32 v198, 0xbfb8aa3b, v106\n\t" \
  "v_mul_f32 v199, 0xbfb8aa3b, v107\n\t" \
  "v_exp_f32 v196, v196\n\t" \
  "v_exp_f32 v197, v197\n\t" \
  "v_exp_f32 v198, v198\n\t" \
  "v_exp_f32 v199, v199\n\t" \
  "v_mfma_f32_32x32x16_bf16 v[116:131], v[16:19], v[152:155], v[116:131]\n\t" \
  "v_mul_f32 v192, v100, v192\n\t" \
  "v_mul_f32 v193, v101, v193\n\t" \
  "v_mul_f32 v194, v102, v194\n\t" \
  "v_mul_f32 v195, v103, v195\n\t" \
  "v_add_f32 v196, 1.0, v196\n\t" \
  "v_add_f32 v197, 1.0, v197\n\t" \
  "v_add_f32 v198, 1.0, v198\n\t" \
  "v_add_f32 v199, 1.0, v199\n\t" \
  "v_cvt_pk_bf16_f32 v184, v192, v193\n\t" \
  "v_cvt_pk_bf16_f32 v185, v194, v195\n\t" \
  "v_mfma_f32_32x32x16_bf16 v[116:131], v[24:27], v[156:159], v[116:131]\n\t" \
  "v_rcp_f32 v196, v196\n\t" \
  "v_rcp_f32 v197, v197\n\t" \
  "v_rcp_f32 v198, v198\n\t" \
  "v_rcp_f32 v199, v199\n\t" \
  "s_waitcnt lgkmcnt(2)\n\t" \
  "v_mfma_f32_32x32x16_bf16 v[68:83], v[200:203], v[176:179], v[68:83]\n\t" \
  "v_mul_f32 v192, 0xbfb8aa3b, v108\n\t" \
  "v_mul_f32 v193, 0xbfb8aa3b, v109\n\t" \
  "v_mul_f32 v194, 0xbfb8aa3b, v110\n\t" \
  "v_mul_f32 v195, 0xbfb8aa3b, v111\n\t" \
  "v_exp_f32 v192, v192\n\t" \
  "v_exp_f32 v193, v193\n\t" \
  "v_exp_f32 v194, v194\n\t" \
  "v_exp_f32 v195, v195\n\t" \
  "v_mfma_f32_32x32x16_bf16 v[116:131], v[32:35], v[160:163], v[116:131]\n\t" \
  "v_mul_f32 v196, v104, v196\n\t" \
  "v_mul_f32 v197, v105, v197\n\t" \
  "v_mul_f32 v198, v106, v198\n\t" \
  "v_mul_f32 v199, v107, v199\n\t" \
  "v_mul_f32 v230, v230, v132\n\t" \
  "v_mul_f32 v231, v231, v132\n\t" \
  "v_cvt_pk_bf16_f32 v230, v230, v231\n\t" \
  "ds_write_b16 v245, v230 offset:7456\n\t" \
  "ds_write_b16_d16_hi v241, v230 offset:7584\n\t" \
  "ds_read_b128 v[200:203], v246 offset:7456\n\t" \
  "ds_read_b128 v[208:211], v246 offset:7488\n\t" \
  "v_mov_b32 v230, v228\n\t" \
  "v_mov_b32 v231, v229\n\t" \
  "v_add_f32 v192, 1.0, v192\n\t" \
  "v_add_f32 v193, 1.0, v193\n\t" \
  "v_add_f32 v194, 1.0, v194\n\t" \
  "v_add_f32 v195, 1.0, v195\n\t" \
  "v_cvt_pk_bf16_f32 v186, v196, v197\n\t" \
  "v_cvt_pk_bf16_f32 v187, v198, v199\n\t" \
  "v_mfma_f32_32x32x16_bf16 v[116:131], v[40:43], v[168:171], v[116:131]\n\t" \
  "v_rcp_f32 v192, v192\n\t" \
  "v_rcp_f32 v193, v193\n\t" \
  "v_rcp_f32 v194, v194\n\t" \
  "v_rcp_f32 v195, v195\n\t" \
  "s_sub_u32 %[nt], %[nt], 1\n\t" \
  "v_mov_b32 v215, %[nt]\n\t" \
  "v_readfirstlane_b32 %[tmp], v214\n\t" \
  "s_mov_b64 exec, 1\n\t" \
  "ds_write_b32 v212, v215\n\t" \
  "s_mov_b64 exec, -1\n\t" \
  "s_sub_i32 %[tmp], %[nt], %[tmp]\n\t" \
  "s_add_i32 %[tmp], %[tmp], %[thr]\n\t" \
  "s_cmp_gt_i32 %[tmp], 0\n\t" \
  "s_setprio 0\n\t" \
  "s_cbranch_scc0 3f\n\t" \
  "s_setprio 1\n\t" \
  "3:\n\t" \
  "v_mfma_f32_32x32x16_bf16 v[116:131], v[92:95], v[164:167], v[116:131]\n\t" \
  "v_mul_f32 v196, 0xbfb8aa3b, v112\n\t" \
  "v_mul_f32 v197, 0xbfb8aa3b, v113\n\t" \
  "v_mul_f32 v198, 0xbfb8aa3b, v114\n\t" \
  "v_mul_f32 v199, 0xbfb8aa3b, v115\n\t" \
  "v_exp_f32 v196, v196\n\t" \
  "v_exp_f32 v197, v197\n\t" \
  "v_exp_f32 v198, v198\n\t" \
  "v_exp_f32 v199, v199\n\t" \
  "v_mfma_f32_32x32x16_bf16 v[116:131], v[48:51], v[172:175], v[116:131]\n\t" \
  "v_mul_f32 v192, v108, v192\n\t" \
  "v_mul_f32 v193, v109, v193\n\t" \
  "v_mul_f32 v194, v110, v194\n\t" \
  "v_mul_f32 v195, v111, v195\n\t" \
  "s_waitcnt vmcnt(0)\n\t" \
  "ds_read_b128 v[148:151], v234\n\t" \
  "ds_read_b128 v[152:155], v235\n\t" \
  "ds_read_b128 v[156:159], v236\n\t" \
  "ds_read_b128 v[160:163], v237\n\t" \
  "ds_read_b128 v[164:167], v238 offset:4096\n\t" \
  "ds_read_b64 v[224:225], v239 offset:5120\n\t" \
  "ds_write_b128 v243, v[184:187] offset:0\n\t" \
  "v_add_f32 v196, 1.0, v196\n\t" \
  "v_add_f32 v197, 1.0, v197\n\t" \
  "v_add_f32 v198, 1.0, v198\n\t" \
  "v_add_f32 v199, 1.0, v199\n\t" \
  "v_cvt_pk_bf16_f32 v188, v192, v193\n\t" \
  "v_cvt_pk_bf16_f32 v189, v194, v195\n\t" \
  "v_mad_u32_u16 v220, v220, %[c128], v248\n\t" \
  "v_mad_u32_u16 v221, v221, %[c128], v249\n\t" \
  "v_mad_u32_u16 v222, v222, %[c128], v248\n\t" \
  "s_waitcnt lgkmcnt(12)\n\t" \
  "v_mfma_f32_32x32x16_bf16 v[68:83], v[204:207], v[180:183], v[68:83]\n\t" \
  "v_rcp_f32 v196, v196\n\t" \
  "v_rcp_f32 v197, v197\n\t" \
  "v_rcp_f32 v198, v198\n\t" \
  "v_rcp_f32 v199, v199\n\t" \
  "v_mad_u32_u16 v223, v223, %[c128], v249\n\t" \
  "v_or_b32 v226, v226, v250\n\t" \
  "v_mad_u32_u16 v227, v227, 6, v251\n\t" \
  "s_waitcnt lgkmcnt(9)\n\t" \
  "v_mfma_f32_16x16x32_bf16 v[84:87], v[200:203], v[176:179], v[84:87]\n\t" \
  "v_mul_f32 v192, 0xbfb8aa3b, v116\n\t" \
  "v_mul_f32 v193, 0xbfb8aa3b, v117\n\t" \
  "v_mul_f32 v194, 0xbfb8aa3b, v118\n\t" \
  "v_mul_f32 v195, 0xbfb8aa3b, v119\n\t" \
  "v_exp_f32 v192, v192\n\t" \
  "v_exp_f32 v193, v193\n\t" \
  "v_exp_f32 v194, v194\n\t" \
  "v_exp_f32 v195, v195\n\t" \
  "s_waitcnt lgkmcnt(8)\n\t" \
  "v_mfma_f32_16x16x32_bf16 v[84:87], v[208:211], v[180:183], v[84:87]\n\t" \
  "v_mul_f32 v196, v112, v196\n\t" \
  "v_mul_f32 v197, v113, v197\n\t" \
  "v_mul_f32 v198, v114, v198\n\t" \
  "v_mul_f32 v199, v115, v199\n\t" \
  "s_waitcnt lgkmcnt(1)\n\t" \
  "s_cmp_lt_i32 %[nt], 1\n\t" \
  "s_cbranch_scc1 4f\n\t" \
  "s_mov_b32 m0, %[b2]\n\t" \
  "s_nop 0\n\t" \
  "global_load_lds_dwordx4 v226, %[ea]\n\t" \
  "global_load_lds_dword v227, %[pos] offset:1024\n\t" \
  "s_mov_b32 m0, %[b1]\n\t" \
  "s_nop 0\n\t" \
  "global_load_lds_dwordx4 v220, %[h0]\n\t" \
  "global_load_lds_dwordx4 v221, %[h1] offset:1024\n\t" \
  "global_load_lds_dwordx4 v222, %[h2] offset:2048\n\t" \
  "global_load_lds_dwordx4 v223, %[h3] offset:3072\n\t" \
  "4:\n\t" \
  "v_add_f32 v192, 1.0, v192\n\t" \
  "v_add_f32 v193, 1.0, v193\n\t" \
  "v_add_f32 v194, 1.0, v194\n\t" \
  "v_add_f32 v195, 1.0, v195\n\t" \
  "v_cvt_pk_bf16_f32 v190, v196, v197\n\t" \
  "v_cvt_pk_bf16_f32 v191, v198, v199\n\t" \
  "v_lshrrev_b32 v232, 20, v232\n\t" \
  "v_add_u32 v220, %[posd], v232\n\t" \
  "v_add_u32 v221, v240, v232\n\t" \
  "ds_write_b128 v243, v[188:191] offset:16\n\t" \
  "ds_read_b128 v[176:179], v242 offset:5376\n\t" \
  "ds_read_b128 v[180:183], v242 offset:5408\n\t" \
  "ds_write_b16 v233, v255 offset:5376\n\t" \
  "ds_read_b128 v[216:219], v220\n\t" \
  "ds_read_b128 v[172:175], v221 offset:32\n\t" \
  "ds_read_b128 v[168:171], v221\n\t" \
  "ds_read_b64_tr_b16 v[200:201], v244 offset:0\n\t" \
  "ds_read_b64_tr_b16 v[202:203], v244 offset:320\n\t" \
  "ds_read_b64_tr_b16 v[204:205], v244 offset:1280\n\t" \
  "ds_read_b64_tr_b16 v[206:207], v244 offset:1600\n\t" \
  "v_add_u32 v233, v241, v232\n\t" \
  "ds_write_b16_d16_hi v233, v255 offset:5376\n\t" \
  "v_mfma_f32_16x16x32_bf16 v[132:135], v[96:99], v[184:187], 0\n\t" \
  "v_rcp_f32 v192, v192\n\t" \
  "v_rcp_f32 v193, v193\n\t" \
  "v_rcp_f32 v194, v194\n\t" \
  "v_rcp_f32 v195, v195\n\t" \
  "v_mfma_f32_32x32x16_bf16 v[100:115], v[4:7], v[148:151], 0\n\t" \
  "v_mul_f32 v196, 0xbfb8aa3b, v120\n\t" \
  "v_mul_f32 v197, 0xbfb8aa3b, v121\n\t" \
  "v_mul_f32 v198, 0xbfb8aa3b, v122\n\t" \
  "v_mul_f32 v199, 0xbfb8aa3b, v123\n\t" \
  "v_exp_f32 v196, v196\n\t" \
  "v_exp_f32 v197, v197\n\t" \
  "v_exp_f32 v198, v198\n\t" \
  "v_exp_f32 v199, v199\n\t" \
  "v_mfma_f32_32x32x16_bf16 v[100:115], v[12:15], v[152:155], v[100:115]\n\t" \
  "v_mul_f32 v192, v116, v192\n\t" \
  "v_mul_f32 v193, v117, v193\n\t" \
  "v_mul_f32 v194, v118, v194\n\t" \
  "v_mul_f32 v195, v119, v195\n\t" \
  "v_add_f32 v196, 1.0, v196\n\t" \
  "v_add_f32 v197, 1.0, v197\n\t" \
  "v_add_f32 v198, 1.0, v198\n\t" \
  "v_add_f32 v199, 1.0, v199\n\t" \
  "v_cvt_pk_bf16_f32 v184, v192, v193\n\t" \
  "v_cvt_pk_bf16_f32 v185, v194, v195\n\t" \
  "v_and_b32 v225, 0xffff0000, v225\n\t" \
  "s_waitcnt lgkmcnt(7)\n\t" \
  "v_lshlrev_b32 v219, 16, v224\n\t" \
  "v_and_b32 v224, 0xffff0000, v224\n\t" \
  "v_sub_f32 v229, v218, v225\n\t" \
  "v_sub_f32 v216, v216, v219\n\t" \
  "v_sub_f32 v217, v217, v224\n\t" \
  "v_mfma_f32_32x32x16_bf16 v[100:115], v[20:23], v[156:159], v[100:115]\n\t" \
  "v_rcp_f32 v196, v196\n\t" \
  "v_rcp_f32 v197, v197\n\t" \
  "v_rcp_f32 v198, v198\n\t" \
  "v_rcp_f32 v199, v199\n\t" \
  "v_mul_f32 v218, v229, v229\n\t" \
  "v_cndmask_b32 v228, v216, v217, vcc\n\t" \
  "v_fmac_f32 v218, v216, v216\n\t" \
  "v_fmac_f32 v218, v217, v217\n\t" \
  "v_cvt_pk_bf16_f32 v218, v218, v218\n\t" \
  "s_waitcnt lgkmcnt(6)\n\t" \
  "v_cndmask_b32_sdwa v175, v175, v218, vcc dst_sel:WORD_1 dst_unused:UNUSED_PRESERVE src0_sel:WORD_1 src1_sel:WORD_1\n\t" \
  "v_mfma_f32_16x16x32_bf16 v[132:135], v[136:139], v[188:191], v[132:135]\n\t" \
  "v_mul_f32 v192, 0xbfb8aa3b, v124\n\t" \
  "v_mul_f32 v193, 0xbfb8aa3b, v125\n\t" \
  "v_mul_f32 v194, 0xbfb8aa3b, v126\n\t" \
  "v_mul_f32 v195, 0xbfb8aa3b, v127\n\t" \
  "v_exp_f32 v192, v192\n\t" \
  "v_exp_f32 v193, v193\n\t" \
  "v_exp_f32 v194, v194\n\t" \
  "v_exp_f32 v195, v195\n\t" \
  "ds_read2_b32 v[220:221], v252 offset0:0 offset1:2\n\t" \
  "ds_read2_b32 v[222:223], v252 offset0:4 offset1:6\n\t" \
  "ds_read_b64 v[226:227], v253\n\t" \
  "ds_read_b32 v232, v254\n\t" \
  "v_mfma_f32_32x32x16_bf16 v[100:115], v[28:31], v[160:163], v[100:115]\n\t" \
  "v_mul_f32 v196, v120, v196\n\t" \
  "v_mul_f32 v197, v121, v197\n\t" \
  "v_mul_f32 v198, v122, v198\n\t" \
  "v_mul_f32 v199, v123, v199\n\t" \
  "v_add_u32 v252, 0x100, v252\n\t" \
  "v_add_u32 v253, 0x100, v253\n\t" \
  "v_add_u32 v254, 0x100, v254\n\t" \
  "v_add_f32 v192, 1.0, v192\n\t" \
  "v_add_f32 v193, 1.0, v193\n\t" \
  "v_add_f32 v194, 1.0, v194\n\t" \
  "v_add_f32 v195, 1.0, v195\n\t" \
  "v_cvt_pk_bf16_f32 v186, v196, v197\n\t" \
  "v_cvt_pk_bf16_f32 v187, v198, v199\n\t" \
  "s_waitcnt lgkmcnt(9)\n\t" \
  "v_mfma_f32_32x32x16_bf16 v[100:115], v[36:39], v[168:171], v[100:115]\n\t" \
  "v_rcp_f32 v192, v192\n\t" \
  "v_rcp_f32 v193, v193\n\t" \
  "v_rcp_f32 v194, v194\n\t" \
  "v_rcp_f32 v195, v195\n\t" \
  "v_mfma_f32_32x32x16_bf16 v[100:115], v[44:47], v[172:175], v[100:115]\n\t" \
  "v_mul_f32 v196, 0xbfb8aa3b, v128\n\t" \
  "v_mul_f32 v197, 0xbfb8aa3b, v129\n\t" \
  "v_mul_f32 v198, 0xbfb8aa3b, v130\n\t" \
  "v_mul_f32 v199, 0xbfb8aa3b, v131\n\t" \
  "v_exp_f32 v196, v196\n\t" \
  "v_exp_f32 v197, v197\n\t" \
  "v_exp_f32 v198, v198\n\t" \
  "v_exp_f32 v199, v199\n\t" \
  "v_mfma_f32_32x32x16_bf16 v[100:115], v[88:91], v[164:167], v[100:115]\n\t" \
  "v_mul_f32 v192, v124, v192\n\t" \
  "v_mul_f32 v193, v125, v193\n\t" \
  "v_mul_f32 v194, v126, v194\n\t" \
  "v_mul_f32 v195, v127, v195\n\t" \
  "ds_write_b128 v243, v[184:187] offset:0\n\t" \
  "v_add_f32 v196, 1.0, v196\n\t" \
  "v_add_f32 v197, 1.0, v197\n\t" \
  "v_add_f32 v198, 1.0, v198\n\t" \
  "v_add_f32 v199, 1.0, v199\n\t" \
  "v_cvt_pk_bf16_f32 v188, v192, v193\n\t" \
  "v_cvt_pk_bf16_f32 v189, v194, v195\n\t" \
  "s_waitcnt lgkmcnt(8)\n\t" \
  "v_mfma_f32_32x32x16_bf16 v[52:67], v[200:203], v[176:179], v[52:67]\n\t" \
  "v_rcp_f32 v196, v196\n\t" \
  "v_rcp_f32 v197, v197\n\t" \
  "v_rcp_f32 v198, v198\n\t" \
  "v_rcp_f32 v199, v199\n\t" \
  "s_waitcnt lgkmcnt(6)\n\t" \
  "v_mfma_f32_32x32x16_bf16 v[52:67], v[204:207], v[180:183], v[52:67]\n\t" \
  "v_mul_f32 v192, 0xbfb8aa3b, v100\n\t" \
  "v_mul_f32 v193, 0xbfb8aa3b, v101\n\t" \
  "v_mul_f32 v194, 0xbfb8aa3b, v102\n\t" \
  "v_mul_f32 v195, 0xbfb8aa3b, v103\n\t" \
  "v_exp_f32 v192, v192\n\t" \
  "v_exp_f32 v193, v193\n\t" \
  "v_exp_f32 v194, v194\n\t" \
  "v_exp_f32 v195, v195\n\t" \
  "s_cmp_eq_u32 %[nt], 0\n\t" \
  "v_mfma_f32_16x16x32_bf16 v[132:135], v[140:143], v[184:187], v[132:135]\n\t" \
  "v_mul_f32 v196, v128, v196\n\t" \
  "v_mul_f32 v197, v129, v197\n\t" \
  "v_mul_f32 v198, v130, v198\n\t" \
  "v_mul_f32 v199, v131, v199\n\t" \
  "v_add_f32 v192, 1.0, v192\n\t" \
  "v_add_f32 v193, 1.0, v193\n\t" \
  "v_add_f32 v194, 1.0, v194\n\t" \
  "v_add_f32 v195, 1.0, v195\n\t" \
  "v_cvt_pk_bf16_f32 v190, v196, v197\n\t" \
  "v_cvt_pk_bf16_f32 v191, v198, v199\n\t" \
  "s_cbranch_scc0 1b\n\t" \
  "s_waitcnt lgkmcnt(0)\n\t" \
  "s_cmp_lg_u32 %[last], 0\n\t" \
  "s_cbranch_scc1 8f\n\t" \
  "ds_write_b128 v243, v[188:191] offset:16\n\t" \
  "ds_read_b64_tr_b16 v[200:201], v244 offset:0\n\t" \
  "ds_read_b64_tr_b16 v[202:203], v244 offset:320\n\t" \
  "ds_read_b64_tr_b16 v[204:205], v244 offset:1280\n\t" \
  "ds_read_b64_tr_b16 v[206:207], v244 offset:1600\n\t" \
  "v_mfma_f32_16x16x32_bf16 v[132:135], v[144:147], v[188:191], v[132:135]\n\t" \
  "s_waitcnt lgkmcnt(0)\n\t" \
  "v_mfma_f32_32x32x16_bf16 v[68:83], v[200:203], v[176:179], v[68:83]\n\t" \
  "v_mfma_f32_32x32x16_bf16 v[68:83], v[204:207], v[180:183], v[68:83]\n\t" \
  "s_nop 8\n\t" \
  "v_mul_f32 v230, v230, v132\n\t" \
  "v_mul_f32 v231, v231, v132\n\t" \
  "v_cvt_pk_bf16_f32 v230, v230, v231\n\t" \
  "ds_write_b16 v245, v230 offset:7456\n\t" \
  "ds_write_b16_d16_hi v241, v230 offset:7584\n\t" \
  "ds_read_b128 v[200:203], v246 offset:7456\n\t" \
  "ds_read_b128 v[208:211], v246 offset:7488\n\t" \
  "s_waitcnt lgkmcnt(0)\n\t" \
  "v_mfma_f32_16x16x32_bf16 v[84:87], v[200:203], v[176:179], v[84:87]\n\t" \
  "v_mfma_f32_16x16x32_bf16 v[84:87], v[208:211], v[180:183], v[84:87]\n\t" \
  "s_branch 9f\n\t" \
  "6:\n\t" \
  "s_cmp_lg_u32 %[last], 0\n\t" \
  "s_cbranch_scc0 9f\n\t" \
  "s_waitcnt lgkmcnt(0)\n\t" \
  "v_cndmask_b32 v224, 0, 24, vcc\n\t" \
  "v_xor_b32 v220, v234, v224\n\t" \
  "v_xor_b32 v221, v235, v224\n\t" \
  "v_xor_b32 v222, v236, v224\n\t" \
  "v_xor_b32 v223, v237, v224\n\t" \
  "v_xor_b32 v216, 16, v220\n\t" \
  "v_xor_b32 v217, 16, v221\n\t" \
  "v_xor_b32 v218, 16, v222\n\t" \
  "v_xor_b32 v219, 16, v223\n\t" \
  "ds_read_b64 v[148:149], v220\n\t" \
  "ds_read_b64 v[150:151], v216\n\t" \
  "ds_read_b64 v[152:153], v221\n\t" \
  "ds_read_b64 v[154:155], v217\n\t" \
  "ds_read_b64 v[156:157], v222\n\t" \
  "ds_read_b64 v[158:159], v218\n\t" \
  "ds_read_b64 v[160:161], v223\n\t" \
  "ds_read_b64 v[162:163], v219\n\t" \
  "ds_read_b128 v[4:7], v247 offset:6144\n\t" \
  "ds_read_b128 v[8:11], v247 offset:7168\n\t" \
  "ds_read_b128 v[12:15], v247 offset:8192\n\t" \
  "ds_read_b128 v[16:19], v247 offset:9216\n\t" \
  "ds_read_b128 v[20:23], v247 offset:10240\n\t" \
  "ds_read_b128 v[24:27], v247 offset:11264\n\t" \
  "v_subrev_u32 v225, %[b1], v241\n\t" \
  "v_lshl_add_u32 v225, v225, 5, v243\n\t" \
  "v_sub_u32 v225, v225, v224\n\t" \
  "s_nop 4\n\t" \
  "v_cvt_pk_bf16_f32 v184, v52, v53\n\t" \
  "v_cvt_pk_bf16_f32 v185, v54, v55\n\t" \
  "v_cvt_pk_bf16_f32 v186, v56, v57\n\t" \
  "v_cvt_pk_bf16_f32 v187, v58, v59\n\t" \
  "v_cvt_pk_bf16_f32 v188, v60, v61\n\t" \
  "v_cvt_pk_bf16_f32 v189, v62, v63\n\t" \
  "v_cvt_pk_bf16_f32 v190, v64, v65\n\t" \
  "v_cvt_pk_bf16_f32 v191, v66, v67\n\t" \
  "v_cvt_pk_bf16_f32 v36, v68, v69\n\t" \
  "v_cvt_pk_bf16_f32 v37, v70, v71\n\t" \
  "v_cvt_pk_bf16_f32 v38, v72, v73\n\t" \
  "v_cvt_pk_bf16_f32 v39, v74, v75\n\t" \
  "v_cvt_pk_bf16_f32 v40, v76, v77\n\t" \
  "v_cvt_pk_bf16_f32 v41, v78, v79\n\t" \
  "v_cvt_pk_bf16_f32 v42, v80, v81\n\t" \
  "v_cvt_pk_bf16_f32 v43, v82, v83\n\t" \
  "s_waitcnt lgkmcnt(5)\n\t" \
  "v_mfma_f32_32x32x16_bf16 v[100:115], v[4:7], v[148:151], 0\n\t" \
  "s_waitcnt lgkmcnt(4)\n\t" \
  "v_mfma_f32_32x32x16_bf16 v[116:131], v[8:11], v[148:151], 0\n\t" \
  "ds_read_b128 v[28:31], v247 offset:12288\n\t" \
  "ds_read_b128 v[32:35], v247 offset:13312\n\t" \
  "s_waitcnt lgkmcnt(5)\n\t" \
  "v_mfma_f32_32x32x16_bf16 v[100:115], v[12:15], v[152:155], v[100:115]\n\t" \
  "s_waitcnt lgkmcnt(4)\n\t" \
  "v_mfma_f32_32x32x16_bf16 v[116:131], v[16:19], v[152:155], v[116:131]\n\t" \
  "ds_read_b128 v[4:7], v247 offset:14336\n\t" \
  "ds_read_b128 v[8:11], v247 offset:15360\n\t" \
  "s_waitcnt lgkmcnt(5)\n\t" \
  "v_mfma_f32_32x32x16_bf16 v[100:115], v[20:23], v[156:159], v[100:115]\n\t" \
  "s_waitcnt lgkmcnt(4)\n\t" \
  "v_mfma_f32_32x32x16_bf16 v[116:131], v[24:27], v[156:159], v[116:131]\n\t" \
  "ds_read_b128 v[12:15], v247 offset:16384\n\t" \
  "ds_read_b128 v[16:19], v247 offset:17408\n\t" \
  "s_waitcnt lgkmcnt(5)\n\t" \
  "v_mfma_f32_32x32x16_bf16 v[100:115], v[28:31], v[160:163], v[100:115]\n\t" \
  "s_waitcnt lgkmcnt(4)\n\t" \
  "v_mfma_f32_32x32x16_bf16 v[116:131], v[32:35], v[160:163], v[116:131]\n\t" \
  "ds_read_b128 v[20:23], v247 offset:18432\n\t" \
  "ds_read_b128 v[24:27], v247 offset:19456\n\t" \
  "s_waitcnt lgkmcnt(5)\n\t" \
  "v_mfma_f32_32x32x16_bf16 v[100:115], v[4:7], v[184:187], v[100:115]\n\t" \
  "ds_read_b128 v[28:31], v247 offset:20480\n\t" \
  "ds_read_b128 v[32:35], v247 offset:21504\n\t" \
  "s_waitcnt lgkmcnt(5)\n\t" \
  "v_mfma_f32_32x32x16_bf16 v[100:115], v[12:15], v[188:191], v[100:115]\n\t" \
  "s_waitcnt lgkmcnt(3)\n\t" \
  "v_mfma_f32_32x32x16_bf16 v[100:115], v[20:23], v[36:39], v[100:115]\n\t" \
  "s_waitcnt lgkmcnt(1)\n\t" \
  "v_mfma_f32_32x32x16_bf16 v[100:115], v[28:31], v[40:43], v[100:115]\n\t" \
  "s_waitcnt lgkmcnt(0)\n\t" \
  "s_branch 10f\n\t" \
  "8:\n\t" \
  "ds_write_b128 v243, v[188:191] offset:16\n\t" \
  "ds_read_b64_tr_b16 v[200:201], v244 offset:0\n\t" \
  "ds_read_b64_tr_b16 v[202:203], v244 offset:320\n\t" \
  "ds_read_b64_tr_b16 v[204:205], v244 offset:1280\n\t" \
  "ds_read_b64_tr_b16 v[206:207], v244 offset:1600\n\t" \
  "v_mfma_f32_16x16x32_bf16 v[132:135], v[144:147], v[188:191], v[132:135]\n\t" \
  "v_cndmask_b32 v224, 0, 24, vcc\n\t" \
  "v_xor_b32 v220, v234, v224\n\t" \
  "v_xor_b32 v221, v235, v224\n\t" \
  "v_xor_b32 v222, v236, v224\n\t" \
  "v_xor_b32 v223, v237, v224\n\t" \
  "v_xor_b32 v216, 16, v220\n\t" \
  "v_xor_b32 v217, 16, v221\n\t" \
  "v_xor_b32 v218, 16, v222\n\t" \
  "v_xor_b32 v219, 16, v223\n\t" \
  "ds_read_b64 v[148:149], v220\n\t" \
  "ds_read_b64 v[150:151], v216\n\t" \
  "ds_read_b64 v[152:153], v221\n\t" \
  "ds_read_b64 v[154:155], v217\n\t" \
  "ds_read_b64 v[156:157], v222\n\t" \
  "ds_read_b64 v[158:159], v218\n\t" \
  "ds_read_b64 v[160:161], v223\n\t" \
  "ds_read_b64 v[162:163], v219\n\t" \
  "ds_read_b128 v[4:7], v247 offset:6144\n\t" \
  "ds_read_b128 v[8:11], v247 offset:7168\n\t" \
  "s_waitcnt lgkmcnt(11)\n\t" \
  "ds_read_b128 v[12:15], v247 offset:8192\n\t" \
  "ds_read_b128 v[16:19], v247 offset:9216\n\t" \
  "ds_read_b128 v[20:23], v247 offset:10240\n\t" \
  "ds_read_b128 v[24:27], v247 offset:11264\n\t" \
  "v_subrev_u32 v225, %[b1], v241\n\t" \
  "v_lshl_add_u32 v225, v225, 5, v243\n\t" \
  "v_sub_u32 v225, v225, v224\n\t" \
  "v_mfma_f32_32x32x16_bf16 v[68:83], v[200:203], v[176:179], v[68:83]\n\t" \
  "s_waitcnt lgkmcnt(5)\n\t" \
  "v_mfma_f32_32x32x16_bf16 v[100:115], v[4:7], v[148:151], 0\n\t" \
  "v_mfma_f32_32x32x16_bf16 v[68:83], v[204:207], v[180:183], v[68:83]\n\t" \
  "s_waitcnt lgkmcnt(4)\n\t" \
  "v_mfma_f32_32x32x16_bf16 v[116:131], v[8:11], v[148:151], 0\n\t" \
  "ds_read_b128 v[28:31], v247 offset:12288\n\t" \
  "ds_read_b128 v[32:35], v247 offset:13312\n\t" \
  "s_waitcnt lgkmcnt(5)\n\t" \
  "v_mfma_f32_32x32x16_bf16 v[100:115], v[12:15], v[152:155], v[100:115]\n\t" \
  "v_mul_f32 v230, v230, v132\n\t" \
  "v_mul_f32 v231, v231, v132\n\t" \
  "v_cvt_pk_bf16_f32 v230, v230, v231\n\t" \
  "ds_write_b16 v245, v230 offset:7456\n\t" \
  "ds_write_b16_d16_hi v241, v230 offset:7584\n\t" \
  "ds_read_b128 v[200:203], v246 offset:7456\n\t" \
  "ds_read_b128 v[208:211], v246 offset:7488\n\t" \
  "v_cvt_pk_bf16_f32 v184, v52, v53\n\t" \
  "v_cvt_pk_bf16_f32 v185, v54, v55\n\t" \
  "v_cvt_pk_bf16_f32 v186, v56, v57\n\t" \
  "v_cvt_pk_bf16_f32 v187, v58, v59\n\t" \
  "s_waitcnt lgkmcnt(8)\n\t" \
  "v_mfma_f32_32x32x16_bf16 v[116:131], v[16:19], v[152:155], v[116:131]\n\t" \
  "ds_read_b128 v[4:7], v247 offset:14336\n\t" \
  "ds_read_b128 v[8:11], v247 offset:15360\n\t" \
  "s_waitcnt lgkmcnt(9)\n\t" \
  "v_mfma_f32_32x32x16_bf16 v[100:115], v[20:23], v[156:159], v[100:115]\n\t" \
  "v_cvt_pk_bf16_f32 v188, v60, v61\n\t" \
  "v_cvt_pk_bf16_f32 v189, v62, v63\n\t" \
  "v_cvt_pk_bf16_f32 v190, v64, v65\n\t" \
  "v_cvt_pk_bf16_f32 v191, v66, v67\n\t" \
  "s_waitcnt lgkmcnt(8)\n\t" \
  "v_mfma_f32_32x32x16_bf16 v[116:131], v[24:27], v[156:159], v[116:131]\n\t" \
  "ds_read_b128 v[12:15], v247 offset:16384\n\t" \
  "ds_read_b128 v[16:19], v247 offset:17408\n\t" \
  "s_waitcnt lgkmcnt(9)\n\t" \
  "v_mfma_f32_32x32x16_bf16 v[100:115], v[28:31], v[160:163], v[100:115]\n\t" \
  "v_cvt_pk_bf16_f32 v36, v68, v69\n\t" \
  "v_cvt_pk_bf16_f32 v37, v70, v71\n\t" \
  "v_cvt_pk_bf16_f32 v38, v72, v73\n\t" \
  "v_cvt_pk_bf16_f32 v39, v74, v75\n\t" \
  "s_waitcnt lgkmcnt(5)\n\t" \
  "v_mfma_f32_16x16x32_bf16 v[84:87], v[200:203], v[176:179], v[84:87]\n\t" \
  "v_cvt_pk_bf16_f32 v40, v76, v77\n\t" \
  "v_cvt_pk_bf16_f32 v41, v78, v79\n\t" \
  "v_cvt_pk_bf16_f32 v42, v80, v81\n\t" \
  "v_cvt_pk_bf16_f32 v43, v82, v83\n\t" \
  "v_mfma_f32_32x32x16_bf16 v[116:131], v[32:35], v[160:163], v[116:131]\n\t" \
  "ds_read_b128 v[20:23], v247 offset:18432\n\t" \
  "ds_read_b128 v[24:27], v247 offset:19456\n\t" \
  "s_waitcnt lgkmcnt(6)\n\t" \
  "v_mfma_f32_16x16x32_bf16 v[84:87], v[208:211], v[180:183], v[84:87]\n\t" \
  "s_waitcnt lgkmcnt(5)\n\t" \
  "v_mfma_f32_32x32x16_bf16 v[100:115], v[4:7], v[184:187], v[100:115]\n\t" \
  "ds_read_b128 v[28:31], v247 offset:20480\n\t" \
  "ds_read_b128 v[32:35], v247 offset:21504\n\t" \
  "s_waitcnt lgkmcnt(5)\n\t" \
  "v_mfma_f32_32x32x16_bf16 v[100:115], v[12:15], v[188:191], v[100:115]\n\t" \
  "s_waitcnt lgkmcnt(3)\n\t" \
  "v_mfma_f32_32x32x16_bf16 v[100:115], v[20:23], v[36:39], v[100:115]\n\t" \
  "s_waitcnt lgkmcnt(1)\n\t" \
  "v_mfma_f32_32x32x16_bf16 v[100:115], v[28:31], v[40:43], v[100:115]\n\t" \
  "s_waitcnt lgkmcnt(0)\n\t" \
  "10:\n\t" \
  "v_mov_b32 v212, 0xbfb8aa3b\n\t" \
  "v_mfma_f32_32x32x16_bf16 v[116:131], v[8:11], v[184:187], v[116:131]\n\t" \
  "v_mfma_f32_32x32x16_bf16 v[116:131], v[16:19], v[188:191], v[116:131]\n\t" \
  "s_nop 8\n\t" \
  "v_mul_f32 v192, 0xbfb8aa3b, v100\n\t" \
  "v_mul_f32 v193, 0xbfb8aa3b, v101\n\t" \
  "v_mul_f32 v194, 0xbfb8aa3b, v102\n\t" \
  "v_mul_f32 v195, 0xbfb8aa3b, v103\n\t" \
  "v_exp_f32 v192, v192\n\t" \
  "v_exp_f32 v193, v193\n\t" \
  "v_exp_f32 v194, v194\n\t" \
  "v_exp_f32 v195, v195\n\t" \
  "v_lshlrev_b32 v200, 16, v148\n\t" \
  "v_and_b32 v201, 0xffff0000, v148\n\t" \
  "v_lshlrev_b32 v202, 16, v149\n\t" \
  "v_and_b32 v203, 0xffff0000, v149\n\t" \
  "v_mfma_f32_32x32x16_bf16 v[116:131], v[24:27], v[36:39], v[116:131]\n\t" \
  "v_add_f32 v192, 1.0, v192\n\t" \
  "v_add_f32 v193, 1.0, v193\n\t" \
  "v_add_f32 v194, 1.0, v194\n\t" \
  "v_add_f32 v195, 1.0, v195\n\t" \
  "v_rcp_f32 v192, v192\n\t" \
  "v_rcp_f32 v193, v193\n\t" \
  "v_rcp_f32 v194, v194\n\t" \
  "v_rcp_f32 v195, v195\n\t" \
  "v_mfma_f32_32x32x16_bf16 v[116:131], v[32:35], v[40:43], v[116:131]\n\t" \
  "v_mul_f32 v196, 0xbfb8aa3b, v104\n\t" \
  "v_mul_f32 v197, 0xbfb8aa3b, v105\n\t" \
  "v_mul_f32 v198, 0xbfb8aa3b, v106\n\t" \
  "v_mul_f32 v199, 0xbfb8aa3b, v107\n\t" \
  "v_exp_f32 v196, v196\n\t" \
  "v_exp_f32 v197, v197\n\t" \
  "v_exp_f32 v198, v198\n\t" \
  "v_exp_f32 v199, v199\n\t" \
  "v_fmac_f32 v200, v100, v192\n\t" \
  "v_fmac_f32 v201, v101, v193\n\t" \
  "v_fmac_f32 v202, v102, v194\n\t" \
  "v_fmac_f32 v203, v103, v195\n\t" \
  "v_lshlrev_b32 v204, 16, v150\n\t" \
  "v_and_b32 v205, 0xffff0000, v150\n\t" \
  "v_lshlrev_b32 v206, 16, v151\n\t" \
  "v_and_b32 v207, 0xffff0000, v151\n\t" \
  "v_add_f32 v196, 1.0, v196\n\t" \
  "v_add_f32 v197, 1.0, v197\n\t" \
  "v_add_f32 v198, 1.0, v198\n\t" \
  "v_add_f32 v199, 1.0, v199\n\t" \
  "v_cvt_pk_bf16_f32 v176, v200, v201\n\t" \
  "v_cvt_pk_bf16_f32 v177, v202, v203\n\t" \
  "ds_write_b64 v225, v[176:177] offset:0\n\t" \
  "v_rcp_f32 v196, v196\n\t" \
  "v_rcp_f32 v197, v197\n\t" \
  "v_rcp_f32 v198, v198\n\t" \
  "v_rcp_f32 v199, v199\n\t" \
  "v_mul_f32 v192, 0xbfb8aa3b, v108\n\t" \
  "v_mul_f32 v193, 0xbfb8aa3b, v109\n\t" \
  "v_mul_f32 v194, 0xbfb8aa3b, v110\n\t" \
  "v_mul_f32 v195, 0xbfb8aa3b, v111\n\t" \
  "v_exp_f32 v192, v192\n\t" \
  "v_exp_f32 v193, v193\n\t" \
  "v_exp_f32 v194, v194\n\t" \
  "v_exp_f32 v195, v195\n\t" \
  "v_fmac_f32 v204, v104, v196\n\t" \
  "v_fmac_f32 v205, v105, v197\n\t" \
  "v_fmac_f32 v206, v106, v198\n\t" \
  "v_fmac_f32 v207, v107, v199\n\t" \
  "v_lshlrev_b32 v200, 16, v152\n\t" \
  "v_and_b32 v201, 0xffff0000, v152\n\t" \
  "v_lshlrev_b32 v202, 16, v153\n\t" \
  "v_and_b32 v203, 0xffff0000, v153\n\t" \
  "v_add_f32 v192, 1.0, v192\n\t" \
  "v_add_f32 v193, 1.0, v193\n\t" \
  "v_add_f32 v194, 1.0, v194\n\t" \
  "v_add_f32 v195, 1.0, v195\n\t" \
  "v_cvt_pk_bf16_f32 v178, v204, v205\n\t" \
  "v_cvt_pk_bf16_f32 v179, v206, v207\n\t" \
  "ds_write_b64 v225, v[178:179] offset:16\n\t" \
  "v_rcp_f32 v192, v192\n\t" \
  "v_rcp_f32 v193, v193\n\t" \
  "v_rcp_f32 v194, v194\n\t" \
  "v_rcp_f32 v195, v195\n\t" \
  "v_mul_f32 v196, 0xbfb8aa3b, v112\n\t" \
  "v_mul_f32 v197, 0xbfb8aa3b, v113\n\t" \
  "v_mul_f32 v198, 0xbfb8aa3b, v114\n\t" \
  "v_mul_f32 v199, 0xbfb8aa3b, v115\n\t" \
  "v_exp_f32 v196, v196\n\t" \
  "v_exp_f32 v197, v197\n\t" \
  "v_exp_f32 v198, v198\n\t" \
  "v_exp_f32 v199, v199\n\t" \
  "v_fmac_f32 v200, v108, v192\n\t" \
  "v_fmac_f32 v201, v109, v193\n\t" \
  "v_fmac_f32 v202, v110, v194\n\t" \
  "v_fmac_f32 v203, v111, v195\n\t" \
  "v_lshlrev_b32 v204, 16, v154\n\t" \
  "v_and_b32 v205, 0xffff0000, v154\n\t" \
  "v_lshlrev_b32 v206, 16, v155\n\t" \
  "v_and_b32 v207, 0xffff0000, v155\n\t" \
  "v_add_f32 v196, 1.0, v196\n\t" \
  "v_add_f32 v197, 1.0, v197\n\t" \
  "v_add_f32 v198, 1.0, v198\n\t" \
  "v_add_f32 v199, 1.0, v199\n\t" \
  "v_cvt_pk_bf16_f32 v180, v200, v201\n\t" \
  "v_cvt_pk_bf16_f32 v181, v202, v203\n\t" \
  "ds_write_b64 v225, v[180:181] offset:32\n\t" \
  "v_rcp_f32 v196, v196\n\t" \
  "v_rcp_f32 v197, v197\n\t" \
  "v_rcp_f32 v198, v198\n\t" \
  "v_rcp_f32 v199, v199\n\t" \
  "v_mul_f32 v192, 0xbfb8aa3b, v116\n\t" \
  "v_mul_f32 v193, 0xbfb8aa3b, v117\n\t" \
  "v_mul_f32 v194, 0xbfb8aa3b, v118\n\t" \
  "v_mul_f32 v195, 0xbfb8aa3b, v119\n\t" \
  "v_exp_f32 v192, v192\n\t" \
  "v_exp_f32 v193, v193\n\t" \
  "v_exp_f32 v194, v194\n\t" \
  "v_exp_f32 v195, v195\n\t" \
  "v_fmac_f32 v204, v112, v196\n\t" \
  "v_fmac_f32 v205, v113, v197\n\t" \
  "v_fmac_f32 v206, v114, v198\n\t" \
  "v_fmac_f32 v207, v115, v199\n\t" \
  "v_lshlrev_b32 v200, 16, v156\n\t" \
  "v_and_b32 v201, 0xffff0000, v156\n\t" \
  "v_lshlrev_b32 v202, 16, v157\n\t" \
  "v_and_b32 v203, 0xffff0000, v157\n\t" \
  "v_add_f32 v192, 1.0, v192\n\t" \
  "v_add_f32 v193, 1.0, v193\n\t" \
  "v_add_f32 v194, 1.0, v194\n\t" \
  "v_add_f32 v195, 1.0, v195\n\t" \
  "v_cvt_pk_bf16_f32 v182, v204, v205\n\t" \
  "v_cvt_pk_bf16_f32 v183, v206, v207\n\t" \
  "ds_write_b64 v225, v[182:183] offset:48\n\t" \
  "v_rcp_f32 v192, v192\n\t" \
  "v_rcp_f32 v193, v193\n\t" \
  "v_rcp_f32 v194, v194\n\t" \
  "v_rcp_f32 v195, v195\n\t" \
  "v_mul_f32 v196, 0xbfb8aa3b, v120\n\t" \
  "v_mul_f32 v197, 0xbfb8aa3b, v121\n\t" \
  "v_mul_f32 v198, 0xbfb8aa3b, v122\n\t" \
  "v_mul_f32 v199, 0xbfb8aa3b, v123\n\t" \
  "v_exp_f32 v196, v196\n\t" \
  "v_exp_f32 v197, v197\n\t" \
  "v_exp_f32 v198, v198\n\t" \
  "v_exp_f32 v199, v199\n\t" \
  "v_fmac_f32 v200, v116, v192\n\t" \
  "v_fmac_f32 v201, v117, v193\n\t" \
  "v_fmac_f32 v202, v118, v194\n\t" \
  "v_fmac_f32 v203, v119, v195\n\t" \
  "v_lshlrev_b32 v204, 16, v158\n\t" \
  "v_and_b32 v205, 0xffff0000, v158\n\t" \
  "v_lshlrev_b32 v206, 16, v159\n\t" \
  "v_and_b32 v207, 0xffff0000, v159\n\t" \
  "v_add_f32 v196, 1.0, v196\n\t" \
  "v_add_f32 v197, 1.0, v197\n\t" \
  "v_add_f32 v198, 1.0, v198\n\t" \
  "v_add_f32 v199, 1.0, v199\n\t" \
  "v_cvt_pk_bf16_f32 v176, v200, v201\n\t" \
  "v_cvt_pk_bf16_f32 v177, v202, v203\n\t" \
  "ds_write_b64 v225, v[176:177] offset:64\n\t" \
  "v_rcp_f32 v196, v196\n\t" \
  "v_rcp_f32 v197, v197\n\t" \
  "v_rcp_f32 v198, v198\n\t" \
  "v_rcp_f32 v199, v199\n\t" \
  "v_mul_f32 v192, 0xbfb8aa3b, v124\n\t" \
  "v_mul_f32 v193, 0xbfb8aa3b, v125\n\t" \
  "v_mul_f32 v194, 0xbfb8aa3b, v126\n\t" \
  "v_mul_f32 v195, 0xbfb8aa3b, v127\n\t" \
  "v_exp_f32 v192, v192\n\t" \
  "v_exp_f32 v193, v193\n\t" \
  "v_exp_f32 v194, v194\n\t" \
  "v_exp_f32 v195, v195\n\t" \
  "v_fmac_f32 v204, v120, v196\n\t" \
  "v_fmac_f32 v205, v121, v197\n\t" \
  "v_fmac_f32 v206, v122, v198\n\t" \
  "v_fmac_f32 v207, v123, v199\n\t" \
  "v_lshlrev_b32 v200, 16, v160\n\t" \
  "v_and_b32 v201, 0xffff0000, v160\n\t" \
  "v_lshlrev_b32 v202, 16, v161\n\t" \
  "v_and_b32 v203, 0xffff0000, v161\n\t" \
  "v_add_f32 v192, 1.0, v192\n\t" \
  "v_add_f32 v193, 1.0, v193\n\t" \
  "v_add_f32 v194, 1.0, v194\n\t" \
  "v_add_f32 v195, 1.0, v195\n\t" \
  "v_cvt_pk_bf16_f32 v178, v204, v205\n\t" \
  "v_cvt_pk_bf16_f32 v179, v206, v207\n\t" \
  "ds_write_b64 v225, v[178:179] offset:80\n\t" \
  "v_rcp_f32 v192, v192\n\t" \
  "v_rcp_f32 v193, v193\n\t" \
  "v_rcp_f32 v194, v194\n\t" \
  "v_rcp_f32 v195, v195\n\t" \
  "v_mul_f32 v196, 0xbfb8aa3b, v128\n\t" \
  "v_mul_f32 v197, 0xbfb8aa3b, v129\n\t" \
  "v_mul_f32 v198, 0xbfb8aa3b, v130\n\t" \
  "v_mul_f32 v199, 0xbfb8aa3b, v131\n\t" \
  "v_exp_f32 v196, v196\n\t" \
  "v_exp_f32 v197, v197\n\t" \
  "v_exp_f32 v198, v198\n\t" \
  "v_exp_f32 v199, v199\n\t" \
  "v_fmac_f32 v200, v124, v192\n\t" \
  "v_fmac_f32 v201, v125, v193\n\t" \
  "v_fmac_f32 v202, v126, v194\n\t" \
  "v_fmac_f32 v203, v127, v195\n\t" \
  "v_lshlrev_b32 v204, 16, v162\n\t" \
  "v_and_b32 v205, 0xffff0000, v162\n\t" \
  "v_lshlrev_b32 v206, 16, v163\n\t" \
  "v_and_b32 v207, 0xffff0000, v163\n\t" \
  "v_add_f32 v196, 1.0, v196\n\t" \
  "v_add_f32 v197, 1.0, v197\n\t" \
  "v_add_f32 v198, 1.0, v198\n\t" \
  "v_add_f32 v199, 1.0, v199\n\t" \
  "v_cvt_pk_bf16_f32 v180, v200, v201\n\t" \
  "v_cvt_pk_bf16_f32 v181, v202, v203\n\t" \
  "ds_write_b64 v225, v[180:181] offset:96\n\t" \
  "v_rcp_f32 v196, v196\n\t" \
  "v_rcp_f32 v197, v197\n\t" \
  "v_rcp_f32 v198, v198\n\t" \
  "v_rcp_f32 v199, v199\n\t" \
  "s_nop 0\n\t" \
  "v_fmac_f32 v204, v128, v196\n\t" \
  "v_fmac_f32 v205, v129, v197\n\t" \
  "v_fmac_f32 v206, v130, v198\n\t" \
  "v_fmac_f32 v207, v131, v199\n\t" \
  "v_cvt_pk_bf16_f32 v182, v204, v205\n\t" \
  "v_cvt_pk_bf16_f32 v183, v206, v207\n\t" \
  "ds_write_b64 v225, v[182:183] offset:112\n\t" \
  "9:\n\t" \
  "ds_write_b16 v233, v255 offset:5376\n\t" \
  "s_mov_b32 m0, %[keep]\n\t" \
  "s_setprio 0\n\t" \
  "s_nop 11\n\t" \
  ""
#define LOOP_ASM LOOP_ASM_FOLD

DI u32x4 tr_frag(unsigned p, int half) {
  const s16x4 lo = __builtin_amdgcn_ds_read_tr16_b64_v4i16((lds_s16x4_p)p);
  const s16x4 hi = __builtin_amdgcn_ds_read_tr16_b64_v4i16((lds_s16x4_p)(p + half));
  return __builtin_bit_cast(u32x4, __builtin_shufflevector(lo, hi, 0, 1, 2, 3, 4, 5, 6, 7));
}
DI u32x4 fold(u32x4 v) {
#pragma unroll
  for (int i = 0; i < 4; i++) v[i] = pk_bf16(bf2f(v[i] & 0xffffu) * -LOG2E, __uint_as_float(v[i] & 0xffff0000u) * -LOG2E);
  return v;
}

__global__ __launch_bounds__(512, 2) void egnn_kernel(const char* __restrict__ hmat, const char* __restrict__ pos,
                                                      const char* __restrict__ ea, const int* __restrict__ src,
                                                      const int* __restrict__ dst, const char* __restrict__ we,
                                                      const char* __restrict__ wx, const char* __restrict__ wh,
                                                      char* __restrict__ ws, uint16_t* __restrict__ out, int N, int E STAMP_ARG) {
  const int w = __builtin_amdgcn_readfirstlane(threadIdx.x >> 6);
  const int bkt = (blockIdx.x & 7) * 32 + (blockIdx.x >> 3);
  const int n0 = bkt * NB;
  STAMP(0);
  unsigned wl = L_WAVE + w * WSZ, mi = L_MI + w * MISZ;
  const auto rs_tab = __builtin_amdgcn_make_buffer_rsrc((void*)(ws + WS_TAB), 0, NBK * NBK * 8, 0x00020000);
  const auto rs_rec = __builtin_amdgcn_make_buffer_rsrc((void*)(ws + WS_REC), 0, NBK * CHS, 0x00020000);
  const auto rs_ea = __builtin_amdgcn_make_buffer_rsrc((void*)ea, 0, E * 32, 0x00020000);
  unsigned tag_lo, tag_hi;
  unsigned wdv0, wdv1;

  {
    const int lane = lane_id(), tid = w * 64 + lane, r = lane & 31, h = lane >> 5;
    const int e0 = bkt * CH;
    int ne = max(E - e0, 0);
    asm("" : "+s"(ne));
    ne = min(ne, CH);
    const auto rs_d = __builtin_amdgcn_make_buffer_rsrc((void*)(dst + e0), 0, ne * 4, 0x00020000);
    const auto rs_s = __builtin_amdgcn_make_buffer_rsrc((void*)(src + e0), 0, ne * 4, 0x00020000);
    unsigned d[7], sv[7];
    u32x2 xa, xb;
    unsigned long long ep;
    asm volatile("s_load_dwordx2 %0, %1, 0x0" : "=s"(ep) : "s"(ws + WS_EPOCH) : "memory");
#pragma unroll
    for (int i = 0; i < 7; i++)
      asm volatile("buffer_load_dword %0, %2, %3, %5 offen nt\n\tbuffer_load_dword %1, %2, %4, %5 offen nt"
                   : "=&v"(d[i]), "=&v"(sv[i]) : "v"(tid * 4), "s"(rs_d), "s"(rs_s), "s"(i * 2048) : "memory");
    LDS(unsigned, L_CNT + tid * 4) = 0;
    asm volatile("s_waitcnt lgkmcnt(0)\n\ts_barrier" : "+s"(ep) :: "memory");
    asm volatile("global_load_ushort %0, %4, %5\n\tglobal_load_ushort %1, %4, %5 offset:64\n\t"
                 "global_load_dwordx2 %2, %6, %7\n\tglobal_load_dwordx2 %3, %6, %7 offset:16"
                 : "=&v"(wdv0), "=&v"(wdv1), "=&v"(xa), "=&v"(xb)
                 : "v"(r * 2), "s"(we + 128 * 128), "v"(h * 8), "s"(wx + ((w + 2) & 3) * 32) : "memory");
    {
      const int j = lane >> 3, jl = (j & 3) * 128 + (lane & 7) * 16, jh = (j >> 2) * 128;
      const char* const h0 = wh + (w < 4 ? 16 * w : 64 + 32 * ((w - 4) >> 1) + 4 * (w & 1)) * 128;
      const char* const e0p = we + (w < 4 ? 64 + 16 * w : 16 * (w - 4)) * 128;
      const unsigned oh = jl + jh * (w < 4 ? 8 : 16), oe = jl + jh * (w < 4 ? 4 : 8);
      unsigned keep;
      asm volatile("s_mov_b32 %0, m0\n\ts_mov_b32 m0, %9\n\ts_nop 0\n\t"
                   "global_load_lds_dwordx4 %1, %4\n\tglobal_load_lds_dwordx4 %1, %5 offset:1024\n\t"
                   "global_load_lds_dwordx4 %2, %6 offset:2048\n\tglobal_load_lds_dwordx4 %2, %7 offset:3072\n\t"
                   "s_add_u32 m0, %9, 0x1000\n\ts_nop 0\n\tglobal_load_lds_dwordx4 %3, %8\n\ts_mov_b32 m0, %0"
                   : "=&s"(keep)
                   : "v"(oh), "v"(oe), "v"(w < 2 ? (lane >> 2) * 128 + (lane & 3) * 16 : 0),
                     "s"(h0), "s"(h0 + (w < 4 ? 4 : 8) * 128 - 1024), "s"(e0p - 2048), "s"(e0p + (w < 4 ? 8 : 4) * 128 - 3072),
                     "s"(we + 129 * 128 + (w & 1) * 64), "s"(wl)
                   : "memory", "scc");
    }
    unsigned ra[5], keep;
    {
      const unsigned cc0 = ((lane & 7) ^ (lane >> 4)) * 16, cc1 = ((lane & 7) ^ (4 + (lane >> 4))) * 16;
      const int nr = n0 + w + (lane & 56);
      ra[0] = min(nr, N - 1) * 128 + cc0; ra[1] = min(nr + 64, N - 1) * 128 + cc1; ra[2] = min(nr + 128, N - 1) * 128 + cc0;
      ra[3] = min(n0 + w + 192, N - 1) * 128 + cc1; ra[4] = mul24(min(n0 + w + min(lane >> 1, 24) * 8, N - 1), 6) + (lane & 1) * 2;
      asm volatile("" : "+v"(ra[0]), "+v"(ra[1]), "+v"(ra[2]), "+v"(ra[3]), "+v"(ra[4]));
    }
    asm volatile("s_waitcnt vmcnt(9)" : "+v"(d[0]), "+v"(d[1]), "+v"(d[2]), "+v"(d[3]), "+v"(d[4]), "+v"(d[5]), "+v"(d[6]),
                 "+v"(sv[0]), "+v"(sv[1]), "+v"(sv[2]), "+v"(sv[3]), "+v"(sv[4]), "+v"(sv[5]), "+v"(sv[6]) :: "memory");
    STAMP(10);
    {
      asm volatile("s_mov_b32 %0, m0\n\t"
                   "s_mov_b32 m0, %8\n\ts_nop 0\n\tglobal_load_lds_dwordx4 %1, %6\n\t"
                   "s_add_u32 m0, %8, 0x400\n\ts_nop 0\n\tglobal_load_lds_dwordx4 %2, %6\n\t"
                   "s_add_u32 m0, %8, 0x800\n\ts_nop 0\n\tglobal_load_lds_dwordx4 %3, %6\n\t"
                   "s_add_u32 m0, %8, 0xc00\n\ts_nop 0\n\tglobal_load_lds_dwordx4 %4, %6\n\t"
                   "s_add_u32 m0, %8, 0x1000\n\ts_nop 0\n\tglobal_load_lds_dword %5, %7\n\ts_mov_b32 m0, %0"
                   : "=&s"(keep)
                   : "v"(ra[0]), "v"(ra[1]), "v"(ra[2]), "v"(ra[3]), "v"(ra[4]), "s"(hmat), "s"(pos), "s"(mi)
                   : "memory", "scc");
    }
    unsigned ent, total;
    auto sort = [&](auto NS_, auto FULL_) __attribute__((always_inline)) {
      constexpr int NS = decltype(NS_)::value;
      constexpr bool FULL = decltype(FULL_)::value;
      unsigned bk[NS], rank[NS];
      u32x2 rec[NS];
#pragma unroll
      for (int i = 0; i < NS; i++) {
        const int el = i * 512 + tid;
        const unsigned b = mul24(d[i], 85599u) >> 24;
        const unsigned dl = d[i] - mul24(b, NB);
        bk[i] = (FULL || el < ne ? b : NBK + lane) * 4;
        rank[i] = __hip_atomic_fetch_add(&LDS(unsigned, L_CNT + bk[i]), 8u, __ATOMIC_RELAXED, __HIP_MEMORY_SCOPE_WORKGROUP);
        rec[i][0] = el;
        rec[i][1] = ((mul24(dl >> 3, 1272u) + dl) << 16) | sv[i] | 0x80000u;
      }
      LDS(unsigned, L_BASE + (NBK + lane) * 4) = 0x40000000u;
      barrier_lds();
      u32x4 cv = LDS(u32x4, L_CNT + lane * 16);
#pragma unroll
      for (int i = 0; i < 4; i++) cv[i] = (cv[i] + 8) & ~15u;
      const unsigned sum = cv[0] + cv[1] + cv[2] + cv[3], incl = wave_scan_incl(sum);
      const unsigned ex = incl - sum + L_REC;
      total = __builtin_amdgcn_readlane(incl, 63);
      const u32x4 bv = {ex, ex + cv[0], ex + cv[0] + cv[1], ex + cv[0] + cv[1] + cv[2]};
      LDS(u32x4, L_BASE + lane * 16) = bv;
      unsigned bs[NS];
#pragma unroll
      for (int i = 0; i < NS; i++) bs[i] = LDS(unsigned, L_BASE + bk[i]);
      ent = ((LDS(unsigned, L_BASE + (tid & 255) * 4) - L_REC) >> 4) | (LDS(unsigned, L_CNT + (tid & 255) * 4) << 9);
      const unsigned lim = L_REC + 8 * SEG + lane * 8;
#pragma unroll
      for (int i = 0; i < NS; i++) LDS(u32x2, FULL ? bs[i] + rank[i] : min(bs[i] + rank[i], lim)) = rec[i];
      barrier_lds();
    };
    if (ne == CH && w != 0) sort(std::integral_constant<int, 6>(), std::true_type());
    else sort(std::integral_constant<int, 7>(), std::false_type());
    LDS(unsigned, L_CNT + tid * 4) = 0;
    STAMP(11);
    tag_lo = (unsigned)(ep + 1) & 0xffu;
    tag_hi = (unsigned)((ep + 1) >> 8);
    {
      const u32x2 g = {ent | (tag_lo << 24), tag_hi};
      __builtin_amdgcn_raw_buffer_store_b64(g, rs_tab, tid < NBK ? bkt * (NBK * 8) + tid * 8 : 0x80000000u, 0, 16);
      const auto rs_out = __builtin_amdgcn_make_buffer_rsrc((void*)(ws + WS_REC + (size_t)bkt * CHS), 0, __builtin_amdgcn_readfirstlane(total), 0x00020000);
#pragma unroll
      for (int i = 0; i < 4; i++) {
        const u32x4 v = LDS(u32x4, L_REC + (i * 512 + tid) * 16);
        const u32x4 o = {v[1], v[3], v[0] | ((v[2] & 0xfffu) << 12) | (tag_lo << 24), tag_hi};
        __builtin_amdgcn_raw_buffer_store_b128(o, rs_out, (i * 512 + tid) * 16, 0, 16);
      }
    }
    STAMP(12);
    asm volatile("s_waitcnt vmcnt(10)" : "+v"(wdv0), "+v"(wdv1), "+v"(xa), "+v"(xb) :: "memory");
    {
      const unsigned tp = wl + (8 * h + ((lane >> 2) & 3)) * 128 + ((lane >> 4) & 1) * 32 + (lane & 3) * 8;
      u32x4 f[2], g[2];
#pragma unroll
      for (int t = 0; t < 2; t++) { f[t] = tr_frag(tp + 64 * t, 512); g[t] = tr_frag(tp + 2048 + 64 * t, 512); }
#pragma unroll
      for (int t = 0; t < 2; t++) {
        if (w < 4) { f[t] = fold(f[t]); g[t] = fold(g[t]); }
        LDS(u32x4, L_WH + (2 * w + t) * 1024 + lane * 16) = f[t];
        LDS(u32x4, wl + 2048 + t * 1024 + lane * 16) = g[t];
      }
      u32x4 x;
      if (w < 2) x = fold(tr_frag(wl + W_STE + (8 * h + ((lane >> 2) & 3)) * 64 + ((lane >> 4) & 1) * 32 + (lane & 3) * 8, 256));
      else {
        const unsigned m = (lane & 3) == 0 && ((lane >> 2) & 1) == ((lane >> 4) & 1) ? ~0u : 0u;
        x = (u32x4){xa[0] & m, xa[1] & m, xb[0] & m, xb[1] & m};
      }
      if (w < 6) LDS(u32x4, L_WJ + w * 1024 + lane * 16) = x;
    }
    if (tid >= 256 && tid < 256 + 26 * 4) {
      const int t = tid - 256, row = t >> 2, sx = (t >> 1) & 1, hh = t & 1;
      const int x = row < DUMMY ? row - 16 * sx - 4 * hh : -1;
      u32x4 v;
      v[0] = x == 0 ? 0x3f80u : x == 1 ? 0x3f800000u : 0u;  v[1] = x == 2 ? 0x3f80u : x == 3 ? 0x3f800000u : 0u;
      v[2] = x == 8 ? 0x3f80u : x == 9 ? 0x3f800000u : 0u;  v[3] = x == 10 ? 0x3f80u : x == 11 ? 0x3f800000u : 0u;
      LDS(u32x4, L_LUT + row * 80 + (t & 3) * 16) = v;
    }
    {
      const u32x4 z4 = {0u, 0u, 0u, 0u};
#pragma unroll
      for (int i = 0; i < 3; i++) { const int o = i * 1024 + lane * 16; if (o < 26 * 80 + 256) LDS(u32x4, wl + W_SI + o) = z4; }
    }
    barrier_lds();
    STAMP(14);
  }

  f32x16 Y0, Y1; f32x4 Yp = {0.f, 0.f, 0.f, 0.f};
#pragma unroll
  for (int i = 0; i < 16; i++) { Y0[i] = 0.f; Y1[i] = 0.f; }
  u32x32 W2; u32x16 PA; u32x8 W3; u32x4 WX0, WX1, WX2, WX3;
  int jbase = 0, part = -1, grp = w, gother = w;

  auto round = [&](bool first) __attribute__((always_inline)) -> bool {
    const int lane = lane_id(), tid = w * 64 + lane, r = lane & 31, h = lane >> 5;
    const unsigned zr = opaque(0);
    if (!first) { LDS(unsigned, L_CNT + tid * 4) = zr; barrier_lds(); }
    LDS(unsigned, L_BASE + lane * 4) = 0x40000000u;
    unsigned tb[4];
    {
      u32x2 t;
      for (int spin = 0;; spin++) {
        asm volatile("buffer_load_dwordx2 %0, %1, %2, %3 offen sc1\n\ts_waitcnt vmcnt(0)"
                     : "=&v"(t) : "v"((w * 32 + r) * (NBK * 8)), "s"(rs_tab), "s"(bkt * 8) : "memory");
        if (__builtin_amdgcn_ballot_w64(t[1] != tag_hi || (t[0] >> 24) != tag_lo) == 0) break;
        if (spin > (1 << 20)) { t[0] = 0; break; }
        __builtin_amdgcn_s_sleep(POLL_SLEEP);
      }
      LDS(unsigned, L_WAVE + w * WSZ + W_STE + r * 4) = t[0];
      const u32x4 t0 = LDS(u32x4, L_WAVE + w * WSZ + W_STE + (lane >> 3) * 16);
#pragma unroll
      for (int p = 0; p < 4; p++) tb[p] = t0[p];
    }
    STAMP(8);
    u32x4 gr[6];
    unsigned ad[6], left[6], eb[6];
    unsigned lmax = 0, nov = 0;
    bool big;
    const int ovmax = part >= 0 ? LG : 2 * LG, cap = 2 * LG + 2 * ovmax;
    {
      const unsigned jj = lane & (LG - 1), c0 = (unsigned)tid / LG * 4, oob = 0x80000000u + zr;
      const unsigned o0 = mul24(c0, CHS) + (jbase / 2 + jj) * 16, eb0 = mul24(c0, CH);
      unsigned off[4], len[4];
#pragma unroll
      for (int p = 0; p < 4; p++) {
        len[p] = (tb[p] >> 12) & 0xfffu;
        if (part >= 0 && part != 4 * p + (w & 3)) len[p] = 0;
        off[p] = o0 + (tb[p] & 0xfffu) * 16 + p * CHS;
        left[p] = len[p] - (jbase + 2 * jj);
        ad[p] = (int)left[p] > 0 ? off[p] : oob;
        eb[p] = eb0 + p * CH;
        asm volatile("buffer_load_dwordx4 %0, %1, %2, 0 offen sc1" : "=v"(gr[p]) : "v"(ad[p]), "s"(rs_rec) : "memory");
        lmax = max(lmax, tb[p] & 0xfff000u);
      }
      asm volatile("" : "+v"(len[0]), "+v"(len[1]), "+v"(len[2]), "+v"(len[3]));
      ad[4] = oob; ad[5] = oob; left[4] = zr; left[5] = zr; eb[4] = zr; eb[5] = zr;
      unsigned ov[4], s1 = 0;
#pragma unroll
      for (int p = 0; p < 4; p++) {
        ov[p] = (unsigned)min(max(((int)len[p] - (jbase + 2 * LG) + 1) >> 1, 0), ovmax);
        const bool sel = jj - nov < ov[p];
        ad[4] = sel ? off[p] + (LG - nov) * 16 : ad[4];
        left[4] = sel ? len[p] - (jbase + 2 * LG) - 2 * (jj - nov) : left[4];
        eb[4] = sel ? eb0 + p * CH : eb[4];
        nov += ov[p];
      }
      asm volatile("buffer_load_dwordx4 %0, %1, %2, 0 offen sc1" : "=v"(gr[4]) : "v"(ad[4]), "s"(rs_rec) : "memory");
      gr[5] = (u32x4){zr, zr, zr, zr};
      big = __builtin_amdgcn_ballot_w64(nov > LG) != 0;
      if (__builtin_expect(big, 0)) {
#pragma unroll
        for (int p = 0; p < 4; p++) {
          const bool sel = jj + LG - s1 < ov[p];
          ad[5] = sel ? off[p] + (2 * LG - s1) * 16 : ad[5];
          left[5] = sel ? len[p] - (jbase + 2 * LG) - 2 * (jj + LG - s1) : left[5];
          eb[5] = sel ? eb0 + p * CH : eb[5];
          s1 += ov[p];
        }
        asm volatile("buffer_load_dwordx4 %0, %1, %2, 0 offen sc1" : "+v"(gr[5]) : "v"(ad[5]), "s"(rs_rec) : "memory");
      }
    }
    unsigned sink = zr;
    STAMP(7);
    u32x4 w1f[8];
    if (first) {
#pragma unroll
      for (int i = 0; i < 8; i++) w1f[i] = LDS(u32x4, L_WAVE + (4 + (i >> 1)) * WSZ + 2048 + (i & 1) * 1024 + lane * 16);
#pragma unroll
      for (int i = 0; i < 8; i++) {
        const u32x4 v = LDS(u32x4, L_WAVE + (i >> 1) * WSZ + 2048 + (i & 1) * 1024 + lane * 16);
#pragma unroll
        for (int j = 0; j < 4; j++) W2[4 * i + j] = v[j];
      }
#pragma unroll
      for (int i = 0; i < 2; i++) {
        const u32x4 v = LDS(u32x4, L_WJ + i * 1024 + lane * 16);
#pragma unroll
        for (int j = 0; j < 4; j++) W3[4 * i + j] = v[j];
      }
      WX0 = LDS(u32x4, L_WJ + 2048 + lane * 16); WX1 = LDS(u32x4, L_WJ + 3072 + lane * 16);
      WX2 = LDS(u32x4, L_WJ + 4096 + lane * 16); WX3 = LDS(u32x4, L_WJ + 5120 + lane * 16);
    }
    STAMP(13);
    if ((lmax >> 12) > (unsigned)(jbase + cap) || nov > 2 * LG) __hip_atomic_fetch_or(&LDS(unsigned, L_FLAG), nov > 2 * LG ? 3u : 1u, __ATOMIC_RELAXED, __HIP_MEMORY_SCOPE_WORKGROUP);
    unsigned mk1 = (lane & 7) >= 1 ? ~zr : zr, mk2 = (lane & 7) >= 2 ? ~zr : zr, sg0 = L_REC + (lane >> 3) * SEG;
    unsigned lim = L_REC + 8 * SEG + lane * 8, own = n0 + min(8 * (r & 3) + (r >> 2), 24) * 8;
    unsigned ce = ((lane & 1) ^ ((lane >> 4) & 1)) * 16, cp = (lane & 1) * 2;
    unsigned cc0 = ((lane & 7) ^ (lane >> 4)) * 16, cc1 = ((lane & 7) ^ (4 + (lane >> 4))) * 16;
    unsigned q_s4 = L_REC + (lane >> 3) * 32 + 4;
    unsigned q_r2 = L_REC + (4 * ((lane >> 1) & 7) + (lane >> 4)) * 8;
    unsigned q_r1 = L_REC + (4 * (r & 7) + (r >> 3)) * 8 + 4;
    asm volatile("" : "+v"(mk1), "+v"(mk2), "+v"(sg0), "+v"(lim), "+v"(own), "+v"(ce), "+v"(cp), "+v"(cc0), "+v"(cc1), "+v"(q_s4),
                 "+v"(q_r2), "+v"(q_r1));
    asm volatile("s_waitcnt vmcnt(0)" : "+v"(gr[0]), "+v"(gr[1]), "+v"(gr[2]), "+v"(gr[3]), "+v"(gr[4]), "+v"(gr[5]) :: "memory");
    for (int spin = 0;; spin++) {
      bool bad = false;
#pragma unroll
      for (int p = 0; p < 6; p++) bad |= ((int)left[p] > 0) & ((gr[p][3] != tag_hi) | ((gr[p][2] >> 24) != tag_lo));
      if (__builtin_amdgcn_ballot_w64(bad) == 0 || spin > (1 << 20)) break;
      asm volatile("buffer_load_dwordx4 %0, %6, %12, 0 offen sc1\n\tbuffer_load_dwordx4 %1, %7, %12, 0 offen sc1\n\t"
                   "buffer_load_dwordx4 %2, %8, %12, 0 offen sc1\n\tbuffer_load_dwordx4 %3, %9, %12, 0 offen sc1\n\t"
                   "buffer_load_dwordx4 %4, %10, %12, 0 offen sc1\n\tbuffer_load_dwordx4 %5, %11, %12, 0 offen sc1\n\ts_waitcnt vmcnt(0)"
                   : "=&v"(gr[0]), "=&v"(gr[1]), "=&v"(gr[2]), "=&v"(gr[3]), "=&v"(gr[4]), "=&v"(gr[5])
                   : "v"(ad[0]), "v"(ad[1]), "v"(ad[2]), "v"(ad[3]), "v"(ad[4]), "v"(ad[5]), "s"(rs_rec) : "memory");
    }
    STAMP(1);
    u32x2 rc[12];
#pragma unroll
    for (int p = 0; p < 6; p++) {
      rc[2 * p] = (u32x2){(eb[p] + (gr[p][2] & 0xfffu)) << 5, (int)left[p] > 0 ? gr[p][0] : 0u};
      rc[2 * p + 1] = (u32x2){(eb[p] + ((gr[p][2] >> 12) & 0xfffu)) << 5, (int)left[p] > 1 ? gr[p][1] : 0u};
    }
    unsigned rk[12], ky[12];
#pragma unroll
    for (int p = 0; p < 12; p++) {
      if (p == 10 && __builtin_expect(!big, 1)) { ky[10] = ky[11] = 0; rk[10] = rk[11] = 0; break; }
      ky[p] = max((rc[p][1] >> 9) & 0x7fcu, (unsigned)lane * 4);
      rk[p] = __hip_atomic_fetch_add(&LDS(unsigned, L_CNT + ky[p]), 8u + zr, __ATOMIC_RELAXED, __HIP_MEMORY_SCOPE_WORKGROUP);
    }
    if (EPF) {
#pragma unroll
      for (int p = 0; p < 10; p++) {
        const unsigned a = (ky[p] & 0x47cu) - 0x400u < (unsigned)EPF * 4 ? rc[p][0] : 0x80000000u + zr;
        asm volatile("buffer_load_dword %0, %1, %2, 0 offen" : "+{v3}"(sink) : "v"(a), "s"(rs_ea) : "memory");
      }
    }
    barrier_lds();
    STAMP(2);
    unsigned g;
    const unsigned flagv = LDS(unsigned, L_FLAG);
    {
      const u32x4 cv = LDS(u32x4, L_CNT + 1024 + lane * 16);
      const unsigned sum = cv[0] + cv[1] + cv[2] + cv[3];
      g = sum + (__builtin_amdgcn_update_dpp(0, (int)sum, 0x111, 0xf, 0xf, true) & mk1);
      g += __builtin_amdgcn_update_dpp(0, (int)g, 0x112, 0xf, 0xf, true) & mk2;
      g += __builtin_amdgcn_update_dpp(0, (int)g, 0x114, 0xf, 0xa, false);
      const unsigned ex = g - sum + sg0;
      const u32x4 bv = {ex, ex + cv[0], ex + cv[0] + cv[1], ex + cv[0] + cv[1] + cv[2]};
      LDS(u32x4, L_BASE + 1024 + lane * 16) = bv;
    }
    unsigned bs[10];
#pragma unroll
    for (int p = 0; p < 10; p++) bs[p] = LDS(unsigned, L_BASE + ky[p]);
    const unsigned flag = __builtin_amdgcn_readfirstlane(flagv);
    const bool again = flag & 1;
    const bool over = (flag & 2) || __builtin_amdgcn_ballot_w64(g > (unsigned)(SEG - 256)) != 0;
    if (__builtin_expect(!over, 1)) {
#pragma unroll
      for (int p = 0; p < 10; p++) LDS(u32x2, min(bs[p] + rk[p], lim)) = rc[p];
      if (__builtin_expect(big, 0)) {
        LDS(u32x2, min(LDS(unsigned, L_BASE + ky[10]) + rk[10], lim)) = rc[10];
        LDS(u32x2, min(LDS(unsigned, L_BASE + ky[11]) + rk[11], lim)) = rc[11];
      }
    }
    if (first) {
      const unsigned key = (g << 3) | (lane >> 3);
      unsigned ks[8], rank = 0;
#pragma unroll
      for (int k = 0; k < 8; k++) ks[k] = __builtin_amdgcn_readlane(key, 8 * k + 7);
#pragma unroll
      for (int k = 0; k < 8; k++) rank += ks[k] > key;
      const unsigned long long sel = 0x8080808080808080ull;
      grp = __builtin_ctzll(__builtin_amdgcn_ballot_w64(rank == (unsigned)(w < 4 ? w : 11 - w)) & sel) >> 3;
      gother = __builtin_ctzll(__builtin_amdgcn_ballot_w64(rank == (unsigned)(w < 4 ? 7 - w : w - 4)) & sel) >> 3;
      wl = L_WAVE + grp * WSZ; mi = L_MI + grp * MISZ;
    }
    unsigned segn = over ? 0u : (unsigned)__builtin_amdgcn_readlane(g, 8 * grp + 7);
    const unsigned seg0 = grp * SEG;
    const int nt = (segn + 255) >> 8;
    {
      const u32x2 pad = {zr, DUMMY * (80u << 20)}, row = {zr, min(own + grp, (unsigned)N - 1) | (DUMMY * (80u << 20))};
      LDS(u32x2, L_REC + seg0 + segn + r * 8) = pad;
      LDS(u32x2, L_REC + seg0 + nt * 256 + r * 8) = row;
    }
    if (lane == 0) LDS(unsigned, wl + W_SI + DUMMY * 80 + 76) = nt;
    barrier_lds();
    STAMP(3);

    const unsigned o_s4 = q_s4 + seg0, o_r2 = q_r2 + seg0, o_r1 = q_r1 + seg0;
    {
      unsigned ns[4];
#pragma unroll
      for (int i = 0; i < 4; i++) ns[i] = LDS(uint16_t, o_s4 + i * 8);
      const u32x2 nr2 = LDS(u32x2, o_r2);
      dma_tile(hmat, ea, pos, ns[0] * 128 + cc0, ns[1] * 128 + cc1, ns[2] * 128 + cc0, ns[3] * 128 + cc1,
               nr2[0] | ce, (nr2[1] & 0xffffu) * 6 + cp, wl);
    }
    if (first) {
      u32x2 hb[8];
#pragma unroll
      for (int i = 0; i < 8; i++) hb[i] = LDS(u32x2, mi + r * 128 + ((i ^ ((r >> 1) & 7)) * 16) + h * 8);
      const u32x2 pg = LDS(u32x2, mi + 4096 + r * 8);
#pragma unroll
      for (int t = 0; t < 2; t++) {
        f32x16 P;
#pragma unroll
        for (int i = 0; i < 16; i++) P[i] = 0.f;
#pragma unroll
        for (int s = 0; s < 4; s++) {
          const u32x4 a = {hb[2 * s][0], hb[2 * s][1], hb[2 * s + 1][0], hb[2 * s + 1][1]};
          P = MFMA(as_bf(a), as_bf(w1f[2 * s + t]), P);
        }
        if (h) P[15] = bf2f(t ? wdv1 : wdv0);
#pragma unroll
        for (int i = 0; i < 16; i++) P[i] *= -LOG2E;
#pragma unroll
        for (int s = 0; s < 2; s++)
#pragma unroll
          for (int j = 0; j < 4; j++) PA[4 * (2 * s + t) + j] = pk_bf16(P[8 * s + 2 * j], P[8 * s + 2 * j + 1]);
      }
      const f32x4 pv = {bf2f(pg[0] & 0xffffu), __uint_as_float(pg[0] & 0xffff0000u), __uint_as_float(pg[1] & 0xffff0000u), 0.f};
      if (lane < DUMMY) LDS(f32x4, wl + W_SI + lane * 80 + 64) = pv;
      if (bkt == 0 && tid == 0) {
        const u32x2 e1 = {tag_lo | (tag_hi << 8), tag_hi >> 24};
        asm volatile("global_store_dwordx2 %0, %1, %2 sc1" :: "v"(zr), "v"(e1), "s"(ws + WS_EPOCH) : "memory");
      }
    }
    const bool last_round = !over && !again && (part < 0 || part == 15);
    STAMP(4);

    {
      u32x16 A0; u32x8 A1;
      A0[0] = zr; A0[1] = zr;
#pragma unroll
      for (int s = 0; s < 4; s++) A0[2 + s] = wl + W_STH + r * 128 + (((2 * s + h) ^ ((r >> 1) & 7)) * 16);
      A0[6] = wl + r * 32 + ((h ^ ((r >> 3) & 1)) * 16);
      A0[7] = wl + r * 8;
      A0[8] = L_LUT + h * 16;
      A0[9] = wl + r * 2;
      A0[10] = wl + min(r, DUMMY) * 80 + h * 16;
      A0[11] = mi + r * MIL + h * 32;
      A0[12] = mi + (8 * h + ((lane >> 2) & 3)) * MIL + (4 * ((lane >> 4) & 1) + (lane & 3)) * 8;
      A0[13] = wl + h * 64 + r * 2;
      {
        const int row = (lane & 15) - 4 * ((lane >> 4) & 1);
        A0[14] = wl + ((unsigned)row < 3 ? row : 3) * 64 + h * 16;
      }
      A0[15] = L_WJ + lane * 16;
      A1[0] = cc0; A1[1] = cc1; A1[2] = ce; A1[3] = cp;
      A1[4] = o_s4 + 256; A1[5] = o_r2 + 256; A1[6] = o_r1;
      A1[7] = 0x3f800000u + zr;
      unsigned cnt = nt, keep;
      const int lastv = __builtin_amdgcn_readfirstlane(opaque(last_round ? 1 : 0));
      const int thr = w >> 2;
      u32x2 A2 = {wl + W_SI + DUMMY * 80 + 76, (unsigned)(L_WAVE + gother * WSZ + W_SI + DUMMY * 80 + 76)};
      unsigned tmp;
      asm volatile(LOOP_ASM
                   : "+{v[52:67]}"(Y0), "+{v[68:83]}"(Y1), "+{v[84:87]}"(Yp), "+{v[232:247]}"(A0), "+{v[248:255]}"(A1), "+{v3}"(sink),
                     "+{v[212:213]}"(A2), [tmp] "=&s"(tmp),
                     [nt] "+s"(cnt), [keep] "=&s"(keep)
                   : "{v[4:35]}"(W2), "{v[36:51]}"(PA), "{v[88:95]}"(W3), "{v[96:99]}"(WX0), "{v[136:139]}"(WX1), "{v[140:143]}"(WX2), "{v[144:147]}"(WX3),
                     [h0] "s"(hmat), [h1] "s"(hmat - 1024), [h2] "s"(hmat - 2048), [h3] "s"(hmat - 3072), [ea] "s"(ea), [pos] "s"(pos - 1024),
                     [b1] "s"(wl), [b2] "s"(wl + W_STE), [posd] "s"(wl + W_SI + 64), [c128] "s"(128u), [thr] "s"(thr), [last] "s"(lastv),
                     [hsel] "s"(0xffffffff00000000ull)
                   : "memory", "scc", "vcc", CLOBBER_V);
    }
    if (over) part = 0;
    else if (part >= 0 && part < 15) part++;
    else { part = -1; jbase += cap; }
    return last_round;
  };
  if (__builtin_expect(!round(true), 0)) {
    do {
      wait_vm0();
      barrier_lds();
      jbase = __builtin_amdgcn_readfirstlane(jbase); part = __builtin_amdgcn_readfirstlane(part);
    } while (!round(false));
  }
  STAMP(5);

  {
    const int lane = lane_id(), r = lane & 31, h = lane >> 5;
    Yp[0] *= -LN2; Yp[1] *= -LN2; Yp[2] *= -LN2;
    const auto rs_o = __builtin_amdgcn_make_buffer_rsrc((void*)out, 0, N * 128, 0x00020000);
#pragma unroll
    for (int i = 0; i < 4; i++) {
      const int q = 8 * i + (lane >> 3), dl = q * 8 + grp;
      const u32x4 v = LDS(u32x4, mi + q * MIP + (lane & 7) * 16);
      if (dl < NB) __builtin_amdgcn_raw_buffer_store_b128(v, rs_o, (n0 + dl) * 128 + (lane & 7) * 16, 0, OUT_AUX);
    }
    const int dl = r * 8 + grp;
    if (h == 0 && dl < NB && n0 + dl < N) {
      const f32x4 pd = LDS(f32x4, wl + W_SI + r * 80 + 64);
      uint16_t* po = out + (size_t)N * 64 + (size_t)(n0 + dl) * 3;
      const unsigned xy = pk_bf16(pd[0] + Yp[0], pd[1] + Yp[1]), zz = pk_bf16(pd[2] + Yp[2], 0.f);
      po[0] = (uint16_t)xy; po[1] = (uint16_t)(xy >> 16); po[2] = (uint16_t)zz;
    }
  }
  STAMP(6);
}

}

#define STAMP_PASS1
#define STAMP_PASS2

extern "C" void kernel_launch(void* const* d_in, const int* in_sizes, int n_in, void* d_out, int out_size,
                              void* d_ws, size_t ws_size, hipStream_t stream) {
  const int N = in_sizes[0] / 64, E = in_sizes[3];
  if (N > NBK * NB || E > NBK * CH || N < 1 || E < 1) return;
  (void)hipFuncSetAttribute((const void*)egnn_kernel, hipFuncAttributeMaxDynamicSharedMemorySize, L_TOTAL);
  egnn_kernel<<<NBK, 512, L_TOTAL, stream>>>((const char*)d_in[0], (const char*)d_in[1], (const char*)d_in[2], (const int*)d_in[3],
                                             (const int*)d_in[4], (const char*)d_in[5], (const char*)d_in[6], (const char*)d_in[7],
                                             (char*)d_ws, (uint16_t*)d_out, N, E STAMP_PASS2);
}
```

```cpp
#include <hip/hip_runtime.h>
#include <stdint.h>
#include <type_traits>

#define FOLD_LOG2E 1

namespace {

typedef __bf16 bf16x8 __attribute__((ext_vector_type(8)));
typedef __bf16 bf16x2 __attribute__((ext_vector_type(2)));
typedef float f32x16 __attribute__((ext_vector_type(16)));
typedef float f32x4 __attribute__((ext_vector_type(4)));
typedef unsigned u32x4 __attribute__((ext_vector_type(4)));
typedef unsigned u32x2 __attribute__((ext_vector_type(2)));
typedef unsigned u32x8 __attribute__((ext_vector_type(8)));
typedef unsigned u32x16 __attribute__((ext_vector_type(16)));
typedef unsigned u32x32 __attribute__((ext_vector_type(32)));
typedef short s16x4 __attribute__((ext_vector_type(4)));
typedef __attribute__((address_space(3))) s16x4* lds_s16x4_p;

#define DI __device__ __forceinline__
#define MFMA(a, b, c) __builtin_amdgcn_mfma_f32_32x32x16_bf16((a), (b), (c), 0, 0, 0)

#define POLL_SLEEP 2
#define EPF 2
#define OUT_AUX 16
constexpr int NBK = 256;
constexpr int LG = 8;
constexpr int NB = 196;
constexpr int CH = 3136;
constexpr float LOG2E = 1.4426950408889634f;
constexpr float LN2 = 0.6931471805599453f;

constexpr size_t WS_EPOCH = 0;
constexpr size_t WS_TAB = 256;
constexpr size_t WS_REC = WS_TAB + (size_t)NBK * NBK * 8;
constexpr int CHS = (CH + NBK) * 8;

constexpr int W_STH = 0, W_STE = 4096, W_STP = 5120, W_SI = 5376, W_TI = W_SI + 26 * 80, WSZ = W_TI + 256;
constexpr int DUMMY = 25;
constexpr int MIP = 144, MISZ = 32 * MIP;
constexpr int MIL = 80;
constexpr int SEG = 17 * 256;
constexpr int L_CNT = 0;
constexpr int L_BASE = L_CNT + 2048;
constexpr int L_FLAG = L_CNT + 512;
constexpr int L_REC = L_BASE + 2048;
constexpr int L_LUT = L_REC + 8 * SEG + 512;
constexpr int L_WAVE = L_LUT + 26 * 80;
constexpr int L_MI = L_WAVE + 8 * WSZ;
constexpr int L_WJ = L_MI + 8 * MISZ;
constexpr int L_WH = L_WJ + 6 * 1024;
constexpr int L_TOTAL = L_WH + 16 * 1024;
static_assert(L_TOTAL <= 163840, "LDS");
static_assert(WSZ % 16 == 0 && L_MI % 16 == 0 && L_LUT % 16 == 0, "align");

#pragma clang diagnostic ignored "-Wint-to-pointer-cast"
#define LDS(T, off) (*(__attribute__((address_space(3))) T*)(unsigned)(off))
DI unsigned mul24(unsigned a, unsigned b) { return (unsigned)__umul24(a, b); }
DI float bf2f(unsigned lo16) { return __uint_as_float(lo16 << 16); }
DI unsigned pk_bf16(float a, float b) { bf16x2 v = {(__bf16)a, (__bf16)b}; return __builtin_bit_cast(unsigned, v); }
DI bf16x8 as_bf(u32x4 v) { return __builtin_bit_cast(bf16x8, v); }
DI int opaque(int x) { asm volatile("" : "+v"(x)); return x; }
DI int lane_id() {
  int x; asm volatile("v_mbcnt_lo_u32_b32 %0, -1, 0\n\tv_mbcnt_hi_u32_b32 %0, -1, %0" : "=v"(x)); return x;
}
DI void wait_vm0() { asm volatile("s_waitcnt vmcnt(0)" ::: "memory"); }
DI void barrier_lds() { asm volatile("s_waitcnt lgkmcnt(0)\n\ts_barrier" ::: "memory"); }
DI void dma_tile(const void* hm, const void* ea, const void* ps, unsigned o0, unsigned o1, unsigned o2, unsigned o3,
                 unsigned oe, unsigned op, unsigned l) {
  unsigned keep;
  asm volatile("s_mov_b32 %0, m0\n\t"
               "s_add_u32 m0, %10, 0x1000\n\ts_nop 0\n\tglobal_load_lds_dwordx4 %5, %8\n\t"
               "s_mov_b32 m0, %10\n\ts_nop 0\n\tglobal_load_lds_dwordx4 %1, %7\n\t"
               "s_add_u32 m0, %10, 0x400\n\ts_nop 0\n\tglobal_load_lds_dwordx4 %2, %7\n\t"
               "s_add_u32 m0, %10, 0x800\n\ts_nop 0\n\tglobal_load_lds_dwordx4 %3, %7\n\t"
               "s_add_u32 m0, %10, 0xc00\n\ts_nop 0\n\tglobal_load_lds_dwordx4 %4, %7\n\t"
               "s_add_u32 m0, %10, 0x1400\n\ts_nop 0\n\tglobal_load_lds_dword %6, %9\n\t"
               "s_mov_b32 m0, %0"
               : "=&s"(keep) : "v"(o0), "v"(o1), "v"(o2), "v"(o3), "v"(oe), "v"(op), "s"(hm), "s"(ea), "s"(ps), "s"(l)
               : "memory", "scc");
}
template <int CTRL, int ROWMASK> DI unsigned dpp_add(unsigned x) {
  return x + (unsigned)__builtin_amdgcn_update_dpp(0, (int)x, CTRL, ROWMASK, 0xf, false);
}
DI unsigned wave_scan_incl(unsigned x) {
  x = dpp_add<0x111, 0xf>(x); x = dpp_add<0x112, 0xf>(x); x = dpp_add<0x114, 0xf>(x); x = dpp_add<0x118, 0xf>(x);
  x = dpp_add<0x142, 0xa>(x); x = dpp_add<0x143, 0xc>(x);
  return x;
}
#define STAMP(i)
#define STAMP_ARG

#define CLOBBER_V "v100", "v101", "v102", "v103", "v104", "v105", "v106", "v107", "v108", "v109", "v110", "v111", "v112", "v113", "v114", "v115", "v116", "v117", "v118", "v119", "v120", "v121", "v122", "v123", "v124", "v125", "v126", "v127", "v128", "v129", "v130", "v131", "v132", "v133", "v134", "v135", "v148", "v149", "v150", "v151", "v152", "v153", "v154", "v155", "v156", "v157", "v158", "v159", "v160", "v161", "v162", "v163", "v164", "v165", "v166", "v167", "v168", "v169", "v170", "v171", "v172", "v173", "v174", "v175", "v176", "v177", "v178", "v179", "v180", "v181", "v182", "v183", "v184", "v185", "v186", "v187", "v188", "v189", "v190", "v191", "v192", "v193", "v194", "v195", "v196", "v197", "v198", "v199", "v200", "v201", "v202", "v203", "v204", "v205", "v206", "v207", "v208", "v209", "v210", "v211", "v214", "v215", "v216", "v217", "v218", "v219", "v220", "v221", "v222", "v223", "v224", "v225", "v226", "v227", "v228", "v229", "v230", "v231"
#define LOOP_ASM_FOLD \
  "s_mov_b32 %[keep], m0\n\t" \
  "s_mov_b64 vcc, %[hsel]\n\t" \
  "ds_read2_b32 v[220:221], v252 offset0:0 offset1:2\n\t" \
  "ds_read2_b32 v[222:223], v252 offset0:4 offset1:6\n\t" \
  "ds_read_b64 v[226:227], v253\n\t" \
  "ds_read_b32 v232, v254\n\t" \
  "v_mov_b32 v176, 0\n\t" \
  "v_mov_b32 v177, 0\n\t" \
  "v_mov_b32 v178, 0\n\t" \
  "v_mov_b32 v179, 0\n\t" \
  "v_mov_b32 v180, 0\n\t" \
  "v_mov_b32 v181, 0\n\t" \
  "v_mov_b32 v182, 0\n\t" \
  "v_mov_b32 v183, 0\n\t" \
  "v_mov_b32 v184, 0\n\t" \
  "v_mov_b32 v185, 0\n\t" \
  "v_mov_b32 v186, 0\n\t" \
  "v_mov_b32 v187, 0\n\t" \
  "v_mov_b32 v188, 0\n\t" \
  "v_mov_b32 v189, 0\n\t" \
  "v_mov_b32 v190, 0\n\t" \
  "v_mov_b32 v191, 0\n\t" \
  "v_mov_b32 v230, 0\n\t" \
  "v_mov_b32 v231, 0\n\t" \
  "v_mov_b32 v208, 0\n\t" \
  "v_mov_b32 v209, 0\n\t" \
  "v_mov_b32 v210, 0\n\t" \
  "v_mov_b32 v211, 0\n\t" \
  "ds_write_b128 v243, v[184:187] offset:0\n\t" \
  "s_nop 0\n\t" \
  "v_mfma_f32_16x16x32_bf16 v[132:135], v[184:187], v[188:191], 0\n\t" \
  "v_add_u32 v252, 0x100, v252\n\t" \
  "v_add_u32 v253, 0x100, v253\n\t" \
  "v_add_u32 v254, 0x100, v254\n\t" \
  "s_waitcnt lgkmcnt(0)\n\t" \
  "v_lshrrev_b32 v232, 20, v232\n\t" \
  "v_add_u32 v233, %[posd], v232\n\t" \
  "ds_read_b128 v[216:219], v233\n\t" \
  "v_add_u32 v233, v240, v232\n\t" \
  "ds_read_b128 v[172:175], v233 offset:32\n\t" \
  "ds_read_b128 v[168:171], v233\n\t" \
  "v_add_u32 v233, v241, v232\n\t" \
  "ds_write_b16_d16_hi v233, v255 offset:5376\n\t" \
  "v_mad_u32_u16 v220, v220, %[c128], v248\n\t" \
  "v_mad_u32_u16 v221, v221, %[c128], v249\n\t" \
  "v_mad_u32_u16 v222, v222, %[c128], v248\n\t" \
  "v_mad_u32_u16 v223, v223, %[c128], v249\n\t" \
  "v_or_b32 v226, v226, v250\n\t" \
  "v_mad_u32_u16 v227, v227, 6, v251\n\t" \
  "s_waitcnt vmcnt(0)\n\t" \
  "ds_read_b128 v[148:151], v234\n\t" \
  "ds_read_b128 v[152:155], v235\n\t" \
  "ds_read_b128 v[156:159], v236\n\t" \
  "ds_read_b128 v[160:163], v237\n\t" \
  "ds_read_b128 v[164:167], v238 offset:4096\n\t" \
  "ds_read_b64 v[224:225], v239 offset:5120\n\t" \
  "s_waitcnt lgkmcnt(0)\n\t" \
  "s_cmp_lt_i32 %[nt], 1\n\t" \
  "s_cbranch_scc1 7f\n\t" \
  "s_mov_b32 m0, %[b2]\n\t" \
  "s_nop 0\n\t" \
  "global_load_lds_dwordx4 v226, %[ea]\n\t" \
  "global_load_lds_dword v227, %[pos] offset:1024\n\t" \
  "s_mov_b32 m0, %[b1]\n\t" \
  "s_nop 0\n\t" \
  "global_load_lds_dwordx4 v220, %[h0]\n\t" \
  "global_load_lds_dwordx4 v221, %[h1] offset:1024\n\t" \
  "global_load_lds_dwordx4 v222, %[h2] offset:2048\n\t" \
  "global_load_lds_dwordx4 v223, %[h3] offset:3072\n\t" \
  "7:\n\t" \
  "ds_read2_b32 v[220:221], v252 offset0:0 offset1:2\n\t" \
  "ds_read2_b32 v[222:223], v252 offset0:4 offset1:6\n\t" \
  "ds_read_b64 v[226:227], v253\n\t" \
  "ds_read_b32 v232, v254\n\t" \
  "v_add_u32 v252, 0x100, v252\n\t" \
  "v_add_u32 v253, 0x100, v253\n\t" \
  "v_add_u32 v254, 0x100, v254\n\t" \
  "v_and_b32 v225, 0xffff0000, v225\n\t" \
  "v_lshlrev_b32 v219, 16, v224\n\t" \
  "v_and_b32 v224, 0xffff0000, v224\n\t" \
  "v_sub_f32 v229, v218, v225\n\t" \
  "v_sub_f32 v216, v216, v219\n\t" \
  "v_sub_f32 v217, v217, v224\n\t" \
  "v_mul_f32 v218, v229, v229\n\t" \
  "v_cndmask_b32 v228, v216, v217, vcc\n\t" \
  "v_fmac_f32 v218, v216, v216\n\t" \
  "v_fmac_f32 v218, v217, v217\n\t" \
  "v_cvt_pk_bf16_f32 v218, v218, v218\n\t" \
  "v_cndmask_b32_sdwa v175, v175, v218, vcc dst_sel:WORD_1 dst_unused:UNUSED_PRESERVE src0_sel:WORD_1 src1_sel:WORD_1\n\t" \
  "v_mfma_f32_32x32x16_bf16 v[100:115], v[4:7], v[148:151], 0\n\t" \
  "v_mfma_f32_32x32x16_bf16 v[100:115], v[12:15], v[152:155], v[100:115]\n\t" \
  "v_mfma_f32_32x32x16_bf16 v[100:115], v[20:23], v[156:159], v[100:115]\n\t" \
  "v_mfma_f32_32x32x16_bf16 v[100:115], v[28:31], v[160:163], v[100:115]\n\t" \
  "v_mfma_f32_32x32x16_bf16 v[100:115], v[36:39], v[168:171], v[100:115]\n\t" \
  "v_mfma_f32_32x32x16_bf16 v[100:115], v[88:91], v[164:167], v[100:115]\n\t" \
  "v_mfma_f32_32x32x16_bf16 v[100:115], v[44:47], v[172:175], v[100:115]\n\t" \
  "s_cmp_eq_u32 %[nt], 0\n\t" \
  "s_nop 10\n\t" \
  "v_exp_f32 v192, v100\n\t" \
  "v_exp_f32 v193, v101\n\t" \
  "v_exp_f32 v194, v102\n\t" \
  "v_exp_f32 v195, v103\n\t" \
  "s_nop 0\n\t" \
  "v_add_f32 v192, 1.0, v192\n\t" \
  "v_add_f32 v193, 1.0, v193\n\t" \
  "v_add_f32 v194, 1.0, v194\n\t" \
  "v_add_f32 v195, 1.0, v195\n\t" \
  "s_cbranch_scc1 6f\n\t" \
  "s_cmp_lt_i32 %[thr], 1\n\t" \
  "s_cbranch_scc1 1f\n\t" \
  "s_setprio 1\n\t" \
  "1:\n\t" \
  "ds_write_b128 v243, v[188:191] offset:16\n\t" \
  "ds_read_b32 v214, v213\n\t" \
  "ds_read_b64_tr_b16 v[200:201], v244 offset:0\n\t" \
  "ds_read_b64_tr_b16 v[202:203], v244 offset:320\n\t" \
  "ds_read_b64_tr_b16 v[204:205], v244 offset:1280\n\t" \
  "ds_read_b64_tr_b16 v[206:207], v244 offset:1600\n\t" \
  "v_mfma_f32_16x16x32_bf16 v[132:135], v[144:147], v[188:191], v[132:135]\n\t" \
  "v_rcp_f32 v192, v192\n\t" \
  "v_rcp_f32 v193, v193\n\t" \
  "v_rcp_f32 v194, v194\n\t" \
  "v_rcp_f32 v195, v195\n\t" \
  "v_mfma_f32_32x32x16_bf16 v[116:131], v[8:11], v[148:151], 0\n\t" \
  "v_exp_f32 v196, v104\n\t" \
  "v_exp_f32 v197, v105\n\t" \
  "v_exp_f32 v198, v106\n\t" \
  "v_exp_f32 v199, v107\n\t" \
  "v_mfma_f32_32x32x16_bf16 v[116:131], v[16:19], v[152:155], v[116:131]\n\t" \
  "v_mul_f32 v192, v100, v192\n\t" \
  "v_mul_f32 v193, v101, v193\n\t" \
  "v_mul_f32 v194, v102, v194\n\t" \
  "v_mul_f32 v195, v103, v195\n\t" \
  "v_add_f32 v196, 1.0, v196\n\t" \
  "v_add_f32 v197, 1.0, v197\n\t" \
  "v_add_f32 v198, 1.0, v198\n\t" \
  "v_add_f32 v199, 1.0, v199\n\t" \
  "v_cvt_pk_bf16_f32 v184, v192, v193\n\t" \
  "v_cvt_pk_bf16_f32 v185, v194, v195\n\t" \
  "v_mfma_f32_32x32x16_bf16 v[116:131], v[24:27], v[156:159], v[116:131]\n\t" \
  "v_rcp_f32 v196, v196\n\t" \
  "v_rcp_f32 v197, v197\n\t" \
  "v_rcp_f32 v198, v198\n\t" \
  "v_rcp_f32 v199, v199\n\t" \
  "s_waitcnt lgkmcnt(2)\n\t" \
  "v_mfma_f32_32x32x16_bf16 v[68:83], v[200:203], v[176:179], v[68:83]\n\t" \
  "v_exp_f32 v192, v108\n\t" \
  "v_exp_f32 v193, v109\n\t" \
  "v_exp_f32 v194, v110\n\t" \
  "v_exp_f32 v195, v111\n\t" \
  "v_mfma_f32_32x32x16_bf16 v[116:131], v[32:35], v[160:163], v[116:131]\n\t" \
  "v_mul_f32 v196, v104, v196\n\t" \
  "v_mul_f32 v197, v105, v197\n\t" \
  "v_mul_f32 v198, v106, v198\n\t" \
  "v_mul_f32 v199, v107, v199\n\t" \
  "v_mul_f32 v230, v230, v132\n\t" \
  "v_mul_f32 v231, v231, v132\n\t" \
  "v_cvt_pk_bf16_f32 v230, v230, v231\n\t" \
  "ds_write_b16 v245, v230 offset:7456\n\t" \
  "ds_write_b16_d16_hi v241, v230 offset:7584\n\t" \
  "ds_read_b128 v[200:203], v246 offset:7456\n\t" \
  "ds_read_b128 v[208:211], v246 offset:7488\n\t" \
  "v_mov_b32 v230, v228\n\t" \
  "v_mov_b32 v231, v229\n\t" \
  "v_add_f32 v192, 1.0, v192\n\t" \
  "v_add_f32 v193, 1.0, v193\n\t" \
  "v_add_f32 v194, 1.0, v194\n\t" \
  "v_add_f32 v195, 1.0, v195\n\t" \
  "v_cvt_pk_bf16_f32 v186, v196, v197\n\t" \
  "v_cvt_pk_bf16_f32 v187, v198, v199\n\t" \
  "v_mfma_f32_32x32x16_bf16 v[116:131], v[40:43], v[168:171], v[116:131]\n\t" \
  "v_rcp_f32 v192, v192\n\t" \
  "v_rcp_f32 v193, v193\n\t" \
  "v_rcp_f32 v194, v194\n\t" \
  "v_rcp_f32 v195, v195\n\t" \
  "s_sub_u32 %[nt], %[nt], 1\n\t" \
  "v_mov_b32 v215, %[nt]\n\t" \
  "v_readfirstlane_b32 %[tmp], v214\n\t" \
  "s_mov_b64 exec, 1\n\t" \
  "ds_write_b32 v212, v215\n\t" \
  "s_mov_b64 exec, -1\n\t" \
  "s_sub_i32 %[tmp], %[nt], %[tmp]\n\t" \
  "s_add_i32 %[tmp], %[tmp], %[thr]\n\t" \
  "s_cmp_gt_i32 %[tmp], 0\n\t" \
  "s_setprio 0\n\t" \
  "s_cbranch_scc0 3f\n\t" \
  "s_setprio 1\n\t" \
  "3:\n\t" \
  "v_mfma_f32_32x32x16_bf16 v[116:131], v[92:95], v[164:167], v[116:131]\n\t" \
  "v_exp_f32 v196, v112\n\t" \
  "v_exp_f32 v197, v113\n\t" \
  "v_exp_f32 v198, v114\n\t" \
  "v_exp_f32 v199, v115\n\t" \
  "v_mfma_f32_32x32x16_bf16 v[116:131], v[48:51], v[172:175], v[116:131]\n\t" \
  "v_mul_f32 v192, v108, v192\n\t" \
  "v_mul_f32 v193, v109, v193\n\t" \
  "v_mul_f32 v194, v110, v194\n\t" \
  "v_mul_f32 v195, v111, v195\n\t" \
  "s_waitcnt vmcnt(0)\n\t" \
  "ds_read_b128 v[148:151], v234\n\t" \
  "ds_read_b128 v[152:155], v235\n\t" \
  "ds_read_b128 v[156:159], v236\n\t" \
  "ds_read_b128 v[160:163], v237\n\t" \
  "ds_read_b128 v[164:167], v238 offset:4096\n\t" \
  "ds_read_b64 v[224:225], v239 offset:5120\n\t" \
  "ds_write_b128 v243, v[184:187] offset:0\n\t" \
  "v_add_f32 v196, 1.0, v196\n\t" \
  "v_add_f32 v197, 1.0, v197\n\t" \
  "v_add_f32 v198, 1.0, v198\n\t" \
  "v_add_f32 v199, 1.0, v199\n\t" \
  "v_cvt_pk_bf16_f32 v188, v192, v193\n\t" \
  "v_cvt_pk_bf16_f32 v189, v194, v195\n\t" \
  "v_mad_u32_u16 v220, v220, %[c128], v248\n\t" \
  "v_mad_u32_u16 v221, v221, %[c128], v249\n\t" \
  "v_mad_u32_u16 v222, v222, %[c128], v248\n\t" \
  "s_waitcnt lgkmcnt(12)\n\t" \
  "v_mfma_f32_32x32x16_bf16 v[68:83], v[204:207], v[180:183], v[68:83]\n\t" \
  "v_rcp_f32 v196, v196\n\t" \
  "v_rcp_f32 v197, v197\n\t" \
  "v_rcp_f32 v198, v198\n\t" \
  "v_rcp_f32 v199, v199\n\t" \
  "v_mad_u32_u16 v223, v223, %[c128], v249\n\t" \
  "v_or_b32 v226, v226, v250\n\t" \
  "v_mad_u32_u16 v227, v227, 6, v251\n\t" \
  "s_waitcnt lgkmcnt(9)\n\t" \
  "v_mfma_f32_16x16x32_bf16 v[84:87], v[200:203], v[176:179], v[84:87]\n\t" \
  "v_exp_f32 v192, v116\n\t" \
  "v_exp_f32 v193, v117\n\t" \
  "v_exp_f32 v194, v118\n\t" \
  "v_exp_f32 v195, v119\n\t" \
  "s_waitcnt lgkmcnt(8)\n\t" \
  "v_mfma_f32_16x16x32_bf16 v[84:87], v[208:211], v[180:183], v[84:87]\n\t" \
  "v_mul_f32 v196, v112, v196\n\t" \
  "v_mul_f32 v197, v113, v197\n\t" \
  "v_mul_f32 v198, v114, v198\n\t" \
  "v_mul_f32 v199, v115, v199\n\t" \
  "s_waitcnt lgkmcnt(1)\n\t" \
  "s_cmp_lt_i32 %[nt], 1\n\t" \
  "s_cbranch_scc1 4f\n\t" \
  "s_mov_b32 m0, %[b2]\n\t" \
  "s_nop 0\n\t" \
  "global_load_lds_dwordx4 v226, %[ea]\n\t" \
  "global_load_lds_dword v227, %[pos] offset:1024\n\t" \
  "s_mov_b32 m0, %[b1]\n\t" \
  "s_nop 0\n\t" \
  "global_load_lds_dwordx4 v220, %[h0]\n\t" \
  "global_load_lds_dwordx4 v221, %[h1] offset:1024\n\t" \
  "global_load_lds_dwordx4 v222, %[h2] offset:2048\n\t" \
  "global_load_lds_dwordx4 v223, %[h3] offset:3072\n\t" \
  "4:\n\t" \
  "v_add_f32 v192, 1.0, v192\n\t" \
  "v_add_f32 v193, 1.0, v193\n\t" \
  "v_add_f32 v194, 1.0, v194\n\t" \
  "v_add_f32 v195, 1.0, v195\n\t" \
  "v_cvt_pk_bf16_f32 v190, v196, v197\n\t" \
  "v_cvt_pk_bf16_f32 v191, v198, v199\n\t" \
  "v_lshrrev_b32 v232, 20, v232\n\t" \
  "v_add_u32 v220, %[posd], v232\n\t" \
  "v_add_u32 v221, v240, v232\n\t" \
  "ds_write_b128 v243, v[188:191] offset:16\n\t" \
  "ds_read_b128 v[176:179], v242 offset:5376\n\t" \
  "ds_read_b128 v[180:183], v242 offset:5408\n\t" \
  "ds_write_b16 v233, v255 offset:5376\n\t" \
  "ds_read_b128 v[216:219], v220\n\t" \
  "ds_read_b128 v[172:175], v221 offset:32\n\t" \
  "ds_read_b128 v[168:171], v221\n\t" \
  "ds_read_b64_tr_b16 v[200:201], v244 offset:0\n\t" \
  "ds_read_b64_tr_b16 v[202:203], v244 offset:320\n\t" \
  "ds_read_b64_tr_b16 v[204:205], v244 offset:1280\n\t" \
  "ds_read_b64_tr_b16 v[206:207], v244 offset:1600\n\t" \
  "v_add_u32 v233, v241, v232\n\t" \
  "ds_write_b16_d16_hi v233, v255 offset:5376\n\t" \
  "v_mfma_f32_16x16x32_bf16 v[132:135], v[96:99], v[184:187], 0\n\t" \
  "v_rcp_f32 v192, v192\n\t" \
  "v_rcp_f32 v193, v193\n\t" \
  "v_rcp_f32 v194, v194\n\t" \
  "v_rcp_f32 v195, v195\n\t" \
  "v_mfma_f32_32x32x16_bf16 v[100:115], v[4:7], v[148:151], 0\n\t" \
  "v_exp_f32 v196, v120\n\t" \
  "v_exp_f32 v197, v121\n\t" \
  "v_exp_f32 v198, v122\n\t" \
  "v_exp_f32 v199, v123\n\t" \
  "v_mfma_f32_32x32x16_bf16 v[100:115], v[12:15], v[152:155], v[100:115]\n\t" \
  "v_mul_f32 v192, v116, v192\n\t" \
  "v_mul_f32 v193, v117, v193\n\t" \
  "v_mul_f32 v194, v118, v194\n\t" \
  "v_mul_f32 v195, v119, v195\n\t" \
  "v_add_f32 v196, 1.0, v196\n\t" \
  "v_add_f32 v197, 1.0, v197\n\t" \
  "v_add_f32 v198, 1.0, v198\n\t" \
  "v_add_f32 v199, 1.0, v199\n\t" \
  "v_cvt_pk_bf16_f32 v184, v192, v193\n\t" \
  "v_cvt_pk_bf16_f32 v185, v194, v195\n\t" \
  "v_and_b32 v225, 0xffff0000, v225\n\t" \
  "s_waitcnt lgkmcnt(7)\n\t" \
  "v_lshlrev_b32 v219, 16, v224\n\t" \
  "v_and_b32 v224, 0xffff0000, v224\n\t" \
  "v_sub_f32 v229, v218, v225\n\t" \
  "v_sub_f32 v216, v216, v219\n\t" \
  "v_sub_f32 v217, v217, v224\n\t" \
  "v_mfma_f32_32x32x16_bf16 v[100:115], v[20:23], v[156:159], v[100:115]\n\t" \
  "v_rcp_f32 v196, v196\n\t" \
  "v_rcp_f32 v197, v197\n\t" \
  "v_rcp_f32 v198, v198\n\t" \
  "v_rcp_f32 v199, v199\n\t" \
  "v_mul_f32 v218, v229, v229\n\t" \
  "v_cndmask_b32 v228, v216, v217, vcc\n\t" \
  "v_fmac_f32 v218, v216, v216\n\t" \
  "v_fmac_f32 v218, v217, v217\n\t" \
  "v_cvt_pk_bf16_f32 v218, v218, v218\n\t" \
  "s_waitcnt lgkmcnt(6)\n\t" \
  "v_cndmask_b32_sdwa v175, v175, v218, vcc dst_sel:WORD_1 dst_unused:UNUSED_PRESERVE src0_sel:WORD_1 src1_sel:WORD_1\n\t" \
  "v_mfma_f32_16x16x32_bf16 v[132:135], v[136:139], v[188:191], v[132:135]\n\t" \
  "v_exp_f32 v192, v124\n\t" \
  "v_exp_f32 v193, v125\n\t" \
  "v_exp_f32 v194, v126\n\t" \
  "v_exp_f32 v195, v127\n\t" \
  "ds_read2_b32 v[220:221], v252 offset0:0 offset1:2\n\t" \
  "ds_read2_b32 v[222:223], v252 offset0:4 offset1:6\n\t" \
  "ds_read_b64 v[226:227], v253\n\t" \
  "ds_read_b32 v232, v254\n\t" \
  "v_mfma_f32_32x32x16_bf16 v[100:115], v[28:31], v[160:163], v[100:115]\n\t" \
  "v_mul_f32 v196, v120, v196\n\t" \
  "v_mul_f32 v197, v121, v197\n\t" \
  "v_mul_f32 v198, v122, v198\n\t" \
  "v_mul_f32 v199, v123, v199\n\t" \
  "v_add_u32 v252, 0x100, v252\n\t" \
  "v_add_u32 v253, 0x100, v253\n\t" \
  "v_add_u32 v254, 0x100, v254\n\t" \
  "v_add_f32 v192, 1.0, v192\n\t" \
  "v_add_f32 v193, 1.0, v193\n\t" \
  "v_add_f32 v194, 1.0, v194\n\t" \
  "v_add_f32 v195, 1.0, v195\n\t" \
  "v_cvt_pk_bf16_f32 v186, v196, v197\n\t" \
  "v_cvt_pk_bf16_f32 v187, v198, v199\n\t" \
  "s_waitcnt lgkmcnt(9)\n\t" \
  "v_mfma_f32_32x32x16_bf16 v[100:115], v[36:39], v[168:171], v[100:115]\n\t" \
  "v_rcp_f32 v192, v192\n\t" \
  "v_rcp_f32 v193, v193\n\t" \
  "v_rcp_f32 v194, v194\n\t" \
  "v_rcp_f32 v195, v195\n\t" \
  "v_mfma_f32_32x32x16_bf16 v[100:115], v[44:47], v[172:175], v[100:115]\n\t" \
  "v_exp_f32 v196, v128\n\t" \
  "v_exp_f32 v197, v129\n\t" \
  "v_exp_f32 v198, v130\n\t" \
  "v_exp_f32 v199, v131\n\t" \
  "v_mfma_f32_32x32x16_bf16 v[100:115], v[88:91], v[164:167], v[100:115]\n\t" \
  "v_mul_f32 v192, v124, v192\n\t" \
  "v_mul_f32 v193, v125, v193\n\t" \
  "v_mul_f32 v194, v126, v194\n\t" \
  "v_mul_f32 v195, v127, v195\n\t" \
  "ds_write_b128 v243, v[184:187] offset:0\n\t" \
  "v_add_f32 v196, 1.0, v196\n\t" \
  "v_add_f32 v197, 1.0, v197\n\t" \
  "v_add_f32 v198, 1.0, v198\n\t" \
  "v_add_f32 v199, 1.0, v199\n\t" \
  "v_cvt_pk_bf16_f32 v188, v192, v193\n\t" \
  "v_cvt_pk_bf16_f32 v189, v194, v195\n\t" \
  "s_waitcnt lgkmcnt(8)\n\t" \
  "v_mfma_f32_32x32x16_bf16 v[52:67], v[200:203], v[176:179], v[52:67]\n\t" \
  "v_rcp_f32 v196, v196\n\t" \
  "v_rcp_f32 v197, v197\n\t" \
  "v_rcp_f32 v198, v198\n\t" \
  "v_rcp_f32 v199, v199\n\t" \
  "s_waitcnt lgkmcnt(6)\n\t" \
  "v_mfma_f32_32x32x16_bf16 v[52:67], v[204:207], v[180:183], v[52:67]\n\t" \
  "v_exp_f32 v192, v100\n\t" \
  "v_exp_f32 v193, v101\n\t" \
  "v_exp_f32 v194, v102\n\t" \
  "v_exp_f32 v195, v103\n\t" \
  "s_cmp_eq_u32 %[nt], 0\n\t" \
  "v_mfma_f32_16x16x32_bf16 v[132:135], v[140:143], v[184:187], v[132:135]\n\t" \
  "v_mul_f32 v196, v128, v196\n\t" \
  "v_mul_f32 v197, v129, v197\n\t" \
  "v_mul_f32 v198, v130, v198\n\t" \
  "v_mul_f32 v199, v131, v199\n\t" \
  "v_add_f32 v192, 1.0, v192\n\t" \
  "v_add_f32 v193, 1.0, v193\n\t" \
  "v_add_f32 v194, 1.0, v194\n\t" \
  "v_add_f32 v195, 1.0, v195\n\t" \
  "v_cvt_pk_bf16_f32 v190, v196, v197\n\t" \
  "v_cvt_pk_bf16_f32 v191, v198, v199\n\t" \
  "s_cbranch_scc0 1b\n\t" \
  "s_waitcnt lgkmcnt(0)\n\t" \
  "s_cmp_lg_u32 %[last], 0\n\t" \
  "s_cbranch_scc1 8f\n\t" \
  "ds_write_b128 v243, v[188:191] offset:16\n\t" \
  "ds_read_b64_tr_b16 v[200:201], v244 offset:0\n\t" \
  "ds_read_b64_tr_b16 v[202:203], v244 offset:320\n\t" \
  "ds_read_b64_tr_b16 v[204:205], v244 offset:1280\n\t" \
  "ds_read_b64_tr_b16 v[206:207], v244 offset:1600\n\t" \
  "v_mfma_f32_16x16x32_bf16 v[132:135], v[144:147], v[188:191], v[132:135]\n\t" \
  "s_waitcnt lgkmcnt(0)\n\t" \
  "v_mfma_f32_32x32x16_bf16 v[68:83], v[200:203], v[176:179], v[68:83]\n\t" \
  "v_mfma_f32_32x32x16_bf16 v[68:83], v[204:207], v[180:183], v[68:83]\n\t" \
  "s_nop 8\n\t" \
  "v_mul_f32 v230, v230, v132\n\t" \
  "v_mul_f32 v231, v231, v132\n\t" \
  "v_cvt_pk_bf16_f32 v230, v230, v231\n\t" \
  "ds_write_b16 v245, v230 offset:7456\n\t" \
  "ds_write_b16_d16_hi v241, v230 offset:7584\n\t" \
  "ds_read_b128 v[200:203], v246 offset:7456\n\t" \
  "ds_read_b128 v[208:211], v246 offset:7488\n\t" \
  "s_waitcnt lgkmcnt(0)\n\t" \
  "v_mfma_f32_16x16x32_bf16 v[84:87], v[200:203], v[176:179], v[84:87]\n\t" \
  "v_mfma_f32_16x16x32_bf16 v[84:87], v[208:211], v[180:183], v[84:87]\n\t" \
  "s_branch 9f\n\t" \
  "6:\n\t" \
  "s_cmp_lg_u32 %[last], 0\n\t" \
  "s_cbranch_scc0 9f\n\t" \
  "s_waitcnt lgkmcnt(0)\n\t" \
  "v_cndmask_b32 v224, 0, 24, vcc\n\t" \
  "v_xor_b32 v220, v234, v224\n\t" \
  "v_xor_b32 v221, v235, v224\n\t" \
  "v_xor_b32 v222, v236, v224\n\t" \
  "v_xor_b32 v223, v237, v224\n\t" \
  "v_xor_b32 v216, 16, v220\n\t" \
  "v_xor_b32 v217, 16, v221\n\t" \
  "v_xor_b32 v218, 16, v222\n\t" \
  "v_xor_b32 v219, 16, v223\n\t" \
  "ds_read_b64 v[148:149], v220\n\t" \
  "ds_read_b64 v[150:151], v216\n\t" \
  "ds_read_b64 v[152:153], v221\n\t" \
  "ds_read_b64 v[154:155], v217\n\t" \
  "ds_read_b64 v[156:157], v222\n\t" \
  "ds_read_b64 v[158:159], v218\n\t" \
  "ds_read_b64 v[160:161], v223\n\t" \
  "ds_read_b64 v[162:163], v219\n\t" \
  "ds_read_b128 v[4:7], v247 offset:6144\n\t" \
  "ds_read_b128 v[8:11], v247 offset:7168\n\t" \
  "ds_read_b128 v[12:15], v247 offset:8192\n\t" \
  "ds_read_b128 v[16:19], v247 offset:9216\n\t" \
  "ds_read_b128 v[20:23], v247 offset:10240\n\t" \
  "ds_read_b128 v[24:27], v247 offset:11264\n\t" \
  "v_subrev_u32 v225, %[b1], v241\n\t" \
  "v_lshl_add_u32 v225, v225, 5, v243\n\t" \
  "v_sub_u32 v225, v225, v224\n\t" \
  "s_nop 4\n\t" \
  "v_cvt_pk_bf16_f32 v184, v52, v53\n\t" \
  "v_cvt_pk_bf16_f32 v185, v54, v55\n\t" \
  "v_cvt_pk_bf16_f32 v186, v56, v57\n\t" \
  "v_cvt_pk_bf16_f32 v187, v58, v59\n\t" \
  "v_cvt_pk_bf16_f32 v188, v60, v61\n\t" \
  "v_cvt_pk_bf16_f32 v189, v62, v63\n\t" \
  "v_cvt_pk_bf16_f32 v190, v64, v65\n\t" \
  "v_cvt_pk_bf16_f32 v191, v66, v67\n\t" \
  "v_cvt_pk_bf16_f32 v36, v68, v69\n\t" \
  "v_cvt_pk_bf16_f32 v37, v70, v71\n\t" \
  "v_cvt_pk_bf16_f32 v38, v72, v73\n\t" \
  "v_cvt_pk_bf16_f32 v39, v74, v75\n\t" \
  "v_cvt_pk_bf16_f32 v40, v76, v77\n\t" \
  "v_cvt_pk_bf16_f32 v41, v78, v79\n\t" \
  "v_cvt_pk_bf16_f32 v42, v80, v81\n\t" \
  "v_cvt_pk_bf16_f32 v43, v82, v83\n\t" \
  "s_waitcnt lgkmcnt(5)\n\t" \
  "v_mfma_f32_32x32x16_bf16 v[100:115], v[4:7], v[148:151], 0\n\t" \
  "s_waitcnt lgkmcnt(4)\n\t" \
  "v_mfma_f32_32x32x16_bf16 v[116:131], v[8:11], v[148:151], 0\n\t" \
  "ds_read_b128 v[28:31], v247 offset:12288\n\t" \
  "ds_read_b128 v[32:35], v247 offset:13312\n\t" \
  "s_waitcnt lgkmcnt(5)\n\t" \
  "v_mfma_f32_32x32x16_bf16 v[100:115], v[12:15], v[152:155], v[100:115]\n\t" \
  "s_waitcnt lgkmcnt(4)\n\t" \
  "v_mfma_f32_32x32x16_bf16 v[116:131], v[16:19], v[152:155], v[116:131]\n\t" \
  "ds_read_b128 v[4:7], v247 offset:14336\n\t" \
  "ds_read_b128 v[8:11], v247 offset:15360\n\t" \
  "s_waitcnt lgkmcnt(5)\n\t" \
  "v_mfma_f32_32x32x16_bf16 v[100:115], v[20:23], v[156:159], v[100:115]\n\t" \
  "s_waitcnt lgkmcnt(4)\n\t" \
  "v_mfma_f32_32x32x16_bf16 v[116:131], v[24:27], v[156:159], v[116:131]\n\t" \
  "ds_read_b128 v[12:15], v247 offset:16384\n\t" \
  "ds_read_b128 v[16:19], v247 offset:17408\n\t" \
  "s_waitcnt lgkmcnt(5)\n\t" \
  "v_mfma_f32_32x32x16_bf16 v[100:115], v[28:31], v[160:163], v[100:115]\n\t" \
  "s_waitcnt lgkmcnt(4)\n\t" \
  "v_mfma_f32_32x32x16_bf16 v[116:131], v[32:35], v[160:163], v[116:131]\n\t" \
  "ds_read_b128 v[20:23], v247 offset:18432\n\t" \
  "ds_read_b128 v[24:27], v247 offset:19456\n\t" \
  "s_waitcnt lgkmcnt(5)\n\t" \
  "v_mfma_f32_32x32x16_bf16 v[100:115], v[4:7], v[184:187], v[100:115]\n\t" \
  "ds_read_b128 v[28:31], v247 offset:20480\n\t" \
  "ds_read_b128 v[32:35], v247 offset:21504\n\t" \
  "s_waitcnt lgkmcnt(5)\n\t" \
  "v_mfma_f32_32x32x16_bf16 v[100:115], v[12:15], v[188:191], v[100:115]\n\t" \
  "s_waitcnt lgkmcnt(3)\n\t" \
  "v_mfma_f32_32x32x16_bf16 v[100:115], v[20:23], v[36:39], v[100:115]\n\t" \
  "s_waitcnt lgkmcnt(1)\n\t" \
  "v_mfma_f32_32x32x16_bf16 v[100:115], v[28:31], v[40:43], v[100:115]\n\t" \
  "s_waitcnt lgkmcnt(0)\n\t" \
  "s_branch 10f\n\t" \
  "8:\n\t" \
  "ds_write_b128 v243, v[188:191] offset:16\n\t" \
  "ds_read_b64_tr_b16 v[200:201], v244 offset:0\n\t" \
  "ds_read_b64_tr_b16 v[202:203], v244 offset:320\n\t" \
  "ds_read_b64_tr_b16 v[204:205], v244 offset:1280\n\t" \
  "ds_read_b64_tr_b16 v[206:207], v244 offset:1600\n\t" \
  "v_mfma_f32_16x16x32_bf16 v[132:135], v[144:147], v[188:191], v[132:135]\n\t" \
  "v_cndmask_b32 v224, 0, 24, vcc\n\t" \
  "v_xor_b32 v220, v234, v224\n\t" \
  "v_xor_b32 v221, v235, v224\n\t" \
  "v_xor_b32 v222, v236, v224\n\t" \
  "v_xor_b32 v223, v237, v224\n\t" \
  "v_xor_b32 v216, 16, v220\n\t" \
  "v_xor_b32 v217, 16, v221\n\t" \
  "v_xor_b32 v218, 16, v222\n\t" \
  "v_xor_b32 v219, 16, v223\n\t" \
  "ds_read_b64 v[148:149], v220\n\t" \
  "ds_read_b64 v[150:151], v216\n\t" \
  "ds_read_b64 v[152:153], v221\n\t" \
  "ds_read_b64 v[154:155], v217\n\t" \
  "ds_read_b64 v[156:157], v222\n\t" \
  "ds_read_b64 v[158:159], v218\n\t" \
  "ds_read_b64 v[160:161], v223\n\t" \
  "ds_read_b64 v[162:163], v219\n\t" \
  "ds_read_b128 v[4:7], v247 offset:6144\n\t" \
  "ds_read_b128 v[8:11], v247 offset:7168\n\t" \
  "s_waitcnt lgkmcnt(11)\n\t" \
  "ds_read_b128 v[12:15], v247 offset:8192\n\t" \
  "ds_read_b128 v[16:19], v247 offset:9216\n\t" \
  "ds_read_b128 v[20:23], v247 offset:10240\n\t" \
  "ds_read_b128 v[24:27], v247 offset:11264\n\t" \
  "v_subrev_u32 v225, %[b1], v241\n\t" \
  "v_lshl_add_u32 v225, v225, 5, v243\n\t" \
  "v_sub_u32 v225, v225, v224\n\t" \
  "v_mfma_f32_32x32x16_bf16 v[68:83], v[200:203], v[176:179], v[68:83]\n\t" \
  "s_waitcnt lgkmcnt(5)\n\t" \
  "v_mfma_f32_32x32x16_bf16 v[100:115], v[4:7], v[148:151], 0\n\t" \
  "v_mfma_f32_32x32x16_bf16 v[68:83], v[204:207], v[180:183], v[68:83]\n\t" \
  "s_waitcnt lgkmcnt(4)\n\t" \
  "v_mfma_f32_32x32x16_bf16 v[116:131], v[8:11], v[148:151], 0\n\t" \
  "ds_read_b128 v[28:31], v247 offset:12288\n\t" \
  "ds_read_b128 v[32:35], v247 offset:13312\n\t" \
  "s_waitcnt lgkmcnt(5)\n\t" \
  "v_mfma_f32_32x32x16_bf16 v[100:115], v[12:15], v[152:155], v[100:115]\n\t" \
  "v_mul_f32 v230, v230, v132\n\t" \
  "v_mul_f32 v231, v231, v132\n\t" \
  "v_cvt_pk_bf16_f32 v230, v230, v231\n\t" \
  "ds_write_b16 v245, v230 offset:7456\n\t" \
  "ds_write_b16_d16_hi v241, v230 offset:7584\n\t" \
  "ds_read_b128 v[200:203], v246 offset:7456\n\t" \
  "ds_read_b128 v[208:211], v246 offset:7488\n\t" \
  "v_cvt_pk_bf16_f32 v184, v52, v53\n\t" \
  "v_cvt_pk_bf16_f32 v185, v54, v55\n\t" \
  "v_cvt_pk_bf16_f32 v186, v56, v57\n\t" \
  "v_cvt_pk_bf16_f32 v187, v58, v59\n\t" \
  "s_waitcnt lgkmcnt(8)\n\t" \
  "v_mfma_f32_32x32x16_bf16 v[116:131], v[16:19], v[152:155], v[116:131]\n\t" \
  "ds_read_b128 v[4:7], v247 offset:14336\n\t" \
  "ds_read_b128 v[8:11], v247 offset:15360\n\t" \
  "s_waitcnt lgkmcnt(9)\n\t" \
  "v_mfma_f32_32x32x16_bf16 v[100:115], v[20:23], v[156:159], v[100:115]\n\t" \
  "v_cvt_pk_bf16_f32 v188, v60, v61\n\t" \
  "v_cvt_pk_bf16_f32 v189, v62, v63\n\t" \
  "v_cvt_pk_bf16_f32 v190, v64, v65\n\t" \
  "v_cvt_pk_bf16_f32 v191, v66, v67\n\t" \
  "s_waitcnt lgkmcnt(8)\n\t" \
  "v_mfma_f32_32x32x16_bf16 v[116:131], v[24:27], v[156:159], v[116:131]\n\t" \
  "ds_read_b128 v[12:15], v247 offset:16384\n\t" \
  "ds_read_b128 v[16:19], v247 offset:17408\n\t" \
  "s_waitcnt lgkmcnt(9)\n\t" \
  "v_mfma_f32_32x32x16_bf16 v[100:115], v[28:31], v[160:163], v[100:115]\n\t" \
  "v_cvt_pk_bf16_f32 v36, v68, v69\n\t" \
  "v_cvt_pk_bf16_f32 v37, v70, v71\n\t" \
  "v_cvt_pk_bf16_f32 v38, v72, v73\n\t" \
  "v_cvt_pk_bf16_f32 v39, v74, v75\n\t" \
  "s_waitcnt lgkmcnt(5)\n\t" \
  "v_mfma_f32_16x16x32_bf16 v[84:87], v[200:203], v[176:179], v[84:87]\n\t" \
  "v_cvt_pk_bf16_f32 v40, v76, v77\n\t" \
  "v_cvt_pk_bf16_f32 v41, v78, v79\n\t" \
  "v_cvt_pk_bf16_f32 v42, v80, v81\n\t" \
  "v_cvt_pk_bf16_f32 v43, v82, v83\n\t" \
  "v_mfma_f32_32x32x16_bf16 v[116:131], v[32:35], v[160:163], v[116:131]\n\t" \
  "ds_read_b128 v[20:23], v247 offset:18432\n\t" \
  "ds_read_b128 v[24:27], v247 offset:19456\n\t" \
  "s_waitcnt lgkmcnt(6)\n\t" \
  "v_mfma_f32_16x16x32_bf16 v[84:87], v[208:211], v[180:183], v[84:87]\n\t" \
  "s_waitcnt lgkmcnt(5)\n\t" \
  "v_mfma_f32_32x32x16_bf16 v[100:115], v[4:7], v[184:187], v[100:115]\n\t" \
  "ds_read_b128 v[28:31], v247 offset:20480\n\t" \
  "ds_read_b128 v[32:35], v247 offset:21504\n\t" \
  "s_waitcnt lgkmcnt(5)\n\t" \
  "v_mfma_f32_32x32x16_bf16 v[100:115], v[12:15], v[188:191], v[100:115]\n\t" \
  "s_waitcnt lgkmcnt(3)\n\t" \
  "v_mfma_f32_32x32x16_bf16 v[100:115], v[20:23], v[36:39], v[100:115]\n\t" \
  "s_waitcnt lgkmcnt(1)\n\t" \
  "v_mfma_f32_32x32x16_bf16 v[100:115], v[28:31], v[40:43], v[100:115]\n\t" \
  "s_waitcnt lgkmcnt(0)\n\t" \
  "10:\n\t" \
  "v_mov_b32 v212, 0xbfb8aa3b\n\t" \
  "v_mfma_f32_32x32x16_bf16 v[116:131], v[8:11], v[184:187], v[116:131]\n\t" \
  "v_mfma_f32_32x32x16_bf16 v[116:131], v[16:19], v[188:191], v[116:131]\n\t" \
  "s_nop 8\n\t" \
  "v_exp_f32 v192, v100\n\t" \
  "v_exp_f32 v193, v101\n\t" \
  "v_exp_f32 v194, v102\n\t" \
  "v_exp_f32 v195, v103\n\t" \
  "v_lshlrev_b32 v200, 16, v148\n\t" \
  "v_and_b32 v201, 0xffff0000, v148\n\t" \
  "v_lshlrev_b32 v202, 16, v149\n\t" \
  "v_and_b32 v203, 0xffff0000, v149\n\t" \
  "v_mfma_f32_32x32x16_bf16 v[116:131], v[24:27], v[36:39], v[116:131]\n\t" \
  "v_fmaak_f32 v192, v192, v212, 0xbfb8aa3b\n\t" \
  "v_fmaak_f32 v193, v193, v212, 0xbfb8aa3b\n\t" \
  "v_fmaak_f32 v194, v194, v212, 0xbfb8aa3b\n\t" \
  "v_fmaak_f32 v195, v195, v212, 0xbfb8aa3b\n\t" \
  "v_rcp_f32 v192, v192\n\t" \
  "v_rcp_f32 v193, v193\n\t" \
  "v_rcp_f32 v194, v194\n\t" \
  "v_rcp_f32 v195, v195\n\t" \
  "v_mfma_f32_32x32x16_bf16 v[116:131], v[32:35], v[40:43], v[116:131]\n\t" \
  "v_exp_f32 v196, v104\n\t" \
  "v_exp_f32 v197, v105\n\t" \
  "v_exp_f32 v198, v106\n\t" \
  "v_exp_f32 v199, v107\n\t" \
  "v_fmac_f32 v200, v100, v192\n\t" \
  "v_fmac_f32 v201, v101, v193\n\t" \
  "v_fmac_f32 v202, v102, v194\n\t" \
  "v_fmac_f32 v203, v103, v195\n\t" \
  "v_lshlrev_b32 v204, 16, v150\n\t" \
  "v_and_b32 v205, 0xffff0000, v150\n\t" \
  "v_lshlrev_b32 v206, 16, v151\n\t" \
  "v_and_b32 v207, 0xffff0000, v151\n\t" \
  "v_fmaak_f32 v196, v196, v212, 0xbfb8aa3b\n\t" \
  "v_fmaak_f32 v197, v197, v212, 0xbfb8aa3b\n\t" \
  "v_fmaak_f32 v198, v198, v212, 0xbfb8aa3b\n\t" \
  "v_fmaak_f32 v199, v199, v212, 0xbfb8aa3b\n\t" \
  "v_cvt_pk_bf16_f32 v176, v200, v201\n\t" \
  "v_cvt_pk_bf16_f32 v177, v202, v203\n\t" \
  "ds_write_b64 v225, v[176:177] offset:0\n\t" \
  "v_rcp_f32 v196, v196\n\t" \
  "v_rcp_f32 v197, v197\n\t" \
  "v_rcp_f32 v198, v198\n\t" \
  "v_rcp_f32 v199, v199\n\t" \
  "v_exp_f32 v192, v108\n\t" \
  "v_exp_f32 v193, v109\n\t" \
  "v_exp_f32 v194, v110\n\t" \
  "v_exp_f32 v195, v111\n\t" \
  "v_fmac_f32 v204, v104, v196\n\t" \
  "v_fmac_f32 v205, v105, v197\n\t" \
  "v_fmac_f32 v206, v106, v198\n\t" \
  "v_fmac_f32 v207, v107, v199\n\t" \
  "v_lshlrev_b32 v200, 16, v152\n\t" \
  "v_and_b32 v201, 0xffff0000, v152\n\t" \
  "v_lshlrev_b32 v202, 16, v153\n\t" \
  "v_and_b32 v203, 0xffff0000, v153\n\t" \
  "v_fmaak_f32 v192, v192, v212, 0xbfb8aa3b\n\t" \
  "v_fmaak_f32 v193, v193, v212, 0xbfb8aa3b\n\t" \
  "v_fmaak_f32 v194, v194, v212, 0xbfb8aa3b\n\t" \
  "v_fmaak_f32 v195, v195, v212, 0xbfb8aa3b\n\t" \
  "v_cvt_pk_bf16_f32 v178, v204, v205\n\t" \
  "v_cvt_pk_bf16_f32 v179, v206, v207\n\t" \
  "ds_write_b64 v225, v[178:179] offset:16\n\t" \
  "v_rcp_f32 v192, v192\n\t" \
  "v_rcp_f32 v193, v193\n\t" \
  "v_rcp_f32 v194, v194\n\t" \
  "v_rcp_f32 v195, v195\n\t" \
  "v_exp_f32 v196, v112\n\t" \
  "v_exp_f32 v197, v113\n\t" \
  "v_exp_f32 v198, v114\n\t" \
  "v_exp_f32 v199, v115\n\t" \
  "v_fmac_f32 v200, v108, v192\n\t" \
  "v_fmac_f32 v201, v109, v193\n\t" \
  "v_fmac_f32 v202, v110, v194\n\t" \
  "v_fmac_f32 v203, v111, v195\n\t" \
  "v_lshlrev_b32 v204, 16, v154\n\t" \
  "v_and_b32 v205, 0xffff0000, v154\n\t" \
  "v_lshlrev_b32 v206, 16, v155\n\t" \
  "v_and_b32 v207, 0xffff0000, v155\n\t" \
  "v_fmaak_f32 v196, v196, v212, 0xbfb8aa3b\n\t" \
  "v_fmaak_f32 v197, v197, v212, 0xbfb8aa3b\n\t" \
  "v_fmaak_f32 v198, v198, v212, 0xbfb8aa3b\n\t" \
  "v_fmaak_f32 v199, v199, v212, 0xbfb8aa3b\n\t" \
  "v_cvt_pk_bf16_f32 v180, v200, v201\n\t" \
  "v_cvt_pk_bf16_f32 v181, v202, v203\n\t" \
  "ds_write_b64 v225, v[180:181] offset:32\n\t" \
  "v_rcp_f32 v196, v196\n\t" \
  "v_rcp_f32 v197, v197\n\t" \
  "v_rcp_f32 v198, v198\n\t" \
  "v_rcp_f32 v199, v199\n\t" \
  "v_exp_f32 v192, v116\n\t" \
  "v_exp_f32 v193, v117\n\t" \
  "v_exp_f32 v194, v118\n\t" \
  "v_exp_f32 v195, v119\n\t" \
  "v_fmac_f32 v204, v112, v196\n\t" \
  "v_fmac_f32 v205, v113, v197\n\t" \
  "v_fmac_f32 v206, v114, v198\n\t" \
  "v_fmac_f32 v207, v115, v199\n\t" \
  "v_lshlrev_b32 v200, 16, v156\n\t" \
  "v_and_b32 v201, 0xffff0000, v156\n\t" \
  "v_lshlrev_b32 v202, 16, v157\n\t" \
  "v_and_b32 v203, 0xffff0000, v157\n\t" \
  "v_fmaak_f32 v192, v192, v212, 0xbfb8aa3b\n\t" \
  "v_fmaak_f32 v193, v193, v212, 0xbfb8aa3b\n\t" \
  "v_fmaak_f32 v194, v194, v212, 0xbfb8aa3b\n\t" \
  "v_fmaak_f32 v195, v195, v212, 0xbfb8aa3b\n\t" \
  "v_cvt_pk_bf16_f32 v182, v204, v205\n\t" \
  "v_cvt_pk_bf16_f32 v183, v206, v207\n\t" \
  "ds_write_b64 v225, v[182:183] offset:48\n\t" \
  "v_rcp_f32 v192, v192\n\t" \
  "v_rcp_f32 v193, v193\n\t" \
  "v_rcp_f32 v194, v194\n\t" \
  "v_rcp_f32 v195, v195\n\t" \
  "v_exp_f32 v196, v120\n\t" \
  "v_exp_f32 v197, v121\n\t" \
  "v_exp_f32 v198, v122\n\t" \
  "v_exp_f32 v199, v123\n\t" \
  "v_fmac_f32 v200, v116, v192\n\t" \
  "v_fmac_f32 v201, v117, v193\n\t" \
  "v_fmac_f32 v202, v118, v194\n\t" \
  "v_fmac_f32 v203, v119, v195\n\t" \
  "v_lshlrev_b32 v204, 16, v158\n\t" \
  "v_and_b32 v205, 0xffff0000, v158\n\t" \
  "v_lshlrev_b32 v206, 16, v159\n\t" \
  "v_and_b32 v207, 0xffff0000, v159\n\t" \
  "v_fmaak_f32 v196, v196, v212, 0xbfb8aa3b\n\t" \
  "v_fmaak_f32 v197, v197, v212, 0xbfb8aa3b\n\t" \
  "v_fmaak_f32 v198, v198, v212, 0xbfb8aa3b\n\t" \
  "v_fmaak_f32 v199, v199, v212, 0xbfb8aa3b\n\t" \
  "v_cvt_pk_bf16_f32 v176, v200, v201\n\t" \
  "v_cvt_pk_bf16_f32 v177, v202, v203\n\t" \
  "ds_write_b64 v225, v[176:177] offset:64\n\t" \
  "v_rcp_f32 v196, v196\n\t" \
  "v_rcp_f32 v197, v197\n\t" \
  "v_rcp_f32 v198, v198\n\t" \
  "v_rcp_f32 v199, v199\n\t" \
  "v_exp_f32 v192, v124\n\t" \
  "v_exp_f32 v193, v125\n\t" \
  "v_exp_f32 v194, v126\n\t" \
  "v_exp_f32 v195, v127\n\t" \
  "v_fmac_f32 v204, v120, v196\n\t" \
  "v_fmac_f32 v205, v121, v197\n\t" \
  "v_fmac_f32 v206, v122, v198\n\t" \
  "v_fmac_f32 v207, v123, v199\n\t" \
  "v_lshlrev_b32 v200, 16, v160\n\t" \
  "v_and_b32 v201, 0xffff0000, v160\n\t" \
  "v_lshlrev_b32 v202, 16, v161\n\t" \
  "v_and_b32 v203, 0xffff0000, v161\n\t" \
  "v_fmaak_f32 v192, v192, v212, 0xbfb8aa3b\n\t" \
  "v_fmaak_f32 v193, v193, v212, 0xbfb8aa3b\n\t" \
  "v_fmaak_f32 v194, v194, v212, 0xbfb8aa3b\n\t" \
  "v_fmaak_f32 v195, v195, v212, 0xbfb8aa3b\n\t" \
  "v_cvt_pk_bf16_f32 v178, v204, v205\n\t" \
  "v_cvt_pk_bf16_f32 v179, v206, v207\n\t" \
  "ds_write_b64 v225, v[178:179] offset:80\n\t" \
  "v_rcp_f32 v192, v192\n\t" \
  "v_rcp_f32 v193, v193\n\t" \
  "v_rcp_f32 v194, v194\n\t" \
  "v_rcp_f32 v195, v195\n\t" \
  "v_exp_f32 v196, v128\n\t" \
  "v_exp_f32 v197, v129\n\t" \
  "v_exp_f32 v198, v130\n\t" \
  "v_exp_f32 v199, v131\n\t" \
  "v_fmac_f32 v200, v124, v192\n\t" \
  "v_fmac_f32 v201, v125, v193\n\t" \
  "v_fmac_f32 v202, v126, v194\n\t" \
  "v_fmac_f32 v203, v127, v195\n\t" \
  "v_lshlrev_b32 v204, 16, v162\n\t" \
  "v_and_b32 v205, 0xffff0000, v162\n\t" \
  "v_lshlrev_b32 v206, 16, v163\n\t" \
  "v_and_b32 v207, 0xffff0000, v163\n\t" \
  "v_fmaak_f32 v196, v196, v212, 0xbfb8aa3b\n\t" \
  "v_fmaak_f32 v197, v197, v212, 0xbfb8aa3b\n\t" \
  "v_fmaak_f32 v198, v198, v212, 0xbfb8aa3b\n\t" \
  "v_fmaak_f32 v199, v199, v212, 0xbfb8aa3b\n\t" \
  "v_cvt_pk_bf16_f32 v180, v200, v201\n\t" \
  "v_cvt_pk_bf16_f32 v181, v202, v203\n\t" \
  "ds_write_b64 v225, v[180:181] offset:96\n\t" \
  "v_rcp_f32 v196, v196\n\t" \
  "v_rcp_f32 v197, v197\n\t" \
  "v_rcp_f32 v198, v198\n\t" \
  "v_rcp_f32 v199, v199\n\t" \
  "s_nop 0\n\t" \
  "v_fmac_f32 v204, v128, v196\n\t" \
  "v_fmac_f32 v205, v129, v197\n\t" \
  "v_fmac_f32 v206, v130, v198\n\t" \
  "v_fmac_f32 v207, v131, v199\n\t" \
  "v_cvt_pk_bf16_f32 v182, v204, v205\n\t" \
  "v_cvt_pk_bf16_f32 v183, v206, v207\n\t" \
  "ds_write_b64 v225, v[182:183] offset:112\n\t" \
  "9:\n\t" \
  "ds_write_b16 v233, v255 offset:5376\n\t" \
  "s_mov_b32 m0, %[keep]\n\t" \
  "s_setprio 0\n\t" \
  "s_nop 11\n\t" \
  ""
#define LOOP_ASM_PLAIN \
  "s_mov_b32 %[keep], m0\n\t" \
  "s_mov_b64 vcc, %[hsel]\n\t" \
  "ds_read2_b32 v[220:221], v252 offset0:0 offset1:2\n\t" \
  "ds_read2_b32 v[222:223], v252 offset0:4 offset1:6\n\t" \
  "ds_read_b64 v[226:227], v253\n\t" \
  "ds_read_b32 v232, v254\n\t" \
  "v_mov_b32 v176, 0\n\t" \
  "v_mov_b32 v177, 0\n\t" \
  "v_mov_b32 v178, 0\n\t" \
  "v_mov_b32 v179, 0\n\t" \
  "v_mov_b32 v180, 0\n\t" \
  "v_mov_b32 v181, 0\n\t" \
  "v_mov_b32 v182, 0\n\t" \
  "v_mov_b32 v183, 0\n\t" \
  "v_mov_b32 v184, 0\n\t" \
  "v_mov_b32 v185, 0\n\t" \
  "v_mov_b32 v186, 0\n\t" \
  "v_mov_b32 v187, 0\n\t" \
  "v_mov_b32 v188, 0\n\t" \
  "v_mov_b32 v189, 0\n\t" \
  "v_mov_b32 v190, 0\n\t" \
  "v_mov_b32 v191, 0\n\t" \
  "v_mov_b32 v230, 0\n\t" \
  "v_mov_b32 v231, 0\n\t" \
  "v_mov_b32 v208, 0\n\t" \
  "v_mov_b32 v209, 0\n\t" \
  "v_mov_b32 v210, 0\n\t" \
  "v_mov_b32 v211, 0\n\t" \
  "ds_write_b128 v243, v[184:187] offset:0\n\t" \
  "s_nop 0\n\t" \
  "v_mfma_f32_16x16x32_bf16 v[132:135], v[184:187], v[188:191], 0\n\t" \
  "v_add_u32 v252, 0x100, v252\n\t" \
  "v_add_u32 v253, 0x100, v253\n\t" \
  "v_add_u32 v254, 0x100, v254\n\t" \
  "s_waitcnt lgkmcnt(0)\n\t" \
  "v_lshrrev_b32 v232, 20, v232\n\t" \
  "v_add_u32 v233, %[posd], v232\n\t" \
  "ds_read_b128 v[216:219], v233\n\t" \
  "v_add_u32 v233, v240, v232\n\t" \
  "ds_read_b128 v[172:175], v233 offset:32\n\t" \
  "ds_read_b128 v[168:171], v233\n\t" \
  "v_add_u32 v233, v241, v232\n\t" \
  "ds_write_b16_d16_hi v233, v255 offset:5376\n\t" \
  "v_mad_u32_u16 v220, v220, %[c128], v248\n\t" \
  "v_mad_u32_u16 v221, v221, %[c128], v249\n\t" \
  "v_mad_u32_u16 v222, v222, %[c128], v248\n\t" \
  "v_mad_u32_u16 v223, v223, %[c128], v249\n\t" \
  "v_or_b32 v226, v226, v250\n\t" \
  "v_mad_u32_u16 v227, v227, 6, v251\n\t" \
  "s_waitcnt vmcnt(0)\n\t" \
  "ds_read_b128 v[148:151], v234\n\t" \
  "ds_read_b128 v[152:155], v235\n\t" \
  "ds_read_b128 v[156:159], v236\n\t" \
  "ds_read_b128 v[160:163], v237\n\t" \
  "ds_read_b128 v[164:167], v238 offset:4096\n\t" \
  "ds_read_b64 v[224:225], v239 offset:5120\n\t" \
  "s_waitcnt lgkmcnt(0)\n\t" \
  "s_cmp_lt_i32 %[nt], 1\n\t" \
  "s_cbranch_scc1 7f\n\t" \
  "s_mov_b32 m0, %[b2]\n\t" \
  "s_nop 0\n\t" \
  "global_load_lds_dwordx4 v226, %[ea]\n\t" \
  "global_load_lds_dword v227, %[pos] offset:1024\n\t" \
  "s_mov_b32 m0, %[b1]\n\t" \
  "s_nop 0\n\t" \
  "global_load_lds_dwordx4 v220, %[h0]\n\t" \
  "global_load_lds_dwordx4 v221, %[h1] offset:1024\n\t" \
  "global_load_lds_dwordx4 v222, %[h2] offset:2048\n\t" \
  "global_load_lds_dwordx4 v223, %[h3] offset:3072\n\t" \
  "7:\n\t" \
  "ds_read2_b32 v[220:221], v252 offset0:0 offset1:2\n\t" \
  "ds_read2_b32 v[222:223], v252 offset0:4 offset1:6\n\t" \
  "ds_read_b64 v[226:227], v253\n\t" \
  "ds_read_b32 v232, v254\n\t" \
  "v_add_u32 v252, 0x100, v252\n\t" \
  "v_add_u32 v253, 0x100, v253\n\t" \
  "v_add_u32 v254, 0x100, v254\n\t" \
  "v_and_b32 v225, 0xffff0000, v225\n\t" \
  "v_lshlrev_b32 v219, 16, v224\n\t" \
  "v_and_b32 v224, 0xffff0000, v224\n\t" \
  "v_sub_f32 v229, v218, v225\n\t" \
  "v_sub_f32 v216, v216, v219\n\t" \
  "v_sub_f32 v217, v217, v224\n\t" \
  "v_mul_f32 v218, v229, v229\n\t" \
  "v_cndmask_b32 v228, v216, v217, vcc\n\t" \
  "v_fmac_f32 v218, v216, v216\n\t" \
  "v_fmac_f32 v218, v217, v217\n\t" \
  "v_cvt_pk_bf16_f32 v218, v218, v218\n\t" \
  "v_cndmask_b32_sdwa v175, v175, v218, vcc dst_sel:WORD_1 dst_unused:UNUSED_PRESERVE src0_sel:WORD_1 src1_sel:WORD_1\n\t" \
  "v_mfma_f32_32x32x16_bf16 v[100:115], v[4:7], v[148:151], 0\n\t" \
  "v_mfma_f32_32x32x16_bf16 v[100:115], v[12:15], v[152:155], v[100:115]\n\t" \
  "v_mfma_f32_32x32x16_bf16 v[100:115], v[20:23], v[156:159], v[100:115]\n\t" \
  "v_mfma_f32_32x32x16_bf16 v[100:115], v[28:31], v[160:163], v[100:115]\n\t" \
  "v_mfma_f32_32x32x16_bf16 v[100:115], v[36:39], v[168:171], v[100:115]\n\t" \
  "v_mfma_f32_32x32x16_bf16 v[100:115], v[88:91], v[164:167], v[100:115]\n\t" \
  "v_mfma_f32_32x32x16_bf16 v[100:115], v[44:47], v[172:175], v[100:115]\n\t" \
  "s_cmp_eq_u32 %[nt], 0\n\t" \
  "s_nop 10\n\t" \
  "v_mul_f32 v192, 0xbfb8aa3b, v100\n\t" \
  "v_mul_f32 v193, 0xbfb8aa3b, v101\n\t" \
  "v_mul_f32 v194, 0xbfb8aa3b, v102\n\t" \
  "v_mul_f32 v195, 0xbfb8aa3b, v103\n\t" \
  "v_exp_f32 v192, v192\n\t" \
  "v_exp_f32 v193, v193\n\t" \
  "v_exp_f32 v194, v194\n\t" \
  "v_exp_f32 v195, v195\n\t" \
  "s_nop 0\n\t" \
  "v_add_f32 v192, 1.0, v192\n\t" \
  "v_add_f32 v193, 1.0, v193\n\t" \
  "v_add_f32 v194, 1.0, v194\n\t" \
  "v_add_f32 v195, 1.0, v195\n\t" \
  "s_cbranch_scc1 6f\n\t" \
  "s_cmp_lt_i32 %[thr], 1\n\t" \
  "s_cbranch_scc1 1f\n\t" \
  "s_setprio 1\n\t" \
  "1:\n\t" \
  "ds_write_b128 v243, v[188:191] offset:16\n\t" \
  "ds_read_b32 v214, v213\n\t" \
  "ds_read_b64_tr_b16 v[200:201], v244 offset:0\n\t" \
  "ds_read_b64_tr_b16 v[202:203], v244 offset:320\n\t" \
  "ds_read_b64_tr_b16 v[204:205], v244 offset:1280\n\t" \
  "ds_read_b64_tr_b16 v[206:207], v244 offset:1600\n\t" \
  "v_mfma_f32_16x16x32_bf16 v[132:135], v[144:147], v[188:191], v[132:135]\n\t" \
  "v_rcp_f32 v192, v192\n\t" \
  "v_rcp_f32 v193, v193\n\t" \
  "v_rcp_f32 v194, v194\n\t" \
  "v_rcp_f32 v195, v195\n\t" \
  "v_mfma_f32_32x32x16_bf16 v[116:131], v[8:11], v[148:151], 0\n\t" \
  "v_mul_f32 v196, 0xbfb8aa3b, v104\n\t" \
  "v_mul_f32 v197, 0xbfb8aa3b, v105\n\t" \
  "v_mul_f32 v198, 0xbfb8aa3b, v106\n\t" \
  "v_mul_f32 v199, 0xbfb8aa3b, v107\n\t" \
  "v_exp_f32 v196, v196\n\t" \
  "v_exp_f32 v197, v197\n\t" \
  "v_exp_f32 v198, v198\n\t" \
  "v_exp_f32 v199, v199\n\t" \
  "v_mfma_f32_32x32x16_bf16 v[116:131], v[16:19], v[152:155], v[116:131]\n\t" \
  "v_mul_f32 v192, v100, v192\n\t" \
  "v_mul_f32 v193, v101, v193\n\t" \
  "v_mul_f32 v194, v102, v194\n\t" \
  "v_mul_f32 v195, v103, v195\n\t" \
  "v_add_f32 v196, 1.0, v196\n\t" \
  "v_add_f32 v197, 1.0, v197\n\t" \
  "v_add_f32 v198, 1.0, v198\n\t" \
  "v_add_f32 v199, 1.0, v199\n\t" \
  "v_cvt_pk_bf16_f32 v184, v192, v193\n\t" \
  "v_cvt_pk_bf16_f32 v185, v194, v195\n\t" \
  "v_mfma_f32_32x32x16_bf16 v[116:131], v[24:27], v[156:159], v[116:131]\n\t" \
  "v_rcp_f32 v196, v196\n\t" \
  "v_rcp_f32 v197, v197\n\t" \
  "v_rcp_f32 v198, v198\n\t" \
  "v_rcp_f32 v199, v199\n\t" \
  "s_waitcnt lgkmcnt(2)\n\t" \
  "v_mfma_f32_32x32x16_bf16 v[68:83], v[200:203], v[176:179], v[68:83]\n\t" \
  "v_mul_f32 v192, 0xbfb8aa3b, v108\n\t" \
  "v_mul_f32 v193, 0xbfb8aa3b, v109\n\t" \
  "v_mul_f32 v194, 0xbfb8aa3b, v110\n\t" \
  "v_mul_f32 v195, 0xbfb8aa3b, v111\n\t" \
  "v_exp_f32 v192, v192\n\t" \
  "v_exp_f32 v193, v193\n\t" \
  "v_exp_f32 v194, v194\n\t" \
  "v_exp_f32 v195, v195\n\t" \
  "v_mfma_f32_32x32x16_bf16 v[116:131], v[32:35], v[160:163], v[116:131]\n\t" \
  "v_mul_f32 v196, v104, v196\n\t" \
  "v_mul_f32 v197, v105, v197\n\t" \
  "v_mul_f32 v198, v106, v198\n\t" \
  "v_mul_f32 v199, v107, v199\n\t" \
  "v_mul_f32 v230, v230, v132\n\t" \
  "v_mul_f32 v231, v231, v132\n\t" \
  "v_cvt_pk_bf16_f32 v230, v230, v231\n\t" \
  "ds_write_b16 v245, v230 offset:7456\n\t" \
  "ds_write_b16_d16_hi v241, v230 offset:7584\n\t" \
  "ds_read_b128 v[200:203], v246 offset:7456\n\t" \
  "ds_read_b128 v[208:211], v246 offset:7488\n\t" \
  "v_mov_b32 v230, v228\n\t" \
  "v_mov_b32 v231, v229\n\t" \
  "v_add_f32 v192, 1.0, v192\n\t" \
  "v_add_f32 v193, 1.0, v193\n\t" \
  "v_add_f32 v194, 1.0, v194\n\t" \
  "v_add_f32 v195, 1.0, v195\n\t" \
  "v_cvt_pk_bf16_f32 v186, v196, v197\n\t" \
  "v_cvt_pk_bf16_f32 v187, v198, v199\n\t" \
  "v_mfma_f32_32x32x16_bf16 v[116:131], v[40:43], v[168:171], v[116:131]\n\t" \
  "v_rcp_f32 v192, v192\n\t" \
  "v_rcp_f32 v193, v193\n\t" \
  "v_rcp_f32 v194, v194\n\t" \
  "v_rcp_f32 v195, v195\n\t" \
  "s_sub_u32 %[nt], %[nt], 1\n\t" \
  "v_mov_b32 v215, %[nt]\n\t" \
  "v_readfirstlane_b32 %[tmp], v214\n\t" \
  "s_mov_b64 exec, 1\n\t" \
  "ds_write_b32 v212, v215\n\t" \
  "s_mov_b64 exec, -1\n\t" \
  "s_sub_i32 %[tmp], %[nt], %[tmp]\n\t" \
  "s_add_i32 %[tmp], %[tmp], %[thr]\n\t" \
  "s_cmp_gt_i32 %[tmp], 0\n\t" \
  "s_setprio 0\n\t" \
  "s_cbranch_scc0 3f\n\t" \
  "s_setprio 1\n\t" \
  "3:\n\t" \
  "v_mfma_f32_32x32x16_bf16 v[116:131], v[92:95], v[164:167], v[116:131]\n\t" \
  "v_mul_f32 v196, 0xbfb8aa3b, v112\n\t" \
  "v_mul_f32 v197, 0xbfb8aa3b, v113\n\t" \
  "v_mul_f32 v198, 0xbfb8aa3b, v114\n\t" \
  "v_mul_f32 v199, 0xbfb8aa3b, v115\n\t" \
  "v_exp_f32 v196, v196\n\t" \
  "v_exp_f32 v197, v197\n\t" \
  "v_exp_f32 v198, v198\n\t" \
  "v_exp_f32 v199, v199\n\t" \
  "v_mfma_f32_32x32x16_bf16 v[116:131], v[48:51], v[172:175], v[116:131]\n\t" \
  "v_mul_f32 v192, v108, v192\n\t" \
  "v_mul_f32 v193, v109, v193\n\t" \
  "v_mul_f32 v194, v110, v194\n\t" \
  "v_mul_f32 v195, v111, v195\n\t" \
  "s_waitcnt vmcnt(0)\n\t" \
  "ds_read_b128 v[148:151], v234\n\t" \
  "ds_read_b128 v[152:155], v235\n\t" \
  "ds_read_b128 v[156:159], v236\n\t" \
  "ds_read_b128 v[160:163], v237\n\t" \
  "ds_read_b128 v[164:167], v238 offset:4096\n\t" \
  "ds_read_b64 v[224:225], v239 offset:5120\n\t" \
  "ds_write_b128 v243, v[184:187] offset:0\n\t" \
  "v_add_f32 v196, 1.0, v196\n\t" \
  "v_add_f32 v197, 1.0, v197\n\t" \
  "v_add_f32 v198, 1.0, v198\n\t" \
  "v_add_f32 v199, 1.0, v199\n\t" \
  "v_cvt_pk_bf16_f32 v188, v192, v193\n\t" \
  "v_cvt_pk_bf16_f32 v189, v194, v195\n\t" \
  "v_mad_u32_u16 v220, v220, %[c128], v248\n\t" \
  "v_mad_u32_u16 v221, v221, %[c128], v249\n\t" \
  "v_mad_u32_u16 v222, v222, %[c128], v248\n\t" \
  "s_waitcnt lgkmcnt(12)\n\t" \
  "v_mfma_f32_32x32x16_bf16 v[68:83], v[204:207], v[180:183], v[68:83]\n\t" \
  "v_rcp_f32 v196, v196\n\t" \
  "v_rcp_f32 v197, v197\n\t" \
  "v_rcp_f32 v198, v198\n\t" \
  "v_rcp_f32 v199, v199\n\t" \
  "v_mad_u32_u16 v223, v223, %[c128], v249\n\t" \
  "v_or_b32 v226, v226, v250\n\t" \
  "v_mad_u32_u16 v227, v227, 6, v251\n\t" \
  "s_waitcnt lgkmcnt(9)\n\t" \
  "v_mfma_f32_16x16x32_bf16 v[84:87], v[200:203], v[176:179], v[84:87]\n\t" \
  "v_mul_f32 v192, 0xbfb8aa3b, v116\n\t" \
  "v_mul_f32 v193, 0xbfb8aa3b, v117\n\t" \
  "v_mul_f32 v194, 0xbfb8aa3b, v118\n\t" \
  "v_mul_f32 v195, 0xbfb8aa3b, v119\n\t" \
  "v_exp_f32 v192, v192\n\t" \
  "v_exp_f32 v193, v193\n\t" \
  "v_exp_f32 v194, v194\n\t" \
  "v_exp_f32 v195, v195\n\t" \
  "s_waitcnt lgkmcnt(8)\n\t" \
  "v_mfma_f32_16x16x32_bf16 v[84:87], v[208:211], v[180:183], v[84:87]\n\t" \
  "v_mul_f32 v196, v112, v196\n\t" \
  "v_mul_f32 v197, v113, v197\n\t" \
  "v_mul_f32 v198, v114, v198\n\t" \
  "v_mul_f32 v199, v115, v199\n\t" \
  "s_waitcnt lgkmcnt(1)\n\t" \
  "s_cmp_lt_i32 %[nt], 1\n\t" \
  "s_cbranch_scc1 4f\n\t" \
  "s_mov_b32 m0, %[b2]\n\t" \
  "s_nop 0\n\t" \
  "global_load_lds_dwordx4 v226, %[ea]\n\t" \
  "global_load_lds_dword v227, %[pos] offset:1024\n\t" \
  "s_mov_b32 m0, %[b1]\n\t" \
  "s_nop 0\n\t" \
  "global_load_lds_dwordx4 v220, %[h0]\n\t" \
  "global_load_lds_dwordx4 v221, %[h1] offset:1024\n\t" \
  "global_load_lds_dwordx4 v222, %[h2] offset:2048\n\t" \
  "global_load_lds_dwordx4 v223, %[h3] offset:3072\n\t" \
  "4:\n\t" \
  "v_add_f32 v192, 1.0, v192\n\t" \
  "v_add_f32 v193, 1.0, v193\n\t" \
  "v_add_f32 v194, 1.0, v194\n\t" \
  "v_add_f32 v195, 1.0, v195\n\t" \
  "v_cvt_pk_bf16_f32 v190, v196, v197\n\t" \
  "v_cvt_pk_bf16_f32 v191, v198, v199\n\t" \
  "v_lshrrev_b32 v232, 20, v232\n\t" \
  "v_add_u32 v220, %[posd], v232\n\t" \
  "v_add_u32 v221, v240, v232\n\t" \
  "ds_write_b128 v243, v[188:191] offset:16\n\t" \
  "ds_read_b128 v[176:179], v242 offset:5376\n\t" \
  "ds_read_b128 v[180:183], v242 offset:5408\n\t" \
  "ds_write_b16 v233, v255 offset:5376\n\t" \
  "ds_read_b128 v[216:219], v220\n\t" \
  "ds_read_b128 v[172:175], v221 offset:32\n\t" \
  "ds_read_b128 v[168:171], v221\n\t" \
  "ds_read_b64_tr_b16 v[200:201], v244 offset:0\n\t" \
  "ds_read_b64_tr_b16 v[202:203], v244 offset:320\n\t" \
  "ds_read_b64_tr_b16 v[204:205], v244 offset:1280\n\t" \
  "ds_read_b64_tr_b16 v[206:207], v244 offset:1600\n\t" \
  "v_add_u32 v233, v241, v232\n\t" \
  "ds_write_b16_d16_hi v233, v255 offset:5376\n\t" \
  "v_mfma_f32_16x16x32_bf16 v[132:135], v[96:99], v[184:187], 0\n\t" \
  "v_rcp_f32 v192, v192\n\t" \
  "v_rcp_f32 v193, v193\n\t" \
  "v_rcp_f32 v194, v194\n\t" \
  "v_rcp_f32 v195, v195\n\t" \
  "v_mfma_f32_32x32x16_bf16 v[100:115], v[4:7], v[148:151], 0\n\t" \
  "v_mul_f32 v196, 0xbfb8aa3b, v120\n\t" \
  "v_mul_f32 v197, 0xbfb8aa3b, v121\n\t" \
  "v_mul_f32 v198, 0xbfb8aa3b, v122\n\t" \
  "v_mul_f32 v199, 0xbfb8aa3b, v123\n\t" \
  "v_exp_f32 v196, v196\n\t" \
  "v_exp_f32 v197, v197\n\t" \
  "v_exp_f32 v198, v198\n\t" \
  "v_exp_f32 v199, v199\n\t" \
  "v_mfma_f32_32x32x16_bf16 v[100:115], v[12:15], v[152:155], v[100:115]\n\t" \
  "v_mul_f32 v192, v116, v192\n\t" \
  "v_mul_f32 v193, v117, v193\n\t" \
  "v_mul_f32 v194, v118, v194\n\t" \
  "v_mul_f32 v195, v119, v195\n\t" \
  "v_add_f32 v196, 1.0, v196\n\t" \
  "v_add_f32 v197, 1.0, v197\n\t" \
  "v_add_f32 v198, 1.0, v198\n\t" \
  "v_add_f32 v199, 1.0, v199\n\t" \
  "v_cvt_pk_bf16_f32 v184, v192, v193\n\t" \
  "v_cvt_pk_bf16_f32 v185, v194, v195\n\t" \
  "v_and_b32 v225, 0xffff0000, v225\n\t" \
  "s_waitcnt lgkmcnt(7)\n\t" \
  "v_lshlrev_b32 v219, 16, v224\n\t" \
  "v_and_b32 v224, 0xffff0000, v224\n\t" \
  "v_sub_f32 v229, v218, v225\n\t" \
  "v_sub_f32 v216, v216, v219\n\t" \
  "v_sub_f32 v217, v217, v224\n\t" \
  "v_mfma_f32_32x32x16_bf16 v[100:115], v[20:23], v[156:159], v[100:115]\n\t" \
  "v_rcp_f32 v196, v196\n\t" \
  "v_rcp_f32 v197, v197\n\t" \
  "v_rcp_f32 v198, v198\n\t" \
  "v_rcp_f32 v199, v199\n\t" \
  "v_mul_f32 v218, v229, v229\n\t" \
  "v_cndmask_b32 v228, v216, v217, vcc\n\t" \
  "v_fmac_f32 v218, v216, v216\n\t" \
  "v_fmac_f32 v218, v217, v217\n\t" \
  "v_cvt_pk_bf16_f32 v218, v218, v218\n\t" \
  "s_waitcnt lgkmcnt(6)\n\t" \
  "v_cndmask_b32_sdwa v175, v175, v218, vcc dst_sel:WORD_1 dst_unused:UNUSED_PRESERVE src0_sel:WORD_1 src1_sel:WORD_1\n\t" \
  "v_mfma_f32_16x16x32_bf16 v[132:135], v[136:139], v[188:191], v[132:135]\n\t" \
  "v_mul_f32 v192, 0xbfb8aa3b, v124\n\t" \
  "v_mul_f32 v193, 0xbfb8aa3b, v125\n\t" \
  "v_mul_f32 v194, 0xbfb8aa3b, v126\n\t" \
  "v_mul_f32 v195, 0xbfb8aa3b, v127\n\t" \
  "v_exp_f32 v192, v192\n\t" \
  "v_exp_f32 v193, v193\n\t" \
  "v_exp_f32 v194, v194\n\t" \
  "v_exp_f32 v195, v195\n\t" \
  "ds_read2_b32 v[220:221], v252 offset0:0 offset1:2\n\t" \
  "ds_read2_b32 v[222:223], v252 offset0:4 offset1:6\n\t" \
  "ds_read_b64 v[226:227], v253\n\t" \
  "ds_read_b32 v232, v254\n\t" \
  "v_mfma_f32_32x32x16_bf16 v[100:115], v[28:31], v[160:163], v[100:115]\n\t" \
  "v_mul_f32 v196, v120, v196\n\t" \
  "v_mul_f32 v197, v121, v197\n\t" \
  "v_mul_f32 v198, v122, v198\n\t" \
  "v_mul_f32 v199, v123, v199\n\t" \
  "v_add_u32 v252, 0x100, v252\n\t" \
  "v_add_u32 v253, 0x100, v253\n\t" \
  "v_add_u32 v254, 0x100, v254\n\t" \
  "v_add_f32 v192, 1.0, v192\n\t" \
  "v_add_f32 v193, 1.0, v193\n\t" \
  "v_add_f32 v194, 1.0, v194\n\t" \
  "v_add_f32 v195, 1.0, v195\n\t" \
  "v_cvt_pk_bf16_f32 v186, v196, v197\n\t" \
  "v_cvt_pk_bf16_f32 v187, v198, v199\n\t" \
  "s_waitcnt lgkmcnt(9)\n\t" \
  "v_mfma_f32_32x32x16_bf16 v[100:115], v[36:39], v[168:171], v[100:115]\n\t" \
  "v_rcp_f32 v192, v192\n\t" \
  "v_rcp_f32 v193, v193\n\t" \
  "v_rcp_f32 v194, v194\n\t" \
  "v_rcp_f32 v195, v195\n\t" \
  "v_mfma_f32_32x32x16_bf16 v[100:115], v[44:47], v[172:175], v[100:115]\n\t" \
  "v_mul_f32 v196, 0xbfb8aa3b, v128\n\t" \
  "v_mul_f32 v197, 0xbfb8aa3b, v129\n\t" \
  "v_mul_f32 v198, 0xbfb8aa3b, v130\n\t" \
  "v_mul_f32 v199, 0xbfb8aa3b, v131\n\t" \
  "v_exp_f32 v196, v196\n\t" \
  "v_exp_f32 v197, v197\n\t" \
  "v_exp_f32 v198, v198\n\t" \
  "v_exp_f32 v199, v199\n\t" \
  "v_mfma_f32_32x32x16_bf16 v[100:115], v[88:91], v[164:167], v[100:115]\n\t" \
  "v_mul_f32 v192, v124, v192\n\t" \
  "v_mul_f32 v193, v125, v193\n\t" \
  "v_mul_f32 v194, v126, v194\n\t" \
  "v_mul_f32 v195, v127, v195\n\t" \
  "ds_write_b128 v243, v[184:187] offset:0\n\t" \
  "v_add_f32 v196, 1.0, v196\n\t" \
  "v_add_f32 v197, 1.0, v197\n\t" \
  "v_add_f32 v198, 1.0, v198\n\t" \
  "v_add_f32 v199, 1.0, v199\n\t" \
  "v_cvt_pk_bf16_f32 v188, v192, v193\n\t" \
  "v_cvt_pk_bf16_f32 v189, v194, v195\n\t" \
  "s_waitcnt lgkmcnt(8)\n\t" \
  "v_mfma_f32_32x32x16_bf16 v[52:67], v[200:203], v[176:179], v[52:67]\n\t" \
  "v_rcp_f32 v196, v196\n\t" \
  "v_rcp_f32 v197, v197\n\t" \
  "v_rcp_f32 v198, v198\n\t" \
  "v_rcp_f32 v199, v199\n\t" \
  "s_waitcnt lgkmcnt(6)\n\t" \
  "v_mfma_f32_32x32x16_bf16 v[52:67], v[204:207], v[180:183], v[52:67]\n\t" \
  "v_mul_f32 v192, 0xbfb8aa3b, v100\n\t" \
  "v_mul_f32 v193, 0xbfb8aa3b, v101\n\t" \
  "v_mul_f32 v194, 0xbfb8aa3b, v102\n\t" \
  "v_mul_f32 v195, 0xbfb8aa3b, v103\n\t" \
  "v_exp_f32 v192, v192\n\t" \
  "v_exp_f32 v193, v193\n\t" \
  "v_exp_f32 v194, v194\n\t" \
  "v_exp_f32 v195, v195\n\t" \
  "s_cmp_eq_u32 %[nt], 0\n\t" \
  "v_mfma_f32_16x16x32_bf16 v[132:135], v[140:143], v[184:187], v[132:135]\n\t" \
  "v_mul_f32 v196, v128, v196\n\t" \
  "v_mul_f32 v197, v129, v197\n\t" \
  "v_mul_f32 v198, v130, v198\n\t" \
  "v_mul_f32 v199, v131, v199\n\t" \
  "v_add_f32 v192, 1.0, v192\n\t" \
  "v_add_f32 v193, 1.0, v193\n\t" \
  "v_add_f32 v194, 1.0, v194\n\t" \
  "v_add_f32 v195, 1.0, v195\n\t" \
  "v_cvt_pk_bf16_f32 v190, v196, v197\n\t" \
  "v_cvt_pk_bf16_f32 v191, v198, v199\n\t" \
  "s_cbranch_scc0 1b\n\t" \
  "s_waitcnt lgkmcnt(0)\n\t" \
  "s_cmp_lg_u32 %[last], 0\n\t" \
  "s_cbranch_scc1 8f\n\t" \
  "ds_write_b128 v243, v[188:191] offset:16\n\t" \
  "ds_read_b64_tr_b16 v[200:201], v244 offset:0\n\t" \
  "ds_read_b64_tr_b16 v[202:203], v244 offset:320\n\t" \
  "ds_read_b64_tr_b16 v[204:205], v244 offset:1280\n\t" \
  "ds_read_b64_tr_b16 v[206:207], v244 offset:1600\n\t" \
  "v_mfma_f32_16x16x32_bf16 v[132:135], v[144:147], v[188:191], v[132:135]\n\t" \
  "s_waitcnt lgkmcnt(0)\n\t" \
  "v_mfma_f32_32x32x16_bf16 v[68:83], v[200:203], v[176:179], v[68:83]\n\t" \
  "v_mfma_f32_32x32x16_bf16 v[68:83], v[204:207], v[180:183], v[68:83]\n\t" \
  "s_nop 8\n\t" \
  "v_mul_f32 v230, v230, v132\n\t" \
  "v_mul_f32 v231, v231, v132\n\t" \
  "v_cvt_pk_bf16_f32 v230, v230, v231\n\t" \
  "ds_write_b16 v245, v230 offset:7456\n\t" \
  "ds_write_b16_d16_hi v241, v230 offset:7584\n\t" \
  "ds_read_b128 v[200:203], v246 offset:7456\n\t" \
  "ds_read_b128 v[208:211], v246 offset:7488\n\t" \
  "s_waitcnt lgkmcnt(0)\n\t" \
  "v_mfma_f32_16x16x32_bf16 v[84:87], v[200:203], v[176:179], v[84:87]\n\t" \
  "v_mfma_f32_16x16x32_bf16 v[84:87], v[208:211], v[180:183], v[84:87]\n\t" \
  "s_branch 9f\n\t" \
  "6:\n\t" \
  "s_cmp_lg_u32 %[last], 0\n\t" \
  "s_cbranch_scc0 9f\n\t" \
  "s_waitcnt lgkmcnt(0)\n\t" \
  "v_cndmask_b32 v224, 0, 24, vcc\n\t" \
  "v_xor_b32 v220, v234, v224\n\t" \
  "v_xor_b32 v221, v235, v224\n\t" \
  "v_xor_b32 v222, v236, v224\n\t" \
  "v_xor_b32 v223, v237, v224\n\t" \
  "v_xor_b32 v216, 16, v220\n\t" \
  "v_xor_b32 v217, 16, v221\n\t" \
  "v_xor_b32 v218, 16, v222\n\t" \
  "v_xor_b32 v219, 16, v223\n\t" \
  "ds_read_b64 v[148:149], v220\n\t" \
  "ds_read_b64 v[150:151], v216\n\t" \
  "ds_read_b64 v[152:153], v221\n\t" \
  "ds_read_b64 v[154:155], v217\n\t" \
  "ds_read_b64 v[156:157], v222\n\t" \
  "ds_read_b64 v[158:159], v218\n\t" \
  "ds_read_b64 v[160:161], v223\n\t" \
  "ds_read_b64 v[162:163], v219\n\t" \
  "ds_read_b128 v[4:7], v247 offset:6144\n\t" \
  "ds_read_b128 v[8:11], v247 offset:7168\n\t" \
  "ds_read_b128 v[12:15], v247 offset:8192\n\t" \
  "ds_read_b128 v[16:19], v247 offset:9216\n\t" \
  "ds_read_b128 v[20:23], v247 offset:10240\n\t" \
  "ds_read_b128 v[24:27], v247 offset:11264\n\t" \
  "v_subrev_u32 v225, %[b1], v241\n\t" \
  "v_lshl_add_u32 v225, v225, 5, v243\n\t" \
  "v_sub_u32 v225, v225, v224\n\t" \
  "s_nop 4\n\t" \
  "v_cvt_pk_bf16_f32 v184, v52, v53\n\t" \
  "v_cvt_pk_bf16_f32 v185, v54, v55\n\t" \
  "v_cvt_pk_bf16_f32 v186, v56, v57\n\t" \
  "v_cvt_pk_bf16_f32 v187, v58, v59\n\t" \
  "v_cvt_pk_bf16_f32 v188, v60, v61\n\t" \
  "v_cvt_pk_bf16_f32 v189, v62, v63\n\t" \
  "v_cvt_pk_bf16_f32 v190, v64, v65\n\t" \
  "v_cvt_pk_bf16_f32 v191, v66, v67\n\t" \
  "v_cvt_pk_bf16_f32 v36, v68, v69\n\t" \
  "v_cvt_pk_bf16_f32 v37, v70, v71\n\t" \
  "v_cvt_pk_bf16_f32 v38, v72, v73\n\t" \
  "v_cvt_pk_bf16_f32 v39, v74, v75\n\t" \
  "v_cvt_pk_bf16_f32 v40, v76, v77\n\t" \
  "v_cvt_pk_bf16_f32 v41, v78, v79\n\t" \
  "v_cvt_pk_bf16_f32 v42, v80, v81\n\t" \
  "v_cvt_pk_bf16_f32 v43, v82, v83\n\t" \
  "s_waitcnt lgkmcnt(5)\n\t" \
  "v_mfma_f32_32x32x16_bf16 v[100:115], v[4:7], v[148:151], 0\n\t" \
  "s_waitcnt lgkmcnt(4)\n\t" \
  "v_mfma_f32_32x32x16_bf16 v[116:131], v[8:11], v[148:151], 0\n\t" \
  "ds_read_b128 v[28:31], v247 offset:12288\n\t" \
  "ds_read_b128 v[32:35], v247 offset:13312\n\t" \
  "s_waitcnt lgkmcnt(5)\n\t" \
  "v_mfma_f32_32x32x16_bf16 v[100:115], v[12:15], v[152:155], v[100:115]\n\t" \
  "s_waitcnt lgkmcnt(4)\n\t" \
  "v_mfma_f32_32x32x16_bf16 v[116:131], v[16:19], v[152:155], v[116:131]\n\t" \
  "ds_read_b128 v[4:7], v247 offset:14336\n\t" \
  "ds_read_b128 v[8:11], v247 offset:15360\n\t" \
  "s_waitcnt lgkmcnt(5)\n\t" \
  "v_mfma_f32_32x32x16_bf16 v[100:115], v[20:23], v[156:159], v[100:115]\n\t" \
  "s_waitcnt lgkmcnt(4)\n\t" \
  "v_mfma_f32_32x32x16_bf16 v[116:131], v[24:27], v[156:159], v[116:131]\n\t" \
  "ds_read_b128 v[12:15], v247 offset:16384\n\t" \
  "ds_read_b128 v[16:19], v247 offset:17408\n\t" \
  "s_waitcnt lgkmcnt(5)\n\t" \
  "v_mfma_f32_32x32x16_bf16 v[100:115], v[28:31], v[160:163], v[100:115]\n\t" \
  "s_waitcnt lgkmcnt(4)\n\t" \
  "v_mfma_f32_32x32x16_bf16 v[116:131], v[32:35], v[160:163], v[116:131]\n\t" \
  "ds_read_b128 v[20:23], v247 offset:18432\n\t" \
  "ds_read_b128 v[24:27], v247 offset:19456\n\t" \
  "s_waitcnt lgkmcnt(5)\n\t" \
  "v_mfma_f32_32x32x16_bf16 v[100:115], v[4:7], v[184:187], v[100:115]\n\t" \
  "ds_read_b128 v[28:31], v247 offset:20480\n\t" \
  "ds_read_b128 v[32:35], v247 offset:21504\n\t" \
  "s_waitcnt lgkmcnt(5)\n\t" \
  "v_mfma_f32_32x32x16_bf16 v[100:115], v[12:15], v[188:191], v[100:115]\n\t" \
  "s_waitcnt lgkmcnt(3)\n\t" \
  "v_mfma_f32_32x32x16_bf16 v[100:115], v[20:23], v[36:39], v[100:115]\n\t" \
  "s_waitcnt lgkmcnt(1)\n\t" \
  "v_mfma_f32_32x32x16_bf16 v[100:115], v[28:31], v[40:43], v[100:115]\n\t" \
  "s_waitcnt lgkmcnt(0)\n\t" \
  "s_branch 10f\n\t" \
  "8:\n\t" \
  "ds_write_b128 v243, v[188:191] offset:16\n\t" \
  "ds_read_b64_tr_b16 v[200:201], v244 offset:0\n\t" \
  "ds_read_b64_tr_b16 v[202:203], v244 offset:320\n\t" \
  "ds_read_b64_tr_b16 v[204:205], v244 offset:1280\n\t" \
  "ds_read_b64_tr_b16 v[206:207], v244 offset:1600\n\t" \
  "v_mfma_f32_16x16x32_bf16 v[132:135], v[144:147], v[188:191], v[132:135]\n\t" \
  "v_cndmask_b32 v224, 0, 24, vcc\n\t" \
  "v_xor_b32 v220, v234, v224\n\t" \
  "v_xor_b32 v221, v235, v224\n\t" \
  "v_xor_b32 v222, v236, v224\n\t" \
  "v_xor_b32 v223, v237, v224\n\t" \
  "v_xor_b32 v216, 16, v220\n\t" \
  "v_xor_b32 v217, 16, v221\n\t" \
  "v_xor_b32 v218, 16, v222\n\t" \
  "v_xor_b32 v219, 16, v223\n\t" \
  "ds_read_b64 v[148:149], v220\n\t" \
  "ds_read_b64 v[150:151], v216\n\t" \
  "ds_read_b64 v[152:153], v221\n\t" \
  "ds_read_b64 v[154:155], v217\n\t" \
  "ds_read_b64 v[156:157], v222\n\t" \
  "ds_read_b64 v[158:159], v218\n\t" \
  "ds_read_b64 v[160:161], v223\n\t" \
  "ds_read_b64 v[162:163], v219\n\t" \
  "ds_read_b128 v[4:7], v247 offset:6144\n\t" \
  "ds_read_b128 v[8:11], v247 offset:7168\n\t" \
  "s_waitcnt lgkmcnt(11)\n\t" \
  "ds_read_b128 v[12:15], v247 offset:8192\n\t" \
  "ds_read_b128 v[16:19], v247 offset:9216\n\t" \
  "ds_read_b128 v[20:23], v247 offset:10240\n\t" \
  "ds_read_b128 v[24:27], v247 offset:11264\n\t" \
  "v_subrev_u32 v225, %[b1], v241\n\t" \
  "v_lshl_add_u32 v225, v225, 5, v243\n\t" \
  "v_sub_u32 v225, v225, v224\n\t" \
  "v_mfma_f32_32x32x16_bf16 v[68:83], v[200:203], v[176:179], v[68:83]\n\t" \
  "s_waitcnt lgkmcnt(5)\n\t" \
  "v_mfma_f32_32x32x16_bf16 v[100:115], v[4:7], v[148:151], 0\n\t" \
  "v_mfma_f32_32x32x16_bf16 v[68:83], v[204:207], v[180:183], v[68:83]\n\t" \
  "s_waitcnt lgkmcnt(4)\n\t" \
  "v_mfma_f32_32x32x16_bf16 v[116:131], v[8:11], v[148:151], 0\n\t" \
  "ds_read_b128 v[28:31], v247 offset:12288\n\t" \
  "ds_read_b128 v[32:35], v247 offset:13312\n\t" \
  "s_waitcnt lgkmcnt(5)\n\t" \
  "v_mfma_f32_32x32x16_bf16 v[100:115], v[12:15], v[152:155], v[100:115]\n\t" \
  "v_mul_f32 v230, v230, v132\n\t" \
  "v_mul_f32 v231, v231, v132\n\t" \
  "v_cvt_pk_bf16_f32 v230, v230, v231\n\t" \
  "ds_write_b16 v245, v230 offset:7456\n\t" \
  "ds_write_b16_d16_hi v241, v230 offset:7584\n\t" \
  "ds_read_b128 v[200:203], v246 offset:7456\n\t" \
  "ds_read_b128 v[208:211], v246 offset:7488\n\t" \
  "v_cvt_pk_bf16_f32 v184, v52, v53\n\t" \
  "v_cvt_pk_bf16_f32 v185, v54, v55\n\t" \
  "v_cvt_pk_bf16_f32 v186, v56, v57\n\t" \
  "v_cvt_pk_bf16_f32 v187, v58, v59\n\t" \
  "s_waitcnt lgkmcnt(8)\n\t" \
  "v_mfma_f32_32x32x16_bf16 v[116:131], v[16:19], v[152:155], v[116:131]\n\t" \
  "ds_read_b128 v[4:7], v247 offset:14336\n\t" \
  "ds_read_b128 v[8:11], v247 offset:15360\n\t" \
  "s_waitcnt lgkmcnt(9)\n\t" \
  "v_mfma_f32_32x32x16_bf16 v[100:115], v[20:23], v[156:159], v[100:115]\n\t" \
  "v_cvt_pk_bf16_f32 v188, v60, v61\n\t" \
  "v_cvt_pk_bf16_f32 v189, v62, v63\n\t" \
  "v_cvt_pk_bf16_f32 v190, v64, v65\n\t" \
  "v_cvt_pk_bf16_f32 v191, v66, v67\n\t" \
  "s_waitcnt lgkmcnt(8)\n\t" \
  "v_mfma_f32_32x32x16_bf16 v[116:131], v[24:27], v[156:159], v[116:131]\n\t" \
  "ds_read_b128 v[12:15], v247 offset:16384\n\t" \
  "ds_read_b128 v[16:19], v247 offset:17408\n\t" \
  "s_waitcnt lgkmcnt(9)\n\t" \
  "v_mfma_f32_32x32x16_bf16 v[100:115], v[28:31], v[160:163], v[100:115]\n\t" \
  "v_cvt_pk_bf16_f32 v36, v68, v69\n\t" \
  "v_cvt_pk_bf16_f32 v37, v70, v71\n\t" \
  "v_cvt_pk_bf16_f32 v38, v72, v73\n\t" \
  "v_cvt_pk_bf16_f32 v39, v74, v75\n\t" \
  "s_waitcnt lgkmcnt(5)\n\t" \
  "v_mfma_f32_16x16x32_bf16 v[84:87], v[200:203], v[176:179], v[84:87]\n\t" \
  "v_cvt_pk_bf16_f32 v40, v76, v77\n\t" \
  "v_cvt_pk_bf16_f32 v41, v78, v79\n\t" \
  "v_cvt_pk_bf16_f32 v42, v80, v81\n\t" \
  "v_cvt_pk_bf16_f32 v43, v82, v83\n\t" \
  "v_mfma_f32_32x32x16_bf16 v[116:131], v[32:35], v[160:163], v[116:131]\n\t" \
  "ds_read_b128 v[20:23], v247 offset:18432\n\t" \
  "ds_read_b128 v[24:27], v247 offset:19456\n\t" \
  "s_waitcnt lgkmcnt(6)\n\t" \
  "v_mfma_f32_16x16x32_bf16 v[84:87], v[208:211], v[180:183], v[84:87]\n\t" \
  "s_waitcnt lgkmcnt(5)\n\t" \
  "v_mfma_f32_32x32x16_bf16 v[100:115], v[4:7], v[184:187], v[100:115]\n\t" \
  "ds_read_b128 v[28:31], v247 offset:20480\n\t" \
  "ds_read_b128 v[32:35], v247 offset:21504\n\t" \
  "s_waitcnt lgkmcnt(5)\n\t" \
  "v_mfma_f32_32x32x16_bf16 v[100:115], v[12:15], v[188:191], v[100:115]\n\t" \
  "s_waitcnt lgkmcnt(3)\n\t" \
  "v_mfma_f32_32x32x16_bf16 v[100:115], v[20:23], v[36:39], v[100:115]\n\t" \
  "s_waitcnt lgkmcnt(1)\n\t" \
  "v_mfma_f32_32x32x16_bf16 v[100:115], v[28:31], v[40:43], v[100:115]\n\t" \
  "s_waitcnt lgkmcnt(0)\n\t" \
  "10:\n\t" \
  "v_mov_b32 v212, 0xbfb8aa3b\n\t" \
  "v_mfma_f32_32x32x16_bf16 v[116:131], v[8:11], v[184:187], v[116:131]\n\t" \
  "v_mfma_f32_32x32x16_bf16 v[116:131], v[16:19], v[188:191], v[116:131]\n\t" \
  "s_nop 8\n\t" \
  "v_mul_f32 v192, 0xbfb8aa3b, v100\n\t" \
  "v_mul_f32 v193, 0xbfb8aa3b, v101\n\t" \
  "v_mul_f32 v194, 0xbfb8aa3b, v102\n\t" \
  "v_mul_f32 v195, 0xbfb8aa3b, v103\n\t" \
  "v_exp_f32 v192, v192\n\t" \
  "v_exp_f32 v193, v193\n\t" \
  "v_exp_f32 v194, v194\n\t" \
  "v_exp_f32 v195, v195\n\t" \
  "v_lshlrev_b32 v200, 16, v148\n\t" \
  "v_and_b32 v201, 0xffff0000, v148\n\t" \
  "v_lshlrev_b32 v202, 16, v149\n\t" \
  "v_and_b32 v203, 0xffff0000, v149\n\t" \
  "v_mfma_f32_32x32x16_bf16 v[116:131], v[24:27], v[36:39], v[116:131]\n\t" \
  "v_add_f32 v192, 1.0, v192\n\t" \
  "v_add_f32 v193, 1.0, v193\n\t" \
  "v_add_f32 v194, 1.0, v194\n\t" \
  "v_add_f32 v195, 1.0, v195\n\t" \
  "v_rcp_f32 v192, v192\n\t" \
  "v_rcp_f32 v193, v193\n\t" \
  "v_rcp_f32 v194, v194\n\t" \
  "v_rcp_f32 v195, v195\n\t" \
  "v_mfma_f32_32x32x16_bf16 v[116:131], v[32:35], v[40:43], v[116:131]\n\t" \
  "v_mul_f32 v196, 0xbfb8aa3b, v104\n\t" \
  "v_mul_f32 v197, 0xbfb8aa3b, v105\n\t" \
  "v_mul_f32 v198, 0xbfb8aa3b, v106\n\t" \
  "v_mul_f32 v199, 0xbfb8aa3b, v107\n\t" \
  "v_exp_f32 v196, v196\n\t" \
  "v_exp_f32 v197, v197\n\t" \
  "v_exp_f32 v198, v198\n\t" \
  "v_exp_f32 v199, v199\n\t" \
  "v_fmac_f32 v200, v100, v192\n\t" \
  "v_fmac_f32 v201, v101, v193\n\t" \
  "v_fmac_f32 v202, v102, v194\n\t" \
  "v_fmac_f32 v203, v103, v195\n\t" \
  "v_lshlrev_b32 v204, 16, v150\n\t" \
  "v_and_b32 v205, 0xffff0000, v150\n\t" \
  "v_lshlrev_b32 v206, 16, v151\n\t" \
  "v_and_b32 v207, 0xffff0000, v151\n\t" \
  "v_add_f32 v196, 1.0, v196\n\t" \
  "v_add_f32 v197, 1.0, v197\n\t" \
  "v_add_f32 v198, 1.0, v198\n\t" \
  "v_add_f32 v199, 1.0, v199\n\t" \
  "v_cvt_pk_bf16_f32 v176, v200, v201\n\t" \
  "v_cvt_pk_bf16_f32 v177, v202, v203\n\t" \
  "ds_write_b64 v225, v[176:177] offset:0\n\t" \
  "v_rcp_f32 v196, v196\n\t" \
  "v_rcp_f32 v197, v197\n\t" \
  "v_rcp_f32 v198, v198\n\t" \
  "v_rcp_f32 v199, v199\n\t" \
  "v_mul_f32 v192, 0xbfb8aa3b, v108\n\t" \
  "v_mul_f32 v193, 0xbfb8aa3b, v109\n\t" \
  "v_mul_f32 v194, 0xbfb8aa3b, v110\n\t" \
  "v_mul_f32 v195, 0xbfb8aa3b, v111\n\t" \
  "v_exp_f32 v192, v192\n\t" \
  "v_exp_f32 v193, v193\n\t" \
  "v_exp_f32 v194, v194\n\t" \
  "v_exp_f32 v195, v195\n\t" \
  "v_fmac_f32 v204, v104, v196\n\t" \
  "v_fmac_f32 v205, v105, v197\n\t" \
  "v_fmac_f32 v206, v106, v198\n\t" \
  "v_fmac_f32 v207, v107, v199\n\t" \
  "v_lshlrev_b32 v200, 16, v152\n\t" \
  "v_and_b32 v201, 0xffff0000, v152\n\t" \
  "v_lshlrev_b32 v202, 16, v153\n\t" \
  "v_and_b32 v203, 0xffff0000, v153\n\t" \
  "v_add_f32 v192, 1.0, v192\n\t" \
  "v_add_f32 v193, 1.0, v193\n\t" \
  "v_add_f32 v194, 1.0, v194\n\t" \
  "v_add_f32 v195, 1.0, v195\n\t" \
  "v_cvt_pk_bf16_f32 v178, v204, v205\n\t" \
  "v_cvt_pk_bf16_f32 v179, v206, v207\n\t" \
  "ds_write_b64 v225, v[178:179] offset:16\n\t" \
  "v_rcp_f32 v192, v192\n\t" \
  "v_rcp_f32 v193, v193\n\t" \
  "v_rcp_f32 v194, v194\n\t" \
  "v_rcp_f32 v195, v195\n\t" \
  "v_mul_f32 v196, 0xbfb8aa3b, v112\n\t" \
  "v_mul_f32 v197, 0xbfb8aa3b, v113\n\t" \
  "v_mul_f32 v198, 0xbfb8aa3b, v114\n\t" \
  "v_mul_f32 v199, 0xbfb8aa3b, v115\n\t" \
  "v_exp_f32 v196, v196\n\t" \
  "v_exp_f32 v197, v197\n\t" \
  "v_exp_f32 v198, v198\n\t" \
  "v_exp_f32 v199, v199\n\t" \
  "v_fmac_f32 v200, v108, v192\n\t" \
  "v_fmac_f32 v201, v109, v193\n\t" \
  "v_fmac_f32 v202, v110, v194\n\t" \
  "v_fmac_f32 v203, v111, v195\n\t" \
  "v_lshlrev_b32 v204, 16, v154\n\t" \
  "v_and_b32 v205, 0xffff0000, v154\n\t" \
  "v_lshlrev_b32 v206, 16, v155\n\t" \
  "v_and_b32 v207, 0xffff0000, v155\n\t" \
  "v_add_f32 v196, 1.0, v196\n\t" \
  "v_add_f32 v197, 1.0, v197\n\t" \
  "v_add_f32 v198, 1.0, v198\n\t" \
  "v_add_f32 v199, 1.0, v199\n\t" \
  "v_cvt_pk_bf16_f32 v180, v200, v201\n\t" \
  "v_cvt_pk_bf16_f32 v181, v202, v203\n\t" \
  "ds_write_b64 v225, v[180:181] offset:32\n\t" \
  "v_rcp_f32 v196, v196\n\t" \
  "v_rcp_f32 v197, v197\n\t" \
  "v_rcp_f32 v198, v198\n\t" \
  "v_rcp_f32 v199, v199\n\t" \
  "v_mul_f32 v192, 0xbfb8aa3b, v116\n\t" \
  "v_mul_f32 v193, 0xbfb8aa3b, v117\n\t" \
  "v_mul_f32 v194, 0xbfb8aa3b, v118\n\t" \
  "v_mul_f32 v195, 0xbfb8aa3b, v119\n\t" \
  "v_exp_f32 v192, v192\n\t" \
  "v_exp_f32 v193, v193\n\t" \
  "v_exp_f32 v194, v194\n\t" \
  "v_exp_f32 v195, v195\n\t" \
  "v_fmac_f32 v204, v112, v196\n\t" \
  "v_fmac_f32 v205, v113, v197\n\t" \
  "v_fmac_f32 v206, v114, v198\n\t" \
  "v_fmac_f32 v207, v115, v199\n\t" \
  "v_lshlrev_b32 v200, 16, v156\n\t" \
  "v_and_b32 v201, 0xffff0000, v156\n\t" \
  "v_lshlrev_b32 v202, 16, v157\n\t" \
  "v_and_b32 v203, 0xffff0000, v157\n\t" \
  "v_add_f32 v192, 1.0, v192\n\t" \
  "v_add_f32 v193, 1.0, v193\n\t" \
  "v_add_f32 v194, 1.0, v194\n\t" \
  "v_add_f32 v195, 1.0, v195\n\t" \
  "v_cvt_pk_bf16_f32 v182, v204, v205\n\t" \
  "v_cvt_pk_bf16_f32 v183, v206, v207\n\t" \
  "ds_write_b64 v225, v[182:183] offset:48\n\t" \
  "v_rcp_f32 v192, v192\n\t" \
  "v_rcp_f32 v193, v193\n\t" \
  "v_rcp_f32 v194, v194\n\t" \
  "v_rcp_f32 v195, v195\n\t" \
  "v_mul_f32 v196, 0xbfb8aa3b, v120\n\t" \
  "v_mul_f32 v197, 0xbfb8aa3b, v121\n\t" \
  "v_mul_f32 v198, 0xbfb8aa3b, v122\n\t" \
  "v_mul_f32 v199, 0xbfb8aa3b, v123\n\t" \
  "v_exp_f32 v196, v196\n\t" \
  "v_exp_f32 v197, v197\n\t" \
  "v_exp_f32 v198, v198\n\t" \
  "v_exp_f32 v199, v199\n\t" \
  "v_fmac_f32 v200, v116, v192\n\t" \
  "v_fmac_f32 v201, v117, v193\n\t" \
  "v_fmac_f32 v202, v118, v194\n\t" \
  "v_fmac_f32 v203, v119, v195\n\t" \
  "v_lshlrev_b32 v204, 16, v158\n\t" \
  "v_and_b32 v205, 0xffff0000, v158\n\t" \
  "v_lshlrev_b32 v206, 16, v159\n\t" \
  "v_and_b32 v207, 0xffff0000, v159\n\t" \
  "v_add_f32 v196, 1.0, v196\n\t" \
  "v_add_f32 v197, 1.0, v197\n\t" \
  "v_add_f32 v198, 1.0, v198\n\t" \
  "v_add_f32 v199, 1.0, v199\n\t" \
  "v_cvt_pk_bf16_f32 v176, v200, v201\n\t" \
  "v_cvt_pk_bf16_f32 v177, v202, v203\n\t" \
  "ds_write_b64 v225, v[176:177] offset:64\n\t" \
  "v_rcp_f32 v196, v196\n\t" \
  "v_rcp_f32 v197, v197\n\t" \
  "v_rcp_f32 v198, v198\n\t" \
  "v_rcp_f32 v199, v199\n\t" \
  "v_mul_f32 v192, 0xbfb8aa3b, v124\n\t" \
  "v_mul_f32 v193, 0xbfb8aa3b, v125\n\t" \
  "v_mul_f32 v194, 0xbfb8aa3b, v126\n\t" \
  "v_mul_f32 v195, 0xbfb8aa3b, v127\n\t" \
  "v_exp_f32 v192, v192\n\t" \
  "v_exp_f32 v193, v193\n\t" \
  "v_exp_f32 v194, v194\n\t" \
  "v_exp_f32 v195, v195\n\t" \
  "v_fmac_f32 v204, v120, v196\n\t" \
  "v_fmac_f32 v205, v121, v197\n\t" \
  "v_fmac_f32 v206, v122, v198\n\t" \
  "v_fmac_f32 v207, v123, v199\n\t" \
  "v_lshlrev_b32 v200, 16, v160\n\t" \
  "v_and_b32 v201, 0xffff0000, v160\n\t" \
  "v_lshlrev_b32 v202, 16, v161\n\t" \
  "v_and_b32 v203, 0xffff0000, v161\n\t" \
  "v_add_f32 v192, 1.0, v192\n\t" \
  "v_add_f32 v193, 1.0, v193\n\t" \
  "v_add_f32 v194, 1.0, v194\n\t" \
  "v_add_f32 v195, 1.0, v195\n\t" \
  "v_cvt_pk_bf16_f32 v178, v204, v205\n\t" \
  "v_cvt_pk_bf16_f32 v179, v206, v207\n\t" \
  "ds_write_b64 v225, v[178:179] offset:80\n\t" \
  "v_rcp_f32 v192, v192\n\t" \
  "v_rcp_f32 v193, v193\n\t" \
  "v_rcp_f32 v194, v194\n\t" \
  "v_rcp_f32 v195, v195\n\t" \
  "v_mul_f32 v196, 0xbfb8aa3b, v128\n\t" \
  "v_mul_f32 v197, 0xbfb8aa3b, v129\n\t" \
  "v_mul_f32 v198, 0xbfb8aa3b, v130\n\t" \
  "v_mul_f32 v199, 0xbfb8aa3b, v131\n\t" \
  "v_exp_f32 v196, v196\n\t" \
  "v_exp_f32 v197, v197\n\t" \
  "v_exp_f32 v198, v198\n\t" \
  "v_exp_f32 v199, v199\n\t" \
  "v_fmac_f32 v200, v124, v192\n\t" \
  "v_fmac_f32 v201, v125, v193\n\t" \
  "v_fmac_f32 v202, v126, v194\n\t" \
  "v_fmac_f32 v203, v127, v195\n\t" \
  "v_lshlrev_b32 v204, 16, v162\n\t" \
  "v_and_b32 v205, 0xffff0000, v162\n\t" \
  "v_lshlrev_b32 v206, 16, v163\n\t" \
  "v_and_b32 v207, 0xffff0000, v163\n\t" \
  "v_add_f32 v196, 1.0, v196\n\t" \
  "v_add_f32 v197, 1.0, v197\n\t" \
  "v_add_f32 v198, 1.0, v198\n\t" \
  "v_add_f32 v199, 1.0, v199\n\t" \
  "v_cvt_pk_bf16_f32 v180, v200, v201\n\t" \
  "v_cvt_pk_bf16_f32 v181, v202, v203\n\t" \
  "ds_write_b64 v225, v[180:181] offset:96\n\t" \
  "v_rcp_f32 v196, v196\n\t" \
  "v_rcp_f32 v197, v197\n\t" \
  "v_rcp_f32 v198, v198\n\t" \
  "v_rcp_f32 v199, v199\n\t" \
  "s_nop 0\n\t" \
  "v_fmac_f32 v204, v128, v196\n\t" \
  "v_fmac_f32 v205, v129, v197\n\t" \
  "v_fmac_f32 v206, v130, v198\n\t" \
  "v_fmac_f32 v207, v131, v199\n\t" \
  "v_cvt_pk_bf16_f32 v182, v204, v205\n\t" \
  "v_cvt_pk_bf16_f32 v183, v206, v207\n\t" \
  "ds_write_b64 v225, v[182:183] offset:112\n\t" \
  "9:\n\t" \
  "ds_write_b16 v233, v255 offset:5376\n\t" \
  "s_mov_b32 m0, %[keep]\n\t" \
  "s_setprio 0\n\t" \
  "s_nop 11\n\t" \
  ""
#define LOOP_ASM LOOP_ASM_FOLD

DI u32x4 tr_frag(unsigned p, int half) {
  const s16x4 lo = __builtin_amdgcn_ds_read_tr16_b64_v4i16((lds_s16x4_p)p);
  const s16x4 hi = __builtin_amdgcn_ds_read_tr16_b64_v4i16((lds_s16x4_p)(p + half));
  return __builtin_bit_cast(u32x4, __builtin_shufflevector(lo, hi, 0, 1, 2, 3, 4, 5, 6, 7));
}
DI u32x4 fold(u32x4 v) {
#pragma unroll
  for (int i = 0; i < 4; i++) v[i] = pk_bf16(bf2f(v[i] & 0xffffu) * -LOG2E, __uint_as_float(v[i] & 0xffff0000u) * -LOG2E);
  return v;
}

__global__ __launch_bounds__(512, 2) void egnn_kernel(const char* __restrict__ hmat, const char* __restrict__ pos,
                                                      const char* __restrict__ ea, const int* __restrict__ src,
                                                      const int* __restrict__ dst, const char* __restrict__ we,
                                                      const char* __restrict__ wx, const char* __restrict__ wh,
                                                      char* __restrict__ ws, uint16_t* __restrict__ out, int N, int E STAMP_ARG) {
  const int w = __builtin_amdgcn_readfirstlane(threadIdx.x >> 6);
  const int bkt = (blockIdx.x & 7) * 32 + (blockIdx.x >> 3);
  const int n0 = bkt * NB;
  STAMP(0);
  unsigned wl = L_WAVE + w * WSZ, mi = L_MI + w * MISZ;
  const auto rs_tab = __builtin_amdgcn_make_buffer_rsrc((void*)(ws + WS_TAB), 0, NBK * NBK * 8, 0x00020000);
  const auto rs_rec = __builtin_amdgcn_make_buffer_rsrc((void*)(ws + WS_REC), 0, NBK * CHS, 0x00020000);
  const auto rs_ea = __builtin_amdgcn_make_buffer_rsrc((void*)ea, 0, E * 32, 0x00020000);
  unsigned tag_lo, tag_hi;
  unsigned wdv0, wdv1;

  {
    const int lane = lane_id(), tid = w * 64 + lane, r = lane & 31, h = lane >> 5;
    const int e0 = bkt * CH;
    int ne = max(E - e0, 0);
    asm("" : "+s"(ne));
    ne = min(ne, CH);
    const auto rs_d = __builtin_amdgcn_make_buffer_rsrc((void*)(dst + e0), 0, ne * 4, 0x00020000);
    const auto rs_s = __builtin_amdgcn_make_buffer_rsrc((void*)(src + e0), 0, ne * 4, 0x00020000);
    unsigned d[7], sv[7];
    u32x2 xa, xb;
    unsigned long long ep;
    asm volatile("s_load_dwordx2 %0, %1, 0x0" : "=s"(ep) : "s"(ws + WS_EPOCH) : "memory");
#pragma unroll
    for (int i = 0; i < 7; i++)
      asm volatile("buffer_load_dword %0, %2, %3, %5 offen nt\n\tbuffer_load_dword %1, %2, %4, %5 offen nt"
                   : "=&v"(d[i]), "=&v"(sv[i]) : "v"(tid * 4), "s"(rs_d), "s"(rs_s), "s"(i * 2048) : "memory");
    LDS(unsigned, L_CNT + tid * 4) = 0;
    asm volatile("s_waitcnt lgkmcnt(0)\n\ts_barrier" : "+s"(ep) :: "memory");
    asm volatile("global_load_ushort %0, %4, %5\n\tglobal_load_ushort %1, %4, %5 offset:64\n\t"
                 "global_load_dwordx2 %2, %6, %7\n\tglobal_load_dwordx2 %3, %6, %7 offset:16"
                 : "=&v"(wdv0), "=&v"(wdv1), "=&v"(xa), "=&v"(xb)
                 : "v"(r * 2), "s"(we + 128 * 128), "v"(h * 8), "s"(wx + ((w + 2) & 3) * 32) : "memory");
    {
      const int j = lane >> 3, jl = (j & 3) * 128 + (lane & 7) * 16, jh = (j >> 2) * 128;
      const char* const h0 = wh + (w < 4 ? 16 * w : 64 + 32 * ((w - 4) >> 1) + 4 * (w & 1)) * 128;
      const char* const e0p = we + (w < 4 ? 64 + 16 * w : 16 * (w - 4)) * 128;
      const unsigned oh = jl + jh * (w < 4 ? 8 : 16), oe = jl + jh * (w < 4 ? 4 : 8);
      unsigned keep;
      asm volatile("s_mov_b32 %0, m0\n\ts_mov_b32 m0, %9\n\ts_nop 0\n\t"
                   "global_load_lds_dwordx4 %1, %4\n\tglobal_load_lds_dwordx4 %1, %5 offset:1024\n\t"
                   "global_load_lds_dwordx4 %2, %6 offset:2048\n\tglobal_load_lds_dwordx4 %2, %7 offset:3072\n\t"
                   "s_add_u32 m0, %9, 0x1000\n\ts_nop 0\n\tglobal_load_lds_dwordx4 %3, %8\n\ts_mov_b32 m0, %0"
                   : "=&s"(keep)
                   : "v"(oh), "v"(oe), "v"(w < 2 ? (lane >> 2) * 128 + (lane & 3) * 16 : 0),
                     "s"(h0), "s"(h0 + (w < 4 ? 4 : 8) * 128 - 1024), "s"(e0p - 2048), "s"(e0p + (w < 4 ? 8 : 4) * 128 - 3072),
                     "s"(we + 129 * 128 + (w & 1) * 64), "s"(wl)
                   : "memory", "scc");
    }
    unsigned ra[5], keep;
    {
      const unsigned cc0 = ((lane & 7) ^ (lane >> 4)) * 16, cc1 = ((lane & 7) ^ (4 + (lane >> 4))) * 16;
      const int nr = n0 + w + (lane & 56);
      ra[0] = min(nr, N - 1) * 128 + cc0; ra[1] = min(nr + 64, N - 1) * 128 + cc1; ra[2] = min(nr + 128, N - 1) * 128 + cc0;
      ra[3] = min(n0 + w + 192, N - 1) * 128 + cc1; ra[4] = mul24(min(n0 + w + min(lane >> 1, 24) * 8, N - 1), 6) + (lane & 1) * 2;
      asm volatile("" : "+v"(ra[0]), "+v"(ra[1]), "+v"(ra[2]), "+v"(ra[3]), "+v"(ra[4]));
    }
    asm volatile("s_waitcnt vmcnt(9)" : "+v"(d[0]), "+v"(d[1]), "+v"(d[2]), "+v"(d[3]), "+v"(d[4]), "+v"(d[5]), "+v"(d[6]),
                 "+v"(sv[0]), "+v"(sv[1]), "+v"(sv[2]), "+v"(sv[3]), "+v"(sv[4]), "+v"(sv[5]), "+v"(sv[6]) :: "memory");
    STAMP(10);
    {
      asm volatile("s_mov_b32 %0, m0\n\t"
                   "s_mov_b32 m0, %8\n\ts_nop 0\n\tglobal_load_lds_dwordx4 %1, %6\n\t"
                   "s_add_u32 m0, %8, 0x400\n\ts_nop 0\n\tglobal_load_lds_dwordx4 %2, %6\n\t"
                   "s_add_u32 m0, %8, 0x800\n\ts_nop 0\n\tglobal_load_lds_dwordx4 %3, %6\n\t"
                   "s_add_u32 m0, %8, 0xc00\n\ts_nop 0\n\tglobal_load_lds_dwordx4 %4, %6\n\t"
                   "s_add_u32 m0, %8, 0x1000\n\ts_nop 0\n\tglobal_load_lds_dword %5, %7\n\ts_mov_b32 m0, %0"
                   : "=&s"(keep)
                   : "v"(ra[0]), "v"(ra[1]), "v"(ra[2]), "v"(ra[3]), "v"(ra[4]), "s"(hmat), "s"(pos), "s"(mi)
                   : "memory", "scc");
    }
    unsigned ent, total;
    auto sort = [&](auto NS_, auto FULL_) __attribute__((always_inline)) {
      constexpr int NS = decltype(NS_)::value;
      constexpr bool FULL = decltype(FULL_)::value;
      unsigned bk[NS], rank[NS];
      u32x2 rec[NS];
#pragma unroll
      for (int i = 0; i < NS; i++) {
        const int el = i * 512 + tid;
        const unsigned b = mul24(d[i], 85599u) >> 24;
        const unsigned dl = d[i] - mul24(b, NB);
        bk[i] = (FULL || el < ne ? b : NBK + lane) * 4;
        rank[i] = __hip_atomic_fetch_add(&LDS(unsigned, L_CNT + bk[i]), 8u, __ATOMIC_RELAXED, __HIP_MEMORY_SCOPE_WORKGROUP);
        rec[i][0] = el;
        rec[i][1] = ((mul24(dl >> 3, 1272u) + dl) << 16) | sv[i] | 0x80000u;
      }
      LDS(unsigned, L_BASE + (NBK + lane) * 4) = 0x40000000u;
      barrier_lds();
      u32x4 cv = LDS(u32x4, L_CNT + lane * 16);
#pragma unroll
      for (int i = 0; i < 4; i++) cv[i] = (cv[i] + 8) & ~15u;
      const unsigned sum = cv[0] + cv[1] + cv[2] + cv[3], incl = wave_scan_incl(sum);
      const unsigned ex = incl - sum + L_REC;
      total = __builtin_amdgcn_readlane(incl, 63);
      const u32x4 bv = {ex, ex + cv[0], ex + cv[0] + cv[1], ex + cv[0] + cv[1] + cv[2]};
      LDS(u32x4, L_BASE + lane * 16) = bv;
      unsigned bs[NS];
#pragma unroll
      for (int i = 0; i < NS; i++) bs[i] = LDS(unsigned, L_BASE + bk[i]);
      ent = ((LDS(unsigned, L_BASE + (tid & 255) * 4) - L_REC) >> 4) | (LDS(unsigned, L_CNT + (tid & 255) * 4) << 9);
      const unsigned lim = L_REC + 8 * SEG + lane * 8;
#pragma unroll
      for (int i = 0; i < NS; i++) LDS(u32x2, FULL ? bs[i] + rank[i] : min(bs[i] + rank[i], lim)) = rec[i];
      barrier_lds();
    };
    if (ne == CH && w != 0) sort(std::integral_constant<int, 6>(), std::true_type());
    else sort(std::integral_constant<int, 7>(), std::false_type());
    LDS(unsigned, L_CNT + tid * 4) = 0;
    STAMP(11);
    tag_lo = (unsigned)(ep + 1) & 0xffu;
    tag_hi = (unsigned)((ep + 1) >> 8);
    {
      const u32x2 g = {ent | (tag_lo << 24), tag_hi};
      __builtin_amdgcn_raw_buffer_store_b64(g, rs_tab, tid < NBK ? bkt * (NBK * 8) + tid * 8 : 0x80000000u, 0, 16);
      const auto rs_out = __builtin_amdgcn_make_buffer_rsrc((void*)(ws + WS_REC + (size_t)bkt * CHS), 0, __builtin_amdgcn_readfirstlane(total), 0x00020000);
#pragma unroll
      for (int i = 0; i < 4; i++) {
        const u32x4 v = LDS(u32x4, L_REC + (i * 512 + tid) * 16);
        const u32x4 o = {v[1], v[3], v[0] | ((v[2] & 0xfffu) << 12) | (tag_lo << 24), tag_hi};
        __builtin_amdgcn_raw_buffer_store_b128(o, rs_out, (i * 512 + tid) * 16, 0, 16);
      }
    }
    STAMP(12);
    asm volatile("s_waitcnt vmcnt(10)" : "+v"(wdv0), "+v"(wdv1), "+v"(xa), "+v"(xb) :: "memory");
    {
      const unsigned tp = wl + (8 * h + ((lane >> 2) & 3)) * 128 + ((lane >> 4) & 1) * 32 + (lane & 3) * 8;
      u32x4 f[2], g[2];
#pragma unroll
      for (int t = 0; t < 2; t++) { f[t] = tr_frag(tp + 64 * t, 512); g[t] = tr_frag(tp + 2048 + 64 * t, 512); }
#pragma unroll
      for (int t = 0; t < 2; t++) {
        if (w < 4) { f[t] = fold(f[t]); g[t] = fold(g[t]); }
        LDS(u32x4, L_WH + (2 * w + t) * 1024 + lane * 16) = f[t];
        LDS(u32x4, wl + 2048 + t * 1024 + lane * 16) = g[t];
      }
      u32x4 x;
      if (w < 2) x = fold(tr_frag(wl + W_STE + (8 * h + ((lane >> 2) & 3)) * 64 + ((lane >> 4) & 1) * 32 + (lane & 3) * 8, 256));
      else {
        const unsigned m = (lane & 3) == 0 && ((lane >> 2) & 1) == ((lane >> 4) & 1) ? ~0u : 0u;
        x = (u32x4){xa[0] & m, xa[1] & m, xb[0] & m, xb[1] & m};
      }
      if (w < 6) LDS(u32x4, L_WJ + w * 1024 + lane * 16) = x;
    }
    if (tid >= 256 && tid < 256 + 26 * 4) {
      const int t = tid - 256, row = t >> 2, sx = (t >> 1) & 1, hh = t & 1;
      const int x = row < DUMMY ? row - 16 * sx - 4 * hh : -1;
      u32x4 v;
      v[0] = x == 0 ? 0x3f80u : x == 1 ? 0x3f800000u : 0u;  v[1] = x == 2 ? 0x3f80u : x == 3 ? 0x3f800000u : 0u;
      v[2] = x == 8 ? 0x3f80u : x == 9 ? 0x3f800000u : 0u;  v[3] = x == 10 ? 0x3f80u : x == 11 ? 0x3f800000u : 0u;
      LDS(u32x4, L_LUT + row * 80 + (t & 3) * 16) = v;
    }
    {
      const u32x4 z4 = {0u, 0u, 0u, 0u};
#pragma unroll
      for (int i = 0; i < 3; i++) { const int o = i * 1024 + lane * 16; if (o < 26 * 80 + 256) LDS(u32x4, wl + W_SI + o) = z4; }
    }
    barrier_lds();
    STAMP(14);
  }

  f32x16 Y0, Y1; f32x4 Yp = {0.f, 0.f, 0.f, 0.f};
#pragma unroll
  for (int i = 0; i < 16; i++) { Y0[i] = 0.f; Y1[i] = 0.f; }
  u32x32 W2; u32x16 PA; u32x8 W3; u32x4 WX0, WX1, WX2, WX3;
  int jbase = 0, part = -1, grp = w, gother = w;

  auto round = [&](bool first) __attribute__((always_inline)) -> bool {
    const int lane = lane_id(), tid = w * 64 + lane, r = lane & 31, h = lane >> 5;
    const unsigned zr = opaque(0);
    if (!first) { LDS(unsigned, L_CNT + tid * 4) = zr; barrier_lds(); }
    LDS(unsigned, L_BASE + lane * 4) = 0x40000000u;
    unsigned tb[4];
    {
      u32x2 t;
      for (int spin = 0;; spin++) {
        asm volatile("buffer_load_dwordx2 %0, %1, %2, %3 offen sc1\n\ts_waitcnt vmcnt(0)"
                     : "=&v"(t) : "v"((w * 32 + r) * (NBK * 8)), "s"(rs_tab), "s"(bkt * 8) : "memory");
        if (__builtin_amdgcn_ballot_w64(t[1] != tag_hi || (t[0] >> 24) != tag_lo) == 0) break;
        if (spin > (1 << 20)) { t[0] = 0; break; }
        __builtin_amdgcn_s_sleep(POLL_SLEEP);
      }
      LDS(unsigned, L_WAVE + w * WSZ + W_STE + r * 4) = t[0];
      const u32x4 t0 = LDS(u32x4, L_WAVE + w * WSZ + W_STE + (lane >> 3) * 16);
#pragma unroll
      for (int p = 0; p < 4; p++) tb[p] = t0[p];
    }
    STAMP(8);
    u32x4 gr[6];
    unsigned ad[6], left[6], eb[6];
    unsigned lmax = 0, nov = 0;
    bool big;
    const int ovmax = part >= 0 ? LG : 2 * LG, cap = 2 * LG + 2 * ovmax;
    {
      const unsigned jj = lane & (LG - 1), c0 = (unsigned)tid / LG * 4, oob = 0x80000000u + zr;
      const unsigned o0 = mul24(c0, CHS) + (jbase / 2 + jj) * 16, eb0 = mul24(c0, CH);
      unsigned off[4], len[4];
#pragma unroll
      for (int p = 0; p < 4; p++) {
        len[p] = (tb[p] >> 12) & 0xfffu;
        if (part >= 0 && part != 4 * p + (w & 3)) len[p] = 0;
        off[p] = o0 + (tb[p] & 0xfffu) * 16 + p * CHS;
        left[p] = len[p] - (jbase + 2 * jj);
        ad[p] = (int)left[p] > 0 ? off[p] : oob;
        eb[p] = eb0 + p * CH;
        asm volatile("buffer_load_dwordx4 %0, %1, %2, 0 offen sc1" : "=v"(gr[p]) : "v"(ad[p]), "s"(rs_rec) : "memory");
        lmax = max(lmax, tb[p] & 0xfff000u);
      }
      asm volatile("" : "+v"(len[0]), "+v"(len[1]), "+v"(len[2]), "+v"(len[3]));
      ad[4] = oob; ad[5] = oob; left[4] = zr; left[5] = zr; eb[4] = zr; eb[5] = zr;
      unsigned ov[4], s1 = 0;
#pragma unroll
      for (int p = 0; p < 4; p++) {
        ov[p] = (unsigned)min(max(((int)len[p] - (jbase + 2 * LG) + 1) >> 1, 0), ovmax);
        const bool sel = jj - nov < ov[p];
        ad[4] = sel ? off[p] + (LG - nov) * 16 : ad[4];
        left[4] = sel ? len[p] - (jbase + 2 * LG) - 2 * (jj - nov) : left[4];
        eb[4] = sel ? eb0 + p * CH : eb[4];
        nov += ov[p];
      }
      asm volatile("buffer_load_dwordx4 %0, %1, %2, 0 offen sc1" : "=v"(gr[4]) : "v"(ad[4]), "s"(rs_rec) : "memory");
      gr[5] = (u32x4){zr, zr, zr, zr};
      big = __builtin_amdgcn_ballot_w64(nov > LG) != 0;
      if (big) {
#pragma unroll
        for (int p = 0; p < 4; p++) {
          const bool sel = jj + LG - s1 < ov[p];
          ad[5] = sel ? off[p] + (2 * LG - s1) * 16 : ad[5];
          left[5] = sel ? len[p] - (jbase + 2 * LG) - 2 * (jj + LG - s1) : left[5];
          eb[5] = sel ? eb0 + p * CH : eb[5];
          s1 += ov[p];
        }
        asm volatile("buffer_load_dwordx4 %0, %1, %2, 0 offen sc1" : "+v"(gr[5]) : "v"(ad[5]), "s"(rs_rec) : "memory");
      }
    }
    unsigned sink = zr;
    STAMP(7);
    u32x4 w1f[8];
    if (first) {
#pragma unroll
      for (int i = 0; i < 8; i++) w1f[i] = LDS(u32x4, L_WAVE + (4 + (i >> 1)) * WSZ + 2048 + (i & 1) * 1024 + lane * 16);
#pragma unroll
      for (int i = 0; i < 8; i++) {
        const u32x4 v = LDS(u32x4, L_WAVE + (i >> 1) * WSZ + 2048 + (i & 1) * 1024 + lane * 16);
#pragma unroll
        for (int j = 0; j < 4; j++) W2[4 * i + j] = v[j];
      }
#pragma unroll
      for (int i = 0; i < 2; i++) {
        const u32x4 v = LDS(u32x4, L_WJ + i * 1024 + lane * 16);
#pragma unroll
        for (int j = 0; j < 4; j++) W3[4 * i + j] = v[j];
      }
      WX0 = LDS(u32x4, L_WJ + 2048 + lane * 16); WX1 = LDS(u32x4, L_WJ + 3072 + lane * 16);
      WX2 = LDS(u32x4, L_WJ + 4096 + lane * 16); WX3 = LDS(u32x4, L_WJ + 5120 + lane * 16);
    }
    STAMP(13);
    if ((lmax >> 12) > (unsigned)(jbase + cap) || nov > 2 * LG) __hip_atomic_fetch_or(&LDS(unsigned, L_FLAG), nov > 2 * LG ? 3u : 1u, __ATOMIC_RELAXED, __HIP_MEMORY_SCOPE_WORKGROUP);
    unsigned mk1 = (lane & 7) >= 1 ? ~zr : zr, mk2 = (lane & 7) >= 2 ? ~zr : zr, sg0 = L_REC + (lane >> 3) * SEG;
    unsigned lim = L_REC + 8 * SEG + lane * 8, own = n0 + min(8 * (r & 3) + (r >> 2), 24) * 8;
    unsigned ce = ((lane & 1) ^ ((lane >> 4) & 1)) * 16, cp = (lane & 1) * 2;
    unsigned cc0 = ((lane & 7) ^ (lane >> 4)) * 16, cc1 = ((lane & 7) ^ (4 + (lane >> 4))) * 16;
    unsigned q_s4 = L_REC + (lane >> 3) * 32 + 4;
    unsigned q_r2 = L_REC + (4 * ((lane >> 1) & 7) + (lane >> 4)) * 8;
    unsigned q_r1 = L_REC + (4 * (r & 7) + (r >> 3)) * 8 + 4;
    asm volatile("" : "+v"(mk1), "+v"(mk2), "+v"(sg0), "+v"(lim), "+v"(own), "+v"(ce), "+v"(cp), "+v"(cc0), "+v"(cc1), "+v"(q_s4),
                 "+v"(q_r2), "+v"(q_r1));
    asm volatile("s_waitcnt vmcnt(0)" : "+v"(gr[0]), "+v"(gr[1]), "+v"(gr[2]), "+v"(gr[3]), "+v"(gr[4]), "+v"(gr[5]) :: "memory");
    for (int spin = 0;; spin++) {
      bool bad = false;
#pragma unroll
      for (int p = 0; p < 6; p++) bad |= ((int)left[p] > 0) & ((gr[p][3] != tag_hi) | ((gr[p][2] >> 24) != tag_lo));
      if (__builtin_amdgcn_ballot_w64(bad) == 0) break;
      if (spin > (1 << 20)) {
#pragma unroll
        for (int p = 0; p < 6; p++) left[p] = zr;
        break;
      }
      asm volatile("buffer_load_dwordx4 %0, %6, %12, 0 offen sc1\n\tbuffer_load_dwordx4 %1, %7, %12, 0 offen sc1\n\t"
                   "buffer_load_dwordx4 %2, %8, %12, 0 offen sc1\n\tbuffer_load_dwordx4 %3, %9, %12, 0 offen sc1\n\t"
                   "buffer_load_dwordx4 %4, %10, %12, 0 offen sc1\n\tbuffer_load_dwordx4 %5, %11, %12, 0 offen sc1\n\ts_waitcnt vmcnt(0)"
                   : "=&v"(gr[0]), "=&v"(gr[1]), "=&v"(gr[2]), "=&v"(gr[3]), "=&v"(gr[4]), "=&v"(gr[5])
                   : "v"(ad[0]), "v"(ad[1]), "v"(ad[2]), "v"(ad[3]), "v"(ad[4]), "v"(ad[5]), "s"(rs_rec) : "memory");
    }
    STAMP(1);
    u32x2 rc[12];
#pragma unroll
    for (int p = 0; p < 6; p++) {
      rc[2 * p] = (u32x2){(eb[p] + (gr[p][2] & 0xfffu)) << 5, (int)left[p] > 0 ? gr[p][0] : 0u};
      rc[2 * p + 1] = (u32x2){(eb[p] + ((gr[p][2] >> 12) & 0xfffu)) << 5, (int)left[p] > 1 ? gr[p][1] : 0u};
    }
    unsigned rk[12], ky[12];
#pragma unroll
    for (int p = 0; p < 12; p++) {
      if (p == 10 && !big) { ky[10] = ky[11] = 0; rk[10] = rk[11] = 0; break; }
      ky[p] = max((rc[p][1] >> 9) & 0x7fcu, (unsigned)lane * 4);
      rk[p] = __hip_atomic_fetch_add(&LDS(unsigned, L_CNT + ky[p]), 8u + zr, __ATOMIC_RELAXED, __HIP_MEMORY_SCOPE_WORKGROUP);
    }
    if (EPF) {
#pragma unroll
      for (int p = 0; p < 10; p++) {
        const unsigned a = (ky[p] & 0x47cu) - 0x400u < (unsigned)EPF * 4 ? rc[p][0] : 0x80000000u + zr;
        asm volatile("buffer_load_dword %0, %1, %2, 0 offen" : "+{v3}"(sink) : "v"(a), "s"(rs_ea) : "memory");
      }
    }
    barrier_lds();
    STAMP(2);
    unsigned g;
    const unsigned flagv = LDS(unsigned, L_FLAG);
    {
      const u32x4 cv = LDS(u32x4, L_CNT + 1024 + lane * 16);
      const unsigned sum = cv[0] + cv[1] + cv[2] + cv[3];
      g = sum + (__builtin_amdgcn_update_dpp(0, (int)sum, 0x111, 0xf, 0xf, true) & mk1);
      g += __builtin_amdgcn_update_dpp(0, (int)g, 0x112, 0xf, 0xf, true) & mk2;
      g += __builtin_amdgcn_update_dpp(0, (int)g, 0x114, 0xf, 0xa, false);
      const unsigned ex = g - sum + sg0;
      const u32x4 bv = {ex, ex + cv[0], ex + cv[0] + cv[1], ex + cv[0] + cv[1] + cv[2]};
      LDS(u32x4, L_BASE + 1024 + lane * 16) = bv;
    }
    unsigned bs[10];
#pragma unroll
    for (int p = 0; p < 10; p++) bs[p] = LDS(unsigned, L_BASE + ky[p]);
    const unsigned flag = __builtin_amdgcn_readfirstlane(flagv);
    const bool again = flag & 1;
    const bool over = (flag & 2) || __builtin_amdgcn_ballot_w64(g > (unsigned)(SEG - 256)) != 0;
    if (!over) {
#pragma unroll
      for (int p = 0; p < 10; p++) LDS(u32x2, min(bs[p] + rk[p], lim)) = rc[p];
      if (big) {
        LDS(u32x2, min(LDS(unsigned, L_BASE + ky[10]) + rk[10], lim)) = rc[10];
        LDS(u32x2, min(LDS(unsigned, L_BASE + ky[11]) + rk[11], lim)) = rc[11];
      }
    }
    if (first) {
      const unsigned key = (g << 3) | (lane >> 3);
      unsigned ks[8], rank = 0;
#pragma unroll
      for (int k = 0; k < 8; k++) ks[k] = __builtin_amdgcn_readlane(key, 8 * k + 7);
#pragma unroll
      for (int k = 0; k < 8; k++) rank += ks[k] > key;
      const unsigned long long sel = 0x8080808080808080ull;
      grp = __builtin_ctzll(__builtin_amdgcn_ballot_w64(rank == (unsigned)(w < 4 ? w : 11 - w)) & sel) >> 3;
      gother = __builtin_ctzll(__builtin_amdgcn_ballot_w64(rank == (unsigned)(w < 4 ? 7 - w : w - 4)) & sel) >> 3;
      wl = L_WAVE + grp * WSZ; mi = L_MI + grp * MISZ;
    }
    unsigned segn = over ? 0u : (unsigned)__builtin_amdgcn_readlane(g, 8 * grp + 7);
    const unsigned seg0 = grp * SEG;
    const int nt = (segn + 255) >> 8;
    {
      const u32x2 pad = {zr, DUMMY * (80u << 20)}, row = {zr, min(own + grp, (unsigned)N - 1) | (DUMMY * (80u << 20))};
      LDS(u32x2, L_REC + seg0 + segn + r * 8) = pad;
      LDS(u32x2, L_REC + seg0 + nt * 256 + r * 8) = row;
    }
    if (lane == 0) LDS(unsigned, wl + W_SI + DUMMY * 80 + 76) = nt;
    barrier_lds();
    STAMP(3);

    const unsigned o_s4 = q_s4 + seg0, o_r2 = q_r2 + seg0, o_r1 = q_r1 + seg0;
    {
      unsigned ns[4];
#pragma unroll
      for (int i = 0; i < 4; i++) ns[i] = LDS(uint16_t, o_s4 + i * 8);
      const u32x2 nr2 = LDS(u32x2, o_r2);
      dma_tile(hmat, ea, pos, ns[0] * 128 + cc0, ns[1] * 128 + cc1, ns[2] * 128 + cc0, ns[3] * 128 + cc1,
               nr2[0] | ce, (nr2[1] & 0xffffu) * 6 + cp, wl);
    }
    if (first) {
      u32x2 hb[8];
#pragma unroll
      for (int i = 0; i < 8; i++) hb[i] = LDS(u32x2, mi + r * 128 + ((i ^ ((r >> 1) & 7)) * 16) + h * 8);
      const u32x2 pg = LDS(u32x2, mi + 4096 + r * 8);
#pragma unroll
      for (int t = 0; t < 2; t++) {
        f32x16 P;
#pragma unroll
        for (int i = 0; i < 16; i++) P[i] = 0.f;
#pragma unroll
        for (int s = 0; s < 4; s++) {
          const u32x4 a = {hb[2 * s][0], hb[2 * s][1], hb[2 * s + 1][0], hb[2 * s + 1][1]};
          P = MFMA(as_bf(a), as_bf(w1f[2 * s + t]), P);
        }
        if (h) P[15] = bf2f(t ? wdv1 : wdv0);
#pragma unroll
        for (int i = 0; i < 16; i++) P[i] *= -LOG2E;
#pragma unroll
        for (int s = 0; s < 2; s++)
#pragma unroll
          for (int j = 0; j < 4; j++) PA[4 * (2 * s + t) + j] = pk_bf16(P[8 * s + 2 * j], P[8 * s + 2 * j + 1]);
      }
      const f32x4 pv = {bf2f(pg[0] & 0xffffu), __uint_as_float(pg[0] & 0xffff0000u), __uint_as_float(pg[1] & 0xffff0000u), 0.f};
      if (lane < DUMMY) LDS(f32x4, wl + W_SI + lane * 80 + 64) = pv;
      if (bkt == 0 && tid == 0) {
        const u32x2 e1 = {tag_lo | (tag_hi << 8), tag_hi >> 24};
        asm volatile("global_store_dwordx2 %0, %1, %2 sc1" :: "v"(zr), "v"(e1), "s"(ws + WS_EPOCH) : "memory");
      }
    }
    const bool last_round = !over && !again && (part < 0 || part == 15);
    STAMP(4);

    {
      u32x16 A0; u32x8 A1;
      A0[0] = zr; A0[1] = zr;
#pragma unroll
      for (int s = 0; s < 4; s++) A0[2 + s] = wl + W_STH + r * 128 + (((2 * s + h) ^ ((r >> 1) & 7)) * 16);
      A0[6] = wl + r * 32 + ((h ^ ((r >> 3) & 1)) * 16);
      A0[7] = wl + r * 8;
      A0[8] = L_LUT + h * 16;
      A0[9] = wl + r * 2;
      A0[10] = wl + min(r, DUMMY) * 80 + h * 16;
      A0[11] = mi + r * MIL + h * 32;
      A0[12] = mi + (8 * h + ((lane >> 2) & 3)) * MIL + (4 * ((lane >> 4) & 1) + (lane & 3)) * 8;
      A0[13] = wl + h * 64 + r * 2;
      {
        const int row = (lane & 15) - 4 * ((lane >> 4) & 1);
        A0[14] = wl + ((unsigned)row < 3 ? row : 3) * 64 + h * 16;
      }
      A0[15] = L_WJ + lane * 16;
      A1[0] = cc0; A1[1] = cc1; A1[2] = ce; A1[3] = cp;
      A1[4] = o_s4 + 256; A1[5] = o_r2 + 256; A1[6] = o_r1;
      A1[7] = 0x3f800000u + zr;
      unsigned cnt = nt, keep;
      const int lastv = __builtin_amdgcn_readfirstlane(opaque(last_round ? 1 : 0));
      const int thr = w >> 2;
      u32x2 A2 = {wl + W_SI + DUMMY * 80 + 76, (unsigned)(L_WAVE + gother * WSZ + W_SI + DUMMY * 80 + 76)};
      unsigned tmp;
      asm volatile(LOOP_ASM
                   : "+{v[52:67]}"(Y0), "+{v[68:83]}"(Y1), "+{v[84:87]}"(Yp), "+{v[232:247]}"(A0), "+{v[248:255]}"(A1), "+{v3}"(sink),
                     "+{v[212:213]}"(A2), [tmp] "=&s"(tmp),
                     [nt] "+s"(cnt), [keep] "=&s"(keep)
                   : "{v[4:35]}"(W2), "{v[36:51]}"(PA), "{v[88:95]}"(W3), "{v[96:99]}"(WX0), "{v[136:139]}"(WX1), "{v[140:143]}"(WX2), "{v[144:147]}"(WX3),
                     [h0] "s"(hmat), [h1] "s"(hmat - 1024), [h2] "s"(hmat - 2048), [h3] "s"(hmat - 3072), [ea] "s"(ea), [pos] "s"(pos - 1024),
                     [b1] "s"(wl), [b2] "s"(wl + W_STE), [posd] "s"(wl + W_SI + 64), [c128] "s"(128u), [thr] "s"(thr), [last] "s"(lastv),
                     [hsel] "s"(0xffffffff00000000ull)
                   : "memory", "scc", "vcc", CLOBBER_V);
    }
    if (over) part = 0;
    else if (part >= 0 && part < 15) part++;
    else { part = -1; jbase += cap; }
    return last_round;
  };
  if (!round(true)) {
    do {
      wait_vm0();
      barrier_lds();
      jbase = __builtin_amdgcn_readfirstlane(jbase); part = __builtin_amdgcn_readfirstlane(part);
    } while (!round(false));
  }
  STAMP(5);

  {
    const int lane = lane_id(), r = lane & 31, h = lane >> 5;
    Yp[0] *= -LN2; Yp[1] *= -LN2; Yp[2] *= -LN2;
    const auto rs_o = __builtin_amdgcn_make_buffer_rsrc((void*)out, 0, N * 128, 0x00020000);
#pragma unroll
    for (int i = 0; i < 4; i++) {
      const int q = 8 * i + (lane >> 3), dl = q * 8 + grp;
      const u32x4 v = LDS(u32x4, mi + q * MIP + (lane & 7) * 16);
      if (dl < NB) __builtin_amdgcn_raw_buffer_store_b128(v, rs_o, (n0 + dl) * 128 + (lane & 7) * 16, 0, OUT_AUX);
    }
    const int dl = r * 8 + grp;
    if (h == 0 && dl < NB && n0 + dl < N) {
      const f32x4 pd = LDS(f32x4, wl + W_SI + r * 80 + 64);
      uint16_t* po = out + (size_t)N * 64 + (size_t)(n0 + dl) * 3;
      const unsigned xy = pk_bf16(pd[0] + Yp[0], pd[1] + Yp[1]), zz = pk_bf16(pd[2] + Yp[2], 0.f);
      po[0] = (uint16_t)xy; po[1] = (uint16_t)(xy >> 16); po[2] = (uint16_t)zz;
    }
  }
  STAMP(6);
}

}

#define STAMP_PASS1
#define STAMP_PASS2

extern "C" void kernel_launch(void* const* d_in, const int* in_sizes, int n_in, void* d_out, int out_size,
                              void* d_ws, size_t ws_size, hipStream_t stream) {
  const int N = in_sizes[0] / 64, E = in_sizes[3];
  if (N > NBK * NB || E > NBK * CH || N < 1 || E < 1) return;
  (void)hipFuncSetAttribute((const void*)egnn_kernel, hipFuncAttributeMaxDynamicSharedMemorySize, L_TOTAL);
  egnn_kernel<<<NBK, 512, L_TOTAL, stream>>>((const char*)d_in[0], (const char*)d_in[1], (const char*)d_in[2], (const int*)d_in[3],
                                             (const int*)d_in[4], (const char*)d_in[5], (const char*)d_in[6], (const char*)d_in[7],
                                             (char*)d_ws, (uint16_t*)d_out, N, E STAMP_PASS2);
}
```
